```python
import torch

def _softmax(x, axis=-1):
    return torch.softmax(x, dim=axis)
def _gelu(x):
    return torch.nn.functional.gelu(x, approximate="tanh")
def _silu(x):
    return torch.nn.functional.silu(x)
def _sigmoid(x):
    return torch.sigmoid(x)
def _layernorm(x, axis=-1, eps=1e-5):
    m = x.mean(dim=axis, keepdim=True)
    v = x.var(dim=axis, keepdim=True, unbiased=False)
    return (x - m) / torch.sqrt(v + eps)
def _scatter_add(tgt, idx, src, axis=0):
    return tgt.clone().index_add_(axis, idx, src)
def _segment_sum(src, seg, n):
    out = torch.zeros((n,) + src.shape[1:], dtype=src.dtype)
    return out.index_add_(0, seg, src)
def _pad(x, pw):
    flat = tuple(v for pair in reversed(pw) for v in pair)
    return torch.nn.functional.pad(x, flat)

kernel_name = 'recurrent_scan__linear_kernel_hedgehog__dense_activation_gelu__prompt'

def setup_inputs():

    _w = (lambda a: (lambda t: t.to(torch.bfloat16) if t.is_floating_point() else t)(torch.as_tensor(a)))
    def _A_setup():
        B, S, D, H = 1, 2048, 576, 32
        Dh = D // H
        return {
            "x": _w((lambda *s: torch.randn(*s, dtype=torch.bfloat16) * 0.05)(B, S, D)),
            "wqkv": _w((lambda *s: torch.randn(*s, dtype=torch.bfloat16) * 0.4)(D, 3 * D)),
            "w_phi": _w((lambda *s: torch.randn(*s, dtype=torch.bfloat16) * 0.4)(H, Dh, Dh) * (Dh ** -0.5)),
            "wo": _w((lambda *s: torch.randn(*s, dtype=torch.bfloat16) * 0.2)(D, D)),
        }
    _A_in = _A_setup()

    def _B_setup():
        B, S, D, F = 1, 2048, 576, 4608
        return {
            "x": _w((lambda *s: torch.randn(*s, dtype=torch.bfloat16) * 0.05)(B, S, D)),
            "w1": _w((lambda *s: torch.randn(*s, dtype=torch.bfloat16) * 0.4)(D, F) * (D ** -0.5)),
            "b1": _w((lambda *s: torch.randn(*s, dtype=torch.bfloat16) * 12.8)(F) * 0.01),
            "w2": _w((lambda *s: torch.randn(*s, dtype=torch.bfloat16) * 0.1)(F, D) * (F ** -0.5)),
            "b2": _w((lambda *s: torch.randn(*s, dtype=torch.bfloat16) * 0.8)(D) * 0.01),
        }
    _B_in = _B_setup()

    return {
        "A_x": _A_in["x"],
        "A_wqkv": _A_in["wqkv"],
        "A_w_phi": _A_in["w_phi"],
        "A_wo": _A_in["wo"],
        "B_x": _B_in["x"],
        "B_w1": _B_in["w1"],
        "B_b1": _B_in["b1"],
        "B_w2": _B_in["w2"],
        "B_b2": _B_in["b2"],
    }

def reference(A_x, A_wqkv, A_w_phi, A_wo, B_x, B_w1, B_b1, B_w2, B_b2):
    def _A_ref(x, wqkv, w_phi, wo):
        B, S, D = x.shape
        H = 32; Dh = D // H
        qkv = (lambda x, *d: x.permute(*d))((x @ wqkv).reshape(B, S, 3, H, Dh), 2, 0, 3, 1, 4)
        q, k, v = qkv[0], qkv[1], qkv[2]
        pq = torch.einsum("bhsd,hdf->bhsf", q, w_phi)
        pk = torch.einsum("bhsd,hdf->bhsf", k, w_phi)
        phi_q = torch.concatenate([_softmax(pq, axis=-1), _softmax(-pq, axis=-1)], axis=-1)
        phi_k = torch.concatenate([_softmax(pk, axis=-1), _softmax(-pk, axis=-1)], axis=-1)
        kv = torch.einsum("bhsf,bhsd->bhfd", phi_k, v)
        z  = torch.einsum("bhsf->bhf", phi_k)
        num = torch.einsum("bhqf,bhfd->bhqd", phi_q, kv)
        den = torch.einsum("bhqf,bhf->bhq", phi_q, z)[..., None] + 1e-6
        o = num / den
        return (lambda x, *d: x.permute(*d))(o, 0, 2, 1, 3).reshape(B, S, D) @ wo

    def _B_ref(x, w1, b1, w2, b2):
        h = torch.einsum("bsd,df->bsf", x, w1) + b1
        h = _gelu(h)
        return torch.einsum("bsf,fd->bsd", h, w2) + b2

    x = A_x
    xn = _layernorm(x, axis=-1)
    s = _A_ref(xn, A_wqkv, A_w_phi, A_wo)
    g = _silu(_B_ref(xn, B_w1, B_b1, B_w2, B_b2))
    return x + s * g
```

```cpp
#include <hip/hip_runtime.h>
#include <hip/hip_bf16.h>
#include <stdint.h>

constexpr int S_ = 2048, D_ = 576, H_ = 32, DH_ = 18, F_ = 4608, NQKV_ = 1728;
constexpr int KPH_ = 40;
constexpr int KPHI_ = H_ * KPH_;
constexpr int SPLITK_ = 4;

typedef short bf16x8 __attribute__((ext_vector_type(8)));
typedef short s16x4 __attribute__((ext_vector_type(4)));
typedef float f32x4 __attribute__((ext_vector_type(4)));
typedef unsigned u32x4 __attribute__((ext_vector_type(4)));
typedef unsigned u32x2 __attribute__((ext_vector_type(2)));
typedef __attribute__((address_space(3))) s16x4* lds_s16x4_p;
typedef unsigned short u16;

__device__ __forceinline__ float bf2f(u16 v) { return __uint_as_float(((unsigned)v) << 16); }
__device__ __forceinline__ u16 f2bf(float f) {
  __hip_bfloat16 b = __float2bfloat16(f);
  return *reinterpret_cast<u16*>(&b);
}
__device__ __forceinline__ unsigned pack2(float lo, float hi) {
  return (unsigned)f2bf(lo) | ((unsigned)f2bf(hi) << 16);
}
__device__ __forceinline__ float rbf(float f) { return bf2f(f2bf(f)); }

__device__ __forceinline__ void glds16(const void* g, void* l) {
  __builtin_amdgcn_global_load_lds((const unsigned*)g, (unsigned*)l, 16, 0, 0);
}

__device__ __forceinline__ bf16x8 swap_halves_if(bf16x8 v, bool odd) {
  bf16x8 s = __builtin_shufflevector(v, v, 4, 5, 6, 7, 0, 1, 2, 3);
  return odd ? s : v;
}

template <int RB>
__device__ __forceinline__ bf16x8 wfrag(const char* img, int o0, int o1, int off) {
  s16x4 a = __builtin_amdgcn_ds_read_tr16_b64_v4i16((lds_s16x4_p)(img + o0 + off));
  s16x4 b = __builtin_amdgcn_ds_read_tr16_b64_v4i16((lds_s16x4_p)(img + o1 + off));
  return __builtin_shufflevector(a, b, 0, 1, 2, 3, 4, 5, 6, 7);
}

struct G1Src {
  const u16* wqkv;
  const u16* w1;
};

struct G1Stage {
  const u16* p[3];
  int st[3];
};

template <int NT>
__device__ __forceinline__ void g1_setup(const G1Src& src, int tile0, int wid, int lane, G1Stage& sg) {
  constexpr int NCH = 64 * NT * 2, NI = NCH / 64, PER = (NI + 3) / 4;
#pragma unroll
  for (int i = 0; i < PER; ++i) {
    int ins = wid + 4 * i;
    if (ins >= NI) ins -= NI;
    int c = ins * 64 + lane;
    int row = c / (2 * NT), cc = c % (2 * NT);
    int col = tile0 * 16 + cc * 8;
    bool isq = col < NQKV_;
    sg.p[i] = isq ? (src.wqkv + (size_t)row * NQKV_ + col) : (src.w1 + (size_t)row * F_ + (col - NQKV_));
    sg.st[i] = isq ? 64 * NQKV_ : 64 * F_;
  }
}

template <int NT>
__device__ __forceinline__ void g1_issue(G1Stage& sg, char* buf, int wid, int lane) {
  constexpr int NCH = 64 * NT * 2, NI = NCH / 64, PER = (NI + 3) / 4;
#pragma unroll
  for (int i = 0; i < PER; ++i) {
    int ins = wid + 4 * i;
    if (ins >= NI) ins -= NI;
    glds16(sg.p[i], buf + (ins * 64 + lane) * 16);
    sg.p[i] += sg.st[i];
  }
}

template <int NT, int NTN>
__device__ __forceinline__ void g1_step(const G1Src& src, G1Stage& sg, int tile0, int tileN, bool hasN, char* smem, int& bufsel,
                                        const bf16x8 (&xa)[2][18], int wid, int lane, int m0,
                                        u16* __restrict__ qkv, u16* __restrict__ hbuf, const u16* __restrict__ b1) {
  constexpr int RB = NT * 32;
  constexpr int BUFB = 64 * 5 * 32;
  const int g = lane >> 4, q = (lane & 15) >> 2, p = lane & 3;
  const int o0 = (8 * g + 4 * (g & 1) + q) * RB + p * 8;
  const int o1 = (8 * g + 4 * (1 - (g & 1)) + q) * RB + p * 8;
  f32x4 acc[2][NT];
#pragma unroll
  for (int r = 0; r < 2; ++r)
#pragma unroll
    for (int n = 0; n < NT; ++n) acc[r][n] = (f32x4){0.f, 0.f, 0.f, 0.f};

#pragma unroll
  for (int kc = 0; kc < 9; ++kc) {
    __syncthreads();
    char* cur = smem + bufsel * BUFB;
    char* nxt = smem + (bufsel ^ 1) * BUFB;
    if (kc < 8) g1_issue<NT>(sg, nxt, wid, lane);
    else if (hasN) { g1_setup<NTN>(src, tileN, wid, lane, sg); g1_issue<NTN>(sg, nxt, wid, lane); }
#pragma unroll
    for (int ks = 0; ks < 2; ++ks) {
#pragma unroll
      for (int n = 0; n < NT; ++n) {
        bf16x8 w = wfrag<RB>(cur, o0, o1, ks * 32 * RB + n * 32);
#pragma unroll
        for (int r = 0; r < 2; ++r)
          acc[r][n] = __builtin_amdgcn_mfma_f32_16x16x32_bf16(w, xa[r][kc * 2 + ks], acc[r][n], 0, 0, 0);
      }
    }
    bufsel ^= 1;
  }
#pragma unroll
  for (int n = 0; n < NT; ++n) {
    const int tile = tile0 + n;
    const int ncol = tile * 16 + 4 * g;
    if (tile < NQKV_ / 16) {
#pragma unroll
      for (int r = 0; r < 2; ++r) {
        const int m = m0 + wid * 32 + r * 16 + (lane & 15);
        u32x2 v = {pack2(acc[r][n][0], acc[r][n][1]), pack2(acc[r][n][2], acc[r][n][3])};
        *reinterpret_cast<u32x2*>(qkv + (size_t)m * NQKV_ + ncol) = v;
      }
    } else {
      const int fcol = ncol - NQKV_;
      u32x2 bb = *reinterpret_cast<const u32x2*>(b1 + fcol);
      float bv[4] = {bf2f((u16)(bb[0] & 0xffff)), bf2f((u16)(bb[0] >> 16)), bf2f((u16)(bb[1] & 0xffff)), bf2f((u16)(bb[1] >> 16))};
#pragma unroll
      for (int r = 0; r < 2; ++r) {
        const int m = m0 + wid * 32 + r * 16 + (lane & 15);
        float o[4];
#pragma unroll
        for (int j = 0; j < 4; ++j) {
          float v = acc[r][n][j] + bv[j];
          float u = 0.7978845608028654f * (v + 0.044715f * v * v * v);
          float e = __expf(-2.f * u);
          o[j] = v / (1.f + e);
        }
        u32x2 v = {pack2(o[0], o[1]), pack2(o[2], o[3])};
        *reinterpret_cast<u32x2*>(hbuf + (size_t)m * F_ + fcol) = v;
      }
    }
  }
}

__global__ __launch_bounds__(256, 2) void k1_gemm1(const u16* __restrict__ x, const u16* __restrict__ wqkv,
                                                   const u16* __restrict__ w1, const u16* __restrict__ b1,
                                                   u16* __restrict__ qkv, u16* __restrict__ hbuf) {
  __shared__ __attribute__((aligned(16))) char smem[2 * 64 * 5 * 32];
  const int tid = threadIdx.x, wid = tid >> 6, lane = tid & 63;
  const int bid = blockIdx.x;
  const int mt = bid >> 5, grp = bid & 31;
  const int m0 = mt * 128;
  int tile0 = (grp * 396) >> 5;
  if (tile0 > 396 - 13) tile0 = 396 - 13;
  G1Src src{wqkv, w1};
  int bufsel = 0;
  G1Stage sg;
  g1_setup<5>(src, tile0, wid, lane, sg);
  g1_issue<5>(sg, smem, wid, lane);

  const int g = lane >> 4;
  const bool odd = g & 1;
  bf16x8 xa[2][18];
#pragma unroll
  for (int r = 0; r < 2; ++r) {
    const int m = m0 + wid * 32 + r * 16 + (lane & 15);
    const u16* xp = x + (size_t)m * D_ + 8 * g;
#pragma unroll
    for (int ks = 0; ks < 18; ++ks) xa[r][ks] = *reinterpret_cast<const bf16x8*>(xp + ks * 32);
  }
#pragma unroll
  for (int r = 0; r < 2; ++r) {
    float s = 0.f;
#pragma unroll
    for (int ks = 0; ks < 18; ++ks) {
      asm volatile("" : "+v"(xa[r][ks]), "+v"(s));
#pragma unroll
      for (int j = 0; j < 8; ++j) s += bf2f((u16)xa[r][ks][j]);
    }
    s += __shfl_xor(s, 16);
    s += __shfl_xor(s, 32);
    const float mean = s * (1.f / D_);
    float ss = 0.f;
#pragma unroll
    for (int ks = 0; ks < 18; ++ks) {
      asm volatile("" : "+v"(xa[r][ks]), "+v"(ss));
#pragma unroll
      for (int j = 0; j < 8; ++j) {
        float d = bf2f((u16)xa[r][ks][j]) - mean;
        ss += d * d;
      }
    }
    ss += __shfl_xor(ss, 16);
    ss += __shfl_xor(ss, 32);
    const float rstd = rsqrtf(ss * (1.f / D_) + 1e-5f);
#pragma unroll
    for (int ks = 0; ks < 18; ++ks) {
      asm volatile("" : "+v"(xa[r][ks]));
      u32x4 pk;
#pragma unroll
      for (int j = 0; j < 4; ++j) {
        float a = (bf2f((u16)xa[r][ks][2 * j]) - mean) * rstd;
        float b = (bf2f((u16)xa[r][ks][2 * j + 1]) - mean) * rstd;
        pk[j] = pack2(a, b);
      }
      xa[r][ks] = swap_halves_if(*reinterpret_cast<bf16x8*>(&pk), odd);
      asm volatile("" : "+v"(xa[r][ks]));
    }
  }
  g1_step<5, 5>(src, sg, tile0, tile0 + 5, true, smem, bufsel, xa, wid, lane, m0, qkv, hbuf, b1);
  g1_step<5, 3>(src, sg, tile0 + 5, tile0 + 10, true, smem, bufsel, xa, wid, lane, m0, qkv, hbuf, b1);
  g1_step<3, 3>(src, sg, tile0 + 10, tile0 + 10, false, smem, bufsel, xa, wid, lane, m0, qkv, hbuf, b1);
}

constexpr int G2_NT = 9, G2_RB = G2_NT * 32, G2_BUFB = 64 * G2_RB;

__device__ __forceinline__ void g2_issue(const u16* __restrict__ w, int ldw, int n0, int krow0, char* buf, int wid, int lane) {
  constexpr int NCH = 64 * G2_NT * 2, NI = NCH / 64, PER = (NI + 3) / 4;
#pragma unroll
  for (int i = 0; i < PER; ++i) {
    int ins = wid + 4 * i;
    if (ins >= NI) ins -= NI;
    int c = ins * 64 + lane;
    int row = c / (2 * G2_NT), cc = c % (2 * G2_NT);
    glds16(w + (size_t)(krow0 + row) * ldw + n0 + cc * 8, buf + c * 16);
  }
}

__device__ void head_kv_block(int h, const u16* __restrict__ qkv, const u16* __restrict__ wphi, const u16* __restrict__ wo,
                              u16* __restrict__ mcat, float* __restrict__ zbuf, char* smem) {
  float* wph = reinterpret_cast<float*>(smem);
  u16* phiT = reinterpret_cast<u16*>(smem + 1440);
  u16* vT = reinterpret_cast<u16*>(smem + 1440 + 25344);
  float* kvs = reinterpret_cast<float*>(smem + 1440 + 25344 + 16896);
  const int tid = threadIdx.x, wid = tid >> 6, lane = tid & 63;
  for (int i = tid; i < 18 * 20; i += 256) {
    int d = i / 20, f = i % 20;
    wph[i] = (f < 18) ? bf2f(wphi[(h * 18 + d) * 18 + f]) : 0.f;
  }
  for (int i = tid; i < 12 * 264; i += 256) phiT[36 * 264 + i] = 0;
  for (int i = tid; i < 14 * 264; i += 256) vT[18 * 264 + i] = (i < 264) ? (u16)0x3f80 : (u16)0;
  __syncthreads();
  f32x4 acc[3][2];
#pragma unroll
  for (int a = 0; a < 3; ++a)
#pragma unroll
    for (int b = 0; b < 2; ++b) acc[a][b] = (f32x4){0.f, 0.f, 0.f, 0.f};
#pragma unroll 1
  for (int ch = 0; ch < 8; ++ch) {
    asm volatile("" ::: "memory");
    const int row = ch * 256 + tid;
    const u16* kp = qkv + (size_t)row * NQKV_ + D_ + h * 18;
    const u16* vp = qkv + (size_t)row * NQKV_ + 2 * D_ + h * 18;
    float kk[18];
#pragma unroll
    for (int d = 0; d < 9; ++d) {
      unsigned t = *reinterpret_cast<const unsigned*>(kp + 2 * d);
      kk[2 * d] = bf2f((u16)(t & 0xffff));
      kk[2 * d + 1] = bf2f((u16)(t >> 16));
    }
    u16 vv[18];
#pragma unroll
    for (int d = 0; d < 9; ++d) {
      unsigned t = *reinterpret_cast<const unsigned*>(vp + 2 * d);
      vv[2 * d] = (u16)(t & 0xffff);
      vv[2 * d + 1] = (u16)(t >> 16);
    }
    float pk[20];
#pragma unroll
    for (int f = 0; f < 20; ++f) pk[f] = 0.f;
#pragma unroll
    for (int d = 0; d < 18; ++d) {
#pragma unroll
      for (int f4 = 0; f4 < 5; ++f4) {
        f32x4 w = *reinterpret_cast<const f32x4*>(wph + d * 20 + f4 * 4);
#pragma unroll
        for (int j = 0; j < 4; ++j) pk[f4 * 4 + j] += kk[d] * w[j];
      }
    }
    float mx = -1e30f, mn = 1e30f;
#pragma unroll
    for (int f = 0; f < 18; ++f) {
      pk[f] = rbf(pk[f]);
      mx = fmaxf(mx, pk[f]);
      mn = fminf(mn, pk[f]);
    }
    float ep[18], en[18], sp = 0.f, sn = 0.f;
#pragma unroll
    for (int f = 0; f < 18; ++f) {
      ep[f] = __expf(pk[f] - mx);
      en[f] = __expf(mn - pk[f]);
      sp += ep[f];
      sn += en[f];
    }
    const float rp = 1.f / sp, rn = 1.f / sn;
    __syncthreads();
#pragma unroll
    for (int f = 0; f < 18; ++f) {
      phiT[f * 264 + tid] = f2bf(ep[f] * rp);
      phiT[(18 + f) * 264 + tid] = f2bf(en[f] * rn);
      vT[f * 264 + tid] = vv[f];
    }
    __syncthreads();
#pragma unroll
    for (int ks = 0; ks < 2; ++ks) {
      const int s0 = wid * 64 + ks * 32 + 8 * (lane >> 4);
      bf16x8 bv[2];
#pragma unroll
      for (int b = 0; b < 2; ++b) bv[b] = *reinterpret_cast<const bf16x8*>(vT + (b * 16 + (lane & 15)) * 264 + s0);
#pragma unroll
      for (int a = 0; a < 3; ++a) {
        bf16x8 av = *reinterpret_cast<const bf16x8*>(phiT + (a * 16 + (lane & 15)) * 264 + s0);
#pragma unroll
        for (int b = 0; b < 2; ++b) acc[a][b] = __builtin_amdgcn_mfma_f32_16x16x32_bf16(av, bv[b], acc[a][b], 0, 0, 0);
      }
    }
  }
#pragma unroll
  for (int a = 0; a < 3; ++a)
#pragma unroll
    for (int b = 0; b < 2; ++b)
#pragma unroll
      for (int j = 0; j < 4; ++j)
        kvs[(wid * 48 + a * 16 + (lane >> 4) * 4 + j) * 32 + b * 16 + (lane & 15)] = acc[a][b][j];
  __syncthreads();
  for (int i = tid; i < 48 * 32; i += 256) {
    float v = kvs[i] + kvs[48 * 32 + i] + kvs[2 * 48 * 32 + i] + kvs[3 * 48 * 32 + i];
    kvs[i] = rbf(v);
  }
  __syncthreads();
  if (tid < 36) zbuf[h * 36 + tid] = kvs[tid * 32 + 18];
  {
    float w[3][18];
#pragma unroll
    for (int c = 0; c < 3; ++c) {
      const int n = tid + 256 * c;
#pragma unroll
      for (int d = 0; d < 18; ++d) w[c][d] = (n < D_) ? bf2f(wo[(size_t)(h * 18 + d) * D_ + n]) : 0.f;
    }
#pragma unroll 1
    for (int f = 0; f < KPH_; ++f) {
      float a[3] = {0.f, 0.f, 0.f};
      if (f < 36) {
#pragma unroll
        for (int d = 0; d < 18; ++d) {
          const float kvv = kvs[f * 32 + d];
#pragma unroll
          for (int c = 0; c < 3; ++c) a[c] += kvv * w[c][d];
        }
      }
#pragma unroll
      for (int c = 0; c < 3; ++c) {
        const int n = tid + 256 * c;
        if (n < D_) mcat[(size_t)(h * KPH_ + f) * D_ + n] = f2bf(a[c]);
      }
    }
  }
}

__device__ void head_q_block(int h, const u16* __restrict__ qkv, const u16* __restrict__ wphi, u16* __restrict__ phi, char* smem) {
  float* wph = reinterpret_cast<float*>(smem);
  const int tid = threadIdx.x;
  for (int i = tid; i < 18 * 20; i += 256) {
    int d = i / 20, f = i % 20;
    wph[i] = (f < 18) ? bf2f(wphi[(h * 18 + d) * 18 + f]) : 0.f;
  }
  __syncthreads();
#pragma unroll 1
  for (int ch = 0; ch < 8; ++ch) {
    asm volatile("" ::: "memory");
    const int row = ch * 256 + tid;
    const u16* kp = qkv + (size_t)row * NQKV_ + h * 18;
    float kk[18];
#pragma unroll
    for (int d = 0; d < 9; ++d) {
      unsigned t = *reinterpret_cast<const unsigned*>(kp + 2 * d);
      kk[2 * d] = bf2f((u16)(t & 0xffff));
      kk[2 * d + 1] = bf2f((u16)(t >> 16));
    }
    float pk[20];
#pragma unroll
    for (int f = 0; f < 20; ++f) pk[f] = 0.f;
#pragma unroll
    for (int d = 0; d < 18; ++d) {
#pragma unroll
      for (int f4 = 0; f4 < 5; ++f4) {
        f32x4 w = *reinterpret_cast<const f32x4*>(wph + d * 20 + f4 * 4);
#pragma unroll
        for (int j = 0; j < 4; ++j) pk[f4 * 4 + j] += kk[d] * w[j];
      }
    }
    float mx = -1e30f, mn = 1e30f;
#pragma unroll
    for (int f = 0; f < 18; ++f) {
      pk[f] = rbf(pk[f]);
      mx = fmaxf(mx, pk[f]);
      mn = fminf(mn, pk[f]);
    }
    float ep[18], en[18], sp = 0.f, sn = 0.f;
#pragma unroll
    for (int f = 0; f < 18; ++f) {
      ep[f] = __expf(pk[f] - mx);
      en[f] = __expf(mn - pk[f]);
      sp += ep[f];
      sn += en[f];
    }
    const float rp = 1.f / sp, rn = 1.f / sn;
    unsigned o[20];
#pragma unroll
    for (int f = 0; f < 9; ++f) o[f] = pack2(ep[2 * f] * rp, ep[2 * f + 1] * rp);
#pragma unroll
    for (int f = 0; f < 9; ++f) o[9 + f] = pack2(en[2 * f] * rn, en[2 * f + 1] * rn);
    o[18] = 0;
    o[19] = 0;
    u16* op = phi + (size_t)row * KPHI_ + h * KPH_;
#pragma unroll
    for (int c = 0; c < 5; ++c) {
      u32x4 v = {o[4 * c], o[4 * c + 1], o[4 * c + 2], o[4 * c + 3]};
      *reinterpret_cast<u32x4*>(op + c * 8) = v;
    }
  }
}

__global__ __launch_bounds__(256, 2) void k2_gemm2(const u16* __restrict__ hbuf, const u16* __restrict__ w2,
                                                   float* __restrict__ part, const u16* __restrict__ qkv,
                                                   const u16* __restrict__ wphi, const u16* __restrict__ wo,
                                                   u16* __restrict__ phi, u16* __restrict__ mcat, float* __restrict__ zbuf) {
  __shared__ __attribute__((aligned(16))) char smem[68352];
  const int tid = threadIdx.x, wid = tid >> 6, lane = tid & 63;
  int bid = blockIdx.x;
  if (bid < 32) { head_kv_block(bid, qkv, wphi, wo, mcat, zbuf, smem); return; }
  if (bid < 64) { head_q_block(bid - 32, qkv, wphi, phi, smem); return; }
  bid -= 64;
  const int sp = bid & 3, nt4 = (bid >> 2) & 3, mt = bid >> 4;
  const int m0 = mt * 128, n0 = nt4 * 144, k0 = sp * 1152;
  const int g = lane >> 4, q = (lane & 15) >> 2, p = lane & 3;
  const bool odd = g & 1;
  const int o0 = (8 * g + 4 * (g & 1) + q) * G2_RB + p * 8;
  const int o1 = (8 * g + 4 * (1 - (g & 1)) + q) * G2_RB + p * 8;
  f32x4 acc[2][G2_NT];
#pragma unroll
  for (int r = 0; r < 2; ++r)
#pragma unroll
    for (int n = 0; n < G2_NT; ++n) acc[r][n] = (f32x4){0.f, 0.f, 0.f, 0.f};
  const u16* ap[2];
#pragma unroll
  for (int r = 0; r < 2; ++r) ap[r] = hbuf + (size_t)(m0 + wid * 32 + r * 16 + (lane & 15)) * F_ + k0 + 8 * g;
  g2_issue(w2, D_, n0, k0, smem, wid, lane);
  bf16x8 an[2][2];
#pragma unroll
  for (int r = 0; r < 2; ++r)
#pragma unroll
    for (int ks = 0; ks < 2; ++ks) an[r][ks] = *reinterpret_cast<const bf16x8*>(ap[r] + ks * 32);
  for (int st = 0; st < 18; ++st) {
    __syncthreads();
    char* cur = smem + (st & 1) * G2_BUFB;
    char* nxt = smem + ((st + 1) & 1) * G2_BUFB;
    bf16x8 ac[2][2];
#pragma unroll
    for (int r = 0; r < 2; ++r)
#pragma unroll
      for (int ks = 0; ks < 2; ++ks) ac[r][ks] = swap_halves_if(an[r][ks], odd);
    if (st < 17) {
      g2_issue(w2, D_, n0, k0 + (st + 1) * 64, nxt, wid, lane);
#pragma unroll
      for (int r = 0; r < 2; ++r)
#pragma unroll
        for (int ks = 0; ks < 2; ++ks) an[r][ks] = *reinterpret_cast<const bf16x8*>(ap[r] + (st + 1) * 64 + ks * 32);
    }
#pragma unroll
    for (int ks = 0; ks < 2; ++ks) {
#pragma unroll
      for (int n = 0; n < G2_NT; ++n) {
        bf16x8 w = wfrag<G2_RB>(cur, o0, o1, ks * 32 * G2_RB + n * 32);
#pragma unroll
        for (int r = 0; r < 2; ++r)
          acc[r][n] = __builtin_amdgcn_mfma_f32_16x16x32_bf16(w, ac[r][ks], acc[r][n], 0, 0, 0);
      }
    }
  }
#pragma unroll
  for (int r = 0; r < 2; ++r) {
    const int m = m0 + wid * 32 + r * 16 + (lane & 15);
    float* pp = part + ((size_t)sp * S_ + m) * D_ + n0 + 4 * g;
#pragma unroll
    for (int n = 0; n < G2_NT; ++n) *reinterpret_cast<f32x4*>(pp + n * 16) = acc[r][n];
  }
}

constexpr int G3_NT = 3, G3_RB = G3_NT * 32, G3_BUFB = 64 * G3_RB;

__device__ __forceinline__ void g3_issue(const u16* __restrict__ w, int n0, int krow0, char* buf, int wid, int lane) {
  constexpr int NCH = 64 * G3_NT * 2, NI = NCH / 64, PER = (NI + 3) / 4;
#pragma unroll
  for (int i = 0; i < PER; ++i) {
    int ins = wid + 4 * i;
    if (ins >= NI) ins -= NI;
    int c = ins * 64 + lane;
    int row = c / (2 * G3_NT), cc = c % (2 * G3_NT);
    glds16(w + (size_t)(krow0 + row) * D_ + n0 + cc * 8, buf + c * 16);
  }
}

__device__ __forceinline__ bf16x8 scale_frag(bf16x8 v, float s, bool odd) {
  u32x4 pk;
#pragma unroll
  for (int j = 0; j < 4; ++j) pk[j] = pack2(bf2f((u16)v[2 * j]) * s, bf2f((u16)v[2 * j + 1]) * s);
  return swap_halves_if(*reinterpret_cast<bf16x8*>(&pk), odd);
}

__global__ __launch_bounds__(256, 2) void k3_final(const u16* __restrict__ phi, const u16* __restrict__ mcat,
                                                   const float* __restrict__ zbuf, const float* __restrict__ part,
                                                   const u16* __restrict__ b2, const u16* __restrict__ x,
                                                   u16* __restrict__ out) {
  __shared__ __attribute__((aligned(16))) char smem[2 * G3_BUFB + 128 * 33 * 4 + 32 * 36 * 4];
  float* rden = reinterpret_cast<float*>(smem + 2 * G3_BUFB);
  float* zs = reinterpret_cast<float*>(smem + 2 * G3_BUFB + 128 * 33 * 4);
  const int tid = threadIdx.x, wid = tid >> 6, lane = tid & 63;
  const int bid = blockIdx.x;
  const int nt = bid % 12, mt = bid / 12;
  const int m0 = mt * 128, n0 = nt * 48;
  g3_issue(mcat, n0, 0, smem, wid, lane);
  for (int i = tid; i < 32 * 36; i += 256) zs[i] = zbuf[i];
  __syncthreads();
  for (int i = tid; i < 128 * 32; i += 256) {
    const int r = i >> 5, h = i & 31;
    const u16* pp = phi + (size_t)(m0 + r) * KPHI_ + h * KPH_;
    float d = 0.f;
#pragma unroll
    for (int c = 0; c < 5; ++c) {
      bf16x8 v = *reinterpret_cast<const bf16x8*>(pp + c * 8);
#pragma unroll
      for (int j = 0; j < 8; ++j)
        if (c * 8 + j < 36) d += bf2f((u16)v[j]) * zs[h * 36 + c * 8 + j];
    }
    rden[r * 33 + h] = 1.f / (rbf(d) + 1e-6f);
  }
  const int g = lane >> 4, q = (lane & 15) >> 2, p = lane & 3;
  const bool odd = g & 1;
  const int o0 = (8 * g + 4 * (g & 1) + q) * G3_RB + p * 8;
  const int o1 = (8 * g + 4 * (1 - (g & 1)) + q) * G3_RB + p * 8;
  f32x4 acc[2][G3_NT];
#pragma unroll
  for (int r = 0; r < 2; ++r)
#pragma unroll
    for (int n = 0; n < G3_NT; ++n) acc[r][n] = (f32x4){0.f, 0.f, 0.f, 0.f};
  const u16* ap[2];
  int rl[2];
#pragma unroll
  for (int r = 0; r < 2; ++r) {
    rl[r] = wid * 32 + r * 16 + (lane & 15);
    ap[r] = phi + (size_t)(m0 + rl[r]) * KPHI_ + 8 * g;
  }
  bf16x8 an[2][2];
#pragma unroll
  for (int r = 0; r < 2; ++r)
#pragma unroll
    for (int ks = 0; ks < 2; ++ks) an[r][ks] = *reinterpret_cast<const bf16x8*>(ap[r] + ks * 32);
  for (int st = 0; st < 20; ++st) {
    __syncthreads();
    char* cur = smem + (st & 1) * G3_BUFB;
    char* nxt = smem + ((st + 1) & 1) * G3_BUFB;
    bf16x8 ac[2][2];
#pragma unroll
    for (int r = 0; r < 2; ++r)
#pragma unroll
      for (int ks = 0; ks < 2; ++ks) {
        const int kidx = st * 64 + ks * 32 + 8 * g;
        const int h = kidx / KPH_;
        ac[r][ks] = scale_frag(an[r][ks], rden[rl[r] * 33 + h], odd);
      }
    if (st < 19) {
      g3_issue(mcat, n0, (st + 1) * 64, nxt, wid, lane);
#pragma unroll
      for (int r = 0; r < 2; ++r)
#pragma unroll
        for (int ks = 0; ks < 2; ++ks) an[r][ks] = *reinterpret_cast<const bf16x8*>(ap[r] + (st + 1) * 64 + ks * 32);
    }
#pragma unroll
    for (int ks = 0; ks < 2; ++ks) {
#pragma unroll
      for (int n = 0; n < G3_NT; ++n) {
        bf16x8 w = wfrag<G3_RB>(cur, o0, o1, ks * 32 * G3_RB + n * 32);
#pragma unroll
        for (int r = 0; r < 2; ++r)
          acc[r][n] = __builtin_amdgcn_mfma_f32_16x16x32_bf16(w, ac[r][ks], acc[r][n], 0, 0, 0);
      }
    }
  }
#pragma unroll
  for (int r = 0; r < 2; ++r) {
    const int m = m0 + rl[r];
#pragma unroll
    for (int n = 0; n < G3_NT; ++n) {
      const int ncol = n0 + n * 16 + 4 * g;
      f32x4 y = *reinterpret_cast<const f32x4*>(part + (size_t)m * D_ + ncol);
#pragma unroll
      for (int s = 1; s < SPLITK_; ++s) {
        f32x4 t = *reinterpret_cast<const f32x4*>(part + ((size_t)s * S_ + m) * D_ + ncol);
        y += t;
      }
      u32x2 bb = *reinterpret_cast<const u32x2*>(b2 + ncol);
      u32x2 xx = *reinterpret_cast<const u32x2*>(x + (size_t)m * D_ + ncol);
      float bv[4] = {bf2f((u16)(bb[0] & 0xffff)), bf2f((u16)(bb[0] >> 16)), bf2f((u16)(bb[1] & 0xffff)), bf2f((u16)(bb[1] >> 16))};
      float xv[4] = {bf2f((u16)(xx[0] & 0xffff)), bf2f((u16)(xx[0] >> 16)), bf2f((u16)(xx[1] & 0xffff)), bf2f((u16)(xx[1] >> 16))};
      float o[4];
#pragma unroll
      for (int j = 0; j < 4; ++j) {
        float yy = y[j] + bv[j];
        float gg = yy / (1.f + __expf(-yy));
        o[j] = xv[j] + acc[r][n][j] * gg;
      }
      u32x2 v = {pack2(o[0], o[1]), pack2(o[2], o[3])};
      *reinterpret_cast<u32x2*>(out + (size_t)m * D_ + ncol) = v;
    }
  }
}

extern "C" void kernel_launch(void* const* d_in, const int* in_sizes, int n_in, void* d_out, int out_size, void* d_ws,
                              size_t ws_size, hipStream_t stream) {
  const u16* x = (const u16*)d_in[0];
  const u16* wqkv = (const u16*)d_in[1];
  const u16* wphi = (const u16*)d_in[2];
  const u16* wo = (const u16*)d_in[3];
  const u16* w1 = (const u16*)d_in[5];
  const u16* b1 = (const u16*)d_in[6];
  const u16* w2 = (const u16*)d_in[7];
  const u16* b2 = (const u16*)d_in[8];
  u16* out = (u16*)d_out;
  char* ws = (char*)d_ws;
  size_t off = 0;
  u16* qkv = (u16*)(ws + off); off += (size_t)S_ * NQKV_ * 2;
  u16* hbuf = (u16*)(ws + off); off += (size_t)S_ * F_ * 2;
  float* part = (float*)(ws + off); off += (size_t)SPLITK_ * S_ * D_ * 4;
  u16* phi = (u16*)(ws + off); off += (size_t)S_ * KPHI_ * 2;
  u16* mcat = (u16*)(ws + off); off += (size_t)KPHI_ * D_ * 2;
  float* zbuf = (float*)(ws + off); off += 32 * 36 * 4;

  k1_gemm1<<<512, 256, 0, stream>>>(x, wqkv, w1, b1, qkv, hbuf);
  k2_gemm2<<<64 + 256, 256, 0, stream>>>(hbuf, w2, part, qkv, wphi, wo, phi, mcat, zbuf);
  k3_final<<<16 * 12, 256, 0, stream>>>(phi, mcat, zbuf, part, b2, x, out);
}
```

```cpp
#include <hip/hip_runtime.h>
#include <hip/hip_bf16.h>
#include <stdint.h>

constexpr int S_ = 2048, D_ = 576, H_ = 32, DH_ = 18, F_ = 4608, NQKV_ = 1728;
constexpr int NPH_ = 1152;
constexpr int KPH_ = 40;
constexpr int KPHI_ = H_ * KPH_;
constexpr int SPLITK_ = 4;

typedef short bf16x8 __attribute__((ext_vector_type(8)));
typedef short s16x4 __attribute__((ext_vector_type(4)));
typedef float f32x4 __attribute__((ext_vector_type(4)));
typedef unsigned u32x4 __attribute__((ext_vector_type(4)));
typedef unsigned u32x2 __attribute__((ext_vector_type(2)));
typedef __attribute__((address_space(3))) s16x4* lds_s16x4_p;
typedef unsigned short u16;

#define WAIT_VM(n) asm volatile("s_waitcnt vmcnt(" #n ")" ::: "memory")
#define WAIT_LGKM0() asm volatile("s_waitcnt lgkmcnt(0)" ::: "memory")
#define BAR() __builtin_amdgcn_s_barrier()

template <int N>
__device__ __forceinline__ void wait_vm() {
  static_assert(N >= 0 && N < 64, "vmcnt range");
  asm volatile("s_waitcnt vmcnt(%0)" ::"n"(N) : "memory");
}

__device__ __forceinline__ float bf2f(u16 v) { return __uint_as_float(((unsigned)v) << 16); }
__device__ __forceinline__ u16 f2bf(float f) {
  __hip_bfloat16 b = __float2bfloat16(f);
  return *reinterpret_cast<u16*>(&b);
}
__device__ __forceinline__ unsigned pack2(float lo, float hi) {
  return (unsigned)f2bf(lo) | ((unsigned)f2bf(hi) << 16);
}
__device__ __forceinline__ float rbf(float f) { return bf2f(f2bf(f)); }
__device__ __forceinline__ float lo16(unsigned u) { return __uint_as_float(u << 16); }
__device__ __forceinline__ float hi16(unsigned u) { return __uint_as_float(u & 0xffff0000u); }

__device__ __forceinline__ void glds16(const void* g, void* l) {
  __builtin_amdgcn_global_load_lds((const unsigned*)g, (unsigned*)l, 16, 0, 0);
}

__device__ __forceinline__ bf16x8 swap_halves_if(bf16x8 v, bool odd) {
  bf16x8 s = __builtin_shufflevector(v, v, 4, 5, 6, 7, 0, 1, 2, 3);
  return odd ? s : v;
}

__device__ __forceinline__ bf16x8 wfrag(const char* img, int o0, int o1, int off) {
  s16x4 a = __builtin_amdgcn_ds_read_tr16_b64_v4i16((lds_s16x4_p)(img + o0 + off));
  s16x4 b = __builtin_amdgcn_ds_read_tr16_b64_v4i16((lds_s16x4_p)(img + o1 + off));
  return __builtin_shufflevector(a, b, 0, 1, 2, 3, 4, 5, 6, 7);
}

__global__ __launch_bounds__(256) void k0_prep(const u16* __restrict__ x, const u16* __restrict__ wqkv,
                                               const u16* __restrict__ wphi, u16* __restrict__ xn,
                                               u16* __restrict__ wfold) {
  const int tid = threadIdx.x, wid = tid >> 6, lane = tid & 63;
  const int bid = blockIdx.x;
  if (bid < 512) {
    const int row = bid * 4 + wid;
    const u16* xp = x + (size_t)row * D_;
    u32x4 a = *reinterpret_cast<const u32x4*>(xp + lane * 8);
    u32x4 b = {0u, 0u, 0u, 0u};
    if (lane < 8) b = *reinterpret_cast<const u32x4*>(xp + 512 + lane * 8);
    float v[16];
#pragma unroll
    for (int j = 0; j < 4; ++j) {
      v[2 * j] = lo16(a[j]); v[2 * j + 1] = hi16(a[j]);
      v[8 + 2 * j] = lo16(b[j]); v[8 + 2 * j + 1] = hi16(b[j]);
    }
    float s = 0.f;
#pragma unroll
    for (int j = 0; j < 16; ++j) s += v[j];
#pragma unroll
    for (int o = 32; o >= 1; o >>= 1) s += __shfl_xor(s, o);
    const float mean = s * (1.f / D_);
    float ss = 0.f;
#pragma unroll
    for (int j = 0; j < 8; ++j) { float d = v[j] - mean; ss += d * d; }
    if (lane < 8) {
#pragma unroll
      for (int j = 8; j < 16; ++j) { float d = v[j] - mean; ss += d * d; }
    }
#pragma unroll
    for (int o = 32; o >= 1; o >>= 1) ss += __shfl_xor(ss, o);
    const float rstd = rsqrtf(ss * (1.f / D_) + 1e-5f);
    u32x4 oa, ob;
#pragma unroll
    for (int j = 0; j < 4; ++j) {
      oa[j] = pack2((v[2 * j] - mean) * rstd, (v[2 * j + 1] - mean) * rstd);
      ob[j] = pack2((v[8 + 2 * j] - mean) * rstd, (v[8 + 2 * j + 1] - mean) * rstd);
    }
    *reinterpret_cast<u32x4*>(xn + (size_t)row * D_ + lane * 8) = oa;
    if (lane < 8) *reinterpret_cast<u32x4*>(xn + (size_t)row * D_ + 512 + lane * 8) = ob;
    return;
  }
  const int k0 = (bid - 512) * 2;
  for (int i = tid; i < 2 * NPH_; i += 256) {
    const int k = k0 + i / NPH_, c = i % NPH_;
    const int hh = c / 18, f = c % 18;
    const int h = hh & 31;
    const u16* wp = wqkv + (size_t)k * NQKV_ + hh * 18;
    const u16* pp = wphi + (size_t)h * 324 + f;
    float acc = 0.f;
#pragma unroll
    for (int d = 0; d < 18; ++d) acc += bf2f(wp[d]) * bf2f(pp[d * 18]);
    wfold[(size_t)k * NPH_ + c] = f2bf(acc);
  }
}

struct G1Src {
  const u16* wfold;
  const u16* wqkv;
  const u16* w1;
};
struct G1Stage {
  const u16* p[3];
  int st[3];
};
constexpr int G1_BUFB = 64 * 5 * 32;
constexpr int G1_RING = 4;

template <int NT>
__device__ __forceinline__ void g1_setup(const G1Src& src, int tile0, int wid, int lane, G1Stage& sg) {
  constexpr int NCH = 64 * NT * 2, NI = NCH / 64, PER = (NI + 3) / 4;
#pragma unroll
  for (int i = 0; i < PER; ++i) {
    int ins = wid + 4 * i;
    if (ins >= NI) ins -= NI;
    int c = ins * 64 + lane;
    int row = c / (2 * NT), cc = c % (2 * NT);
    int col = tile0 * 16 + cc * 8;
    const u16* p;
    int ld;
    if (col < NPH_) { p = src.wfold + col; ld = NPH_; }
    else if (col < NQKV_) { p = src.wqkv + col; ld = NQKV_; }
    else { p = src.w1 + (col - NQKV_); ld = F_; }
    sg.p[i] = p + (size_t)row * ld;
    sg.st[i] = 64 * ld;
  }
}

template <int NT>
__device__ __forceinline__ void g1_issue(G1Stage& sg, char* buf, int wid, int lane) {
  constexpr int NCH = 64 * NT * 2, NI = NCH / 64, PER = (NI + 3) / 4;
#pragma unroll
  for (int i = 0; i < PER; ++i) {
    int ins = wid + 4 * i;
    if (ins >= NI) ins -= NI;
    glds16(sg.p[i], buf + (ins * 64 + lane) * 16);
    sg.p[i] += sg.st[i];
  }
}
template <int NT> struct G1Per { static constexpr int v = ((64 * NT * 2) / 64 + 3) / 4; };

template <int NT, int NTN, int GS0>
__device__ __forceinline__ void g1_step(const G1Src& src, G1Stage& sg, int tile0, char* smem,
                                        const bf16x8 (&xa)[2][18], int wid, int lane, int m0,
                                        u16* __restrict__ qkv, u16* __restrict__ hbuf, const float* __restrict__ bias_lds) {
  constexpr int RB = NT * 32;
  const int g = lane >> 4, q = (lane & 15) >> 2, p = lane & 3;
  const int o0 = (8 * g + 4 * (g & 1) + q) * RB + p * 8;
  const int o1 = (8 * g + 4 * (1 - (g & 1)) + q) * RB + p * 8;
  f32x4 acc[2][NT];
#pragma unroll
  for (int r = 0; r < 2; ++r)
#pragma unroll
    for (int n = 0; n < NT; ++n) acc[r][n] = (f32x4){0.f, 0.f, 0.f, 0.f};

#pragma unroll
  for (int kc = 0; kc < 9; ++kc) {
    constexpr int PC = G1Per<NT>::v, PN = (NTN > 0) ? G1Per<(NTN > 0 ? NTN : 1)>::v : 0;
    if (kc <= 6) wait_vm<2 * PC>();
    else if (kc == 7) wait_vm<PC + PN>();
    else wait_vm<2 * PN>();
    WAIT_LGKM0();
    BAR();
    if (kc + 3 < 9) g1_issue<NT>(sg, smem + ((GS0 + kc + 3) % G1_RING) * G1_BUFB, wid, lane);
    else if (NTN > 0) {
      if (kc + 3 == 9) g1_setup<(NTN > 0 ? NTN : 1)>(src, tile0 + NT, wid, lane, sg);
      g1_issue<(NTN > 0 ? NTN : 1)>(sg, smem + ((GS0 + kc + 3) % G1_RING) * G1_BUFB, wid, lane);
    }
    const char* cur = smem + ((GS0 + kc) % G1_RING) * G1_BUFB;
#pragma unroll
    for (int ks = 0; ks < 2; ++ks) {
#pragma unroll
      for (int n = 0; n < NT; ++n) {
        bf16x8 w = wfrag(cur, o0, o1, ks * 32 * RB + n * 32);
#pragma unroll
        for (int r = 0; r < 2; ++r)
          acc[r][n] = __builtin_amdgcn_mfma_f32_16x16x32_bf16(w, xa[r][kc * 2 + ks], acc[r][n], 0, 0, 0);
      }
    }
  }
#pragma unroll
  for (int n = 0; n < NT; ++n) {
    const int tile = tile0 + n;
    const int ncol = tile * 16 + 4 * g;
    if (tile < NQKV_ / 16) {
#pragma unroll
      for (int r = 0; r < 2; ++r) {
        const int m = m0 + wid * 32 + r * 16 + (lane & 15);
        u32x2 v = {pack2(acc[r][n][0], acc[r][n][1]), pack2(acc[r][n][2], acc[r][n][3])};
        *reinterpret_cast<u32x2*>(qkv + (size_t)m * NQKV_ + ncol) = v;
      }
    } else {
      const int fcol = ncol - NQKV_;
      f32x4 bv = *reinterpret_cast<const f32x4*>(bias_lds + (GS0 / 9) * 80 + n * 16 + 4 * g);
#pragma unroll
      for (int r = 0; r < 2; ++r) {
        const int m = m0 + wid * 32 + r * 16 + (lane & 15);
        float o[4];
#pragma unroll
        for (int j = 0; j < 4; ++j) {
          float v = acc[r][n][j] + bv[j];
          float u = 0.7978845608028654f * (v + 0.044715f * v * v * v);
          float e = __expf(-2.f * u);
          o[j] = v * __builtin_amdgcn_rcpf(1.f + e);
        }
        u32x2 v = {pack2(o[0], o[1]), pack2(o[2], o[3])};
        *reinterpret_cast<u32x2*>(hbuf + (size_t)m * F_ + fcol) = v;
      }
    }
  }
}

__global__ __launch_bounds__(256, 2) void k1_gemm1(const u16* __restrict__ xn, const u16* __restrict__ wfold,
                                                   const u16* __restrict__ wqkv, const u16* __restrict__ w1,
                                                   const u16* __restrict__ b1, u16* __restrict__ qkv,
                                                   u16* __restrict__ hbuf) {
  __shared__ __attribute__((aligned(16))) char smem[G1_RING * G1_BUFB + 240 * 4];
  float* bias_lds = reinterpret_cast<float*>(smem + G1_RING * G1_BUFB);
  const int tid = threadIdx.x, wid = tid >> 6, lane = tid & 63;
  const int bid = blockIdx.x;
  const int mt = bid >> 5, grp = bid & 31;
  const int m0 = mt * 128;
  int tile0 = (grp * 396) >> 5;
  if (tile0 > 396 - 13) tile0 = 396 - 13;
  G1Src src{wfold, wqkv, w1};
  G1Stage sg;
  g1_setup<5>(src, tile0, wid, lane, sg);
  g1_issue<5>(sg, smem + 0 * G1_BUFB, wid, lane);
  g1_issue<5>(sg, smem + 1 * G1_BUFB, wid, lane);
  g1_issue<5>(sg, smem + 2 * G1_BUFB, wid, lane);
  if (tid < 208) {
    int col = tile0 * 16 + tid - NQKV_;
    bias_lds[tid] = (col >= 0) ? bf2f(b1[col]) : 0.f;
  }
  const int g = lane >> 4;
  const bool odd = g & 1;
  bf16x8 xa[2][18];
#pragma unroll
  for (int r = 0; r < 2; ++r) {
    const int m = m0 + wid * 32 + r * 16 + (lane & 15);
    const u16* xp = xn + (size_t)m * D_ + 8 * g;
#pragma unroll
    for (int ks = 0; ks < 18; ++ks) xa[r][ks] = swap_halves_if(*reinterpret_cast<const bf16x8*>(xp + ks * 32), odd);
  }
  WAIT_VM(0);
  WAIT_LGKM0();
  g1_step<5, 5, 0>(src, sg, tile0, smem, xa, wid, lane, m0, qkv, hbuf, bias_lds);
  g1_step<5, 3, 9>(src, sg, tile0 + 5, smem, xa, wid, lane, m0, qkv, hbuf, bias_lds);
  g1_step<3, 0, 18>(src, sg, tile0 + 10, smem, xa, wid, lane, m0, qkv, hbuf, bias_lds);
}

constexpr int G2_NT = 9, G2_RB = G2_NT * 32;
constexpr int G2_WB = 32 * G2_RB;
constexpr int G2_AB = 128 * 64;
constexpr int G2_BUFB = G2_WB + G2_AB;
constexpr int G2_RING = 4;
constexpr int G2_STAGES = 36;

__device__ __forceinline__ void softmax_pm(const float (&pk)[18], float (&ep)[18], float (&en)[18]) {
  float mx = pk[0], mn = pk[0];
#pragma unroll
  for (int f = 1; f < 18; ++f) {
    mx = fmaxf(mx, pk[f]);
    mn = fminf(mn, pk[f]);
  }
  float sp = 0.f, sn = 0.f;
#pragma unroll
  for (int f = 0; f < 18; ++f) {
    ep[f] = __expf(pk[f] - mx);
    en[f] = __expf(mn - pk[f]);
    sp += ep[f];
    sn += en[f];
  }
  const float rp = 1.f / sp, rn = 1.f / sn;
#pragma unroll
  for (int f = 0; f < 18; ++f) {
    ep[f] *= rp;
    en[f] *= rn;
  }
}

__device__ void head_kv_block(int h, const u16* __restrict__ qkv, const u16* __restrict__ wo,
                              u16* __restrict__ mcat, float* __restrict__ zbuf, char* smem) {
  u16* phiT = reinterpret_cast<u16*>(smem);
  u16* vT = reinterpret_cast<u16*>(smem + 25344);
  float* kvs = reinterpret_cast<float*>(smem + 25344 + 16896);
  const int tid = threadIdx.x, wid = tid >> 6, lane = tid & 63;
  for (int i = tid; i < 12 * 264; i += 256) phiT[36 * 264 + i] = 0;
  for (int i = tid; i < 14 * 264; i += 256) vT[18 * 264 + i] = (i < 264) ? (u16)0x3f80 : (u16)0;
  f32x4 acc[3][2];
#pragma unroll
  for (int a = 0; a < 3; ++a)
#pragma unroll
    for (int b = 0; b < 2; ++b) acc[a][b] = (f32x4){0.f, 0.f, 0.f, 0.f};
#pragma unroll 1
  for (int ch = 0; ch < 8; ++ch) {
    const int row = ch * 256 + tid;
    const u16* kp = qkv + (size_t)row * NQKV_ + D_ + h * 18;
    const u16* vp = qkv + (size_t)row * NQKV_ + 2 * D_ + h * 18;
    float pk[18];
    unsigned vv[9];
#pragma unroll
    for (int d = 0; d < 9; ++d) {
      unsigned t = *reinterpret_cast<const unsigned*>(kp + 2 * d);
      pk[2 * d] = lo16(t);
      pk[2 * d + 1] = hi16(t);
      vv[d] = *reinterpret_cast<const unsigned*>(vp + 2 * d);
    }
    float ep[18], en[18];
    softmax_pm(pk, ep, en);
    __syncthreads();
#pragma unroll
    for (int f = 0; f < 18; ++f) {
      phiT[f * 264 + tid] = f2bf(ep[f]);
      phiT[(18 + f) * 264 + tid] = f2bf(en[f]);
      vT[f * 264 + tid] = (u16)((f & 1) ? (vv[f >> 1] >> 16) : (vv[f >> 1] & 0xffff));
    }
    __syncthreads();
#pragma unroll
    for (int ks = 0; ks < 2; ++ks) {
      const int s0 = wid * 64 + ks * 32 + 8 * (lane >> 4);
      bf16x8 bv[2];
#pragma unroll
      for (int b = 0; b < 2; ++b) bv[b] = *reinterpret_cast<const bf16x8*>(vT + (b * 16 + (lane & 15)) * 264 + s0);
#pragma unroll
      for (int a = 0; a < 3; ++a) {
        bf16x8 av = *reinterpret_cast<const bf16x8*>(phiT + (a * 16 + (lane & 15)) * 264 + s0);
#pragma unroll
        for (int b = 0; b < 2; ++b) acc[a][b] = __builtin_amdgcn_mfma_f32_16x16x32_bf16(av, bv[b], acc[a][b], 0, 0, 0);
      }
    }
  }
  __syncthreads();
#pragma unroll
  for (int a = 0; a < 3; ++a)
#pragma unroll
    for (int b = 0; b < 2; ++b)
#pragma unroll
      for (int j = 0; j < 4; ++j)
        kvs[(wid * 48 + a * 16 + (lane >> 4) * 4 + j) * 32 + b * 16 + (lane & 15)] = acc[a][b][j];
  u16* woimg = reinterpret_cast<u16*>(smem);
  u16* kvb = reinterpret_cast<u16*>(smem + 36864);
  for (int i = tid; i < 32 * 72; i += 256) {
    const int r = i / 72, c = i % 72;
    u32x4 v = {0u, 0u, 0u, 0u};
    if (r < 18) v = *reinterpret_cast<const u32x4*>(wo + (size_t)(h * 18 + r) * D_ + c * 8);
    *reinterpret_cast<u32x4*>(woimg + r * 576 + c * 8) = v;
  }
  __syncthreads();
  for (int i = tid; i < 48 * 32; i += 256) {
    float v = kvs[i] + kvs[48 * 32 + i] + kvs[2 * 48 * 32 + i] + kvs[3 * 48 * 32 + i];
    const int f = i >> 5, d = i & 31;
    if (d == 18 && f < 36) zbuf[h * 36 + f] = rbf(v);
    kvb[i] = (d < 18 && f < 36) ? f2bf(v) : (u16)0;
  }
  __syncthreads();
  {
    const int g = lane >> 4, q = (lane & 15) >> 2, p = lane & 3;
    constexpr int RB = 576 * 2;
    const int o0 = (8 * g + q) * RB + p * 8, o1 = (8 * g + 4 + q) * RB + p * 8;
    bf16x8 kb[3];
#pragma unroll
    for (int a = 0; a < 3; ++a) kb[a] = *reinterpret_cast<const bf16x8*>(kvb + (a * 16 + (lane & 15)) * 32 + 8 * g);
    for (int nt = wid; nt < 36; nt += 4) {
      bf16x8 w = wfrag(reinterpret_cast<const char*>(woimg), o0, o1, nt * 32);
#pragma unroll
      for (int a = 0; a < 3; ++a) {
        f32x4 c = __builtin_amdgcn_mfma_f32_16x16x32_bf16(w, kb[a], (f32x4){0.f, 0.f, 0.f, 0.f}, 0, 0, 0);
        const int f = a * 16 + (lane & 15);
        if (f < KPH_) {
          u32x2 v = {pack2(c[0], c[1]), pack2(c[2], c[3])};
          *reinterpret_cast<u32x2*>(mcat + (size_t)(h * KPH_ + f) * D_ + nt * 16 + 4 * g) = v;
        }
      }
    }
  }
}

__device__ void head_q_block(int hb, const u16* __restrict__ qkv, u16* __restrict__ phi) {
  const int h = hb >> 1, tid = threadIdx.x;
#pragma unroll 1
  for (int ch = 0; ch < 4; ++ch) {
    const int row = (hb & 1) * 1024 + ch * 256 + tid;
    const u16* kp = qkv + (size_t)row * NQKV_ + h * 18;
    float pk[18];
#pragma unroll
    for (int d = 0; d < 9; ++d) {
      unsigned t = *reinterpret_cast<const unsigned*>(kp + 2 * d);
      pk[2 * d] = lo16(t);
      pk[2 * d + 1] = hi16(t);
    }
    float ep[18], en[18];
    softmax_pm(pk, ep, en);
    unsigned o[20];
#pragma unroll
    for (int f = 0; f < 9; ++f) o[f] = pack2(ep[2 * f], ep[2 * f + 1]);
#pragma unroll
    for (int f = 0; f < 9; ++f) o[9 + f] = pack2(en[2 * f], en[2 * f + 1]);
    o[18] = 0;
    o[19] = 0;
    u16* op = phi + (size_t)row * KPHI_ + h * KPH_;
#pragma unroll
    for (int c = 0; c < 5; ++c) {
      u32x4 v = {o[4 * c], o[4 * c + 1], o[4 * c + 2], o[4 * c + 3]};
      *reinterpret_cast<u32x4*>(op + c * 8) = v;
    }
  }
}

constexpr int K2_HEADBLK = 96;

__global__ __launch_bounds__(256, 2) void k2_gemm2(const u16* __restrict__ hbuf, const u16* __restrict__ w2,
                                                   float* __restrict__ part, const u16* __restrict__ qkv,
                                                   const u16* __restrict__ wo, u16* __restrict__ phi,
                                                   u16* __restrict__ mcat, float* __restrict__ zbuf) {
  __shared__ __attribute__((aligned(16))) char smem[G2_RING * G2_BUFB];
  const int tid = threadIdx.x, wid = tid >> 6, lane = tid & 63;
  int bid = blockIdx.x;
  if (bid < 32) { head_kv_block(bid, qkv, wo, mcat, zbuf, smem); return; }
  if (bid < K2_HEADBLK) { head_q_block(bid - 32, qkv, phi); return; }
  bid -= K2_HEADBLK;
  const int xcd = bid & 7, loc = bid >> 3;
  const int mt = xcd * 2 + (loc >> 4), nt4 = (loc >> 2) & 3, sp = loc & 3;
  const int m0 = mt * 128, n0 = nt4 * 144, k0 = sp * 1152;
  const int g = lane >> 4, q = (lane & 15) >> 2, p = lane & 3;
  const bool odd = g & 1;
  const int o0 = (8 * g + 4 * (g & 1) + q) * G2_RB + p * 8;
  const int o1 = (8 * g + 4 * (1 - (g & 1)) + q) * G2_RB + p * 8;
  f32x4 acc[2][G2_NT];
#pragma unroll
  for (int r = 0; r < 2; ++r)
#pragma unroll
    for (int n = 0; n < G2_NT; ++n) acc[r][n] = (f32x4){0.f, 0.f, 0.f, 0.f};
  const u16* wp[3];
  int wdst[3];
#pragma unroll
  for (int i = 0; i < 3; ++i) {
    int ins = wid + 4 * i;
    if (ins >= 9) ins -= 9;
    int c = ins * 64 + lane;
    int row = c / 18, cc = c % 18;
    wp[i] = w2 + (size_t)(k0 + row) * D_ + n0 + cc * 8;
    wdst[i] = c * 16;
  }
  const u16* apg[2];
  int adst[2];
#pragma unroll
  for (int i = 0; i < 2; ++i) {
    int row = wid * 32 + i * 16 + (lane >> 2), pos = lane & 3;
    int ch = pos ^ ((4 - ((row >> 2) & 3)) & 3);
    apg[i] = hbuf + (size_t)(m0 + row) * F_ + k0 + ch * 8;
    adst[i] = G2_WB + row * 64 + pos * 16;
  }
  int aoff[2];
#pragma unroll
  for (int r = 0; r < 2; ++r) {
    int row = wid * 32 + r * 16 + (lane & 15);
    aoff[r] = G2_WB + row * 64 + ((g ^ ((4 - ((row >> 2) & 3)) & 3)) * 16);
  }
  auto issue = [&](int slot) {
    char* buf = smem + slot * G2_BUFB;
#pragma unroll
    for (int i = 0; i < 3; ++i) { glds16(wp[i], buf + wdst[i]); wp[i] += 32 * D_; }
#pragma unroll
    for (int i = 0; i < 2; ++i) { glds16(apg[i], buf + adst[i]); apg[i] += 32; }
  };
  issue(0);
  issue(1);
  issue(2);
#pragma unroll 1
  for (int st0 = 0; st0 < G2_STAGES; st0 += 4) {
#pragma unroll
    for (int u = 0; u < 4; ++u) {
      const int st = st0 + u;
      if (st < G2_STAGES - 2) WAIT_VM(10);
      else if (st == G2_STAGES - 2) WAIT_VM(5);
      else WAIT_VM(0);
      WAIT_LGKM0();
      BAR();
      if (st + 3 < G2_STAGES) issue((u + 3) & 3);
      const char* cur = smem + u * G2_BUFB;
      bf16x8 ac[2];
#pragma unroll
      for (int r = 0; r < 2; ++r) ac[r] = swap_halves_if(*reinterpret_cast<const bf16x8*>(cur + aoff[r]), odd);
#pragma unroll
      for (int n = 0; n < G2_NT; ++n) {
        bf16x8 w = wfrag(cur, o0, o1, n * 32);
#pragma unroll
        for (int r = 0; r < 2; ++r) acc[r][n] = __builtin_amdgcn_mfma_f32_16x16x32_bf16(w, ac[r], acc[r][n], 0, 0, 0);
      }
    }
  }
#pragma unroll
  for (int r = 0; r < 2; ++r) {
    const int m = m0 + wid * 32 + r * 16 + (lane & 15);
    float* pp = part + ((size_t)sp * S_ + m) * D_ + n0 + 4 * g;
#pragma unroll
    for (int n = 0; n < G2_NT; ++n) *reinterpret_cast<f32x4*>(pp + n * 16) = acc[r][n];
  }
}

constexpr int G3_NT = 3, G3_RB = G3_NT * 32;
constexpr int G3_WB = 32 * G3_RB;
constexpr int G3_AB = 128 * 64;
constexpr int G3_BUFB = G3_WB + G3_AB;
constexpr int G3_RING = 4;
constexpr int G3_STAGES = 40;

__device__ __forceinline__ bf16x8 scale_frag(bf16x8 v, float s, bool odd) {
  u32x4 in = *reinterpret_cast<u32x4*>(&v);
  u32x4 pk;
#pragma unroll
  for (int j = 0; j < 4; ++j) pk[j] = pack2(lo16(in[j]) * s, hi16(in[j]) * s);
  return swap_halves_if(*reinterpret_cast<bf16x8*>(&pk), odd);
}

__global__ __launch_bounds__(256, 2) void k3_final(const u16* __restrict__ phi, const u16* __restrict__ mcat,
                                                   const float* __restrict__ zbuf, const float* __restrict__ part,
                                                   const u16* __restrict__ b2, const u16* __restrict__ x,
                                                   u16* __restrict__ out) {
  __shared__ __attribute__((aligned(16))) char smem[G3_RING * G3_BUFB + 128 * 33 * 4 + 32 * 36 * 4];
  float* rden = reinterpret_cast<float*>(smem + G3_RING * G3_BUFB);
  float* zs = reinterpret_cast<float*>(smem + G3_RING * G3_BUFB + 128 * 33 * 4);
  const int tid = threadIdx.x, wid = tid >> 6, lane = tid & 63;
  const int bid = blockIdx.x;
  const int xcd = bid & 7, loc = bid >> 3;
  const int mt = xcd * 2 + loc / 12, nt = loc % 12;
  const int m0 = mt * 128, n0 = nt * 48;
  const int g = lane >> 4, q = (lane & 15) >> 2, p = lane & 3;
  const bool odd = g & 1;
  const u16* wp;
  int wdst;
  {
    int ins = wid < 3 ? wid : 0;
    int c = ins * 64 + lane;
    int row = c / 6, cc = c % 6;
    wp = mcat + (size_t)row * D_ + n0 + cc * 8;
    wdst = c * 16;
  }
  const u16* apg[2];
  int adst[2];
#pragma unroll
  for (int i = 0; i < 2; ++i) {
    int row = wid * 32 + i * 16 + (lane >> 2), pos = lane & 3;
    int ch = pos ^ ((4 - ((row >> 2) & 3)) & 3);
    apg[i] = phi + (size_t)(m0 + row) * KPHI_ + ch * 8;
    adst[i] = G3_WB + row * 64 + pos * 16;
  }
  auto issue = [&](int slot) {
    char* buf = smem + slot * G3_BUFB;
    glds16(wp, buf + wdst);
    wp += 32 * D_;
#pragma unroll
    for (int i = 0; i < 2; ++i) { glds16(apg[i], buf + adst[i]); apg[i] += 32; }
  };
  issue(0);
  issue(1);
  issue(2);
  for (int i = tid; i < 32 * 36; i += 256) zs[i] = zbuf[i];
  __syncthreads();
  for (int i = tid; i < 128 * 32; i += 256) {
    const int r = i >> 5, h = i & 31;
    const u16* pp = phi + (size_t)(m0 + r) * KPHI_ + h * KPH_;
    float d = 0.f;
#pragma unroll
    for (int c = 0; c < 5; ++c) {
      u32x4 v = *reinterpret_cast<const u32x4*>(pp + c * 8);
#pragma unroll
      for (int j = 0; j < 4; ++j) {
        if (c * 8 + 2 * j < 36) d += lo16(v[j]) * zs[h * 36 + c * 8 + 2 * j];
        if (c * 8 + 2 * j + 1 < 36) d += hi16(v[j]) * zs[h * 36 + c * 8 + 2 * j + 1];
      }
    }
    rden[r * 33 + h] = 1.f / (rbf(d) + 1e-6f);
  }
  const int o0 = (8 * g + 4 * (g & 1) + q) * G3_RB + p * 8;
  const int o1 = (8 * g + 4 * (1 - (g & 1)) + q) * G3_RB + p * 8;
  f32x4 acc[2][G3_NT];
#pragma unroll
  for (int r = 0; r < 2; ++r)
#pragma unroll
    for (int n = 0; n < G3_NT; ++n) acc[r][n] = (f32x4){0.f, 0.f, 0.f, 0.f};
  int aoff[2], rl[2];
#pragma unroll
  for (int r = 0; r < 2; ++r) {
    rl[r] = wid * 32 + r * 16 + (lane & 15);
    aoff[r] = G3_WB + rl[r] * 64 + ((g ^ ((4 - ((rl[r] >> 2) & 3)) & 3)) * 16);
  }
  __syncthreads();
#pragma unroll 1
  for (int st0 = 0; st0 < G3_STAGES; st0 += 4) {
#pragma unroll
    for (int u = 0; u < 4; ++u) {
      const int st = st0 + u;
      if (st < G3_STAGES - 2) WAIT_VM(6);
      else if (st == G3_STAGES - 2) WAIT_VM(3);
      else WAIT_VM(0);
      WAIT_LGKM0();
      BAR();
      if (st + 3 < G3_STAGES) issue((u + 3) & 3);
      const char* cur = smem + u * G3_BUFB;
      const int h = (st * 32 + 8 * g) / KPH_;
      bf16x8 ac[2];
#pragma unroll
      for (int r = 0; r < 2; ++r)
        ac[r] = scale_frag(*reinterpret_cast<const bf16x8*>(cur + aoff[r]), rden[rl[r] * 33 + h], odd);
#pragma unroll
      for (int n = 0; n < G3_NT; ++n) {
        bf16x8 w = wfrag(cur, o0, o1, n * 32);
#pragma unroll
        for (int r = 0; r < 2; ++r) acc[r][n] = __builtin_amdgcn_mfma_f32_16x16x32_bf16(w, ac[r], acc[r][n], 0, 0, 0);
      }
    }
  }
#pragma unroll
  for (int r = 0; r < 2; ++r) {
    const int m = m0 + rl[r];
#pragma unroll
    for (int n = 0; n < G3_NT; ++n) {
      const int ncol = n0 + n * 16 + 4 * g;
      f32x4 y = *reinterpret_cast<const f32x4*>(part + (size_t)m * D_ + ncol);
#pragma unroll
      for (int s = 1; s < SPLITK_; ++s) {
        f32x4 t = *reinterpret_cast<const f32x4*>(part + ((size_t)s * S_ + m) * D_ + ncol);
        y += t;
      }
      u32x2 bb = *reinterpret_cast<const u32x2*>(b2 + ncol);
      u32x2 xx = *reinterpret_cast<const u32x2*>(x + (size_t)m * D_ + ncol);
      float bv[4] = {lo16(bb[0]), hi16(bb[0]), lo16(bb[1]), hi16(bb[1])};
      float xv[4] = {lo16(xx[0]), hi16(xx[0]), lo16(xx[1]), hi16(xx[1])};
      float o[4];
#pragma unroll
      for (int j = 0; j < 4; ++j) {
        float yy = y[j] + bv[j];
        float gg = yy / (1.f + __expf(-yy));
        o[j] = xv[j] + acc[r][n][j] * gg;
      }
      u32x2 v = {pack2(o[0], o[1]), pack2(o[2], o[3])};
      *reinterpret_cast<u32x2*>(out + (size_t)m * D_ + ncol) = v;
    }
  }
}

extern "C" void kernel_launch(void* const* d_in, const int* in_sizes, int n_in, void* d_out, int out_size, void* d_ws,
                              size_t ws_size, hipStream_t stream) {
  const u16* x = (const u16*)d_in[0];
  const u16* wqkv = (const u16*)d_in[1];
  const u16* wphi = (const u16*)d_in[2];
  const u16* wo = (const u16*)d_in[3];
  const u16* w1 = (const u16*)d_in[5];
  const u16* b1 = (const u16*)d_in[6];
  const u16* w2 = (const u16*)d_in[7];
  const u16* b2 = (const u16*)d_in[8];
  u16* out = (u16*)d_out;
  char* ws = (char*)d_ws;
  size_t off = 0;
  u16* xn = (u16*)(ws + off); off += (size_t)S_ * D_ * 2;
  u16* wfold = (u16*)(ws + off); off += (size_t)D_ * NPH_ * 2;
  u16* qkv = (u16*)(ws + off); off += (size_t)S_ * NQKV_ * 2;
  u16* hbuf = (u16*)(ws + off); off += (size_t)S_ * F_ * 2;
  float* part = (float*)(ws + off); off += (size_t)SPLITK_ * S_ * D_ * 4;
  u16* phi = (u16*)(ws + off); off += (size_t)S_ * KPHI_ * 2;
  u16* mcat = (u16*)(ws + off); off += (size_t)KPHI_ * D_ * 2;
  float* zbuf = (float*)(ws + off); off += 32 * 36 * 4;

  k0_prep<<<512 + 288, 256, 0, stream>>>(x, wqkv, wphi, xn, wfold);
  k1_gemm1<<<512, 256, 0, stream>>>(xn, wfold, wqkv, w1, b1, qkv, hbuf);
  k2_gemm2<<<K2_HEADBLK + 256, 256, 0, stream>>>(hbuf, w2, part, qkv, wo, phi, mcat, zbuf);
  k3_final<<<16 * 12, 256, 0, stream>>>(phi, mcat, zbuf, part, b2, x, out);
}
```

```cpp
#include <hip/hip_runtime.h>
#include <hip/hip_bf16.h>
#include <stdint.h>

constexpr int S_ = 2048, D_ = 576, H_ = 32, DH_ = 18, F_ = 4608, NQKV_ = 1728;
constexpr int NPH_ = 1152;
constexpr int KPH_ = 40;
constexpr int KPHI_ = H_ * KPH_;
constexpr int SPLITK_ = 4;

typedef short bf16x8 __attribute__((ext_vector_type(8)));
typedef short s16x4 __attribute__((ext_vector_type(4)));
typedef float f32x4 __attribute__((ext_vector_type(4)));
typedef unsigned u32x4 __attribute__((ext_vector_type(4)));
typedef unsigned u32x2 __attribute__((ext_vector_type(2)));
typedef __attribute__((address_space(3))) s16x4* lds_s16x4_p;
typedef unsigned short u16;
template <int V> struct IC { static constexpr int value = V; };

#define WAIT_VM(n) asm volatile("s_waitcnt vmcnt(" #n ")" ::: "memory")
#define WAIT_LGKM0() asm volatile("s_waitcnt lgkmcnt(0)" ::: "memory")
#define BAR() __builtin_amdgcn_s_barrier()

template <int N>
__device__ __forceinline__ void wait_vm() {
  static_assert(N >= 0 && N < 64, "vmcnt range");
  asm volatile("s_waitcnt vmcnt(%0)" ::"n"(N) : "memory");
}

__device__ __forceinline__ float bf2f(u16 v) { return __uint_as_float(((unsigned)v) << 16); }
__device__ __forceinline__ u16 f2bf(float f) {
  __hip_bfloat16 b = __float2bfloat16(f);
  return *reinterpret_cast<u16*>(&b);
}
__device__ __forceinline__ unsigned pack2(float lo, float hi) {
  return (unsigned)f2bf(lo) | ((unsigned)f2bf(hi) << 16);
}
__device__ __forceinline__ float rbf(float f) { return bf2f(f2bf(f)); }
__device__ __forceinline__ float lo16(unsigned u) { return __uint_as_float(u << 16); }
__device__ __forceinline__ float hi16(unsigned u) { return __uint_as_float(u & 0xffff0000u); }

__device__ __forceinline__ void glds16(const void* g, void* l) {
  __builtin_amdgcn_global_load_lds((const unsigned*)g, (unsigned*)l, 16, 0, 0);
}

__device__ __forceinline__ void l2_touch(const void* g, void* ldummy) {
  __builtin_amdgcn_global_load_lds((const unsigned*)g, (unsigned*)ldummy, 4, 0, 0);
}

__device__ __forceinline__ bf16x8 swap_halves_if(bf16x8 v, bool odd) {
  bf16x8 s = __builtin_shufflevector(v, v, 4, 5, 6, 7, 0, 1, 2, 3);
  return odd ? s : v;
}

__device__ __forceinline__ bf16x8 wfrag(const char* img, int o0, int o1, int off) {
  s16x4 a = __builtin_amdgcn_ds_read_tr16_b64_v4i16((lds_s16x4_p)(img + o0 + off));
  s16x4 b = __builtin_amdgcn_ds_read_tr16_b64_v4i16((lds_s16x4_p)(img + o1 + off));
  return __builtin_shufflevector(a, b, 0, 1, 2, 3, 4, 5, 6, 7);
}

struct WF { s16x4 lo, hi; };
template <int OFF>
__device__ __forceinline__ void tr_rd(s16x4& r, unsigned addr) {
  asm volatile("ds_read_b64_tr_b16 %0, %1 offset:%2" : "=v"(r) : "v"(addr), "n"(OFF) : "memory");
}
template <int OFF>
__device__ __forceinline__ void b128_rd(bf16x8& r, unsigned addr) {
  asm volatile("ds_read_b128 %0, %1 offset:%2" : "=v"(r) : "v"(addr), "n"(OFF) : "memory");
}
template <int N>
__device__ __forceinline__ void wait_lgkm() {
  static_assert(N >= 0 && N <= 15, "lgkmcnt range");
  asm volatile("s_waitcnt lgkmcnt(%0)" ::"n"(N) : "memory");
}
__device__ __forceinline__ void pin(WF& w) { asm volatile("" : "+v"(w.lo), "+v"(w.hi)); }
__device__ __forceinline__ void pin(bf16x8& w) { asm volatile("" : "+v"(w)); }
__device__ __forceinline__ bf16x8 cat(const WF& w) { return __builtin_shufflevector(w.lo, w.hi, 0, 1, 2, 3, 4, 5, 6, 7); }
__device__ __forceinline__ unsigned lds_addr(const void* p) {
  return (unsigned)(uintptr_t)(__attribute__((address_space(3))) const char*)p;
}

__global__ __launch_bounds__(256) void k0_prep(const u16* __restrict__ x, const u16* __restrict__ wqkv,
                                               const u16* __restrict__ wphi, u16* __restrict__ xn,
                                               u16* __restrict__ wfold) {
  const int tid = threadIdx.x, wid = tid >> 6, lane = tid & 63;
  const int bid = blockIdx.x;
  if (bid < 512) {
    const int row = bid * 4 + wid;
    const u16* xp = x + (size_t)row * D_;
    u32x4 a = *reinterpret_cast<const u32x4*>(xp + lane * 8);
    u32x4 b = {0u, 0u, 0u, 0u};
    if (lane < 8) b = *reinterpret_cast<const u32x4*>(xp + 512 + lane * 8);
    float v[16];
#pragma unroll
    for (int j = 0; j < 4; ++j) {
      v[2 * j] = lo16(a[j]); v[2 * j + 1] = hi16(a[j]);
      v[8 + 2 * j] = lo16(b[j]); v[8 + 2 * j + 1] = hi16(b[j]);
    }
    float s = 0.f;
#pragma unroll
    for (int j = 0; j < 16; ++j) s += v[j];
#pragma unroll
    for (int o = 32; o >= 1; o >>= 1) s += __shfl_xor(s, o);
    const float mean = s * (1.f / D_);
    float ss = 0.f;
#pragma unroll
    for (int j = 0; j < 8; ++j) { float d = v[j] - mean; ss += d * d; }
    if (lane < 8) {
#pragma unroll
      for (int j = 8; j < 16; ++j) { float d = v[j] - mean; ss += d * d; }
    }
#pragma unroll
    for (int o = 32; o >= 1; o >>= 1) ss += __shfl_xor(ss, o);
    const float rstd = rsqrtf(ss * (1.f / D_) + 1e-5f);
    u32x4 oa, ob;
#pragma unroll
    for (int j = 0; j < 4; ++j) {
      oa[j] = pack2((v[2 * j] - mean) * rstd, (v[2 * j + 1] - mean) * rstd);
      ob[j] = pack2((v[8 + 2 * j] - mean) * rstd, (v[8 + 2 * j + 1] - mean) * rstd);
    }
    *reinterpret_cast<u32x4*>(xn + (size_t)row * D_ + lane * 8) = oa;
    if (lane < 8) *reinterpret_cast<u32x4*>(xn + (size_t)row * D_ + 512 + lane * 8) = ob;
    return;
  }
  {
    const int i = (bid - 512) * 256 + tid;
    const int k = i / NPH_, c = i % NPH_;
    const int hh = c / 18, f = c % 18;
    const int h = hh & 31;
    const u16* wp = wqkv + (size_t)k * NQKV_ + hh * 18;
    const u16* pp = wphi + (size_t)h * 324 + f;
    u16 wv[18], pv[18];
#pragma unroll
    for (int d = 0; d < 18; ++d) { wv[d] = wp[d]; pv[d] = pp[d * 18]; }
    float acc = 0.f;
#pragma unroll
    for (int d = 0; d < 18; ++d) acc += bf2f(wv[d]) * bf2f(pv[d]);
    wfold[(size_t)k * NPH_ + c] = f2bf(acc);
  }
}

struct G1Src {
  const u16* wfold;
  const u16* wqkv;
  const u16* w1;
};
struct G1Stage {
  const u16* p[3];
  int st[3];
};
constexpr int G1_BUFB = 64 * 5 * 32;
constexpr int G1_RING = 4;

template <int NT>
__device__ __forceinline__ void g1_setup(const G1Src& src, int tile0, int wid, int lane, G1Stage& sg) {
  constexpr int NCH = 64 * NT * 2, NI = NCH / 64, PER = (NI + 3) / 4;
#pragma unroll
  for (int i = 0; i < PER; ++i) {
    int ins = wid + 4 * i;
    if (ins >= NI) ins -= NI;
    int c = ins * 64 + lane;
    int row = c / (2 * NT), cc = c % (2 * NT);
    int col = tile0 * 16 + cc * 8;
    const u16* p;
    int ld;
    if (col < NPH_) { p = src.wfold + col; ld = NPH_; }
    else if (col < NQKV_) { p = src.wqkv + col; ld = NQKV_; }
    else { p = src.w1 + (col - NQKV_); ld = F_; }
    sg.p[i] = p + (size_t)row * ld;
    sg.st[i] = 64 * ld;
  }
}

template <int NT>
__device__ __forceinline__ void g1_issue(G1Stage& sg, char* buf, int wid, int lane) {
  constexpr int NCH = 64 * NT * 2, NI = NCH / 64, PER = (NI + 3) / 4;
#pragma unroll
  for (int i = 0; i < PER; ++i) {
    int ins = wid + 4 * i;
    if (ins >= NI) ins -= NI;
    glds16(sg.p[i], buf + (ins * 64 + lane) * 16);
    sg.p[i] += sg.st[i];
  }
}
template <int NT> struct G1Per { static constexpr int v = ((64 * NT * 2) / 64 + 3) / 4; };

template <int NT, int RB, int BASE, int F0, int CNT, int I = 0>
__device__ __forceinline__ void g1_reads(WF (&w)[4], unsigned a0, unsigned a1) {
  if constexpr (I < CNT) {
    constexpr int f = F0 + I, ks = f / NT, n = f % NT;
    tr_rd<BASE + ks * 32 * RB + n * 32>(w[I].lo, a0);
    tr_rd<BASE + ks * 32 * RB + n * 32>(w[I].hi, a1);
    g1_reads<NT, RB, BASE, F0, CNT, I + 1>(w, a0, a1);
  }
}
template <int NT, int F0, int CNT, int KC>
__device__ __forceinline__ void g1_mma(WF (&w)[4], const bf16x8 (&xa)[2][18], f32x4 (&acc)[2][NT]) {
#pragma unroll
  for (int i = 0; i < CNT; ++i) pin(w[i]);
#pragma unroll
  for (int i = 0; i < CNT; ++i) {
    const int f = F0 + i, ks = f / NT, n = f % NT;
#pragma unroll
    for (int r = 0; r < 2; ++r)
      acc[r][n] = __builtin_amdgcn_mfma_f32_16x16x32_bf16(cat(w[i]), xa[r][KC * 2 + ks], acc[r][n], 0, 0, 0);
  }
}

template <int NT, int NTN, int GS0>
__device__ __forceinline__ void g1_step(const G1Src& src, G1Stage& sg, int tile0, char* smem,
                                        const bf16x8 (&xa)[2][18], int m0,
                                        u16* __restrict__ qkv, u16* __restrict__ hbuf, const float* __restrict__ bias_lds) {
  constexpr int RB = NT * 32;
  int tz = threadIdx.x;
  asm volatile("" : "+v"(tz));
  const int wid = __builtin_amdgcn_readfirstlane(tz >> 6), lane = tz & 63;
  const int g = lane >> 4, q = (lane & 15) >> 2, p = lane & 3;
  const unsigned a0 = lds_addr(smem) + (8 * g + 4 * (g & 1) + q) * RB + p * 8;
  const unsigned a1 = lds_addr(smem) + (8 * g + 4 * (1 - (g & 1)) + q) * RB + p * 8;
  f32x4 acc[2][NT];
#pragma unroll
  for (int r = 0; r < 2; ++r)
#pragma unroll
    for (int n = 0; n < NT; ++n) acc[r][n] = (f32x4){0.f, 0.f, 0.f, 0.f};

  auto stage = [&](auto KC) {
    constexpr int kc = decltype(KC)::value;
    constexpr int PC = G1Per<NT>::v, PN = (NTN > 0) ? G1Per<(NTN > 0 ? NTN : 1)>::v : 0;
    if constexpr (kc <= 6) wait_vm<2 * PC>();
    else if constexpr (kc == 7) wait_vm<PC + PN>();
    else wait_vm<2 * PN>();
    WAIT_LGKM0();
    BAR();
    if constexpr (kc + 3 < 9) g1_issue<NT>(sg, smem + ((GS0 + kc + 3) % G1_RING) * G1_BUFB, wid, lane);
    else if constexpr (NTN > 0) {
      if constexpr (kc + 3 == 9) g1_setup<(NTN > 0 ? NTN : 1)>(src, tile0 + NT, wid, lane, sg);
      g1_issue<(NTN > 0 ? NTN : 1)>(sg, smem + ((GS0 + kc + 3) % G1_RING) * G1_BUFB, wid, lane);
    }
    constexpr int CUR = ((GS0 + kc) % G1_RING) * G1_BUFB;
    constexpr int NF = 2 * NT;
    constexpr int C0 = 4, C1 = (NF - C0 < 4) ? NF - C0 : 4, C2 = NF - C0 - C1;
    WF wA[4], wB[4], wC[4];
    g1_reads<NT, RB, CUR, 0, C0>(wA, a0, a1);
    g1_reads<NT, RB, CUR, C0, C1>(wB, a0, a1);
    wait_lgkm<2 * C1>();
    g1_mma<NT, 0, C0, kc>(wA, xa, acc);
    if constexpr (C2 > 0) {
      g1_reads<NT, RB, CUR, C0 + C1, C2>(wC, a0, a1);
      wait_lgkm<2 * C2>();
    } else {
      wait_lgkm<0>();
    }
    g1_mma<NT, C0, C1, kc>(wB, xa, acc);
    if constexpr (C2 > 0) {
      wait_lgkm<0>();
      g1_mma<NT, C0 + C1, C2, kc>(wC, xa, acc);
    }
  };
  stage(IC<0>{}); stage(IC<1>{}); stage(IC<2>{}); stage(IC<3>{}); stage(IC<4>{});
  stage(IC<5>{}); stage(IC<6>{}); stage(IC<7>{}); stage(IC<8>{});
#pragma unroll
  for (int n = 0; n < NT; ++n) {
    const int tile = tile0 + n;
    const int ncol = tile * 16 + 4 * g;
    if (tile < NQKV_ / 16) {
#pragma unroll
      for (int r = 0; r < 2; ++r) {
        const int m = m0 + wid * 32 + r * 16 + (lane & 15);
        u32x2 v = {pack2(acc[r][n][0], acc[r][n][1]), pack2(acc[r][n][2], acc[r][n][3])};
        *reinterpret_cast<u32x2*>(qkv + (size_t)m * NQKV_ + ncol) = v;
      }
    } else {
      const int fcol = ncol - NQKV_;
      f32x4 bv = *reinterpret_cast<const f32x4*>(bias_lds + (GS0 / 9) * 80 + n * 16 + 4 * g);
#pragma unroll
      for (int r = 0; r < 2; ++r) {
        const int m = m0 + wid * 32 + r * 16 + (lane & 15);
        float o[4];
#pragma unroll
        for (int j = 0; j < 4; ++j) {
          float v = acc[r][n][j] + bv[j];
          float u = 0.7978845608028654f * (v + 0.044715f * v * v * v);
          float e = __expf(-2.f * u);
          o[j] = v * __builtin_amdgcn_rcpf(1.f + e);
        }
        u32x2 v = {pack2(o[0], o[1]), pack2(o[2], o[3])};
        *reinterpret_cast<u32x2*>(hbuf + (size_t)m * F_ + fcol) = v;
      }
    }
  }
}

__global__ __launch_bounds__(256, 2) void k1_gemm1(const u16* __restrict__ xn, const u16* __restrict__ wfold,
                                                   const u16* __restrict__ wqkv, const u16* __restrict__ w1,
                                                   const u16* __restrict__ b1, u16* __restrict__ qkv,
                                                   u16* __restrict__ hbuf) {
  __shared__ __attribute__((aligned(16))) char smem[G1_RING * G1_BUFB + 2048];
  float* bias_lds = reinterpret_cast<float*>(smem + G1_RING * G1_BUFB);
  const int tid = threadIdx.x, wid = tid >> 6, lane = tid & 63;
  const int bid = blockIdx.x;
  const int mt = bid >> 5, grp = bid & 31;
  const int m0 = mt * 128;
  int tile0 = (grp * 396) >> 5;
  if (tile0 > 396 - 13) tile0 = 396 - 13;
  G1Src src{wfold, wqkv, w1};
  {
    int idx = mt * 256 + tid;
    if (idx >= 576 * 7) idx -= 576 * 7;
    const int row = idx / 7, seg = idx % 7;
    const int col = tile0 * 16 + seg * 32;
    const u16* pp;
    if (col < NPH_) pp = wfold + (size_t)row * NPH_ + col;
    else if (col < NQKV_) pp = wqkv + (size_t)row * NQKV_ + col;
    else pp = w1 + (size_t)row * F_ + (col - NQKV_);
    l2_touch(pp, smem + G1_RING * G1_BUFB + 1024 + tid * 4);
  }
  G1Stage sg;
  g1_setup<5>(src, tile0, wid, lane, sg);
  g1_issue<5>(sg, smem + 0 * G1_BUFB, wid, lane);
  g1_issue<5>(sg, smem + 1 * G1_BUFB, wid, lane);
  g1_issue<5>(sg, smem + 2 * G1_BUFB, wid, lane);
  if (tid < 208) {
    int col = tile0 * 16 + tid - NQKV_;
    bias_lds[tid] = (col >= 0) ? bf2f(b1[col]) : 0.f;
  }
  const int g = lane >> 4;
  const bool odd = g & 1;
  bf16x8 xa[2][18];
#pragma unroll
  for (int r = 0; r < 2; ++r) {
    const int m = m0 + wid * 32 + r * 16 + (lane & 15);
    const u16* xp = xn + (size_t)m * D_ + 8 * g;
#pragma unroll
    for (int ks = 0; ks < 18; ++ks) xa[r][ks] = swap_halves_if(*reinterpret_cast<const bf16x8*>(xp + ks * 32), odd);
  }
  WAIT_VM(0);
  WAIT_LGKM0();
  g1_step<5, 5, 0>(src, sg, tile0, smem, xa, m0, qkv, hbuf, bias_lds);
  g1_step<5, 3, 9>(src, sg, tile0 + 5, smem, xa, m0, qkv, hbuf, bias_lds);
  g1_step<3, 0, 18>(src, sg, tile0 + 10, smem, xa, m0, qkv, hbuf, bias_lds);
}

constexpr int G2_NT = 9, G2_RB = G2_NT * 32;
constexpr int G2_WB = 32 * G2_RB;
constexpr int G2_AB = 128 * 64;
constexpr int G2_BUFB = G2_WB + G2_AB;
constexpr int G2_RING = 4;
constexpr int G2_STAGES = 36;

__device__ __forceinline__ void softmax_pm(const float (&pk)[18], float (&ep)[18], float (&en)[18]) {
  float mx = pk[0], mn = pk[0];
#pragma unroll
  for (int f = 1; f < 18; ++f) {
    mx = fmaxf(mx, pk[f]);
    mn = fminf(mn, pk[f]);
  }
  float sp = 0.f, sn = 0.f;
#pragma unroll
  for (int f = 0; f < 18; ++f) {
    ep[f] = __expf(pk[f] - mx);
    en[f] = __expf(mn - pk[f]);
    sp += ep[f];
    sn += en[f];
  }
  const float rp = 1.f / sp, rn = 1.f / sn;
#pragma unroll
  for (int f = 0; f < 18; ++f) {
    ep[f] *= rp;
    en[f] *= rn;
  }
}

__device__ void head_kv_block(int h, const u16* __restrict__ qkv, const u16* __restrict__ wo,
                              u16* __restrict__ mcat, float* __restrict__ zbuf, char* smem) {
  u16* phiT = reinterpret_cast<u16*>(smem);
  u16* vT = reinterpret_cast<u16*>(smem + 25344);
  float* kvs = reinterpret_cast<float*>(smem + 25344 + 16896);
  const int tid = threadIdx.x, wid = tid >> 6, lane = tid & 63;
  for (int i = tid; i < 12 * 264; i += 256) phiT[36 * 264 + i] = 0;
  for (int i = tid; i < 14 * 264; i += 256) vT[18 * 264 + i] = (i < 264) ? (u16)0x3f80 : (u16)0;
  f32x4 acc[3][2];
#pragma unroll
  for (int a = 0; a < 3; ++a)
#pragma unroll
    for (int b = 0; b < 2; ++b) acc[a][b] = (f32x4){0.f, 0.f, 0.f, 0.f};
#pragma unroll 1
  for (int ch = 0; ch < 8; ++ch) {
    const int row = ch * 256 + tid;
    const u16* kp = qkv + (size_t)row * NQKV_ + D_ + h * 18;
    const u16* vp = qkv + (size_t)row * NQKV_ + 2 * D_ + h * 18;
    float pk[18];
    unsigned vv[9];
#pragma unroll
    for (int d = 0; d < 9; ++d) {
      unsigned t = *reinterpret_cast<const unsigned*>(kp + 2 * d);
      pk[2 * d] = lo16(t);
      pk[2 * d + 1] = hi16(t);
      vv[d] = *reinterpret_cast<const unsigned*>(vp + 2 * d);
    }
    float ep[18], en[18];
    softmax_pm(pk, ep, en);
    __syncthreads();
#pragma unroll
    for (int f = 0; f < 18; ++f) {
      phiT[f * 264 + tid] = f2bf(ep[f]);
      phiT[(18 + f) * 264 + tid] = f2bf(en[f]);
      vT[f * 264 + tid] = (u16)((f & 1) ? (vv[f >> 1] >> 16) : (vv[f >> 1] & 0xffff));
    }
    __syncthreads();
#pragma unroll
    for (int ks = 0; ks < 2; ++ks) {
      const int s0 = wid * 64 + ks * 32 + 8 * (lane >> 4);
      bf16x8 bv[2];
#pragma unroll
      for (int b = 0; b < 2; ++b) bv[b] = *reinterpret_cast<const bf16x8*>(vT + (b * 16 + (lane & 15)) * 264 + s0);
#pragma unroll
      for (int a = 0; a < 3; ++a) {
        bf16x8 av = *reinterpret_cast<const bf16x8*>(phiT + (a * 16 + (lane & 15)) * 264 + s0);
#pragma unroll
        for (int b = 0; b < 2; ++b) acc[a][b] = __builtin_amdgcn_mfma_f32_16x16x32_bf16(av, bv[b], acc[a][b], 0, 0, 0);
      }
    }
  }
  __syncthreads();
#pragma unroll
  for (int a = 0; a < 3; ++a)
#pragma unroll
    for (int b = 0; b < 2; ++b)
#pragma unroll
      for (int j = 0; j < 4; ++j)
        kvs[(wid * 48 + a * 16 + (lane >> 4) * 4 + j) * 32 + b * 16 + (lane & 15)] = acc[a][b][j];
  u16* woimg = reinterpret_cast<u16*>(smem);
  u16* kvb = reinterpret_cast<u16*>(smem + 36864);
  for (int i = tid; i < 32 * 72; i += 256) {
    const int r = i / 72, c = i % 72;
    u32x4 v = {0u, 0u, 0u, 0u};
    if (r < 18) v = *reinterpret_cast<const u32x4*>(wo + (size_t)(h * 18 + r) * D_ + c * 8);
    *reinterpret_cast<u32x4*>(woimg + r * 576 + c * 8) = v;
  }
  __syncthreads();
  for (int i = tid; i < 48 * 32; i += 256) {
    float v = kvs[i] + kvs[48 * 32 + i] + kvs[2 * 48 * 32 + i] + kvs[3 * 48 * 32 + i];
    const int f = i >> 5, d = i & 31;
    if (d == 18 && f < 36) zbuf[h * 36 + f] = rbf(v);
    kvb[i] = (d < 18 && f < 36) ? f2bf(v) : (u16)0;
  }
  __syncthreads();
  {
    const int g = lane >> 4, q = (lane & 15) >> 2, p = lane & 3;
    constexpr int RB = 576 * 2;
    const int o0 = (8 * g + q) * RB + p * 8, o1 = (8 * g + 4 + q) * RB + p * 8;
    bf16x8 kb[3];
#pragma unroll
    for (int a = 0; a < 3; ++a) kb[a] = *reinterpret_cast<const bf16x8*>(kvb + (a * 16 + (lane & 15)) * 32 + 8 * g);
    for (int nt = wid; nt < 36; nt += 4) {
      bf16x8 w = wfrag(reinterpret_cast<const char*>(woimg), o0, o1, nt * 32);
#pragma unroll
      for (int a = 0; a < 3; ++a) {
        f32x4 c = __builtin_amdgcn_mfma_f32_16x16x32_bf16(w, kb[a], (f32x4){0.f, 0.f, 0.f, 0.f}, 0, 0, 0);
        const int f = a * 16 + (lane & 15);
        if (f < KPH_) {
          u32x2 v = {pack2(c[0], c[1]), pack2(c[2], c[3])};
          *reinterpret_cast<u32x2*>(mcat + (size_t)(h * KPH_ + f) * D_ + nt * 16 + 4 * g) = v;
        }
      }
    }
  }
}

__device__ void head_q_block(int hb, const u16* __restrict__ qkv, u16* __restrict__ phi) {
  const int h = hb >> 1, tid = threadIdx.x;
#pragma unroll 1
  for (int ch = 0; ch < 4; ++ch) {
    const int row = (hb & 1) * 1024 + ch * 256 + tid;
    const u16* kp = qkv + (size_t)row * NQKV_ + h * 18;
    float pk[18];
#pragma unroll
    for (int d = 0; d < 9; ++d) {
      unsigned t = *reinterpret_cast<const unsigned*>(kp + 2 * d);
      pk[2 * d] = lo16(t);
      pk[2 * d + 1] = hi16(t);
    }
    float ep[18], en[18];
    softmax_pm(pk, ep, en);
    unsigned o[20];
#pragma unroll
    for (int f = 0; f < 9; ++f) o[f] = pack2(ep[2 * f], ep[2 * f + 1]);
#pragma unroll
    for (int f = 0; f < 9; ++f) o[9 + f] = pack2(en[2 * f], en[2 * f + 1]);
    o[18] = 0;
    o[19] = 0;
    u16* op = phi + (size_t)row * KPHI_ + h * KPH_;
#pragma unroll
    for (int c = 0; c < 5; ++c) {
      u32x4 v = {o[4 * c], o[4 * c + 1], o[4 * c + 2], o[4 * c + 3]};
      *reinterpret_cast<u32x4*>(op + c * 8) = v;
    }
  }
}

template <int BASE, int N = 0>
__device__ __forceinline__ void g2_wreads(WF (&w)[G2_NT], unsigned a0, unsigned a1) {
  if constexpr (N < G2_NT) {
    tr_rd<BASE + N * 32>(w[N].lo, a0);
    tr_rd<BASE + N * 32>(w[N].hi, a1);
    g2_wreads<BASE, N + 1>(w, a0, a1);
  }
}
template <int BASE>
__device__ __forceinline__ void g2_reads(bf16x8 (&ar)[2], WF (&w)[G2_NT], unsigned aa0, unsigned aa1, unsigned wa0, unsigned wa1) {
  b128_rd<BASE>(ar[0], aa0);
  b128_rd<BASE>(ar[1], aa1);
  g2_wreads<BASE>(w, wa0, wa1);
}

constexpr int K2_HEADBLK = 96;

__global__ __launch_bounds__(256, 2) void k2_gemm2(const u16* __restrict__ hbuf, const u16* __restrict__ w2,
                                                   float* __restrict__ part, const u16* __restrict__ qkv,
                                                   const u16* __restrict__ wo, u16* __restrict__ phi,
                                                   u16* __restrict__ mcat, float* __restrict__ zbuf) {
  __shared__ __attribute__((aligned(16))) char smem[G2_RING * G2_BUFB + 1024];
  const int tid = threadIdx.x, wid = tid >> 6, lane = tid & 63;
  int bid = blockIdx.x;
  if (bid < 32) { head_kv_block(bid, qkv, wo, mcat, zbuf, smem); return; }
  if (bid < K2_HEADBLK) { head_q_block(bid - 32, qkv, phi); return; }
  bid -= K2_HEADBLK;
  const int xcd = bid & 7, loc = bid >> 3;
  const int sp = xcd >> 1, mt = (xcd & 1) * 8 + (loc >> 2), nt4 = loc & 3;
  const int m0 = mt * 128, n0 = nt4 * 144, k0 = sp * 1152;
  {
    char* dummy = smem + G2_RING * G2_BUFB + tid * 4;
#pragma unroll
    for (int i = 0; i < 4; ++i) {
      int idx = (loc * 4 + i) * 256 + tid;
      if (idx < 18432) {
        const int row = idx / 18, seg = idx % 18;
        l2_touch(hbuf + (size_t)((xcd & 1) * 1024 + row) * F_ + k0 + seg * 64, dummy);
      } else {
        int j = idx - 18432;
        if (j >= 10368) j -= 10368;
        const int row = j / 9, seg = j % 9;
        l2_touch(w2 + (size_t)(k0 + row) * D_ + seg * 64, dummy);
      }
    }
  }
  const int g = lane >> 4, q = (lane & 15) >> 2, p = lane & 3;
  const bool odd = g & 1;
  const int o0 = (8 * g + 4 * (g & 1) + q) * G2_RB + p * 8;
  const int o1 = (8 * g + 4 * (1 - (g & 1)) + q) * G2_RB + p * 8;
  f32x4 acc[2][G2_NT];
#pragma unroll
  for (int r = 0; r < 2; ++r)
#pragma unroll
    for (int n = 0; n < G2_NT; ++n) acc[r][n] = (f32x4){0.f, 0.f, 0.f, 0.f};
  const u16* wp[3];
  int wdst[3];
#pragma unroll
  for (int i = 0; i < 3; ++i) {
    int ins = wid + 4 * i;
    if (ins >= 9) ins -= 9;
    int c = ins * 64 + lane;
    int row = c / 18, cc = c % 18;
    wp[i] = w2 + (size_t)(k0 + row) * D_ + n0 + cc * 8;
    wdst[i] = c * 16;
  }
  const u16* apg[2];
  int adst[2];
#pragma unroll
  for (int i = 0; i < 2; ++i) {
    int row = wid * 32 + i * 16 + (lane >> 2), pos = lane & 3;
    int ch = pos ^ ((4 - ((row >> 2) & 3)) & 3);
    apg[i] = hbuf + (size_t)(m0 + row) * F_ + k0 + ch * 8;
    adst[i] = G2_WB + row * 64 + pos * 16;
  }
  int aoff[2];
#pragma unroll
  for (int r = 0; r < 2; ++r) {
    int row = wid * 32 + r * 16 + (lane & 15);
    aoff[r] = G2_WB + row * 64 + ((g ^ ((4 - ((row >> 2) & 3)) & 3)) * 16);
  }
  const unsigned aa0 = lds_addr(smem) + aoff[0], aa1 = lds_addr(smem) + aoff[1];
  const unsigned wa0 = lds_addr(smem) + o0, wa1 = lds_addr(smem) + o1;
  auto issue = [&](int slot) {
    char* buf = smem + slot * G2_BUFB;
#pragma unroll
    for (int i = 0; i < 3; ++i) { glds16(wp[i], buf + wdst[i]); wp[i] += 32 * D_; }
#pragma unroll
    for (int i = 0; i < 2; ++i) { glds16(apg[i], buf + adst[i]); apg[i] += 32; }
  };
  issue(0);
  issue(1);
  issue(2);
#pragma unroll 1
  for (int st0 = 0; st0 < G2_STAGES; st0 += 4) {
#pragma unroll
    for (int u = 0; u < 4; ++u) {
      const int st = st0 + u;
      if (st < G2_STAGES - 2) WAIT_VM(10);
      else if (st == G2_STAGES - 2) WAIT_VM(5);
      else WAIT_VM(0);
      WAIT_LGKM0();
      BAR();
      if (st + 3 < G2_STAGES) issue((u + 3) & 3);
      bf16x8 ar[2];
      WF w[G2_NT];
      if (u == 0) g2_reads<0 * G2_BUFB>(ar, w, aa0, aa1, wa0, wa1);
      if (u == 1) g2_reads<1 * G2_BUFB>(ar, w, aa0, aa1, wa0, wa1);
      if (u == 2) g2_reads<2 * G2_BUFB>(ar, w, aa0, aa1, wa0, wa1);
      if (u == 3) g2_reads<3 * G2_BUFB>(ar, w, aa0, aa1, wa0, wa1);
      wait_lgkm<10>();
      pin(ar[0]); pin(ar[1]);
      bf16x8 ac[2];
#pragma unroll
      for (int r = 0; r < 2; ++r) ac[r] = swap_halves_if(ar[r], odd);
#pragma unroll
      for (int n = 0; n < 4; ++n) pin(w[n]);
#pragma unroll
      for (int n = 0; n < 4; ++n)
#pragma unroll
        for (int r = 0; r < 2; ++r) acc[r][n] = __builtin_amdgcn_mfma_f32_16x16x32_bf16(cat(w[n]), ac[r], acc[r][n], 0, 0, 0);
      wait_lgkm<4>();
#pragma unroll
      for (int n = 4; n < 7; ++n) pin(w[n]);
#pragma unroll
      for (int n = 4; n < 7; ++n)
#pragma unroll
        for (int r = 0; r < 2; ++r) acc[r][n] = __builtin_amdgcn_mfma_f32_16x16x32_bf16(cat(w[n]), ac[r], acc[r][n], 0, 0, 0);
      wait_lgkm<0>();
#pragma unroll
      for (int n = 7; n < 9; ++n) pin(w[n]);
#pragma unroll
      for (int n = 7; n < 9; ++n)
#pragma unroll
        for (int r = 0; r < 2; ++r) acc[r][n] = __builtin_amdgcn_mfma_f32_16x16x32_bf16(cat(w[n]), ac[r], acc[r][n], 0, 0, 0);
    }
  }
#pragma unroll
  for (int r = 0; r < 2; ++r) {
    const int m = m0 + wid * 32 + r * 16 + (lane & 15);
    float* pp = part + ((size_t)sp * S_ + m) * D_ + n0 + 4 * g;
#pragma unroll
    for (int n = 0; n < G2_NT; ++n) *reinterpret_cast<f32x4*>(pp + n * 16) = acc[r][n];
  }
}

constexpr int G3_NT = 3, G3_RB = G3_NT * 32;
constexpr int G3_WB = 32 * G3_RB;
constexpr int G3_AB = 128 * 64;
constexpr int G3_BUFB = G3_WB + G3_AB;
constexpr int G3_RING = 4;
constexpr int G3_STAGES = 40;

__device__ __forceinline__ bf16x8 scale_frag(bf16x8 v, float s, bool odd) {
  u32x4 in = *reinterpret_cast<u32x4*>(&v);
  u32x4 pk;
#pragma unroll
  for (int j = 0; j < 4; ++j) pk[j] = pack2(lo16(in[j]) * s, hi16(in[j]) * s);
  return swap_halves_if(*reinterpret_cast<bf16x8*>(&pk), odd);
}

template <int BASE>
__device__ __forceinline__ void g3_reads(bf16x8 (&ar)[2], WF (&w)[G3_NT], unsigned aa0, unsigned aa1, unsigned wa0, unsigned wa1) {
  b128_rd<BASE>(ar[0], aa0);
  b128_rd<BASE>(ar[1], aa1);
  tr_rd<BASE + 0>(w[0].lo, wa0); tr_rd<BASE + 0>(w[0].hi, wa1);
  tr_rd<BASE + 32>(w[1].lo, wa0); tr_rd<BASE + 32>(w[1].hi, wa1);
  tr_rd<BASE + 64>(w[2].lo, wa0); tr_rd<BASE + 64>(w[2].hi, wa1);
}

__global__ __launch_bounds__(256, 2) void k3_final(const u16* __restrict__ phi, const u16* __restrict__ mcat,
                                                   const float* __restrict__ zbuf, const float* __restrict__ part,
                                                   const u16* __restrict__ b2, const u16* __restrict__ x,
                                                   u16* __restrict__ out) {
  __shared__ __attribute__((aligned(16))) char smem[G3_RING * G3_BUFB + 128 * 33 * 4 + 32 * 36 * 4 + 1024];
  float* rden = reinterpret_cast<float*>(smem + G3_RING * G3_BUFB);
  float* zs = reinterpret_cast<float*>(smem + G3_RING * G3_BUFB + 128 * 33 * 4);
  const int tid = threadIdx.x, wid = tid >> 6, lane = tid & 63;
  const int bid = blockIdx.x;
  const int xcd = bid & 7, loc = bid >> 3;
  const int mt = xcd * 2 + loc / 12, nt = loc % 12;
  const int m0 = mt * 128, n0 = nt * 48;
  const int g = lane >> 4, q = (lane & 15) >> 2, p = lane & 3;
  const bool odd = g & 1;
  {
    char* dummy = smem + G3_RING * G3_BUFB + 128 * 33 * 4 + 32 * 36 * 4 + tid * 4;
#pragma unroll
    for (int i = 0; i < 3; ++i) {
      int idx = (loc * 3 + i) * 256 + tid;
      if (idx < 5120) {
        const int row = idx / 20, seg = idx % 20;
        l2_touch(phi + (size_t)(xcd * 256 + row) * KPHI_ + seg * 64, dummy);
      } else {
        int j = idx - 5120;
        if (j >= 11520) j -= 11520;
        const int row = j / 9, seg = j % 9;
        l2_touch(mcat + (size_t)row * D_ + seg * 64, dummy);
      }
    }
  }
  const u16* wp;
  int wdst;
  {
    int ins = wid < 3 ? wid : 0;
    int c = ins * 64 + lane;
    int row = c / 6, cc = c % 6;
    wp = mcat + (size_t)row * D_ + n0 + cc * 8;
    wdst = c * 16;
  }
  const u16* apg[2];
  int adst[2];
#pragma unroll
  for (int i = 0; i < 2; ++i) {
    int row = wid * 32 + i * 16 + (lane >> 2), pos = lane & 3;
    int ch = pos ^ ((4 - ((row >> 2) & 3)) & 3);
    apg[i] = phi + (size_t)(m0 + row) * KPHI_ + ch * 8;
    adst[i] = G3_WB + row * 64 + pos * 16;
  }
  auto issue = [&](int slot) {
    char* buf = smem + slot * G3_BUFB;
    glds16(wp, buf + wdst);
    wp += 32 * D_;
#pragma unroll
    for (int i = 0; i < 2; ++i) { glds16(apg[i], buf + adst[i]); apg[i] += 32; }
  };
  issue(0);
  issue(1);
  issue(2);
  for (int i = tid; i < 32 * 36; i += 256) zs[i] = zbuf[i];
  __syncthreads();
  for (int i = tid; i < 128 * 32; i += 256) {
    const int r = i >> 5, h = i & 31;
    const u16* pp = phi + (size_t)(m0 + r) * KPHI_ + h * KPH_;
    float d = 0.f;
#pragma unroll
    for (int c = 0; c < 5; ++c) {
      u32x4 v = *reinterpret_cast<const u32x4*>(pp + c * 8);
#pragma unroll
      for (int j = 0; j < 4; ++j) {
        if (c * 8 + 2 * j < 36) d += lo16(v[j]) * zs[h * 36 + c * 8 + 2 * j];
        if (c * 8 + 2 * j + 1 < 36) d += hi16(v[j]) * zs[h * 36 + c * 8 + 2 * j + 1];
      }
    }
    rden[r * 33 + h] = 1.f / (rbf(d) + 1e-6f);
  }
  const int o0 = (8 * g + 4 * (g & 1) + q) * G3_RB + p * 8;
  const int o1 = (8 * g + 4 * (1 - (g & 1)) + q) * G3_RB + p * 8;
  f32x4 acc[2][G3_NT];
#pragma unroll
  for (int r = 0; r < 2; ++r)
#pragma unroll
    for (int n = 0; n < G3_NT; ++n) acc[r][n] = (f32x4){0.f, 0.f, 0.f, 0.f};
  int aoff[2], rl[2];
#pragma unroll
  for (int r = 0; r < 2; ++r) {
    rl[r] = wid * 32 + r * 16 + (lane & 15);
    aoff[r] = G3_WB + rl[r] * 64 + ((g ^ ((4 - ((rl[r] >> 2) & 3)) & 3)) * 16);
  }
  const unsigned aa0 = lds_addr(smem) + aoff[0], aa1 = lds_addr(smem) + aoff[1];
  const unsigned wa0 = lds_addr(smem) + o0, wa1 = lds_addr(smem) + o1;
  __syncthreads();
#pragma unroll 1
  for (int st0 = 0; st0 < G3_STAGES; st0 += 4) {
#pragma unroll
    for (int u = 0; u < 4; ++u) {
      const int st = st0 + u;
      if (st < G3_STAGES - 2) WAIT_VM(6);
      else if (st == G3_STAGES - 2) WAIT_VM(3);
      else WAIT_VM(0);
      WAIT_LGKM0();
      BAR();
      if (st + 3 < G3_STAGES) issue((u + 3) & 3);
      const int h = (st * 32 + 8 * g) / KPH_;
      float sc[2];
#pragma unroll
      for (int r = 0; r < 2; ++r) sc[r] = rden[rl[r] * 33 + h];
      asm volatile("" : "+v"(sc[0]), "+v"(sc[1]));
      bf16x8 ar[2];
      WF w[G3_NT];
      if (u == 0) g3_reads<0 * G3_BUFB>(ar, w, aa0, aa1, wa0, wa1);
      if (u == 1) g3_reads<1 * G3_BUFB>(ar, w, aa0, aa1, wa0, wa1);
      if (u == 2) g3_reads<2 * G3_BUFB>(ar, w, aa0, aa1, wa0, wa1);
      if (u == 3) g3_reads<3 * G3_BUFB>(ar, w, aa0, aa1, wa0, wa1);
      wait_lgkm<6>();
      pin(ar[0]); pin(ar[1]);
      bf16x8 ac[2];
#pragma unroll
      for (int r = 0; r < 2; ++r) ac[r] = scale_frag(ar[r], sc[r], odd);
      wait_lgkm<0>();
#pragma unroll
      for (int n = 0; n < G3_NT; ++n) pin(w[n]);
#pragma unroll
      for (int n = 0; n < G3_NT; ++n)
#pragma unroll
        for (int r = 0; r < 2; ++r) acc[r][n] = __builtin_amdgcn_mfma_f32_16x16x32_bf16(cat(w[n]), ac[r], acc[r][n], 0, 0, 0);
    }
  }
#pragma unroll
  for (int r = 0; r < 2; ++r) {
    const int m = m0 + rl[r];
#pragma unroll
    for (int n = 0; n < G3_NT; ++n) {
      const int ncol = n0 + n * 16 + 4 * g;
      f32x4 y = *reinterpret_cast<const f32x4*>(part + (size_t)m * D_ + ncol);
#pragma unroll
      for (int s = 1; s < SPLITK_; ++s) {
        f32x4 t = *reinterpret_cast<const f32x4*>(part + ((size_t)s * S_ + m) * D_ + ncol);
        y += t;
      }
      u32x2 bb = *reinterpret_cast<const u32x2*>(b2 + ncol);
      u32x2 xx = *reinterpret_cast<const u32x2*>(x + (size_t)m * D_ + ncol);
      float bv[4] = {lo16(bb[0]), hi16(bb[0]), lo16(bb[1]), hi16(bb[1])};
      float xv[4] = {lo16(xx[0]), hi16(xx[0]), lo16(xx[1]), hi16(xx[1])};
      float o[4];
#pragma unroll
      for (int j = 0; j < 4; ++j) {
        float yy = y[j] + bv[j];
        float gg = yy / (1.f + __expf(-yy));
        o[j] = xv[j] + acc[r][n][j] * gg;
      }
      u32x2 v = {pack2(o[0], o[1]), pack2(o[2], o[3])};
      *reinterpret_cast<u32x2*>(out + (size_t)m * D_ + ncol) = v;
    }
  }
}

extern "C" void kernel_launch(void* const* d_in, const int* in_sizes, int n_in, void* d_out, int out_size, void* d_ws,
                              size_t ws_size, hipStream_t stream) {
  const u16* x = (const u16*)d_in[0];
  const u16* wqkv = (const u16*)d_in[1];
  const u16* wphi = (const u16*)d_in[2];
  const u16* wo = (const u16*)d_in[3];
  const u16* w1 = (const u16*)d_in[5];
  const u16* b1 = (const u16*)d_in[6];
  const u16* w2 = (const u16*)d_in[7];
  const u16* b2 = (const u16*)d_in[8];
  u16* out = (u16*)d_out;
  char* ws = (char*)d_ws;
  size_t off = 0;
  u16* xn = (u16*)(ws + off); off += (size_t)S_ * D_ * 2;
  u16* wfold = (u16*)(ws + off); off += (size_t)D_ * NPH_ * 2;
  u16* qkv = (u16*)(ws + off); off += (size_t)S_ * NQKV_ * 2;
  u16* hbuf = (u16*)(ws + off); off += (size_t)S_ * F_ * 2;
  float* part = (float*)(ws + off); off += (size_t)SPLITK_ * S_ * D_ * 4;
  u16* phi = (u16*)(ws + off); off += (size_t)S_ * KPHI_ * 2;
  u16* mcat = (u16*)(ws + off); off += (size_t)KPHI_ * D_ * 2;
  float* zbuf = (float*)(ws + off); off += 32 * 36 * 4;

  k0_prep<<<512 + 2592, 256, 0, stream>>>(x, wqkv, wphi, xn, wfold);
  k1_gemm1<<<512, 256, 0, stream>>>(xn, wfold, wqkv, w1, b1, qkv, hbuf);
  k2_gemm2<<<K2_HEADBLK + 256, 256, 0, stream>>>(hbuf, w2, part, qkv, wo, phi, mcat, zbuf);
  k3_final<<<16 * 12, 256, 0, stream>>>(phi, mcat, zbuf, part, b2, x, out);
}
```

```cpp
#include <hip/hip_runtime.h>
#include <hip/hip_bf16.h>
#include <stdint.h>

constexpr int S_ = 2048, D_ = 576, H_ = 32, DH_ = 18, F_ = 4608, NQKV_ = 1728;
constexpr int NPH_ = 1152;
constexpr int KPH_ = 40;
constexpr int KPHI_ = H_ * KPH_;
constexpr int SPLITK_ = 4;

typedef short bf16x8 __attribute__((ext_vector_type(8)));
typedef short s16x4 __attribute__((ext_vector_type(4)));
typedef float f32x4 __attribute__((ext_vector_type(4)));
typedef unsigned u32x4 __attribute__((ext_vector_type(4)));
typedef unsigned u32x2 __attribute__((ext_vector_type(2)));
typedef __attribute__((address_space(3))) s16x4* lds_s16x4_p;
typedef unsigned short u16;
template <int V> struct IC { static constexpr int value = V; };

#define WAIT_LGKM0() asm volatile("s_waitcnt lgkmcnt(0)" ::: "memory")
#define BAR() __builtin_amdgcn_s_barrier()

template <int N>
__device__ __forceinline__ void wait_vm() {
  static_assert(N >= 0 && N < 64, "vmcnt range");
  asm volatile("s_waitcnt vmcnt(%0)" ::"n"(N) : "memory");
}

__device__ __forceinline__ float bf2f(u16 v) { return __uint_as_float(((unsigned)v) << 16); }
__device__ __forceinline__ u16 f2bf(float f) {
  __hip_bfloat16 b = __float2bfloat16(f);
  return *reinterpret_cast<u16*>(&b);
}
__device__ __forceinline__ unsigned pack2(float lo, float hi) {
  return (unsigned)f2bf(lo) | ((unsigned)f2bf(hi) << 16);
}
__device__ __forceinline__ float rbf(float f) { return bf2f(f2bf(f)); }
__device__ __forceinline__ float lo16(unsigned u) { return __uint_as_float(u << 16); }
__device__ __forceinline__ float hi16(unsigned u) { return __uint_as_float(u & 0xffff0000u); }

__device__ __forceinline__ void glds16(const void* g, void* l) {
  __builtin_amdgcn_global_load_lds((const unsigned*)g, (unsigned*)l, 16, 0, 0);
}

__device__ __forceinline__ bf16x8 swap_halves_if(bf16x8 v, bool odd) {
  bf16x8 s = __builtin_shufflevector(v, v, 4, 5, 6, 7, 0, 1, 2, 3);
  return odd ? s : v;
}

__device__ __forceinline__ bf16x8 wfrag(const char* img, int o0, int o1, int off) {
  s16x4 a = __builtin_amdgcn_ds_read_tr16_b64_v4i16((lds_s16x4_p)(img + o0 + off));
  s16x4 b = __builtin_amdgcn_ds_read_tr16_b64_v4i16((lds_s16x4_p)(img + o1 + off));
  return __builtin_shufflevector(a, b, 0, 1, 2, 3, 4, 5, 6, 7);
}

struct WF { s16x4 lo, hi; };
template <int OFF>
__device__ __forceinline__ void tr_rd(s16x4& r, unsigned addr) {
  asm volatile("ds_read_b64_tr_b16 %0, %1 offset:%2" : "=v"(r) : "v"(addr), "n"(OFF) : "memory");
}
__device__ __forceinline__ void gld16_asm(bf16x8& r, const void* p) {
  asm volatile("global_load_dwordx4 %0, %1, off" : "=v"(r) : "v"(p) : "memory");
}
template <int N>
__device__ __forceinline__ void wait_lgkm() {
  static_assert(N >= 0 && N <= 15, "lgkmcnt range");
  asm volatile("s_waitcnt lgkmcnt(%0)" ::"n"(N) : "memory");
}
__device__ __forceinline__ void pin(WF& w) { asm volatile("" : "+v"(w.lo), "+v"(w.hi)); }
__device__ __forceinline__ void pin(bf16x8& w) { asm volatile("" : "+v"(w)); }
__device__ __forceinline__ bf16x8 cat(const WF& w) { return __builtin_shufflevector(w.lo, w.hi, 0, 1, 2, 3, 4, 5, 6, 7); }
__device__ __forceinline__ unsigned lds_addr(const void* p) {
  return (unsigned)(uintptr_t)(__attribute__((address_space(3))) const char*)p;
}

__global__ __launch_bounds__(256) void k0_prep(const u16* __restrict__ x, const u16* __restrict__ wqkv,
                                               const u16* __restrict__ wphi, u16* __restrict__ xnf,
                                               u16* __restrict__ wfold) {
  __shared__ __attribute__((aligned(16))) float wph[18 * 20];
  const int tid = threadIdx.x, wid = tid >> 6, lane = tid & 63;
  const int bid = blockIdx.x;
  if (bid < 512) {
    const int row = bid * 4 + wid;
    const u16* xp = x + (size_t)row * D_;
    u32x4 a = *reinterpret_cast<const u32x4*>(xp + lane * 8);
    u32x4 b = {0u, 0u, 0u, 0u};
    if (lane < 8) b = *reinterpret_cast<const u32x4*>(xp + 512 + lane * 8);
    float v[16];
#pragma unroll
    for (int j = 0; j < 4; ++j) {
      v[2 * j] = lo16(a[j]); v[2 * j + 1] = hi16(a[j]);
      v[8 + 2 * j] = lo16(b[j]); v[8 + 2 * j + 1] = hi16(b[j]);
    }
    float s = 0.f;
#pragma unroll
    for (int j = 0; j < 16; ++j) s += v[j];
#pragma unroll
    for (int o = 32; o >= 1; o >>= 1) s += __shfl_xor(s, o);
    const float mean = s * (1.f / D_);
    float ss = 0.f;
#pragma unroll
    for (int j = 0; j < 8; ++j) { float d = v[j] - mean; ss += d * d; }
    if (lane < 8) {
#pragma unroll
      for (int j = 8; j < 16; ++j) { float d = v[j] - mean; ss += d * d; }
    }
#pragma unroll
    for (int o = 32; o >= 1; o >>= 1) ss += __shfl_xor(ss, o);
    const float rstd = rsqrtf(ss * (1.f / D_) + 1e-5f);
    u32x4 oa, ob;
#pragma unroll
    for (int j = 0; j < 4; ++j) {
      oa[j] = pack2((v[2 * j] - mean) * rstd, (v[2 * j + 1] - mean) * rstd);
      ob[j] = pack2((v[8 + 2 * j] - mean) * rstd, (v[8 + 2 * j + 1] - mean) * rstd);
    }
    {
      const int c = lane;
      u16* dst = xnf + ((size_t)((row >> 4) * 18 + (c >> 2)) * 64 + (c & 3) * 16 + (row & 15)) * 8;
      *reinterpret_cast<u32x4*>(dst) = oa;
    }
    if (lane < 8) {
      const int c = 64 + lane;
      u16* dst = xnf + ((size_t)((row >> 4) * 18 + (c >> 2)) * 64 + (c & 3) * 16 + (row & 15)) * 8;
      *reinterpret_cast<u32x4*>(dst) = ob;
    }
    return;
  }
  {
    const int fb = bid - 512;
    const int hh = fb / 3, k = (fb % 3) * 192 + tid;
    const int h = hh & 31;
    for (int i = tid; i < 18 * 20; i += 256) {
      const int d = i / 20, f = i % 20;
      wph[i] = (f < 18) ? bf2f(wphi[h * 324 + d * 18 + f]) : 0.f;
    }
    unsigned wv[9];
    if (tid < 192) {
      const u16* wp = wqkv + (size_t)k * NQKV_ + hh * 18;
#pragma unroll
      for (int d = 0; d < 9; ++d) wv[d] = *reinterpret_cast<const unsigned*>(wp + 2 * d);
    }
    __syncthreads();
    if (tid < 192) {
      float acc[20];
#pragma unroll
      for (int f = 0; f < 20; ++f) acc[f] = 0.f;
#pragma unroll
      for (int d = 0; d < 18; ++d) {
        const float w = (d & 1) ? hi16(wv[d >> 1]) : lo16(wv[d >> 1]);
#pragma unroll
        for (int f4 = 0; f4 < 5; ++f4) {
          f32x4 pw = *reinterpret_cast<const f32x4*>(wph + d * 20 + f4 * 4);
#pragma unroll
          for (int j = 0; j < 4; ++j) acc[f4 * 4 + j] += w * pw[j];
        }
      }
      u16* op = wfold + (size_t)k * NPH_ + hh * 18;
#pragma unroll
      for (int f = 0; f < 9; ++f) *reinterpret_cast<unsigned*>(op + 2 * f) = pack2(acc[2 * f], acc[2 * f + 1]);
    }
  }
}

constexpr int G1_BUFB = 64 * 128;
constexpr int G1_RING = 8;
constexpr int G1_XOFF = G1_RING * G1_BUFB;
constexpr int G1_BOFF = G1_XOFF + 576 * 32;
constexpr int G1_SMEM = G1_BOFF + 1024;

template <int BASE, int F0, int CNT, int I = 0>
__device__ __forceinline__ void g1_reads(WF (&w)[4], const unsigned (&ad)[4][2]) {
  if constexpr (I < CNT) {
    constexpr int f = F0 + I, ks = f / 4, n = f % 4;
    tr_rd<BASE + ks * 32 * 128>(w[I].lo, ad[n][0]);
    tr_rd<BASE + ks * 32 * 128>(w[I].hi, ad[n][1]);
    g1_reads<BASE, F0, CNT, I + 1>(w, ad);
  }
}
template <int F0, int KC>
__device__ __forceinline__ void g1_mma(WF (&w)[4], const bf16x8 (&xa)[2][18], f32x4 (&acc)[2][4]) {
#pragma unroll
  for (int i = 0; i < 4; ++i) pin(w[i]);
#pragma unroll
  for (int i = 0; i < 4; ++i) {
    const int f = F0 + i, ks = f / 4, n = f % 4;
#pragma unroll
    for (int r = 0; r < 2; ++r)
      acc[r][n] = __builtin_amdgcn_mfma_f32_16x16x32_bf16(cat(w[i]), xa[r][KC * 2 + ks], acc[r][n], 0, 0, 0);
  }
}

__device__ __forceinline__ float gelu_f(float v) {
  float u = 0.7978845608028654f * (v + 0.044715f * v * v * v);
  float e = __expf(-2.f * u);
  return v * __builtin_amdgcn_rcpf(1.f + e);
}

__device__ __forceinline__ void g1_store_tile(const f32x4& a, int tile, int mrow, int g, const float* bias4,
                                              u16* __restrict__ qkv, u16* __restrict__ hf) {
  if (tile < NQKV_ / 16) {
    u32x2 v = {pack2(a[0], a[1]), pack2(a[2], a[3])};
    *reinterpret_cast<u32x2*>(qkv + (size_t)mrow * NQKV_ + tile * 16 + 4 * g) = v;
  } else {
    f32x4 bv = *reinterpret_cast<const f32x4*>(bias4);
    float o[4];
#pragma unroll
    for (int j = 0; j < 4; ++j) o[j] = gelu_f(a[j] + bv[j]);
    u32x2 v = {pack2(o[0], o[1]), pack2(o[2], o[3])};
    const int T = tile - NQKV_ / 16;
    u16* dst = hf + ((size_t)((mrow >> 4) * 144 + (T >> 1)) * 64 + ((T & 1) * 2 + (g >> 1)) * 16 + (mrow & 15)) * 8 + (g & 1) * 4;
    *reinterpret_cast<u32x2*>(dst) = v;
  }
}

template <int U>
__device__ __forceinline__ void g1_unit(const u16*& wsrc, int& wstride, int unit0, const u16* wfold, const u16* wqkv,
                                        const u16* w1, char* smem, const bf16x8 (&xa)[2][18], int m0,
                                        u16* __restrict__ qkv, u16* __restrict__ hf) {
  int tz = threadIdx.x;
  asm volatile("" : "+v"(tz));
  const int wid = __builtin_amdgcn_readfirstlane(tz >> 6), lane = tz & 63;
  const int g = lane >> 4, q = (lane & 15) >> 2, p = lane & 3;
  unsigned ad[4][2];
#pragma unroll
  for (int t = 0; t < 2; ++t) {
    const int row = 8 * g + 4 * t + q;
    const int f = ((row >> 1) & 1) | (((row >> 3) & 1) << 1);
#pragma unroll
    for (int n = 0; n < 4; ++n) ad[n][t] = lds_addr(smem) + row * 128 + ((n ^ f) * 32) + p * 8;
  }
  f32x4 acc[2][4];
#pragma unroll
  for (int r = 0; r < 2; ++r)
#pragma unroll
    for (int n = 0; n < 4; ++n) acc[r][n] = (f32x4){0.f, 0.f, 0.f, 0.f};

  auto stage = [&](auto KC) {
    constexpr int kc = decltype(KC)::value;
    constexpr int s = U * 9 + kc;
    constexpr int nl = (s + 6 <= 26) ? 6 : (26 - s);
    constexpr int nst = (U > 0 && kc <= 6) ? 8 : 0;
    wait_vm<nl + nst>();
    WAIT_LGKM0();
    BAR();
    if constexpr (s + 7 <= 26) {
      constexpr int sn = s + 7;
      if constexpr (sn % 9 == 0) {
        const int col = (unit0 + sn / 9) * 64;
        const int row = wid * 8 + (lane >> 3);
        const int f = ((row >> 1) & 1) | (((row >> 3) & 1) << 1);
        const int ch = (lane & 7) ^ (f << 1);
        if (col < NPH_) { wsrc = wfold + (size_t)row * NPH_ + col + ch * 8; wstride = 64 * NPH_; }
        else if (col < NQKV_) { wsrc = wqkv + (size_t)row * NQKV_ + col + ch * 8; wstride = 64 * NQKV_; }
        else { wsrc = w1 + (size_t)row * F_ + (col - NQKV_) + ch * 8; wstride = 64 * F_; }
      }
      glds16(wsrc, smem + (sn % G1_RING) * G1_BUFB + wid * 1024 + lane * 16);
      wsrc += wstride;
    }
    constexpr int CUR = (s % G1_RING) * G1_BUFB;
    WF wA[4], wB[4];
    g1_reads<CUR, 0, 4>(wA, ad);
    g1_reads<CUR, 4, 4>(wB, ad);
    wait_lgkm<8>();
    g1_mma<0, kc>(wA, xa, acc);
    wait_lgkm<0>();
    g1_mma<4, kc>(wB, xa, acc);
  };
  stage(IC<0>{}); stage(IC<1>{}); stage(IC<2>{}); stage(IC<3>{}); stage(IC<4>{});
  stage(IC<5>{}); stage(IC<6>{}); stage(IC<7>{}); stage(IC<8>{});
  const float* bias_lds = reinterpret_cast<const float*>(smem + G1_BOFF);
#pragma unroll
  for (int n = 0; n < 4; ++n) {
    const int tile = (unit0 + U) * 4 + n;
#pragma unroll
    for (int r = 0; r < 2; ++r)
      g1_store_tile(acc[r][n], tile, m0 + wid * 32 + r * 16 + (lane & 15), g, bias_lds + (U * 4 + n) * 16 + 4 * g, qkv, hf);
  }
}

__global__ __launch_bounds__(512, 2) void k1_gemm1(const u16* __restrict__ xnf, const u16* __restrict__ wfold,
                                                   const u16* __restrict__ wqkv, const u16* __restrict__ w1,
                                                   const u16* __restrict__ b1, u16* __restrict__ qkv,
                                                   u16* __restrict__ hf) {
  __shared__ __attribute__((aligned(16))) char smem[G1_SMEM];
  float* bias_lds = reinterpret_cast<float*>(smem + G1_BOFF);
  const int tid = threadIdx.x, wid = tid >> 6, lane = tid & 63;
  const int bid = blockIdx.x;
  const int mt = bid >> 5, grp = bid & 31;
  const int m0 = mt * 256;
  const int unit0 = grp * 3;
  const bool extra = grp < 12;
  const int xcol = 6144 + grp * 16;
  const u16* wsrc;
  int wstride;
  {
    const int col = unit0 * 64;
    const int row = wid * 8 + (lane >> 3);
    const int f = ((row >> 1) & 1) | (((row >> 3) & 1) << 1);
    const int ch = (lane & 7) ^ (f << 1);
    if (col < NPH_) { wsrc = wfold + (size_t)row * NPH_ + col + ch * 8; wstride = 64 * NPH_; }
    else if (col < NQKV_) { wsrc = wqkv + (size_t)row * NQKV_ + col + ch * 8; wstride = 64 * NQKV_; }
    else { wsrc = w1 + (size_t)row * F_ + (col - NQKV_) + ch * 8; wstride = 64 * F_; }
  }
#pragma unroll
  for (int s = 0; s < 7; ++s) {
    glds16(wsrc, smem + s * G1_BUFB + wid * 1024 + lane * 16);
    wsrc += wstride;
  }
  if (extra) {
#pragma unroll
    for (int i = 0; i < 3; ++i) {
      int ins = wid + 8 * i;
      if (ins >= 18) ins -= 18;
      const int c = ins * 64 + lane;
      const int pr = c >> 1, half = c & 1;
      const int k = (pr & ~0xC) | ((pr & 4) << 1) | ((pr & 8) >> 1);
      glds16(w1 + (size_t)k * F_ + (xcol - NQKV_) + half * 8, smem + G1_XOFF + c * 16);
    }
  }
  if (tid < 208) {
    const int col = (tid < 192) ? (unit0 * 64 + tid) : (xcol + tid - 192);
    bias_lds[tid] = (col >= NQKV_) ? bf2f(b1[col - NQKV_]) : 0.f;
  }
  bf16x8 xa[2][18];
#pragma unroll
  for (int r = 0; r < 2; ++r) {
    const u16* xp = xnf + ((size_t)((m0 >> 4) + wid * 2 + r) * 18 * 64 + lane) * 8;
#pragma unroll
    for (int ks = 0; ks < 18; ++ks) xa[r][ks] = *reinterpret_cast<const bf16x8*>(xp + ks * 512);
  }
  wait_vm<0>();
  WAIT_LGKM0();
  g1_unit<0>(wsrc, wstride, unit0, wfold, wqkv, w1, smem, xa, m0, qkv, hf);
  g1_unit<1>(wsrc, wstride, unit0, wfold, wqkv, w1, smem, xa, m0, qkv, hf);
  g1_unit<2>(wsrc, wstride, unit0, wfold, wqkv, w1, smem, xa, m0, qkv, hf);
  if (extra) {
    int tz = threadIdx.x;
    asm volatile("" : "+v"(tz));
    const int wid2 = __builtin_amdgcn_readfirstlane(tz >> 6), lane2 = tz & 63;
    const int g = lane2 >> 4, q = (lane2 & 15) >> 2, p = lane2 & 3;
    unsigned ad[2];
#pragma unroll
    for (int t = 0; t < 2; ++t) {
      const int k = 8 * g + 4 * t + q;
      const int pr = (k & ~0xC) | ((k & 4) << 1) | ((k & 8) >> 1);
      ad[t] = lds_addr(smem) + G1_XOFF + pr * 32 + p * 8;
    }
    f32x4 acc[2] = {(f32x4){0.f, 0.f, 0.f, 0.f}, (f32x4){0.f, 0.f, 0.f, 0.f}};
#pragma unroll
    for (int kb = 0; kb < 18; kb += 6) {
      WF w[6];
      tr_rd<0 * 1024>(w[0].lo, ad[0] + kb * 1024); tr_rd<0 * 1024>(w[0].hi, ad[1] + kb * 1024);
      tr_rd<1 * 1024>(w[1].lo, ad[0] + kb * 1024); tr_rd<1 * 1024>(w[1].hi, ad[1] + kb * 1024);
      tr_rd<2 * 1024>(w[2].lo, ad[0] + kb * 1024); tr_rd<2 * 1024>(w[2].hi, ad[1] + kb * 1024);
      tr_rd<3 * 1024>(w[3].lo, ad[0] + kb * 1024); tr_rd<3 * 1024>(w[3].hi, ad[1] + kb * 1024);
      tr_rd<4 * 1024>(w[4].lo, ad[0] + kb * 1024); tr_rd<4 * 1024>(w[4].hi, ad[1] + kb * 1024);
      tr_rd<5 * 1024>(w[5].lo, ad[0] + kb * 1024); tr_rd<5 * 1024>(w[5].hi, ad[1] + kb * 1024);
      wait_lgkm<0>();
#pragma unroll
      for (int i = 0; i < 6; ++i) pin(w[i]);
#pragma unroll
      for (int i = 0; i < 6; ++i)
#pragma unroll
        for (int r = 0; r < 2; ++r)
          acc[r] = __builtin_amdgcn_mfma_f32_16x16x32_bf16(cat(w[i]), xa[r][kb + i], acc[r], 0, 0, 0);
    }
#pragma unroll
    for (int r = 0; r < 2; ++r)
      g1_store_tile(acc[r], xcol / 16, m0 + wid2 * 32 + r * 16 + (lane2 & 15), g, bias_lds + 192 + 4 * g, qkv, hf);
  }
}

constexpr int G2_NT = 9, G2_RB = G2_NT * 32;
constexpr int G2_BUFB = 64 * G2_RB;
constexpr int G2_RING = 4;
constexpr int G2_STAGES = 18;

__device__ __forceinline__ void softmax_pm(const float (&pk)[18], float (&ep)[18], float (&en)[18]) {
  float mx = pk[0], mn = pk[0];
#pragma unroll
  for (int f = 1; f < 18; ++f) {
    mx = fmaxf(mx, pk[f]);
    mn = fminf(mn, pk[f]);
  }
  float sp = 0.f, sn = 0.f;
#pragma unroll
  for (int f = 0; f < 18; ++f) {
    ep[f] = __expf(pk[f] - mx);
    en[f] = __expf(mn - pk[f]);
    sp += ep[f];
    sn += en[f];
  }
  const float rp = 1.f / sp, rn = 1.f / sn;
#pragma unroll
  for (int f = 0; f < 18; ++f) {
    ep[f] *= rp;
    en[f] *= rn;
  }
}

__device__ void head_kv_block(int h, const u16* __restrict__ qkv, const u16* __restrict__ wo,
                              u16* __restrict__ mcat, float* __restrict__ zbuf, char* smem) {
  u16* phiT = reinterpret_cast<u16*>(smem);
  u16* vT = reinterpret_cast<u16*>(smem + 25344);
  float* kvs = reinterpret_cast<float*>(smem + 25344 + 16896);
  const int tid = threadIdx.x, wid = tid >> 6, lane = tid & 63;
  for (int i = tid; i < 12 * 264; i += 256) phiT[36 * 264 + i] = 0;
  for (int i = tid; i < 14 * 264; i += 256) vT[18 * 264 + i] = (i < 264) ? (u16)0x3f80 : (u16)0;
  f32x4 acc[3][2];
#pragma unroll
  for (int a = 0; a < 3; ++a)
#pragma unroll
    for (int b = 0; b < 2; ++b) acc[a][b] = (f32x4){0.f, 0.f, 0.f, 0.f};
#pragma unroll 1
  for (int ch = 0; ch < 8; ++ch) {
    const int row = ch * 256 + tid;
    const u16* kp = qkv + (size_t)row * NQKV_ + D_ + h * 18;
    const u16* vp = qkv + (size_t)row * NQKV_ + 2 * D_ + h * 18;
    float pk[18];
    unsigned vv[9];
#pragma unroll
    for (int d = 0; d < 9; ++d) {
      unsigned t = *reinterpret_cast<const unsigned*>(kp + 2 * d);
      pk[2 * d] = lo16(t);
      pk[2 * d + 1] = hi16(t);
      vv[d] = *reinterpret_cast<const unsigned*>(vp + 2 * d);
    }
    float ep[18], en[18];
    softmax_pm(pk, ep, en);
    __syncthreads();
#pragma unroll
    for (int f = 0; f < 18; ++f) {
      phiT[f * 264 + tid] = f2bf(ep[f]);
      phiT[(18 + f) * 264 + tid] = f2bf(en[f]);
      vT[f * 264 + tid] = (u16)((f & 1) ? (vv[f >> 1] >> 16) : (vv[f >> 1] & 0xffff));
    }
    __syncthreads();
#pragma unroll
    for (int ks = 0; ks < 2; ++ks) {
      const int s0 = wid * 64 + ks * 32 + 8 * (lane >> 4);
      bf16x8 bv[2];
#pragma unroll
      for (int b = 0; b < 2; ++b) bv[b] = *reinterpret_cast<const bf16x8*>(vT + (b * 16 + (lane & 15)) * 264 + s0);
#pragma unroll
      for (int a = 0; a < 3; ++a) {
        bf16x8 av = *reinterpret_cast<const bf16x8*>(phiT + (a * 16 + (lane & 15)) * 264 + s0);
#pragma unroll
        for (int b = 0; b < 2; ++b) acc[a][b] = __builtin_amdgcn_mfma_f32_16x16x32_bf16(av, bv[b], acc[a][b], 0, 0, 0);
      }
    }
  }
  __syncthreads();
#pragma unroll
  for (int a = 0; a < 3; ++a)
#pragma unroll
    for (int b = 0; b < 2; ++b)
#pragma unroll
      for (int j = 0; j < 4; ++j)
        kvs[(wid * 48 + a * 16 + (lane >> 4) * 4 + j) * 32 + b * 16 + (lane & 15)] = acc[a][b][j];
  u16* woimg = reinterpret_cast<u16*>(smem);
  u16* kvb = reinterpret_cast<u16*>(smem + 36864);
  for (int i = tid; i < 32 * 72; i += 256) {
    const int r = i / 72, c = i % 72;
    u32x4 v = {0u, 0u, 0u, 0u};
    if (r < 18) v = *reinterpret_cast<const u32x4*>(wo + (size_t)(h * 18 + r) * D_ + c * 8);
    *reinterpret_cast<u32x4*>(woimg + r * 576 + c * 8) = v;
  }
  __syncthreads();
  for (int i = tid; i < 48 * 32; i += 256) {
    float v = kvs[i] + kvs[48 * 32 + i] + kvs[2 * 48 * 32 + i] + kvs[3 * 48 * 32 + i];
    const int f = i >> 5, d = i & 31;
    if (d == 18 && f < 36) zbuf[h * 36 + f] = rbf(v);
    kvb[i] = (d < 18 && f < 36) ? f2bf(v) : (u16)0;
  }
  __syncthreads();
  {
    const int g = lane >> 4, q = (lane & 15) >> 2, p = lane & 3;
    constexpr int RB = 576 * 2;
    const int o0 = (8 * g + q) * RB + p * 8, o1 = (8 * g + 4 + q) * RB + p * 8;
    bf16x8 kb[3];
#pragma unroll
    for (int a = 0; a < 3; ++a) kb[a] = *reinterpret_cast<const bf16x8*>(kvb + (a * 16 + (lane & 15)) * 32 + 8 * g);
    for (int nt = wid; nt < 36; nt += 4) {
      bf16x8 w = wfrag(reinterpret_cast<const char*>(woimg), o0, o1, nt * 32);
#pragma unroll
      for (int a = 0; a < 3; ++a) {
        f32x4 c = __builtin_amdgcn_mfma_f32_16x16x32_bf16(w, kb[a], (f32x4){0.f, 0.f, 0.f, 0.f}, 0, 0, 0);
        const int f = a * 16 + (lane & 15);
        if (f < KPH_) {
          u32x2 v = {pack2(c[0], c[1]), pack2(c[2], c[3])};
          *reinterpret_cast<u32x2*>(mcat + (size_t)(h * KPH_ + f) * D_ + nt * 16 + 4 * g) = v;
        }
      }
    }
  }
}

__device__ void head_q_block(int hb, const u16* __restrict__ qkv, u16* __restrict__ phif) {
  const int h = hb >> 1, tid = threadIdx.x;
#pragma unroll 1
  for (int ch = 0; ch < 4; ++ch) {
    const int row = (hb & 1) * 1024 + ch * 256 + tid;
    const u16* kp = qkv + (size_t)row * NQKV_ + h * 18;
    float pk[18];
#pragma unroll
    for (int d = 0; d < 9; ++d) {
      unsigned t = *reinterpret_cast<const unsigned*>(kp + 2 * d);
      pk[2 * d] = lo16(t);
      pk[2 * d + 1] = hi16(t);
    }
    float ep[18], en[18];
    softmax_pm(pk, ep, en);
    unsigned o[20];
#pragma unroll
    for (int f = 0; f < 9; ++f) o[f] = pack2(ep[2 * f], ep[2 * f + 1]);
#pragma unroll
    for (int f = 0; f < 9; ++f) o[9 + f] = pack2(en[2 * f], en[2 * f + 1]);
    o[18] = 0;
    o[19] = 0;
#pragma unroll
    for (int c = 0; c < 5; ++c) {
      const int cidx = h * 5 + c;
      u16* dst = phif + ((size_t)((row >> 4) * 40 + (cidx >> 2)) * 64 + (cidx & 3) * 16 + (row & 15)) * 8;
      u32x4 v = {o[4 * c], o[4 * c + 1], o[4 * c + 2], o[4 * c + 3]};
      *reinterpret_cast<u32x4*>(dst) = v;
    }
  }
}

template <int BASE, int N0, int CNT, int I = 0>
__device__ __forceinline__ void g2_wreads(WF (&w)[5], unsigned a0, unsigned a1) {
  if constexpr (I < CNT) {
    tr_rd<BASE + (N0 + I) * 32>(w[I].lo, a0);
    tr_rd<BASE + (N0 + I) * 32>(w[I].hi, a1);
    g2_wreads<BASE, N0, CNT, I + 1>(w, a0, a1);
  }
}
template <int N0, int CNT>
__device__ __forceinline__ void g2_mma(WF (&w)[5], const bf16x8 (&ac)[2], f32x4 (&acc)[2][G2_NT]) {
#pragma unroll
  for (int i = 0; i < CNT; ++i) pin(w[i]);
#pragma unroll
  for (int i = 0; i < CNT; ++i)
#pragma unroll
    for (int r = 0; r < 2; ++r)
      acc[r][N0 + i] = __builtin_amdgcn_mfma_f32_16x16x32_bf16(cat(w[i]), ac[r], acc[r][N0 + i], 0, 0, 0);
}
template <int BASE>
__device__ __forceinline__ void g2_compute(const bf16x8 (&a)[2][2], f32x4 (&acc)[2][G2_NT], unsigned wa0, unsigned wa1) {
  WF wA[5], wB[5];
  g2_wreads<BASE, 0, 5>(wA, wa0, wa1);
  g2_wreads<BASE, 5, 4>(wB, wa0, wa1);
  wait_lgkm<8>();
  bf16x8 c0[2] = {a[0][0], a[1][0]};
  g2_mma<0, 5>(wA, c0, acc);
  g2_wreads<BASE + 32 * G2_RB, 0, 5>(wA, wa0, wa1);
  wait_lgkm<10>();
  g2_mma<5, 4>(wB, c0, acc);
  g2_wreads<BASE + 32 * G2_RB, 5, 4>(wB, wa0, wa1);
  wait_lgkm<8>();
  bf16x8 c1[2] = {a[0][1], a[1][1]};
  g2_mma<0, 5>(wA, c1, acc);
  wait_lgkm<0>();
  g2_mma<5, 4>(wB, c1, acc);
}

constexpr int K2_HEADBLK = 96;

__global__ __launch_bounds__(256, 2) void k2_gemm2(const u16* __restrict__ hf, const u16* __restrict__ w2,
                                                   float* __restrict__ part, const u16* __restrict__ qkv,
                                                   const u16* __restrict__ wo, u16* __restrict__ phif,
                                                   u16* __restrict__ mcat, float* __restrict__ zbuf) {
  __shared__ __attribute__((aligned(16))) char smem[G2_RING * G2_BUFB];
  const int tid = threadIdx.x, wid = tid >> 6, lane = tid & 63;
  int bid = blockIdx.x;
  if (bid < 32) { head_kv_block(bid, qkv, wo, mcat, zbuf, smem); return; }
  if (bid < K2_HEADBLK) { head_q_block(bid - 32, qkv, phif); return; }
  bid -= K2_HEADBLK;
  const int xcd = bid & 7, loc = bid >> 3;
  const int sp = xcd >> 1, mt = (xcd & 1) * 8 + (loc >> 2), nt4 = loc & 3;
  const int m0 = mt * 128, n0 = nt4 * 144, k0 = sp * 1152;
  const int g = lane >> 4, q = (lane & 15) >> 2, p = lane & 3;
  const bool odd = g & 1;
  const unsigned wa0 = lds_addr(smem) + (8 * g + 4 * (g & 1) + q) * G2_RB + p * 8;
  const unsigned wa1 = lds_addr(smem) + (8 * g + 4 * (1 - (g & 1)) + q) * G2_RB + p * 8;
  f32x4 acc[2][G2_NT];
#pragma unroll
  for (int r = 0; r < 2; ++r)
#pragma unroll
    for (int n = 0; n < G2_NT; ++n) acc[r][n] = (f32x4){0.f, 0.f, 0.f, 0.f};
  const u16* wp[5];
  int wdst[5];
#pragma unroll
  for (int i = 0; i < 5; ++i) {
    int ins = wid + 4 * i;
    if (ins >= 18) ins -= 18;
    int c = ins * 64 + lane;
    int row = c / 18, cc = c % 18;
    wp[i] = w2 + (size_t)(k0 + row) * D_ + n0 + cc * 8;
    wdst[i] = c * 16;
  }
  const u16* ap[2];
#pragma unroll
  for (int r = 0; r < 2; ++r) ap[r] = hf + ((size_t)((m0 >> 4) + wid * 2 + r) * 144 + (k0 >> 5)) * 512 + lane * 8;
  bf16x8 an[4][2][2];
  auto issue = [&](auto SL) {
    constexpr int slot = decltype(SL)::value;
    char* buf = smem + slot * G2_BUFB;
#pragma unroll
    for (int i = 0; i < 5; ++i) { glds16(wp[i], buf + wdst[i]); wp[i] += 64 * D_; }
#pragma unroll
    for (int r = 0; r < 2; ++r) {
      gld16_asm(an[slot][r][0], ap[r]);
      gld16_asm(an[slot][r][1], ap[r] + 512);
      ap[r] += 1024;
    }
  };
  issue(IC<0>{});
  issue(IC<1>{});
  issue(IC<2>{});
  auto stage = [&](auto U, auto LAST) {
    constexpr int u = decltype(U)::value;
    constexpr int rem = decltype(LAST)::value;
    if constexpr (rem >= 2) wait_vm<18>();
    else if constexpr (rem == 1) wait_vm<9>();
    else wait_vm<0>();
    WAIT_LGKM0();
    BAR();
    bf16x8 a[2][2];
#pragma unroll
    for (int r = 0; r < 2; ++r)
#pragma unroll
      for (int ks = 0; ks < 2; ++ks) { pin(an[u][r][ks]); a[r][ks] = swap_halves_if(an[u][r][ks], odd); }
    if constexpr (rem >= 3) issue(IC<(u + 3) & 3>{});
    g2_compute<u * G2_BUFB>(a, acc, wa0, wa1);
  };
#pragma unroll 1
  for (int it = 0; it < 3; ++it) {
    stage(IC<0>{}, IC<3>{}); stage(IC<1>{}, IC<3>{}); stage(IC<2>{}, IC<3>{}); stage(IC<3>{}, IC<3>{});
  }
  stage(IC<0>{}, IC<3>{}); stage(IC<1>{}, IC<3>{}); stage(IC<2>{}, IC<3>{});
  stage(IC<3>{}, IC<2>{}); stage(IC<0>{}, IC<1>{}); stage(IC<1>{}, IC<0>{});
#pragma unroll
  for (int r = 0; r < 2; ++r) {
    const int m = m0 + wid * 32 + r * 16 + (lane & 15);
    float* pp = part + ((size_t)sp * S_ + m) * D_ + n0 + 4 * g;
#pragma unroll
    for (int n = 0; n < G2_NT; ++n) *reinterpret_cast<f32x4*>(pp + n * 16) = acc[r][n];
  }
}

constexpr int G3_BUFB = 64 * 128;
constexpr int G3_RING = 4;

__device__ __forceinline__ bf16x8 scale_frag(bf16x8 v, float s) {
  u32x4 in = *reinterpret_cast<u32x4*>(&v);
  u32x4 pk;
#pragma unroll
  for (int j = 0; j < 4; ++j) pk[j] = pack2(lo16(in[j]) * s, hi16(in[j]) * s);
  return *reinterpret_cast<bf16x8*>(&pk);
}

__global__ __launch_bounds__(256, 2) void k3_final(const u16* __restrict__ phif, const u16* __restrict__ mcat,
                                                   const float* __restrict__ zbuf, const float* __restrict__ part,
                                                   const u16* __restrict__ b2, const u16* __restrict__ x,
                                                   u16* __restrict__ out) {
  __shared__ __attribute__((aligned(16))) char smem[G3_RING * G3_BUFB + 64 * 33 * 4 + 32 * 36 * 4];
  float* rden = reinterpret_cast<float*>(smem + G3_RING * G3_BUFB);
  float* zs = reinterpret_cast<float*>(smem + G3_RING * G3_BUFB + 64 * 33 * 4);
  const int tid = threadIdx.x, wid = tid >> 6, lane = tid & 63;
  const int bid = blockIdx.x;
  const int xcd = bid & 7, loc = bid >> 3;
  const int mt = xcd * 4 + loc / 9, nt = loc % 9;
  const int m0 = mt * 64, n0 = nt * 64;
  const int g = lane >> 4, q = (lane & 15) >> 2, p = lane & 3;
  const int wr = wid >> 1, wc = wid & 1;
  const u16* wp[2];
#pragma unroll
  for (int i = 0; i < 2; ++i) {
    const int row = (wid * 2 + i) * 8 + (lane >> 3);
    const int f = ((row >> 1) & 1) | (((row >> 3) & 1) << 1);
    const int ch = (lane & 7) ^ (f << 1);
    wp[i] = mcat + (size_t)row * D_ + n0 + ch * 8;
  }
  const u16* ap[2];
#pragma unroll
  for (int r = 0; r < 2; ++r) ap[r] = phif + ((size_t)((m0 >> 4) + wr * 2 + r) * 40) * 512 + lane * 8;
  bf16x8 an[4][2][2];
  auto issue = [&](auto SL) {
    constexpr int slot = decltype(SL)::value;
    char* buf = smem + slot * G3_BUFB;
#pragma unroll
    for (int i = 0; i < 2; ++i) { glds16(wp[i], buf + (wid * 2 + i) * 1024 + lane * 16); wp[i] += 64 * D_; }
#pragma unroll
    for (int r = 0; r < 2; ++r) {
      gld16_asm(an[slot][r][0], ap[r]);
      gld16_asm(an[slot][r][1], ap[r] + 512);
      ap[r] += 1024;
    }
  };
  issue(IC<0>{});
  issue(IC<1>{});
  issue(IC<2>{});
  for (int i = tid; i < 32 * 36; i += 256) zs[i] = zbuf[i];
  __syncthreads();
  for (int i = tid; i < 64 * 32; i += 256) {
    const int r = i & 63, h = i >> 6;
    const int row = m0 + r;
    float d = 0.f;
#pragma unroll
    for (int c = 0; c < 5; ++c) {
      const int cidx = h * 5 + c;
      u32x4 v = *reinterpret_cast<const u32x4*>(phif + ((size_t)((row >> 4) * 40 + (cidx >> 2)) * 64 + (cidx & 3) * 16 + (row & 15)) * 8);
#pragma unroll
      for (int j = 0; j < 4; ++j) {
        if (c * 8 + 2 * j < 36) d += lo16(v[j]) * zs[h * 36 + c * 8 + 2 * j];
        if (c * 8 + 2 * j + 1 < 36) d += hi16(v[j]) * zs[h * 36 + c * 8 + 2 * j + 1];
      }
    }
    rden[r * 33 + h] = 1.f / (rbf(d) + 1e-6f);
  }
  unsigned ad[2][2];
#pragma unroll
  for (int t = 0; t < 2; ++t) {
    const int row = 8 * g + 4 * t + q;
    const int f = ((row >> 1) & 1) | (((row >> 3) & 1) << 1);
#pragma unroll
    for (int n = 0; n < 2; ++n) ad[n][t] = lds_addr(smem) + row * 128 + (((wc * 2 + n) ^ f) * 32) + p * 8;
  }
  f32x4 acc[2][2];
#pragma unroll
  for (int r = 0; r < 2; ++r)
#pragma unroll
    for (int n = 0; n < 2; ++n) acc[r][n] = (f32x4){0.f, 0.f, 0.f, 0.f};
  int rl[2];
#pragma unroll
  for (int r = 0; r < 2; ++r) rl[r] = wr * 32 + r * 16 + (lane & 15);
  __syncthreads();
  int st = 0;
  auto stage = [&](auto U, auto LAST) {
    constexpr int u = decltype(U)::value;
    constexpr int rem = decltype(LAST)::value;
    if constexpr (rem >= 2) wait_vm<12>();
    else if constexpr (rem == 1) wait_vm<6>();
    else wait_vm<0>();
    WAIT_LGKM0();
    BAR();
    float sc[2][2];
#pragma unroll
    for (int ks = 0; ks < 2; ++ks) {
      const int h = (st * 64 + ks * 32 + 8 * g) / KPH_;
#pragma unroll
      for (int r = 0; r < 2; ++r) sc[r][ks] = rden[rl[r] * 33 + h];
    }
    asm volatile("" : "+v"(sc[0][0]), "+v"(sc[0][1]), "+v"(sc[1][0]), "+v"(sc[1][1]));
    WF w[2][2];
    constexpr int BASE = u * G3_BUFB;
#pragma unroll
    for (int n = 0; n < 2; ++n) {
      tr_rd<BASE>(w[0][n].lo, ad[n][0]); tr_rd<BASE>(w[0][n].hi, ad[n][1]);
    }
#pragma unroll
    for (int n = 0; n < 2; ++n) {
      tr_rd<BASE + 32 * 128>(w[1][n].lo, ad[n][0]); tr_rd<BASE + 32 * 128>(w[1][n].hi, ad[n][1]);
    }
    bf16x8 a[2][2];
#pragma unroll
    for (int r = 0; r < 2; ++r)
#pragma unroll
      for (int ks = 0; ks < 2; ++ks) { pin(an[u][r][ks]); a[r][ks] = scale_frag(an[u][r][ks], sc[r][ks]); }
    if constexpr (rem >= 3) issue(IC<(u + 3) & 3>{});
    wait_lgkm<0>();
#pragma unroll
    for (int ks = 0; ks < 2; ++ks)
#pragma unroll
      for (int n = 0; n < 2; ++n) pin(w[ks][n]);
#pragma unroll
    for (int ks = 0; ks < 2; ++ks)
#pragma unroll
      for (int n = 0; n < 2; ++n)
#pragma unroll
        for (int r = 0; r < 2; ++r)
          acc[r][n] = __builtin_amdgcn_mfma_f32_16x16x32_bf16(cat(w[ks][n]), a[r][ks], acc[r][n], 0, 0, 0);
    ++st;
  };
#pragma unroll 1
  for (int it = 0; it < 4; ++it) {
    stage(IC<0>{}, IC<3>{}); stage(IC<1>{}, IC<3>{}); stage(IC<2>{}, IC<3>{}); stage(IC<3>{}, IC<3>{});
  }
  stage(IC<0>{}, IC<3>{}); stage(IC<1>{}, IC<2>{}); stage(IC<2>{}, IC<1>{}); stage(IC<3>{}, IC<0>{});
#pragma unroll
  for (int r = 0; r < 2; ++r) {
    const int m = m0 + rl[r];
#pragma unroll
    for (int n = 0; n < 2; ++n) {
      const int ncol = n0 + wc * 32 + n * 16 + 4 * g;
      f32x4 y = *reinterpret_cast<const f32x4*>(part + (size_t)m * D_ + ncol);
#pragma unroll
      for (int s = 1; s < SPLITK_; ++s) {
        f32x4 t = *reinterpret_cast<const f32x4*>(part + ((size_t)s * S_ + m) * D_ + ncol);
        y += t;
      }
      u32x2 bb = *reinterpret_cast<const u32x2*>(b2 + ncol);
      u32x2 xx = *reinterpret_cast<const u32x2*>(x + (size_t)m * D_ + ncol);
      float bv[4] = {lo16(bb[0]), hi16(bb[0]), lo16(bb[1]), hi16(bb[1])};
      float xv[4] = {lo16(xx[0]), hi16(xx[0]), lo16(xx[1]), hi16(xx[1])};
      float o[4];
#pragma unroll
      for (int j = 0; j < 4; ++j) {
        float yy = y[j] + bv[j];
        float gg = yy / (1.f + __expf(-yy));
        o[j] = xv[j] + acc[r][n][j] * gg;
      }
      u32x2 v = {pack2(o[0], o[1]), pack2(o[2], o[3])};
      *reinterpret_cast<u32x2*>(out + (size_t)m * D_ + ncol) = v;
    }
  }
}

extern "C" void kernel_launch(void* const* d_in, const int* in_sizes, int n_in, void* d_out, int out_size, void* d_ws,
                              size_t ws_size, hipStream_t stream) {
  const u16* x = (const u16*)d_in[0];
  const u16* wqkv = (const u16*)d_in[1];
  const u16* wphi = (const u16*)d_in[2];
  const u16* wo = (const u16*)d_in[3];
  const u16* w1 = (const u16*)d_in[5];
  const u16* b1 = (const u16*)d_in[6];
  const u16* w2 = (const u16*)d_in[7];
  const u16* b2 = (const u16*)d_in[8];
  u16* out = (u16*)d_out;
  char* ws = (char*)d_ws;
  size_t off = 0;
  u16* xnf = (u16*)(ws + off); off += (size_t)S_ * D_ * 2;
  u16* wfold = (u16*)(ws + off); off += (size_t)D_ * NPH_ * 2;
  u16* qkv = (u16*)(ws + off); off += (size_t)S_ * NQKV_ * 2;
  u16* hf = (u16*)(ws + off); off += (size_t)S_ * F_ * 2;
  float* part = (float*)(ws + off); off += (size_t)SPLITK_ * S_ * D_ * 4;
  u16* phif = (u16*)(ws + off); off += (size_t)S_ * KPHI_ * 2;
  u16* mcat = (u16*)(ws + off); off += (size_t)KPHI_ * D_ * 2;
  float* zbuf = (float*)(ws + off); off += 32 * 36 * 4;

  k0_prep<<<512 + 192, 256, 0, stream>>>(x, wqkv, wphi, xnf, wfold);
  k1_gemm1<<<256, 512, 0, stream>>>(xnf, wfold, wqkv, w1, b1, qkv, hf);
  k2_gemm2<<<K2_HEADBLK + 256, 256, 0, stream>>>(hf, w2, part, qkv, wo, phif, mcat, zbuf);
  k3_final<<<32 * 9, 256, 0, stream>>>(phif, mcat, zbuf, part, b2, x, out);
}
```

```cpp
#include <hip/hip_runtime.h>
#include <hip/hip_bf16.h>
#include <stdint.h>

constexpr int S_ = 2048, D_ = 576, H_ = 32, DH_ = 18, F_ = 4608, NQKV_ = 1728;
constexpr int NPH_ = 1152;
constexpr int KPH_ = 40;
constexpr int KPHI_ = H_ * KPH_;
constexpr int SPLITK_ = 4;

typedef short bf16x8 __attribute__((ext_vector_type(8)));
typedef short s16x4 __attribute__((ext_vector_type(4)));
typedef float f32x4 __attribute__((ext_vector_type(4)));
typedef unsigned u32x4 __attribute__((ext_vector_type(4)));
typedef unsigned u32x2 __attribute__((ext_vector_type(2)));
typedef __attribute__((address_space(3))) s16x4* lds_s16x4_p;
typedef unsigned short u16;
template <int V> struct IC { static constexpr int value = V; };

#define STAMP(kid, i) do { } while (0)
#define WAIT_LGKM0() asm volatile("s_waitcnt lgkmcnt(0)" ::: "memory")
#define BAR() __builtin_amdgcn_s_barrier()

template <int N>
__device__ __forceinline__ void wait_vm() {
  static_assert(N >= 0 && N < 64, "vmcnt range");
  asm volatile("s_waitcnt vmcnt(%0)" ::"n"(N) : "memory");
}

__device__ __forceinline__ float bf2f(u16 v) { return __uint_as_float(((unsigned)v) << 16); }
__device__ __forceinline__ u16 f2bf(float f) {
  __hip_bfloat16 b = __float2bfloat16(f);
  return *reinterpret_cast<u16*>(&b);
}
__device__ __forceinline__ unsigned pack2(float lo, float hi) {
  return (unsigned)f2bf(lo) | ((unsigned)f2bf(hi) << 16);
}
__device__ __forceinline__ float rbf(float f) { return bf2f(f2bf(f)); }
__device__ __forceinline__ float lo16(unsigned u) { return __uint_as_float(u << 16); }
__device__ __forceinline__ float hi16(unsigned u) { return __uint_as_float(u & 0xffff0000u); }

__device__ __forceinline__ void glds16(const void* g, void* l) {
  __builtin_amdgcn_global_load_lds((const unsigned*)g, (unsigned*)l, 16, 0, 0);
}

__device__ __forceinline__ bf16x8 swap_halves_if(bf16x8 v, bool odd) {
  bf16x8 s = __builtin_shufflevector(v, v, 4, 5, 6, 7, 0, 1, 2, 3);
  return odd ? s : v;
}

__device__ __forceinline__ bf16x8 wfrag(const char* img, int o0, int o1, int off) {
  s16x4 a = __builtin_amdgcn_ds_read_tr16_b64_v4i16((lds_s16x4_p)(img + o0 + off));
  s16x4 b = __builtin_amdgcn_ds_read_tr16_b64_v4i16((lds_s16x4_p)(img + o1 + off));
  return __builtin_shufflevector(a, b, 0, 1, 2, 3, 4, 5, 6, 7);
}

struct WF { s16x4 lo, hi; };
template <int OFF>
__device__ __forceinline__ void tr_rd(s16x4& r, unsigned addr) {
  asm volatile("ds_read_b64_tr_b16 %0, %1 offset:%2" : "=v"(r) : "v"(addr), "n"(OFF) : "memory");
}
__device__ __forceinline__ void gld16_asm(bf16x8& r, const void* p) {
  asm volatile("global_load_dwordx4 %0, %1, off" : "=v"(r) : "v"(p) : "memory");
}
template <int N>
__device__ __forceinline__ void wait_lgkm() {
  static_assert(N >= 0 && N <= 15, "lgkmcnt range");
  asm volatile("s_waitcnt lgkmcnt(%0)" ::"n"(N) : "memory");
}
__device__ __forceinline__ void pin(WF& w) { asm volatile("" : "+v"(w.lo), "+v"(w.hi)); }
__device__ __forceinline__ void pin(bf16x8& w) { asm volatile("" : "+v"(w)); }
__device__ __forceinline__ bf16x8 cat(const WF& w) { return __builtin_shufflevector(w.lo, w.hi, 0, 1, 2, 3, 4, 5, 6, 7); }
__device__ __forceinline__ unsigned lds_addr(const void* p) {
  return (unsigned)(uintptr_t)(__attribute__((address_space(3))) const char*)p;
}

__global__ __launch_bounds__(256) void k0_prep(const u16* __restrict__ x, const u16* __restrict__ wqkv,
                                               const u16* __restrict__ wphi, u16* __restrict__ xnf,
                                               u16* __restrict__ wfold) {
  __shared__ __attribute__((aligned(16))) float wph[18 * 20];
  const int tid = threadIdx.x, wid = tid >> 6, lane = tid & 63;
  const int bid = blockIdx.x;
  if (bid < 512) {
    const int row = bid * 4 + wid;
    const u16* xp = x + (size_t)row * D_;
    u32x4 a = *reinterpret_cast<const u32x4*>(xp + lane * 8);
    u32x4 b = {0u, 0u, 0u, 0u};
    if (lane < 8) b = *reinterpret_cast<const u32x4*>(xp + 512 + lane * 8);
    float v[16];
#pragma unroll
    for (int j = 0; j < 4; ++j) {
      v[2 * j] = lo16(a[j]); v[2 * j + 1] = hi16(a[j]);
      v[8 + 2 * j] = lo16(b[j]); v[8 + 2 * j + 1] = hi16(b[j]);
    }
    float s = 0.f;
#pragma unroll
    for (int j = 0; j < 16; ++j) s += v[j];
#pragma unroll
    for (int o = 32; o >= 1; o >>= 1) s += __shfl_xor(s, o);
    const float mean = s * (1.f / D_);
    float ss = 0.f;
#pragma unroll
    for (int j = 0; j < 8; ++j) { float d = v[j] - mean; ss += d * d; }
    if (lane < 8) {
#pragma unroll
      for (int j = 8; j < 16; ++j) { float d = v[j] - mean; ss += d * d; }
    }
#pragma unroll
    for (int o = 32; o >= 1; o >>= 1) ss += __shfl_xor(ss, o);
    const float rstd = rsqrtf(ss * (1.f / D_) + 1e-5f);
    u32x4 oa, ob;
#pragma unroll
    for (int j = 0; j < 4; ++j) {
      oa[j] = pack2((v[2 * j] - mean) * rstd, (v[2 * j + 1] - mean) * rstd);
      ob[j] = pack2((v[8 + 2 * j] - mean) * rstd, (v[8 + 2 * j + 1] - mean) * rstd);
    }
    {
      const int c = lane;
      u16* dst = xnf + ((size_t)((row >> 4) * 18 + (c >> 2)) * 64 + (c & 3) * 16 + (row & 15)) * 8;
      *reinterpret_cast<u32x4*>(dst) = oa;
    }
    if (lane < 8) {
      const int c = 64 + lane;
      u16* dst = xnf + ((size_t)((row >> 4) * 18 + (c >> 2)) * 64 + (c & 3) * 16 + (row & 15)) * 8;
      *reinterpret_cast<u32x4*>(dst) = ob;
    }
    return;
  }
  {
    const int fb = bid - 512;
    const int hh = fb / 3, k = (fb % 3) * 192 + tid;
    const int h = hh & 31;
    for (int i = tid; i < 18 * 20; i += 256) {
      const int d = i / 20, f = i % 20;
      wph[i] = (f < 18) ? bf2f(wphi[h * 324 + d * 18 + f]) : 0.f;
    }
    unsigned wv[9];
    if (tid < 192) {
      const u16* wp = wqkv + (size_t)k * NQKV_ + hh * 18;
#pragma unroll
      for (int d = 0; d < 9; ++d) wv[d] = *reinterpret_cast<const unsigned*>(wp + 2 * d);
    }
    __syncthreads();
    if (tid < 192) {
      float acc[20];
#pragma unroll
      for (int f = 0; f < 20; ++f) acc[f] = 0.f;
#pragma unroll
      for (int d = 0; d < 18; ++d) {
        const float w = (d & 1) ? hi16(wv[d >> 1]) : lo16(wv[d >> 1]);
#pragma unroll
        for (int f4 = 0; f4 < 5; ++f4) {
          f32x4 pw = *reinterpret_cast<const f32x4*>(wph + d * 20 + f4 * 4);
#pragma unroll
          for (int j = 0; j < 4; ++j) acc[f4 * 4 + j] += w * pw[j];
        }
      }
      u16* op = wfold + (size_t)k * NPH_ + hh * 18;
#pragma unroll
      for (int f = 0; f < 9; ++f) *reinterpret_cast<unsigned*>(op + 2 * f) = pack2(acc[2 * f], acc[2 * f + 1]);
    }
  }
}

constexpr int G1_BUFB = 64 * 128;
constexpr int G1_RING = 8;
constexpr int G1_XOFF = G1_RING * G1_BUFB;
constexpr int G1_BOFF = G1_XOFF + 576 * 32;
constexpr int G1_SMEM = G1_BOFF + 1024;
constexpr int G1_NST = 8;

template <int BASE, int F0, int CNT, int I = 0>
__device__ __forceinline__ void g1_reads(WF (&w)[4], const unsigned (&ad)[4][2]) {
  if constexpr (I < CNT) {
    constexpr int f = F0 + I, ks = f / 4, n = f % 4;
    tr_rd<BASE + ks * 32 * 128>(w[I].lo, ad[n][0]);
    tr_rd<BASE + ks * 32 * 128>(w[I].hi, ad[n][1]);
    g1_reads<BASE, F0, CNT, I + 1>(w, ad);
  }
}
template <int F0, int KC>
__device__ __forceinline__ void g1_mma(WF (&w)[4], const bf16x8 (&xa)[2][18], f32x4 (&acc)[2][4]) {
#pragma unroll
  for (int i = 0; i < 4; ++i) pin(w[i]);
#pragma unroll
  for (int i = 0; i < 4; ++i) {
    const int f = F0 + i, ks = f / 4, n = f % 4;
#pragma unroll
    for (int r = 0; r < 2; ++r)
      acc[r][n] = __builtin_amdgcn_mfma_f32_16x16x32_bf16(cat(w[i]), xa[r][KC * 2 + ks], acc[r][n], 0, 0, 0);
  }
}

__device__ __forceinline__ float gelu_f(float v) {
  float u = 0.7978845608028654f * (v + 0.044715f * v * v * v);
  float e = __expf(-2.f * u);
  return v * __builtin_amdgcn_rcpf(1.f + e);
}

__device__ __forceinline__ void g1_store_tile(const f32x4& a, int tile, int mrow, int g, const float* bias4,
                                              u16* __restrict__ qkv, u16* __restrict__ hf) {
  if (tile < NQKV_ / 16) {
    const int col = tile * 16 + 4 * g;
#pragma unroll
    for (int j = 0; j < 2; ++j) {
      const int c = col + 2 * j;
      const int hh = c / 18, f = c - hh * 18;
      *reinterpret_cast<unsigned*>(qkv + ((size_t)hh * S_ + mrow) * 18 + f) = pack2(a[2 * j], a[2 * j + 1]);
    }
  } else {
    f32x4 bv = *reinterpret_cast<const f32x4*>(bias4);
    float o[4];
#pragma unroll
    for (int j = 0; j < 4; ++j) o[j] = gelu_f(a[j] + bv[j]);
    u32x2 v = {pack2(o[0], o[1]), pack2(o[2], o[3])};
    const int T = tile - NQKV_ / 16;
    u16* dst = hf + ((size_t)((mrow >> 4) * 144 + (T >> 1)) * 64 + ((T & 1) * 2 + (g >> 1)) * 16 + (mrow & 15)) * 8 + (g & 1) * 4;
    *reinterpret_cast<u32x2*>(dst) = v;
  }
}

__device__ __forceinline__ void g1_addr(unsigned (&ad)[4][2], char* smem, int lane) {
  const int g = lane >> 4, q = (lane & 15) >> 2, p = lane & 3;
#pragma unroll
  for (int t = 0; t < 2; ++t) {
    const int row = 8 * g + 4 * t + q;
    const int f = ((row >> 1) & 1) | (((row >> 3) & 1) << 1);
#pragma unroll
    for (int n = 0; n < 4; ++n) ad[n][t] = lds_addr(smem) + row * 128 + ((n ^ f) * 32) + p * 8;
  }
}

template <int U>
__device__ __forceinline__ void g1_unit(const u16*& wsrc, int& wstride, int unit0, const u16* wfold, const u16* wqkv,
                                        const u16* w1, char* smem, const bf16x8 (&xa)[2][18], int m0,
                                        u16* __restrict__ qkv, u16* __restrict__ hf, WF (&wA)[4]) {
  int tz = threadIdx.x;
  asm volatile("" : "+v"(tz));
  const int wid = __builtin_amdgcn_readfirstlane(tz >> 6), lane = tz & 63;
  const int g = lane >> 4;
  unsigned ad[4][2];
  g1_addr(ad, smem, lane);
  f32x4 acc[2][4];
#pragma unroll
  for (int r = 0; r < 2; ++r)
#pragma unroll
    for (int n = 0; n < 4; ++n) acc[r][n] = (f32x4){0.f, 0.f, 0.f, 0.f};

  auto stage = [&](auto KC) {
    constexpr int kc = decltype(KC)::value;
    constexpr int s = U * 9 + kc;
    constexpr int CUR = (s % G1_RING) * G1_BUFB;
    WF wB[4];
    g1_reads<CUR, 4, 4>(wB, ad);
    wait_lgkm<8>();
    g1_mma<0, kc>(wA, xa, acc);
    if constexpr (s < 26) {
      constexpr int nl = (s + 6 <= 26) ? 5 : (26 - s - 1);
      constexpr int nst = (U > 0 && kc <= 5) ? G1_NST : 0;
      wait_vm<nl + nst>();
      BAR();
      if constexpr (s + 7 <= 26) {
        constexpr int sn = s + 7;
        if constexpr (sn % 9 == 0) {
          const int col = (unit0 + sn / 9) * 64;
          const int row = wid * 8 + (lane >> 3);
          const int f = ((row >> 1) & 1) | (((row >> 3) & 1) << 1);
          const int ch = (lane & 7) ^ (f << 1);
          if (col < NPH_) { wsrc = wfold + (size_t)row * NPH_ + col + ch * 8; wstride = 64 * NPH_; }
          else if (col < NQKV_) { wsrc = wqkv + (size_t)row * NQKV_ + col + ch * 8; wstride = 64 * NQKV_; }
          else { wsrc = w1 + (size_t)row * F_ + (col - NQKV_) + ch * 8; wstride = 64 * F_; }
        }
        glds16(wsrc, smem + (sn % G1_RING) * G1_BUFB + wid * 1024 + lane * 16);
        wsrc += wstride;
      }
      WF wN[4];
      g1_reads<((s + 1) % G1_RING) * G1_BUFB, 0, 4>(wN, ad);
      wait_lgkm<8>();
      g1_mma<4, kc>(wB, xa, acc);
#pragma unroll
      for (int i = 0; i < 4; ++i) wA[i] = wN[i];
    } else {
      wait_lgkm<0>();
      g1_mma<4, kc>(wB, xa, acc);
    }
  };
  stage(IC<0>{}); stage(IC<1>{}); stage(IC<2>{}); stage(IC<3>{}); stage(IC<4>{});
  stage(IC<5>{}); stage(IC<6>{}); stage(IC<7>{}); stage(IC<8>{});
  const float* bias_lds = reinterpret_cast<const float*>(smem + G1_BOFF);
#pragma unroll
  for (int n = 0; n < 4; ++n) {
    const int tile = (unit0 + U) * 4 + n;
#pragma unroll
    for (int r = 0; r < 2; ++r)
      g1_store_tile(acc[r][n], tile, m0 + wid * 32 + r * 16 + (lane & 15), g, bias_lds + (U * 4 + n) * 16 + 4 * g, qkv, hf);
  }
}

__global__ __launch_bounds__(512, 2) void k1_gemm1(const u16* __restrict__ xnf, const u16* __restrict__ wfold,
                                                   const u16* __restrict__ wqkv, const u16* __restrict__ w1,
                                                   const u16* __restrict__ b1, u16* __restrict__ qkv,
                                                   u16* __restrict__ hf) {
  __shared__ __attribute__((aligned(16))) char smem[G1_SMEM];
  float* bias_lds = reinterpret_cast<float*>(smem + G1_BOFF);
  const int tid = threadIdx.x, wid = tid >> 6, lane = tid & 63;
  const int bid = blockIdx.x;
  const int mt = bid >> 5, grp = bid & 31;
  const int m0 = mt * 256;
  const int unit0 = grp * 3;
  const bool extra = grp < 12;
  STAMP(1, 0);
  const int xcol = 6144 + grp * 16;
  const u16* wsrc;
  int wstride;
  {
    const int col = unit0 * 64;
    const int row = wid * 8 + (lane >> 3);
    const int f = ((row >> 1) & 1) | (((row >> 3) & 1) << 1);
    const int ch = (lane & 7) ^ (f << 1);
    if (col < NPH_) { wsrc = wfold + (size_t)row * NPH_ + col + ch * 8; wstride = 64 * NPH_; }
    else if (col < NQKV_) { wsrc = wqkv + (size_t)row * NQKV_ + col + ch * 8; wstride = 64 * NQKV_; }
    else { wsrc = w1 + (size_t)row * F_ + (col - NQKV_) + ch * 8; wstride = 64 * F_; }
  }
#pragma unroll
  for (int s = 0; s < 7; ++s) {
    glds16(wsrc, smem + s * G1_BUFB + wid * 1024 + lane * 16);
    wsrc += wstride;
  }
  if (extra) {
#pragma unroll
    for (int i = 0; i < 3; ++i) {
      int ins = wid + 8 * i;
      if (ins >= 18) ins -= 18;
      const int c = ins * 64 + lane;
      const int pr = c >> 1, half = c & 1;
      const int k = (pr & ~0xC) | ((pr & 4) << 1) | ((pr & 8) >> 1);
      glds16(w1 + (size_t)k * F_ + (xcol - NQKV_) + half * 8, smem + G1_XOFF + c * 16);
    }
  }
  if (tid < 208) {
    const int col = (tid < 192) ? (unit0 * 64 + tid) : (xcol + tid - 192);
    bias_lds[tid] = (col >= NQKV_) ? bf2f(b1[col - NQKV_]) : 0.f;
  }
  bf16x8 xa[2][18];
#pragma unroll
  for (int r = 0; r < 2; ++r) {
    const u16* xp = xnf + ((size_t)((m0 >> 4) + wid * 2 + r) * 18 * 64 + lane) * 8;
#pragma unroll
    for (int ks = 0; ks < 18; ++ks) xa[r][ks] = *reinterpret_cast<const bf16x8*>(xp + ks * 512);
  }
  wait_vm<0>();
  WAIT_LGKM0();
  BAR();
  WF wA[4];
  {
    unsigned ad[4][2];
    g1_addr(ad, smem, lane);
    g1_reads<0, 0, 4>(wA, ad);
  }
  STAMP(1, 1);
  g1_unit<0>(wsrc, wstride, unit0, wfold, wqkv, w1, smem, xa, m0, qkv, hf, wA);
  STAMP(1, 2);
  g1_unit<1>(wsrc, wstride, unit0, wfold, wqkv, w1, smem, xa, m0, qkv, hf, wA);
  STAMP(1, 3);
  g1_unit<2>(wsrc, wstride, unit0, wfold, wqkv, w1, smem, xa, m0, qkv, hf, wA);
  STAMP(1, 4);
  if (extra) {
    int tz = threadIdx.x;
    asm volatile("" : "+v"(tz));
    const int wid2 = __builtin_amdgcn_readfirstlane(tz >> 6), lane2 = tz & 63;
    const int g = lane2 >> 4, q = (lane2 & 15) >> 2, p = lane2 & 3;
    unsigned ad[2];
#pragma unroll
    for (int t = 0; t < 2; ++t) {
      const int k = 8 * g + 4 * t + q;
      const int pr = (k & ~0xC) | ((k & 4) << 1) | ((k & 8) >> 1);
      ad[t] = lds_addr(smem) + G1_XOFF + pr * 32 + p * 8;
    }
    f32x4 acc[2] = {(f32x4){0.f, 0.f, 0.f, 0.f}, (f32x4){0.f, 0.f, 0.f, 0.f}};
#pragma unroll
    for (int kb = 0; kb < 18; kb += 6) {
      WF w[6];
      tr_rd<0 * 1024>(w[0].lo, ad[0] + kb * 1024); tr_rd<0 * 1024>(w[0].hi, ad[1] + kb * 1024);
      tr_rd<1 * 1024>(w[1].lo, ad[0] + kb * 1024); tr_rd<1 * 1024>(w[1].hi, ad[1] + kb * 1024);
      tr_rd<2 * 1024>(w[2].lo, ad[0] + kb * 1024); tr_rd<2 * 1024>(w[2].hi, ad[1] + kb * 1024);
      tr_rd<3 * 1024>(w[3].lo, ad[0] + kb * 1024); tr_rd<3 * 1024>(w[3].hi, ad[1] + kb * 1024);
      tr_rd<4 * 1024>(w[4].lo, ad[0] + kb * 1024); tr_rd<4 * 1024>(w[4].hi, ad[1] + kb * 1024);
      tr_rd<5 * 1024>(w[5].lo, ad[0] + kb * 1024); tr_rd<5 * 1024>(w[5].hi, ad[1] + kb * 1024);
      wait_lgkm<0>();
#pragma unroll
      for (int i = 0; i < 6; ++i) pin(w[i]);
#pragma unroll
      for (int i = 0; i < 6; ++i)
#pragma unroll
        for (int r = 0; r < 2; ++r)
          acc[r] = __builtin_amdgcn_mfma_f32_16x16x32_bf16(cat(w[i]), xa[r][kb + i], acc[r], 0, 0, 0);
    }
#pragma unroll
    for (int r = 0; r < 2; ++r)
      g1_store_tile(acc[r], xcol / 16, m0 + wid2 * 32 + r * 16 + (lane2 & 15), g, bias_lds + 192 + 4 * g, qkv, hf);
  }
}

constexpr int G2_NT = 9, G2_RB = G2_NT * 32;
constexpr int G2_BUFB = 64 * G2_RB;
constexpr int G2_RING = 4;
constexpr int G2_STAGES = 18;

__device__ __forceinline__ void softmax_pm(const float (&pk)[18], float (&ep)[18], float (&en)[18]) {
  float mx = pk[0], mn = pk[0];
#pragma unroll
  for (int f = 1; f < 18; ++f) {
    mx = fmaxf(mx, pk[f]);
    mn = fminf(mn, pk[f]);
  }
  float sp = 0.f, sn = 0.f;
#pragma unroll
  for (int f = 0; f < 18; ++f) {
    ep[f] = __expf(pk[f] - mx);
    en[f] = __expf(mn - pk[f]);
    sp += ep[f];
    sn += en[f];
  }
  const float rp = 1.f / sp, rn = 1.f / sn;
#pragma unroll
  for (int f = 0; f < 18; ++f) {
    ep[f] *= rp;
    en[f] *= rn;
  }
}

__device__ void head_kv_block(int h, const u16* __restrict__ qkv, const u16* __restrict__ wo,
                              u16* __restrict__ mcat, float* __restrict__ zbuf, char* smem) {
  u16* phiT = reinterpret_cast<u16*>(smem);
  u16* vT = reinterpret_cast<u16*>(smem + 25344);
  float* kvs = reinterpret_cast<float*>(smem + 25344 + 16896);
  const int tid = threadIdx.x, wid = tid >> 6, lane = tid & 63;
  unsigned kr[8][9], vr[8][9];
#pragma unroll
  for (int ch = 0; ch < 8; ++ch) {
    const int row = ch * 256 + tid;
    const unsigned* kp = reinterpret_cast<const unsigned*>(qkv + ((size_t)(32 + h) * S_ + row) * 18);
    const unsigned* vp = reinterpret_cast<const unsigned*>(qkv + ((size_t)(64 + h) * S_ + row) * 18);
#pragma unroll
    for (int d = 0; d < 9; ++d) { kr[ch][d] = kp[d]; vr[ch][d] = vp[d]; }
  }
  for (int i = tid; i < 12 * 264; i += 256) phiT[36 * 264 + i] = 0;
  for (int i = tid; i < 14 * 264; i += 256) vT[18 * 264 + i] = (i < 264) ? (u16)0x3f80 : (u16)0;
  f32x4 acc[3][2];
#pragma unroll
  for (int a = 0; a < 3; ++a)
#pragma unroll
    for (int b = 0; b < 2; ++b) acc[a][b] = (f32x4){0.f, 0.f, 0.f, 0.f};
#pragma unroll
  for (int ch = 0; ch < 8; ++ch) {
    float pk[18];
#pragma unroll
    for (int d = 0; d < 9; ++d) {
      pk[2 * d] = lo16(kr[ch][d]);
      pk[2 * d + 1] = hi16(kr[ch][d]);
    }
    float ep[18], en[18];
    softmax_pm(pk, ep, en);
    __syncthreads();
#pragma unroll
    for (int f = 0; f < 18; ++f) {
      phiT[f * 264 + tid] = f2bf(ep[f]);
      phiT[(18 + f) * 264 + tid] = f2bf(en[f]);
      vT[f * 264 + tid] = (u16)((f & 1) ? (vr[ch][f >> 1] >> 16) : (vr[ch][f >> 1] & 0xffff));
    }
    __syncthreads();
#pragma unroll
    for (int ks = 0; ks < 2; ++ks) {
      const int s0 = wid * 64 + ks * 32 + 8 * (lane >> 4);
      bf16x8 bv[2];
#pragma unroll
      for (int b = 0; b < 2; ++b) bv[b] = *reinterpret_cast<const bf16x8*>(vT + (b * 16 + (lane & 15)) * 264 + s0);
#pragma unroll
      for (int a = 0; a < 3; ++a) {
        bf16x8 av = *reinterpret_cast<const bf16x8*>(phiT + (a * 16 + (lane & 15)) * 264 + s0);
#pragma unroll
        for (int b = 0; b < 2; ++b) acc[a][b] = __builtin_amdgcn_mfma_f32_16x16x32_bf16(av, bv[b], acc[a][b], 0, 0, 0);
      }
    }
  }
  __syncthreads();
#pragma unroll
  for (int a = 0; a < 3; ++a)
#pragma unroll
    for (int b = 0; b < 2; ++b)
#pragma unroll
      for (int j = 0; j < 4; ++j)
        kvs[(wid * 48 + a * 16 + (lane >> 4) * 4 + j) * 32 + b * 16 + (lane & 15)] = acc[a][b][j];
  u16* woimg = reinterpret_cast<u16*>(smem);
  u16* kvb = reinterpret_cast<u16*>(smem + 36864);
  for (int i = tid; i < 32 * 72; i += 256) {
    const int r = i / 72, c = i % 72;
    u32x4 v = {0u, 0u, 0u, 0u};
    if (r < 18) v = *reinterpret_cast<const u32x4*>(wo + (size_t)(h * 18 + r) * D_ + c * 8);
    *reinterpret_cast<u32x4*>(woimg + r * 576 + c * 8) = v;
  }
  __syncthreads();
  for (int i = tid; i < 48 * 32; i += 256) {
    float v = kvs[i] + kvs[48 * 32 + i] + kvs[2 * 48 * 32 + i] + kvs[3 * 48 * 32 + i];
    const int f = i >> 5, d = i & 31;
    if (d == 18 && f < 36) zbuf[h * 36 + f] = rbf(v);
    kvb[i] = (d < 18 && f < 36) ? f2bf(v) : (u16)0;
  }
  __syncthreads();
  {
    const int g = lane >> 4, q = (lane & 15) >> 2, p = lane & 3;
    constexpr int RB = 576 * 2;
    const int o0 = (8 * g + q) * RB + p * 8, o1 = (8 * g + 4 + q) * RB + p * 8;
    bf16x8 kb[3];
#pragma unroll
    for (int a = 0; a < 3; ++a) kb[a] = *reinterpret_cast<const bf16x8*>(kvb + (a * 16 + (lane & 15)) * 32 + 8 * g);
    for (int nt = wid; nt < 36; nt += 4) {
      bf16x8 w = wfrag(reinterpret_cast<const char*>(woimg), o0, o1, nt * 32);
#pragma unroll
      for (int a = 0; a < 3; ++a) {
        f32x4 c = __builtin_amdgcn_mfma_f32_16x16x32_bf16(w, kb[a], (f32x4){0.f, 0.f, 0.f, 0.f}, 0, 0, 0);
        const int f = a * 16 + (lane & 15);
        if (f < KPH_) {
          u32x2 v = {pack2(c[0], c[1]), pack2(c[2], c[3])};
          *reinterpret_cast<u32x2*>(mcat + (size_t)(h * KPH_ + f) * D_ + nt * 16 + 4 * g) = v;
        }
      }
    }
  }
}

__device__ void head_q_block(int hb, const u16* __restrict__ qkv, u16* __restrict__ phif) {
  const int h = hb >> 1, tid = threadIdx.x;
  unsigned kr[4][9];
#pragma unroll
  for (int ch = 0; ch < 4; ++ch) {
    const int row = (hb & 1) * 1024 + ch * 256 + tid;
    const unsigned* kp = reinterpret_cast<const unsigned*>(qkv + ((size_t)h * S_ + row) * 18);
#pragma unroll
    for (int d = 0; d < 9; ++d) kr[ch][d] = kp[d];
  }
#pragma unroll
  for (int ch = 0; ch < 4; ++ch) {
    const int row = (hb & 1) * 1024 + ch * 256 + tid;
    float pk[18];
#pragma unroll
    for (int d = 0; d < 9; ++d) {
      pk[2 * d] = lo16(kr[ch][d]);
      pk[2 * d + 1] = hi16(kr[ch][d]);
    }
    float ep[18], en[18];
    softmax_pm(pk, ep, en);
    unsigned o[20];
#pragma unroll
    for (int f = 0; f < 9; ++f) o[f] = pack2(ep[2 * f], ep[2 * f + 1]);
#pragma unroll
    for (int f = 0; f < 9; ++f) o[9 + f] = pack2(en[2 * f], en[2 * f + 1]);
    o[18] = 0;
    o[19] = 0;
#pragma unroll
    for (int c = 0; c < 5; ++c) {
      const int cidx = h * 5 + c;
      u16* dst = phif + ((size_t)((row >> 4) * 40 + (cidx >> 2)) * 64 + (cidx & 3) * 16 + (row & 15)) * 8;
      u32x4 v = {o[4 * c], o[4 * c + 1], o[4 * c + 2], o[4 * c + 3]};
      *reinterpret_cast<u32x4*>(dst) = v;
    }
  }
}

template <int BASE, int N0, int CNT, int I = 0>
__device__ __forceinline__ void g2_wreads(WF (&w)[5], unsigned a0, unsigned a1) {
  if constexpr (I < CNT) {
    tr_rd<BASE + (N0 + I) * 32>(w[I].lo, a0);
    tr_rd<BASE + (N0 + I) * 32>(w[I].hi, a1);
    g2_wreads<BASE, N0, CNT, I + 1>(w, a0, a1);
  }
}
template <int N0, int CNT>
__device__ __forceinline__ void g2_mma(WF (&w)[5], const bf16x8 (&ac)[2], f32x4 (&acc)[2][G2_NT]) {
#pragma unroll
  for (int i = 0; i < CNT; ++i) pin(w[i]);
#pragma unroll
  for (int i = 0; i < CNT; ++i)
#pragma unroll
    for (int r = 0; r < 2; ++r)
      acc[r][N0 + i] = __builtin_amdgcn_mfma_f32_16x16x32_bf16(cat(w[i]), ac[r], acc[r][N0 + i], 0, 0, 0);
}
constexpr int K2_HEADBLK = 96;

__global__ __launch_bounds__(256, 2) void k2_gemm2(const u16* __restrict__ hf, const u16* __restrict__ w2,
                                                   float* __restrict__ part, const u16* __restrict__ qkv,
                                                   const u16* __restrict__ wo, u16* __restrict__ phif,
                                                   u16* __restrict__ mcat, float* __restrict__ zbuf) {
  __shared__ __attribute__((aligned(16))) char smem[G2_RING * G2_BUFB];
  const int tid = threadIdx.x, wid = tid >> 6, lane = tid & 63;
  int bid = blockIdx.x;
  if (bid < 32) { head_kv_block(bid, qkv, wo, mcat, zbuf, smem); return; }
  if (bid < K2_HEADBLK) { head_q_block(bid - 32, qkv, phif); return; }
  bid -= K2_HEADBLK;
  const int xcd = bid & 7, loc = bid >> 3;
  const int sp = xcd >> 1, mt = (xcd & 1) * 8 + (loc >> 2), nt4 = loc & 3;
  const int m0 = mt * 128, n0 = nt4 * 144, k0 = sp * 1152;
  const int g = lane >> 4, q = (lane & 15) >> 2, p = lane & 3;
  const bool odd = g & 1;
  const unsigned wa0 = lds_addr(smem) + (8 * g + 4 * (g & 1) + q) * G2_RB + p * 8;
  const unsigned wa1 = lds_addr(smem) + (8 * g + 4 * (1 - (g & 1)) + q) * G2_RB + p * 8;
  f32x4 acc[2][G2_NT];
#pragma unroll
  for (int r = 0; r < 2; ++r)
#pragma unroll
    for (int n = 0; n < G2_NT; ++n) acc[r][n] = (f32x4){0.f, 0.f, 0.f, 0.f};
  const u16* wp[5];
  int wdst[5];
#pragma unroll
  for (int i = 0; i < 5; ++i) {
    int ins = wid + 4 * i;
    if (ins >= 18) ins -= 18;
    int c = ins * 64 + lane;
    int row = c / 18, cc = c % 18;
    wp[i] = w2 + (size_t)(k0 + row) * D_ + n0 + cc * 8;
    wdst[i] = c * 16;
  }
  const u16* ap[2];
#pragma unroll
  for (int r = 0; r < 2; ++r) ap[r] = hf + ((size_t)((m0 >> 4) + wid * 2 + r) * 144 + (k0 >> 5)) * 512 + lane * 8;
  bf16x8 an[4][2][2];
  auto issue = [&](auto SL) {
    constexpr int slot = decltype(SL)::value;
    char* buf = smem + slot * G2_BUFB;
#pragma unroll
    for (int i = 0; i < 5; ++i) { glds16(wp[i], buf + wdst[i]); wp[i] += 64 * D_; }
#pragma unroll
    for (int r = 0; r < 2; ++r) {
      gld16_asm(an[slot][r][0], ap[r]);
      gld16_asm(an[slot][r][1], ap[r] + 512);
      ap[r] += 1024;
    }
  };
  issue(IC<0>{});
  issue(IC<1>{});
  issue(IC<2>{});
  wait_vm<18>();
  BAR();
  WF wA[5], wB[5];
  g2_wreads<0, 0, 5>(wA, wa0, wa1);
  auto stage = [&](auto U, auto LAST) {
    constexpr int u = decltype(U)::value;
    constexpr int rem = decltype(LAST)::value;
    constexpr int BASE = u * G2_BUFB;
    bf16x8 c0[2], c1[2];
#pragma unroll
    for (int r = 0; r < 2; ++r) {
      pin(an[u][r][0]); pin(an[u][r][1]);
      c0[r] = swap_halves_if(an[u][r][0], odd);
      c1[r] = swap_halves_if(an[u][r][1], odd);
    }
    g2_wreads<BASE, 5, 4>(wB, wa0, wa1);
    wait_lgkm<8>();
    g2_mma<0, 5>(wA, c0, acc);
    g2_wreads<BASE + 32 * G2_RB, 0, 5>(wA, wa0, wa1);
    wait_lgkm<10>();
    g2_mma<5, 4>(wB, c0, acc);
    g2_wreads<BASE + 32 * G2_RB, 5, 4>(wB, wa0, wa1);
    wait_lgkm<8>();
    g2_mma<0, 5>(wA, c1, acc);
    if constexpr (rem >= 1) {
      if constexpr (rem >= 2) wait_vm<9>(); else wait_vm<0>();
      BAR();
      if constexpr (rem >= 3) issue(IC<(u + 3) & 3>{});
      g2_wreads<((u + 1) & 3) * G2_BUFB, 0, 5>(wA, wa0, wa1);
      wait_lgkm<10>();
    } else {
      wait_lgkm<0>();
    }
    g2_mma<5, 4>(wB, c1, acc);
  };
#pragma unroll 1
  for (int it = 0; it < 3; ++it) {
    stage(IC<0>{}, IC<3>{}); stage(IC<1>{}, IC<3>{}); stage(IC<2>{}, IC<3>{}); stage(IC<3>{}, IC<3>{});
  }
  stage(IC<0>{}, IC<3>{}); stage(IC<1>{}, IC<3>{}); stage(IC<2>{}, IC<3>{});
  stage(IC<3>{}, IC<2>{}); stage(IC<0>{}, IC<1>{}); stage(IC<1>{}, IC<0>{});
#pragma unroll
  for (int r = 0; r < 2; ++r) {
    const int m = m0 + wid * 32 + r * 16 + (lane & 15);
    float* pp = part + ((size_t)sp * S_ + m) * D_ + n0 + 4 * g;
#pragma unroll
    for (int n = 0; n < G2_NT; ++n) *reinterpret_cast<f32x4*>(pp + n * 16) = acc[r][n];
  }
}

constexpr int G3_BUFB = 64 * 128;
constexpr int G3_RING = 4;

__device__ __forceinline__ bf16x8 scale_frag(bf16x8 v, float s) {
  u32x4 in = *reinterpret_cast<u32x4*>(&v);
  u32x4 pk;
#pragma unroll
  for (int j = 0; j < 4; ++j) pk[j] = pack2(lo16(in[j]) * s, hi16(in[j]) * s);
  return *reinterpret_cast<bf16x8*>(&pk);
}

__global__ __launch_bounds__(256, 2) void k3_final(const u16* __restrict__ phif, const u16* __restrict__ mcat,
                                                   const float* __restrict__ zbuf, const float* __restrict__ part,
                                                   const u16* __restrict__ b2, const u16* __restrict__ x,
                                                   u16* __restrict__ out) {
  __shared__ __attribute__((aligned(16))) char smem[G3_RING * G3_BUFB + 64 * 33 * 4 + 32 * 36 * 4];
  float* rden = reinterpret_cast<float*>(smem + G3_RING * G3_BUFB);
  float* zs = reinterpret_cast<float*>(smem + G3_RING * G3_BUFB + 64 * 33 * 4);
  const int tid = threadIdx.x, wid = tid >> 6, lane = tid & 63;
  const int bid = blockIdx.x;
  const int xcd = bid & 7, loc = bid >> 3;
  const int mt = xcd * 4 + loc / 9, nt = loc % 9;
  const int m0 = mt * 64, n0 = nt * 64;
  const int g = lane >> 4, q = (lane & 15) >> 2, p = lane & 3;
  const int wr = wid >> 1, wc = wid & 1;
  STAMP(3, 0);
  const u16* wp[2];
#pragma unroll
  for (int i = 0; i < 2; ++i) {
    const int row = (wid * 2 + i) * 8 + (lane >> 3);
    const int f = ((row >> 1) & 1) | (((row >> 3) & 1) << 1);
    const int ch = (lane & 7) ^ (f << 1);
    wp[i] = mcat + (size_t)row * D_ + n0 + ch * 8;
  }
  const u16* ap[2];
#pragma unroll
  for (int r = 0; r < 2; ++r) ap[r] = phif + ((size_t)((m0 >> 4) + wr * 2 + r) * 40) * 512 + lane * 8;
  bf16x8 an[4][2][2];
  auto issue = [&](auto SL) {
    constexpr int slot = decltype(SL)::value;
    char* buf = smem + slot * G3_BUFB;
#pragma unroll
    for (int i = 0; i < 2; ++i) { glds16(wp[i], buf + (wid * 2 + i) * 1024 + lane * 16); wp[i] += 64 * D_; }
#pragma unroll
    for (int r = 0; r < 2; ++r) {
      gld16_asm(an[slot][r][0], ap[r]);
      gld16_asm(an[slot][r][1], ap[r] + 512);
      ap[r] += 1024;
    }
  };
  issue(IC<0>{});
  issue(IC<1>{});
  issue(IC<2>{});
  for (int i = tid; i < 32 * 36; i += 256) zs[i] = zbuf[i];
  __syncthreads();
  STAMP(3, 1);
  for (int i = tid; i < 64 * 32; i += 256) {
    const int r = i & 63, h = i >> 6;
    const int row = m0 + r;
    float d = 0.f;
#pragma unroll
    for (int c = 0; c < 5; ++c) {
      const int cidx = h * 5 + c;
      u32x4 v = *reinterpret_cast<const u32x4*>(phif + ((size_t)((row >> 4) * 40 + (cidx >> 2)) * 64 + (cidx & 3) * 16 + (row & 15)) * 8);
#pragma unroll
      for (int j = 0; j < 4; ++j) {
        if (c * 8 + 2 * j < 36) d += lo16(v[j]) * zs[h * 36 + c * 8 + 2 * j];
        if (c * 8 + 2 * j + 1 < 36) d += hi16(v[j]) * zs[h * 36 + c * 8 + 2 * j + 1];
      }
    }
    rden[r * 33 + h] = 1.f / (rbf(d) + 1e-6f);
  }
  unsigned ad[2][2];
#pragma unroll
  for (int t = 0; t < 2; ++t) {
    const int row = 8 * g + 4 * t + q;
    const int f = ((row >> 1) & 1) | (((row >> 3) & 1) << 1);
#pragma unroll
    for (int n = 0; n < 2; ++n) ad[n][t] = lds_addr(smem) + row * 128 + (((wc * 2 + n) ^ f) * 32) + p * 8;
  }
  f32x4 acc[2][2];
#pragma unroll
  for (int r = 0; r < 2; ++r)
#pragma unroll
    for (int n = 0; n < 2; ++n) acc[r][n] = (f32x4){0.f, 0.f, 0.f, 0.f};
  int rl[2];
#pragma unroll
  for (int r = 0; r < 2; ++r) rl[r] = wr * 32 + r * 16 + (lane & 15);
  __syncthreads();
  STAMP(3, 2);
  int st = 0;
  auto stage = [&](auto U, auto LAST) {
    constexpr int u = decltype(U)::value;
    constexpr int rem = decltype(LAST)::value;
    if constexpr (rem >= 2) wait_vm<12>();
    else if constexpr (rem == 1) wait_vm<6>();
    else wait_vm<0>();
    WAIT_LGKM0();
    BAR();
    float sc[2][2];
#pragma unroll
    for (int ks = 0; ks < 2; ++ks) {
      const int h = (st * 64 + ks * 32 + 8 * g) / KPH_;
#pragma unroll
      for (int r = 0; r < 2; ++r) sc[r][ks] = rden[rl[r] * 33 + h];
    }
    asm volatile("" : "+v"(sc[0][0]), "+v"(sc[0][1]), "+v"(sc[1][0]), "+v"(sc[1][1]));
    WF w[2][2];
    constexpr int BASE = u * G3_BUFB;
#pragma unroll
    for (int n = 0; n < 2; ++n) {
      tr_rd<BASE>(w[0][n].lo, ad[n][0]); tr_rd<BASE>(w[0][n].hi, ad[n][1]);
    }
#pragma unroll
    for (int n = 0; n < 2; ++n) {
      tr_rd<BASE + 32 * 128>(w[1][n].lo, ad[n][0]); tr_rd<BASE + 32 * 128>(w[1][n].hi, ad[n][1]);
    }
    bf16x8 a[2][2];
#pragma unroll
    for (int r = 0; r < 2; ++r)
#pragma unroll
      for (int ks = 0; ks < 2; ++ks) { pin(an[u][r][ks]); a[r][ks] = scale_frag(an[u][r][ks], sc[r][ks]); }
    if constexpr (rem >= 3) issue(IC<(u + 3) & 3>{});
    wait_lgkm<0>();
#pragma unroll
    for (int ks = 0; ks < 2; ++ks)
#pragma unroll
      for (int n = 0; n < 2; ++n) pin(w[ks][n]);
#pragma unroll
    for (int ks = 0; ks < 2; ++ks)
#pragma unroll
      for (int n = 0; n < 2; ++n)
#pragma unroll
        for (int r = 0; r < 2; ++r)
          acc[r][n] = __builtin_amdgcn_mfma_f32_16x16x32_bf16(cat(w[ks][n]), a[r][ks], acc[r][n], 0, 0, 0);
    ++st;
  };
#pragma unroll 1
  for (int it = 0; it < 4; ++it) {
    stage(IC<0>{}, IC<3>{}); stage(IC<1>{}, IC<3>{}); stage(IC<2>{}, IC<3>{}); stage(IC<3>{}, IC<3>{});
  }
  stage(IC<0>{}, IC<3>{}); stage(IC<1>{}, IC<2>{}); stage(IC<2>{}, IC<1>{}); stage(IC<3>{}, IC<0>{});
  STAMP(3, 3);
#pragma unroll
  for (int r = 0; r < 2; ++r) {
    const int m = m0 + rl[r];
#pragma unroll
    for (int n = 0; n < 2; ++n) {
      const int ncol = n0 + wc * 32 + n * 16 + 4 * g;
      f32x4 y = *reinterpret_cast<const f32x4*>(part + (size_t)m * D_ + ncol);
#pragma unroll
      for (int s = 1; s < SPLITK_; ++s) {
        f32x4 t = *reinterpret_cast<const f32x4*>(part + ((size_t)s * S_ + m) * D_ + ncol);
        y += t;
      }
      u32x2 bb = *reinterpret_cast<const u32x2*>(b2 + ncol);
      u32x2 xx = *reinterpret_cast<const u32x2*>(x + (size_t)m * D_ + ncol);
      float bv[4] = {lo16(bb[0]), hi16(bb[0]), lo16(bb[1]), hi16(bb[1])};
      float xv[4] = {lo16(xx[0]), hi16(xx[0]), lo16(xx[1]), hi16(xx[1])};
      float o[4];
#pragma unroll
      for (int j = 0; j < 4; ++j) {
        float yy = y[j] + bv[j];
        float gg = yy / (1.f + __expf(-yy));
        o[j] = xv[j] + acc[r][n][j] * gg;
      }
      u32x2 v = {pack2(o[0], o[1]), pack2(o[2], o[3])};
      *reinterpret_cast<u32x2*>(out + (size_t)m * D_ + ncol) = v;
    }
  }
  STAMP(3, 4);
}

extern "C" void kernel_launch(void* const* d_in, const int* in_sizes, int n_in, void* d_out, int out_size, void* d_ws,
                              size_t ws_size, hipStream_t stream) {
  const u16* x = (const u16*)d_in[0];
  const u16* wqkv = (const u16*)d_in[1];
  const u16* wphi = (const u16*)d_in[2];
  const u16* wo = (const u16*)d_in[3];
  const u16* w1 = (const u16*)d_in[5];
  const u16* b1 = (const u16*)d_in[6];
  const u16* w2 = (const u16*)d_in[7];
  const u16* b2 = (const u16*)d_in[8];
  u16* out = (u16*)d_out;
  char* ws = (char*)d_ws;
  size_t off = 0;
  u16* xnf = (u16*)(ws + off); off += (size_t)S_ * D_ * 2;
  u16* wfold = (u16*)(ws + off); off += (size_t)D_ * NPH_ * 2;
  u16* qkv = (u16*)(ws + off); off += (size_t)S_ * NQKV_ * 2;
  u16* hf = (u16*)(ws + off); off += (size_t)S_ * F_ * 2;
  float* part = (float*)(ws + off); off += (size_t)SPLITK_ * S_ * D_ * 4;
  u16* phif = (u16*)(ws + off); off += (size_t)S_ * KPHI_ * 2;
  u16* mcat = (u16*)(ws + off); off += (size_t)KPHI_ * D_ * 2;
  float* zbuf = (float*)(ws + off); off += 32 * 36 * 4;

  k0_prep<<<512 + 192, 256, 0, stream>>>(x, wqkv, wphi, xnf, wfold);
  k1_gemm1<<<256, 512, 0, stream>>>(xnf, wfold, wqkv, w1, b1, qkv, hf);
  k2_gemm2<<<K2_HEADBLK + 256, 256, 0, stream>>>(hf, w2, part, qkv, wo, phif, mcat, zbuf);
  k3_final<<<32 * 9, 256, 0, stream>>>(phif, mcat, zbuf, part, b2, x, out);
}
```

```cpp
#include <hip/hip_runtime.h>
#include <hip/hip_bf16.h>
#include <stdint.h>

constexpr int S_ = 2048, D_ = 576, H_ = 32, DH_ = 18, F_ = 4608, NQKV_ = 1728;
constexpr int NPH_ = 1152;
constexpr int KPH_ = 40;
constexpr int KPHI_ = H_ * KPH_;
constexpr int SPLITK_ = 4;

typedef short bf16x8 __attribute__((ext_vector_type(8)));
typedef short s16x4 __attribute__((ext_vector_type(4)));
typedef float f32x4 __attribute__((ext_vector_type(4)));
typedef unsigned u32x4 __attribute__((ext_vector_type(4)));
typedef unsigned u32x2 __attribute__((ext_vector_type(2)));
typedef __attribute__((address_space(3))) s16x4* lds_s16x4_p;
typedef unsigned short u16;
template <int V> struct IC { static constexpr int value = V; };

#define STAMP(kid, i) do { } while (0)
#define WAIT_LGKM0() asm volatile("s_waitcnt lgkmcnt(0)" ::: "memory")
#define BAR() __builtin_amdgcn_s_barrier()

template <int N>
__device__ __forceinline__ void wait_vm() {
  static_assert(N >= 0 && N < 64, "vmcnt range");
  asm volatile("s_waitcnt vmcnt(%0)" ::"n"(N) : "memory");
}

__device__ __forceinline__ float bf2f(u16 v) { return __uint_as_float(((unsigned)v) << 16); }
__device__ __forceinline__ u16 f2bf(float f) {
  __hip_bfloat16 b = __float2bfloat16(f);
  return *reinterpret_cast<u16*>(&b);
}
__device__ __forceinline__ unsigned pack2(float lo, float hi) {
  return (unsigned)f2bf(lo) | ((unsigned)f2bf(hi) << 16);
}
__device__ __forceinline__ float rbf(float f) { return bf2f(f2bf(f)); }
__device__ __forceinline__ float lo16(unsigned u) { return __uint_as_float(u << 16); }
__device__ __forceinline__ float hi16(unsigned u) { return __uint_as_float(u & 0xffff0000u); }

__device__ __forceinline__ void glds16(const void* g, void* l) {
  __builtin_amdgcn_global_load_lds((const unsigned*)g, (unsigned*)l, 16, 0, 0);
}

__device__ __forceinline__ bf16x8 swap_halves_if(bf16x8 v, bool odd) {
  bf16x8 s = __builtin_shufflevector(v, v, 4, 5, 6, 7, 0, 1, 2, 3);
  return odd ? s : v;
}

__device__ __forceinline__ bf16x8 wfrag(const char* img, int o0, int o1, int off) {
  s16x4 a = __builtin_amdgcn_ds_read_tr16_b64_v4i16((lds_s16x4_p)(img + o0 + off));
  s16x4 b = __builtin_amdgcn_ds_read_tr16_b64_v4i16((lds_s16x4_p)(img + o1 + off));
  return __builtin_shufflevector(a, b, 0, 1, 2, 3, 4, 5, 6, 7);
}

struct WF { s16x4 lo, hi; };
template <int OFF>
__device__ __forceinline__ void tr_rd(s16x4& r, unsigned addr) {
  asm volatile("ds_read_b64_tr_b16 %0, %1 offset:%2" : "=v"(r) : "v"(addr), "n"(OFF) : "memory");
}
__device__ __forceinline__ void gld16_asm(bf16x8& r, const void* p) {
  asm volatile("global_load_dwordx4 %0, %1, off" : "=v"(r) : "v"(p) : "memory");
}
template <int N>
__device__ __forceinline__ void wait_lgkm() {
  static_assert(N >= 0 && N <= 15, "lgkmcnt range");
  asm volatile("s_waitcnt lgkmcnt(%0)" ::"n"(N) : "memory");
}
__device__ __forceinline__ void pin(WF& w) { asm volatile("" : "+v"(w.lo), "+v"(w.hi)); }
__device__ __forceinline__ void pin(bf16x8& w) { asm volatile("" : "+v"(w)); }
__device__ __forceinline__ bf16x8 cat(const WF& w) { return __builtin_shufflevector(w.lo, w.hi, 0, 1, 2, 3, 4, 5, 6, 7); }
__device__ __forceinline__ unsigned lds_addr(const void* p) {
  return (unsigned)(uintptr_t)(__attribute__((address_space(3))) const char*)p;
}

__global__ __launch_bounds__(256) void k0_prep(const u16* __restrict__ x, const u16* __restrict__ wqkv,
                                               const u16* __restrict__ wphi, u16* __restrict__ xnf,
                                               u16* __restrict__ wfold) {
  __shared__ __attribute__((aligned(16))) float wph[18 * 20];
  const int tid = threadIdx.x, wid = tid >> 6, lane = tid & 63;
  const int bid = blockIdx.x;
  if (bid < 512) {
    const int row = bid * 4 + wid;
    const u16* xp = x + (size_t)row * D_;
    u32x4 a = *reinterpret_cast<const u32x4*>(xp + lane * 8);
    u32x4 b = {0u, 0u, 0u, 0u};
    if (lane < 8) b = *reinterpret_cast<const u32x4*>(xp + 512 + lane * 8);
    float v[16];
#pragma unroll
    for (int j = 0; j < 4; ++j) {
      v[2 * j] = lo16(a[j]); v[2 * j + 1] = hi16(a[j]);
      v[8 + 2 * j] = lo16(b[j]); v[8 + 2 * j + 1] = hi16(b[j]);
    }
    float s = 0.f;
#pragma unroll
    for (int j = 0; j < 16; ++j) s += v[j];
#pragma unroll
    for (int o = 32; o >= 1; o >>= 1) s += __shfl_xor(s, o);
    const float mean = s * (1.f / D_);
    float ss = 0.f;
#pragma unroll
    for (int j = 0; j < 8; ++j) { float d = v[j] - mean; ss += d * d; }
    if (lane < 8) {
#pragma unroll
      for (int j = 8; j < 16; ++j) { float d = v[j] - mean; ss += d * d; }
    }
#pragma unroll
    for (int o = 32; o >= 1; o >>= 1) ss += __shfl_xor(ss, o);
    const float rstd = rsqrtf(ss * (1.f / D_) + 1e-5f);
    u32x4 oa, ob;
#pragma unroll
    for (int j = 0; j < 4; ++j) {
      oa[j] = pack2((v[2 * j] - mean) * rstd, (v[2 * j + 1] - mean) * rstd);
      ob[j] = pack2((v[8 + 2 * j] - mean) * rstd, (v[8 + 2 * j + 1] - mean) * rstd);
    }
    {
      const int c = lane;
      u16* dst = xnf + ((size_t)((row >> 4) * 18 + (c >> 2)) * 64 + (c & 3) * 16 + (row & 15)) * 8;
      *reinterpret_cast<u32x4*>(dst) = oa;
    }
    if (lane < 8) {
      const int c = 64 + lane;
      u16* dst = xnf + ((size_t)((row >> 4) * 18 + (c >> 2)) * 64 + (c & 3) * 16 + (row & 15)) * 8;
      *reinterpret_cast<u32x4*>(dst) = ob;
    }
    return;
  }
  {
    const int fb = bid - 512;
    const int hh = fb / 3, k = (fb % 3) * 192 + tid;
    const int h = hh & 31;
    for (int i = tid; i < 18 * 20; i += 256) {
      const int d = i / 20, f = i % 20;
      wph[i] = (f < 18) ? bf2f(wphi[h * 324 + d * 18 + f]) : 0.f;
    }
    unsigned wv[9];
    if (tid < 192) {
      const u16* wp = wqkv + (size_t)k * NQKV_ + hh * 18;
#pragma unroll
      for (int d = 0; d < 9; ++d) wv[d] = *reinterpret_cast<const unsigned*>(wp + 2 * d);
    }
    __syncthreads();
    if (tid < 192) {
      float acc[20];
#pragma unroll
      for (int f = 0; f < 20; ++f) acc[f] = 0.f;
#pragma unroll
      for (int d = 0; d < 18; ++d) {
        const float w = (d & 1) ? hi16(wv[d >> 1]) : lo16(wv[d >> 1]);
#pragma unroll
        for (int f4 = 0; f4 < 5; ++f4) {
          f32x4 pw = *reinterpret_cast<const f32x4*>(wph + d * 20 + f4 * 4);
#pragma unroll
          for (int j = 0; j < 4; ++j) acc[f4 * 4 + j] += w * pw[j];
        }
      }
      u16* op = wfold + (size_t)k * NPH_ + hh * 18;
#pragma unroll
      for (int f = 0; f < 9; ++f) *reinterpret_cast<unsigned*>(op + 2 * f) = pack2(acc[2 * f], acc[2 * f + 1]);
    }
  }
}

constexpr int G1_BUFB = 64 * 128;
constexpr int G1_RING = 8;
constexpr int G1_XOFF = G1_RING * G1_BUFB;
constexpr int G1_BOFF = G1_XOFF + 576 * 32;
constexpr int G1_SMEM = G1_BOFF + 1024;
constexpr int G1_NST = 8;

template <int BASE, int F0, int CNT, int I = 0>
__device__ __forceinline__ void g1_reads(WF (&w)[4], const unsigned (&ad)[4][2]) {
  if constexpr (I < CNT) {
    constexpr int f = F0 + I, ks = f / 4, n = f % 4;
    tr_rd<BASE + ks * 32 * 128>(w[I].lo, ad[n][0]);
    tr_rd<BASE + ks * 32 * 128>(w[I].hi, ad[n][1]);
    g1_reads<BASE, F0, CNT, I + 1>(w, ad);
  }
}
template <int F0, int KC>
__device__ __forceinline__ void g1_mma(WF (&w)[4], const bf16x8 (&xa)[2][18], f32x4 (&acc)[2][4]) {
#pragma unroll
  for (int i = 0; i < 4; ++i) pin(w[i]);
#pragma unroll
  for (int i = 0; i < 4; ++i) {
    const int f = F0 + i, ks = f / 4, n = f % 4;
#pragma unroll
    for (int r = 0; r < 2; ++r)
      acc[r][n] = __builtin_amdgcn_mfma_f32_16x16x32_bf16(cat(w[i]), xa[r][KC * 2 + ks], acc[r][n], 0, 0, 0);
  }
}

__device__ __forceinline__ float gelu_f(float v) {
  float u = 0.7978845608028654f * (v + 0.044715f * v * v * v);
  float e = __expf(-2.f * u);
  return v * __builtin_amdgcn_rcpf(1.f + e);
}

__device__ __forceinline__ void g1_store_tile(const f32x4& a, int tile, int mrow, int g, const float* bias4,
                                              u16* __restrict__ qkv, u16* __restrict__ hf) {
  if (tile < NQKV_ / 16) {
    const int col = tile * 16 + 4 * g;
#pragma unroll
    for (int j = 0; j < 2; ++j) {
      const int c = col + 2 * j;
      const int hh = c / 18, f = c - hh * 18;
      *reinterpret_cast<unsigned*>(qkv + ((size_t)hh * S_ + mrow) * 18 + f) = pack2(a[2 * j], a[2 * j + 1]);
    }
  } else {
    f32x4 bv = *reinterpret_cast<const f32x4*>(bias4);
    float o[4];
#pragma unroll
    for (int j = 0; j < 4; ++j) o[j] = gelu_f(a[j] + bv[j]);
    u32x2 v = {pack2(o[0], o[1]), pack2(o[2], o[3])};
    const int T = tile - NQKV_ / 16;
    u16* dst = hf + ((size_t)((mrow >> 4) * 144 + (T >> 1)) * 64 + ((T & 1) * 2 + (g >> 1)) * 16 + (mrow & 15)) * 8 + (g & 1) * 4;
    *reinterpret_cast<u32x2*>(dst) = v;
  }
}

__device__ __forceinline__ void g1_addr(unsigned (&ad)[4][2], char* smem, int lane) {
  const int g = lane >> 4, q = (lane & 15) >> 2, p = lane & 3;
#pragma unroll
  for (int t = 0; t < 2; ++t) {
    const int row = 8 * g + 4 * t + q;
    const int f = ((row >> 1) & 1) | (((row >> 3) & 1) << 1);
#pragma unroll
    for (int n = 0; n < 4; ++n) ad[n][t] = lds_addr(smem) + row * 128 + ((n ^ f) * 32) + p * 8;
  }
}

template <int U>
__device__ __forceinline__ void g1_unit(const u16*& wsrc, int& wstride, int unit0, const u16* wfold, const u16* wqkv,
                                        const u16* w1, char* smem, const bf16x8 (&xa)[2][18], int m0,
                                        u16* __restrict__ qkv, u16* __restrict__ hf, WF (&wA)[4]) {
  int tz = threadIdx.x;
  asm volatile("" : "+v"(tz));
  const int wid = __builtin_amdgcn_readfirstlane(tz >> 6), lane = tz & 63;
  const int g = lane >> 4;
  unsigned ad[4][2];
  g1_addr(ad, smem, lane);
  f32x4 acc[2][4];
#pragma unroll
  for (int r = 0; r < 2; ++r)
#pragma unroll
    for (int n = 0; n < 4; ++n) acc[r][n] = (f32x4){0.f, 0.f, 0.f, 0.f};

  auto stage = [&](auto KC) {
    constexpr int kc = decltype(KC)::value;
    constexpr int s = U * 9 + kc;
    constexpr int CUR = (s % G1_RING) * G1_BUFB;
    WF wB[4];
    g1_reads<CUR, 4, 4>(wB, ad);
    wait_lgkm<8>();
    g1_mma<0, kc>(wA, xa, acc);
    if constexpr (s < 26) {
      constexpr int nl = (s + 6 <= 26) ? 5 : (26 - s - 1);
      constexpr int nst = (U > 0 && kc <= 5) ? G1_NST : 0;
      wait_vm<nl + nst>();
      BAR();
      if constexpr (s + 7 <= 26) {
        constexpr int sn = s + 7;
        if constexpr (sn % 9 == 0) {
          const int col = (unit0 + sn / 9) * 64;
          const int row = wid * 8 + (lane >> 3);
          const int f = ((row >> 1) & 1) | (((row >> 3) & 1) << 1);
          const int ch = (lane & 7) ^ (f << 1);
          if (col < NPH_) { wsrc = wfold + (size_t)row * NPH_ + col + ch * 8; wstride = 64 * NPH_; }
          else if (col < NQKV_) { wsrc = wqkv + (size_t)row * NQKV_ + col + ch * 8; wstride = 64 * NQKV_; }
          else { wsrc = w1 + (size_t)row * F_ + (col - NQKV_) + ch * 8; wstride = 64 * F_; }
        }
        glds16(wsrc, smem + (sn % G1_RING) * G1_BUFB + wid * 1024 + lane * 16);
        wsrc += wstride;
      }
      WF wN[4];
      g1_reads<((s + 1) % G1_RING) * G1_BUFB, 0, 4>(wN, ad);
      wait_lgkm<8>();
      g1_mma<4, kc>(wB, xa, acc);
#pragma unroll
      for (int i = 0; i < 4; ++i) wA[i] = wN[i];
    } else {
      wait_lgkm<0>();
      g1_mma<4, kc>(wB, xa, acc);
    }
  };
  stage(IC<0>{}); stage(IC<1>{}); stage(IC<2>{}); stage(IC<3>{}); stage(IC<4>{});
  stage(IC<5>{}); stage(IC<6>{}); stage(IC<7>{}); stage(IC<8>{});
  const float* bias_lds = reinterpret_cast<const float*>(smem + G1_BOFF);
#pragma unroll
  for (int n = 0; n < 4; ++n) {
    const int tile = (unit0 + U) * 4 + n;
#pragma unroll
    for (int r = 0; r < 2; ++r)
      g1_store_tile(acc[r][n], tile, m0 + wid * 32 + r * 16 + (lane & 15), g, bias_lds + (U * 4 + n) * 16 + 4 * g, qkv, hf);
  }
}

__global__ __launch_bounds__(512, 2) void k1_gemm1(const u16* __restrict__ xnf, const u16* __restrict__ wfold,
                                                   const u16* __restrict__ wqkv, const u16* __restrict__ w1,
                                                   const u16* __restrict__ b1, u16* __restrict__ qkv,
                                                   u16* __restrict__ hf) {
  __shared__ __attribute__((aligned(16))) char smem[G1_SMEM];
  float* bias_lds = reinterpret_cast<float*>(smem + G1_BOFF);
  const int tid = threadIdx.x, wid = tid >> 6, lane = tid & 63;
  const int bid = blockIdx.x;
  const int mt = bid >> 5, grp = bid & 31;
  const int m0 = mt * 256;
  const int unit0 = grp * 3;
  const bool extra = grp < 12;
  STAMP(1, 0);
  const int xcol = 6144 + grp * 16;
  const u16* wsrc;
  int wstride;
  {
    const int col = unit0 * 64;
    const int row = wid * 8 + (lane >> 3);
    const int f = ((row >> 1) & 1) | (((row >> 3) & 1) << 1);
    const int ch = (lane & 7) ^ (f << 1);
    if (col < NPH_) { wsrc = wfold + (size_t)row * NPH_ + col + ch * 8; wstride = 64 * NPH_; }
    else if (col < NQKV_) { wsrc = wqkv + (size_t)row * NQKV_ + col + ch * 8; wstride = 64 * NQKV_; }
    else { wsrc = w1 + (size_t)row * F_ + (col - NQKV_) + ch * 8; wstride = 64 * F_; }
  }
#pragma unroll
  for (int s = 0; s < 7; ++s) {
    glds16(wsrc, smem + s * G1_BUFB + wid * 1024 + lane * 16);
    wsrc += wstride;
  }
  if (extra) {
#pragma unroll
    for (int i = 0; i < 3; ++i) {
      int ins = wid + 8 * i;
      if (ins >= 18) ins -= 18;
      const int c = ins * 64 + lane;
      const int pr = c >> 1, half = c & 1;
      const int k = (pr & ~0xC) | ((pr & 4) << 1) | ((pr & 8) >> 1);
      glds16(w1 + (size_t)k * F_ + (xcol - NQKV_) + half * 8, smem + G1_XOFF + c * 16);
    }
  }
  if (tid < 208) {
    const int col = (tid < 192) ? (unit0 * 64 + tid) : (xcol + tid - 192);
    bias_lds[tid] = (col >= NQKV_) ? bf2f(b1[col - NQKV_]) : 0.f;
  }
  bf16x8 xa[2][18];
#pragma unroll
  for (int r = 0; r < 2; ++r) {
    const u16* xp = xnf + ((size_t)((m0 >> 4) + wid * 2 + r) * 18 * 64 + lane) * 8;
#pragma unroll
    for (int ks = 0; ks < 18; ++ks) xa[r][ks] = *reinterpret_cast<const bf16x8*>(xp + ks * 512);
  }
  wait_vm<0>();
  WAIT_LGKM0();
  BAR();
  WF wA[4];
  {
    unsigned ad[4][2];
    g1_addr(ad, smem, lane);
    g1_reads<0, 0, 4>(wA, ad);
  }
  STAMP(1, 1);
  g1_unit<0>(wsrc, wstride, unit0, wfold, wqkv, w1, smem, xa, m0, qkv, hf, wA);
  STAMP(1, 2);
  g1_unit<1>(wsrc, wstride, unit0, wfold, wqkv, w1, smem, xa, m0, qkv, hf, wA);
  STAMP(1, 3);
  g1_unit<2>(wsrc, wstride, unit0, wfold, wqkv, w1, smem, xa, m0, qkv, hf, wA);
  STAMP(1, 4);
  if (extra) {
    int tz = threadIdx.x;
    asm volatile("" : "+v"(tz));
    const int wid2 = __builtin_amdgcn_readfirstlane(tz >> 6), lane2 = tz & 63;
    const int g = lane2 >> 4, q = (lane2 & 15) >> 2, p = lane2 & 3;
    unsigned ad[2];
#pragma unroll
    for (int t = 0; t < 2; ++t) {
      const int k = 8 * g + 4 * t + q;
      const int pr = (k & ~0xC) | ((k & 4) << 1) | ((k & 8) >> 1);
      ad[t] = lds_addr(smem) + G1_XOFF + pr * 32 + p * 8;
    }
    f32x4 acc[2] = {(f32x4){0.f, 0.f, 0.f, 0.f}, (f32x4){0.f, 0.f, 0.f, 0.f}};
#pragma unroll
    for (int kb = 0; kb < 18; kb += 6) {
      WF w[6];
      tr_rd<0 * 1024>(w[0].lo, ad[0] + kb * 1024); tr_rd<0 * 1024>(w[0].hi, ad[1] + kb * 1024);
      tr_rd<1 * 1024>(w[1].lo, ad[0] + kb * 1024); tr_rd<1 * 1024>(w[1].hi, ad[1] + kb * 1024);
      tr_rd<2 * 1024>(w[2].lo, ad[0] + kb * 1024); tr_rd<2 * 1024>(w[2].hi, ad[1] + kb * 1024);
      tr_rd<3 * 1024>(w[3].lo, ad[0] + kb * 1024); tr_rd<3 * 1024>(w[3].hi, ad[1] + kb * 1024);
      tr_rd<4 * 1024>(w[4].lo, ad[0] + kb * 1024); tr_rd<4 * 1024>(w[4].hi, ad[1] + kb * 1024);
      tr_rd<5 * 1024>(w[5].lo, ad[0] + kb * 1024); tr_rd<5 * 1024>(w[5].hi, ad[1] + kb * 1024);
      wait_lgkm<0>();
#pragma unroll
      for (int i = 0; i < 6; ++i) pin(w[i]);
#pragma unroll
      for (int i = 0; i < 6; ++i)
#pragma unroll
        for (int r = 0; r < 2; ++r)
          acc[r] = __builtin_amdgcn_mfma_f32_16x16x32_bf16(cat(w[i]), xa[r][kb + i], acc[r], 0, 0, 0);
    }
#pragma unroll
    for (int r = 0; r < 2; ++r)
      g1_store_tile(acc[r], xcol / 16, m0 + wid2 * 32 + r * 16 + (lane2 & 15), g, bias_lds + 192 + 4 * g, qkv, hf);
  }
}

constexpr int G2_NT = 9, G2_RB = G2_NT * 32;
constexpr int G2_BUFB = 64 * G2_RB;
constexpr int G2_RING = 4;
constexpr int G2_STAGES = 18;

__device__ __forceinline__ void softmax_pm(const float (&pk)[18], float (&ep)[18], float (&en)[18]) {
  float mx = pk[0], mn = pk[0];
#pragma unroll
  for (int f = 1; f < 18; ++f) {
    mx = fmaxf(mx, pk[f]);
    mn = fminf(mn, pk[f]);
  }
  float sp = 0.f, sn = 0.f;
#pragma unroll
  for (int f = 0; f < 18; ++f) {
    ep[f] = __expf(pk[f] - mx);
    en[f] = __expf(mn - pk[f]);
    sp += ep[f];
    sn += en[f];
  }
  const float rp = 1.f / sp, rn = 1.f / sn;
#pragma unroll
  for (int f = 0; f < 18; ++f) {
    ep[f] *= rp;
    en[f] *= rn;
  }
}

__device__ void head_block(int hb, const u16* __restrict__ qkv, const u16* __restrict__ wo,
                           u16* __restrict__ mcat, u16* __restrict__ phif, char* smem) {
  u16* phiT = reinterpret_cast<u16*>(smem);
  u16* vT = reinterpret_cast<u16*>(smem + 25344);
  float* kvs = reinterpret_cast<float*>(smem + 25344 + 16896);
  float* zs = reinterpret_cast<float*>(smem + 66816);
  const int h = hb >> 1, half = hb & 1;
  const int tid = threadIdx.x, wid = tid >> 6, lane = tid & 63;
  unsigned kr[8][9], vr[8][9], qr[4][9];
#pragma unroll
  for (int ch = 0; ch < 8; ++ch) {
    const int row = ch * 256 + tid;
    const unsigned* kp = reinterpret_cast<const unsigned*>(qkv + ((size_t)(32 + h) * S_ + row) * 18);
    const unsigned* vp = reinterpret_cast<const unsigned*>(qkv + ((size_t)(64 + h) * S_ + row) * 18);
#pragma unroll
    for (int d = 0; d < 9; ++d) { kr[ch][d] = kp[d]; vr[ch][d] = (half == 0) ? vp[d] : 0u; }
  }
#pragma unroll
  for (int ch = 0; ch < 4; ++ch) {
    const int row = half * 1024 + ch * 256 + tid;
    const unsigned* qp = reinterpret_cast<const unsigned*>(qkv + ((size_t)h * S_ + row) * 18);
#pragma unroll
    for (int d = 0; d < 9; ++d) qr[ch][d] = qp[d];
  }
  for (int i = tid; i < 12 * 264; i += 256) phiT[36 * 264 + i] = 0;
  for (int i = tid; i < 14 * 264; i += 256) vT[18 * 264 + i] = (i < 264) ? (u16)0x3f80 : (u16)0;
  f32x4 acc[3][2];
#pragma unroll
  for (int a = 0; a < 3; ++a)
#pragma unroll
    for (int b = 0; b < 2; ++b) acc[a][b] = (f32x4){0.f, 0.f, 0.f, 0.f};
#pragma unroll
  for (int ch = 0; ch < 8; ++ch) {
    float pk[18];
#pragma unroll
    for (int d = 0; d < 9; ++d) {
      pk[2 * d] = lo16(kr[ch][d]);
      pk[2 * d + 1] = hi16(kr[ch][d]);
    }
    float ep[18], en[18];
    softmax_pm(pk, ep, en);
    __syncthreads();
#pragma unroll
    for (int f = 0; f < 18; ++f) {
      phiT[f * 264 + tid] = f2bf(ep[f]);
      phiT[(18 + f) * 264 + tid] = f2bf(en[f]);
      vT[f * 264 + tid] = (u16)((f & 1) ? (vr[ch][f >> 1] >> 16) : (vr[ch][f >> 1] & 0xffff));
    }
    __syncthreads();
#pragma unroll
    for (int ks = 0; ks < 2; ++ks) {
      const int s0 = wid * 64 + ks * 32 + 8 * (lane >> 4);
      bf16x8 bv[2];
#pragma unroll
      for (int b = 0; b < 2; ++b) bv[b] = *reinterpret_cast<const bf16x8*>(vT + (b * 16 + (lane & 15)) * 264 + s0);
#pragma unroll
      for (int a = 0; a < 3; ++a) {
        bf16x8 av = *reinterpret_cast<const bf16x8*>(phiT + (a * 16 + (lane & 15)) * 264 + s0);
#pragma unroll
        for (int b = 0; b < 2; ++b) acc[a][b] = __builtin_amdgcn_mfma_f32_16x16x32_bf16(av, bv[b], acc[a][b], 0, 0, 0);
      }
    }
  }
  __syncthreads();
#pragma unroll
  for (int a = 0; a < 3; ++a)
#pragma unroll
    for (int b = 0; b < 2; ++b)
#pragma unroll
      for (int j = 0; j < 4; ++j)
        kvs[(wid * 48 + a * 16 + (lane >> 4) * 4 + j) * 32 + b * 16 + (lane & 15)] = acc[a][b][j];
  u16* woimg = reinterpret_cast<u16*>(smem);
  u16* kvb = reinterpret_cast<u16*>(smem + 36864);
  if (half == 0) {
    for (int i = tid; i < 32 * 72; i += 256) {
      const int r = i / 72, c = i % 72;
      u32x4 v = {0u, 0u, 0u, 0u};
      if (r < 18) v = *reinterpret_cast<const u32x4*>(wo + (size_t)(h * 18 + r) * D_ + c * 8);
      *reinterpret_cast<u32x4*>(woimg + r * 576 + c * 8) = v;
    }
  }
  __syncthreads();
  for (int i = tid; i < 48 * 32; i += 256) {
    float v = kvs[i] + kvs[48 * 32 + i] + kvs[2 * 48 * 32 + i] + kvs[3 * 48 * 32 + i];
    const int f = i >> 5, d = i & 31;
    if (d == 18 && f < 36) zs[f] = rbf(v);
    kvb[i] = (d < 18 && f < 36) ? f2bf(v) : (u16)0;
  }
  __syncthreads();
  if (half == 0) {
    const int g = lane >> 4, q = (lane & 15) >> 2, p = lane & 3;
    constexpr int RB = 576 * 2;
    const int o0 = (8 * g + q) * RB + p * 8, o1 = (8 * g + 4 + q) * RB + p * 8;
    bf16x8 kb[3];
#pragma unroll
    for (int a = 0; a < 3; ++a) kb[a] = *reinterpret_cast<const bf16x8*>(kvb + (a * 16 + (lane & 15)) * 32 + 8 * g);
    for (int nt = wid; nt < 36; nt += 4) {
      bf16x8 w = wfrag(reinterpret_cast<const char*>(woimg), o0, o1, nt * 32);
#pragma unroll
      for (int a = 0; a < 3; ++a) {
        f32x4 c = __builtin_amdgcn_mfma_f32_16x16x32_bf16(w, kb[a], (f32x4){0.f, 0.f, 0.f, 0.f}, 0, 0, 0);
        const int f = a * 16 + (lane & 15);
        if (f < KPH_) {
          u32x2 v = {pack2(c[0], c[1]), pack2(c[2], c[3])};
          *reinterpret_cast<u32x2*>(mcat + (size_t)(h * KPH_ + f) * D_ + nt * 16 + 4 * g) = v;
        }
      }
    }
  }
  float zr[36];
#pragma unroll
  for (int f = 0; f < 36; ++f) zr[f] = zs[f];
#pragma unroll
  for (int ch = 0; ch < 4; ++ch) {
    const int row = half * 1024 + ch * 256 + tid;
    float pk[18];
#pragma unroll
    for (int d = 0; d < 9; ++d) {
      pk[2 * d] = lo16(qr[ch][d]);
      pk[2 * d + 1] = hi16(qr[ch][d]);
    }
    float ep[18], en[18];
    softmax_pm(pk, ep, en);
    float den = 0.f;
#pragma unroll
    for (int f = 0; f < 18; ++f) {
      ep[f] = rbf(ep[f]);
      en[f] = rbf(en[f]);
      den += ep[f] * zr[f] + en[f] * zr[18 + f];
    }
    const float rd = 1.f / (rbf(den) + 1e-6f);
    unsigned o[20];
#pragma unroll
    for (int f = 0; f < 9; ++f) o[f] = pack2(ep[2 * f] * rd, ep[2 * f + 1] * rd);
#pragma unroll
    for (int f = 0; f < 9; ++f) o[9 + f] = pack2(en[2 * f] * rd, en[2 * f + 1] * rd);
    o[18] = 0;
    o[19] = 0;
#pragma unroll
    for (int c = 0; c < 5; ++c) {
      const int cidx = h * 5 + c;
      u16* dst = phif + ((size_t)((row >> 4) * 40 + (cidx >> 2)) * 64 + (cidx & 3) * 16 + (row & 15)) * 8;
      u32x4 v = {o[4 * c], o[4 * c + 1], o[4 * c + 2], o[4 * c + 3]};
      *reinterpret_cast<u32x4*>(dst) = v;
    }
  }
}

template <int BASE, int N0, int CNT, int I = 0>
__device__ __forceinline__ void g2_wreads(WF (&w)[5], unsigned a0, unsigned a1) {
  if constexpr (I < CNT) {
    tr_rd<BASE + (N0 + I) * 32>(w[I].lo, a0);
    tr_rd<BASE + (N0 + I) * 32>(w[I].hi, a1);
    g2_wreads<BASE, N0, CNT, I + 1>(w, a0, a1);
  }
}
template <int N0, int CNT>
__device__ __forceinline__ void g2_mma(WF (&w)[5], const bf16x8 (&ac)[2], f32x4 (&acc)[2][G2_NT]) {
#pragma unroll
  for (int i = 0; i < CNT; ++i) pin(w[i]);
#pragma unroll
  for (int i = 0; i < CNT; ++i)
#pragma unroll
    for (int r = 0; r < 2; ++r)
      acc[r][N0 + i] = __builtin_amdgcn_mfma_f32_16x16x32_bf16(cat(w[i]), ac[r], acc[r][N0 + i], 0, 0, 0);
}
constexpr int K2_HEADBLK = 64;

__global__ __launch_bounds__(256, 2) void k2_gemm2(const u16* __restrict__ hf, const u16* __restrict__ w2,
                                                   float* __restrict__ part, const u16* __restrict__ qkv,
                                                   const u16* __restrict__ wo, u16* __restrict__ phif,
                                                   u16* __restrict__ mcat) {
  __shared__ __attribute__((aligned(16))) char smem[G2_RING * G2_BUFB];
  const int tid = threadIdx.x, wid = tid >> 6, lane = tid & 63;
  int bid = blockIdx.x;
  if (bid < K2_HEADBLK) { head_block(bid, qkv, wo, mcat, phif, smem); return; }
  bid -= K2_HEADBLK;
  const int xcd = bid & 7, loc = bid >> 3;
  const int sp = xcd >> 1, mt = (xcd & 1) * 8 + (loc >> 2), nt4 = loc & 3;
  const int m0 = mt * 128, n0 = nt4 * 144, k0 = sp * 1152;
  const int g = lane >> 4, q = (lane & 15) >> 2, p = lane & 3;
  const bool odd = g & 1;
  const unsigned wa0 = lds_addr(smem) + (8 * g + 4 * (g & 1) + q) * G2_RB + p * 8;
  const unsigned wa1 = lds_addr(smem) + (8 * g + 4 * (1 - (g & 1)) + q) * G2_RB + p * 8;
  f32x4 acc[2][G2_NT];
#pragma unroll
  for (int r = 0; r < 2; ++r)
#pragma unroll
    for (int n = 0; n < G2_NT; ++n) acc[r][n] = (f32x4){0.f, 0.f, 0.f, 0.f};
  const u16* wp[5];
  int wdst[5];
#pragma unroll
  for (int i = 0; i < 5; ++i) {
    int ins = wid + 4 * i;
    if (ins >= 18) ins -= 18;
    int c = ins * 64 + lane;
    int row = c / 18, cc = c % 18;
    wp[i] = w2 + (size_t)(k0 + row) * D_ + n0 + cc * 8;
    wdst[i] = c * 16;
  }
  const u16* ap[2];
#pragma unroll
  for (int r = 0; r < 2; ++r) ap[r] = hf + ((size_t)((m0 >> 4) + wid * 2 + r) * 144 + (k0 >> 5)) * 512 + lane * 8;
  bf16x8 an[4][2][2];
  auto issue = [&](auto SL) {
    constexpr int slot = decltype(SL)::value;
    char* buf = smem + slot * G2_BUFB;
#pragma unroll
    for (int i = 0; i < 5; ++i) { glds16(wp[i], buf + wdst[i]); wp[i] += 64 * D_; }
#pragma unroll
    for (int r = 0; r < 2; ++r) {
      gld16_asm(an[slot][r][0], ap[r]);
      gld16_asm(an[slot][r][1], ap[r] + 512);
      ap[r] += 1024;
    }
  };
  issue(IC<0>{});
  issue(IC<1>{});
  issue(IC<2>{});
  wait_vm<18>();
  BAR();
  WF wA[5], wB[5];
  g2_wreads<0, 0, 5>(wA, wa0, wa1);
  auto stage = [&](auto U, auto LAST) {
    constexpr int u = decltype(U)::value;
    constexpr int rem = decltype(LAST)::value;
    constexpr int BASE = u * G2_BUFB;
    bf16x8 c0[2], c1[2];
#pragma unroll
    for (int r = 0; r < 2; ++r) {
      pin(an[u][r][0]); pin(an[u][r][1]);
      c0[r] = swap_halves_if(an[u][r][0], odd);
      c1[r] = swap_halves_if(an[u][r][1], odd);
    }
    g2_wreads<BASE, 5, 4>(wB, wa0, wa1);
    wait_lgkm<8>();
    g2_mma<0, 5>(wA, c0, acc);
    g2_wreads<BASE + 32 * G2_RB, 0, 5>(wA, wa0, wa1);
    wait_lgkm<10>();
    g2_mma<5, 4>(wB, c0, acc);
    g2_wreads<BASE + 32 * G2_RB, 5, 4>(wB, wa0, wa1);
    wait_lgkm<8>();
    g2_mma<0, 5>(wA, c1, acc);
    if constexpr (rem >= 1) {
      if constexpr (rem >= 2) wait_vm<9>(); else wait_vm<0>();
      BAR();
      if constexpr (rem >= 3) issue(IC<(u + 3) & 3>{});
      g2_wreads<((u + 1) & 3) * G2_BUFB, 0, 5>(wA, wa0, wa1);
      wait_lgkm<10>();
    } else {
      wait_lgkm<0>();
    }
    g2_mma<5, 4>(wB, c1, acc);
  };
#pragma unroll 1
  for (int it = 0; it < 3; ++it) {
    stage(IC<0>{}, IC<3>{}); stage(IC<1>{}, IC<3>{}); stage(IC<2>{}, IC<3>{}); stage(IC<3>{}, IC<3>{});
  }
  stage(IC<0>{}, IC<3>{}); stage(IC<1>{}, IC<3>{}); stage(IC<2>{}, IC<3>{});
  stage(IC<3>{}, IC<2>{}); stage(IC<0>{}, IC<1>{}); stage(IC<1>{}, IC<0>{});
#pragma unroll
  for (int r = 0; r < 2; ++r) {
    const int m = m0 + wid * 32 + r * 16 + (lane & 15);
    float* pp = part + ((size_t)sp * S_ + m) * D_ + n0 + 4 * g;
#pragma unroll
    for (int n = 0; n < G2_NT; ++n) *reinterpret_cast<f32x4*>(pp + n * 16) = acc[r][n];
  }
}

constexpr int G3_BUFB = 128 * 128;
constexpr int G3_RING = 3;

__global__ __launch_bounds__(256, 2) void k3_final(const u16* __restrict__ phif, const u16* __restrict__ mcat,
                                                   const float* __restrict__ part, const u16* __restrict__ b2,
                                                   const u16* __restrict__ x, u16* __restrict__ out) {
  __shared__ __attribute__((aligned(16))) char smem[G3_RING * G3_BUFB];
  const int tid = threadIdx.x, wid = tid >> 6, lane = tid & 63;
  const int bid = blockIdx.x;
  const int xcd = bid & 7, loc = bid >> 3;
  const int mt = xcd * 4 + loc / 9, nt = loc % 9;
  const int m0 = mt * 64, n0 = nt * 64;
  const int g = lane >> 4, q = (lane & 15) >> 2, p = lane & 3;
  const int wr = wid >> 1, wc = wid & 1;
  STAMP(3, 0);
  const u16* wp[4];
#pragma unroll
  for (int i = 0; i < 4; ++i) {
    const int row = (wid * 4 + i) * 8 + (lane >> 3);
    const int f = ((row >> 1) & 1) | (((row >> 3) & 1) << 1);
    const int ch = (lane & 7) ^ (f << 1);
    wp[i] = mcat + (size_t)row * D_ + n0 + ch * 8;
  }
  const u16* ap[2];
#pragma unroll
  for (int r = 0; r < 2; ++r) ap[r] = phif + ((size_t)((m0 >> 4) + wr * 2 + r) * 40) * 512 + lane * 8;
  bf16x8 an[3][2][4];
  auto issue = [&](auto SL) {
    constexpr int slot = decltype(SL)::value;
    char* buf = smem + slot * G3_BUFB;
#pragma unroll
    for (int i = 0; i < 4; ++i) { glds16(wp[i], buf + (wid * 4 + i) * 1024 + lane * 16); wp[i] += 128 * D_; }
#pragma unroll
    for (int r = 0; r < 2; ++r) {
#pragma unroll
      for (int ks = 0; ks < 4; ++ks) gld16_asm(an[slot][r][ks], ap[r] + ks * 512);
      ap[r] += 2048;
    }
  };
  issue(IC<0>{});
  issue(IC<1>{});
  bf16x8 py[2][2][4];
  u32x2 xx[2][2], bb[2];
#pragma unroll
  for (int r = 0; r < 2; ++r) {
    const int m = m0 + wr * 32 + r * 16 + (lane & 15);
#pragma unroll
    for (int n = 0; n < 2; ++n) {
      const int ncol = n0 + wc * 32 + n * 16 + 4 * g;
#pragma unroll
      for (int sI = 0; sI < SPLITK_; ++sI) gld16_asm(py[r][n][sI], part + ((size_t)sI * S_ + m) * D_ + ncol);
      asm volatile("global_load_dwordx2 %0, %1, off" : "=v"(xx[r][n]) : "v"(x + (size_t)m * D_ + ncol) : "memory");
    }
  }
#pragma unroll
  for (int n = 0; n < 2; ++n)
    asm volatile("global_load_dwordx2 %0, %1, off" : "=v"(bb[n]) : "v"(b2 + n0 + wc * 32 + n * 16 + 4 * g) : "memory");
  unsigned ad[2][2];
#pragma unroll
  for (int t = 0; t < 2; ++t) {
    const int row = 8 * g + 4 * t + q;
    const int f = ((row >> 1) & 1) | (((row >> 3) & 1) << 1);
#pragma unroll
    for (int n = 0; n < 2; ++n) ad[n][t] = lds_addr(smem) + row * 128 + (((wc * 2 + n) ^ f) * 32) + p * 8;
  }
  f32x4 acc[2][2];
#pragma unroll
  for (int r = 0; r < 2; ++r)
#pragma unroll
    for (int n = 0; n < 2; ++n) acc[r][n] = (f32x4){0.f, 0.f, 0.f, 0.f};
  STAMP(3, 1);
  auto stage = [&](auto U, auto WAITN, auto MORE) {
    constexpr int u = decltype(U)::value;
    wait_vm<decltype(WAITN)::value>();
    BAR();
    if constexpr (decltype(MORE)::value) issue(IC<(u + 2) % 3>{});
    constexpr int BASE = u * G3_BUFB;
    WF w[4][2];
#pragma unroll
    for (int n = 0; n < 2; ++n) { tr_rd<BASE + 0 * 4096>(w[0][n].lo, ad[n][0]); tr_rd<BASE + 0 * 4096>(w[0][n].hi, ad[n][1]); }
#pragma unroll
    for (int n = 0; n < 2; ++n) { tr_rd<BASE + 1 * 4096>(w[1][n].lo, ad[n][0]); tr_rd<BASE + 1 * 4096>(w[1][n].hi, ad[n][1]); }
#pragma unroll
    for (int n = 0; n < 2; ++n) { tr_rd<BASE + 2 * 4096>(w[2][n].lo, ad[n][0]); tr_rd<BASE + 2 * 4096>(w[2][n].hi, ad[n][1]); }
#pragma unroll
    for (int n = 0; n < 2; ++n) { tr_rd<BASE + 3 * 4096>(w[3][n].lo, ad[n][0]); tr_rd<BASE + 3 * 4096>(w[3][n].hi, ad[n][1]); }
#pragma unroll
    for (int r = 0; r < 2; ++r)
#pragma unroll
      for (int ks = 0; ks < 4; ++ks) pin(an[u][r][ks]);
    wait_lgkm<8>();
#pragma unroll
    for (int ks = 0; ks < 2; ++ks)
#pragma unroll
      for (int n = 0; n < 2; ++n) pin(w[ks][n]);
#pragma unroll
    for (int ks = 0; ks < 2; ++ks)
#pragma unroll
      for (int n = 0; n < 2; ++n)
#pragma unroll
        for (int r = 0; r < 2; ++r)
          acc[r][n] = __builtin_amdgcn_mfma_f32_16x16x32_bf16(cat(w[ks][n]), an[u][r][ks], acc[r][n], 0, 0, 0);
    wait_lgkm<0>();
#pragma unroll
    for (int ks = 2; ks < 4; ++ks)
#pragma unroll
      for (int n = 0; n < 2; ++n) pin(w[ks][n]);
#pragma unroll
    for (int ks = 2; ks < 4; ++ks)
#pragma unroll
      for (int n = 0; n < 2; ++n)
#pragma unroll
        for (int r = 0; r < 2; ++r)
          acc[r][n] = __builtin_amdgcn_mfma_f32_16x16x32_bf16(cat(w[ks][n]), an[u][r][ks], acc[r][n], 0, 0, 0);
  };
  stage(IC<0>{}, IC<34>{}, IC<1>{});
  stage(IC<1>{}, IC<34>{}, IC<1>{});
  stage(IC<2>{}, IC<12>{}, IC<1>{});
  stage(IC<0>{}, IC<12>{}, IC<1>{});
  stage(IC<1>{}, IC<12>{}, IC<1>{});
  stage(IC<2>{}, IC<12>{}, IC<1>{});
  stage(IC<0>{}, IC<12>{}, IC<1>{});
  stage(IC<1>{}, IC<12>{}, IC<1>{});
  stage(IC<2>{}, IC<12>{}, IC<0>{});
  stage(IC<0>{}, IC<0>{}, IC<0>{});
  STAMP(3, 3);
#pragma unroll
  for (int n = 0; n < 2; ++n) asm volatile("" : "+v"(bb[n]));
#pragma unroll
  for (int r = 0; r < 2; ++r) {
    const int m = m0 + wr * 32 + r * 16 + (lane & 15);
#pragma unroll
    for (int n = 0; n < 2; ++n) {
      const int ncol = n0 + wc * 32 + n * 16 + 4 * g;
      asm volatile("" : "+v"(xx[r][n]));
#pragma unroll
      for (int sI = 0; sI < SPLITK_; ++sI) pin(py[r][n][sI]);
      f32x4 y = *reinterpret_cast<f32x4*>(&py[r][n][0]);
#pragma unroll
      for (int sI = 1; sI < SPLITK_; ++sI) y += *reinterpret_cast<f32x4*>(&py[r][n][sI]);
      float bv[4] = {lo16(bb[n][0]), hi16(bb[n][0]), lo16(bb[n][1]), hi16(bb[n][1])};
      float xv[4] = {lo16(xx[r][n][0]), hi16(xx[r][n][0]), lo16(xx[r][n][1]), hi16(xx[r][n][1])};
      float o[4];
#pragma unroll
      for (int j = 0; j < 4; ++j) {
        float yy = y[j] + bv[j];
        float gg = yy / (1.f + __expf(-yy));
        o[j] = xv[j] + acc[r][n][j] * gg;
      }
      u32x2 v = {pack2(o[0], o[1]), pack2(o[2], o[3])};
      *reinterpret_cast<u32x2*>(out + (size_t)m * D_ + ncol) = v;
    }
  }
  STAMP(3, 4);
}

extern "C" void kernel_launch(void* const* d_in, const int* in_sizes, int n_in, void* d_out, int out_size, void* d_ws,
                              size_t ws_size, hipStream_t stream) {
  const u16* x = (const u16*)d_in[0];
  const u16* wqkv = (const u16*)d_in[1];
  const u16* wphi = (const u16*)d_in[2];
  const u16* wo = (const u16*)d_in[3];
  const u16* w1 = (const u16*)d_in[5];
  const u16* b1 = (const u16*)d_in[6];
  const u16* w2 = (const u16*)d_in[7];
  const u16* b2 = (const u16*)d_in[8];
  u16* out = (u16*)d_out;
  char* ws = (char*)d_ws;
  size_t off = 0;
  u16* xnf = (u16*)(ws + off); off += (size_t)S_ * D_ * 2;
  u16* wfold = (u16*)(ws + off); off += (size_t)D_ * NPH_ * 2;
  u16* qkv = (u16*)(ws + off); off += (size_t)S_ * NQKV_ * 2;
  u16* hf = (u16*)(ws + off); off += (size_t)S_ * F_ * 2;
  float* part = (float*)(ws + off); off += (size_t)SPLITK_ * S_ * D_ * 4;
  u16* phif = (u16*)(ws + off); off += (size_t)S_ * KPHI_ * 2;
  u16* mcat = (u16*)(ws + off); off += (size_t)KPHI_ * D_ * 2;

  k0_prep<<<512 + 192, 256, 0, stream>>>(x, wqkv, wphi, xnf, wfold);
  k1_gemm1<<<256, 512, 0, stream>>>(xnf, wfold, wqkv, w1, b1, qkv, hf);
  k2_gemm2<<<K2_HEADBLK + 256, 256, 0, stream>>>(hf, w2, part, qkv, wo, phif, mcat);
  k3_final<<<32 * 9, 256, 0, stream>>>(phif, mcat, part, b2, x, out);
}
```

```cpp
#include <hip/hip_runtime.h>
#include <hip/hip_bf16.h>
#include <stdint.h>

constexpr int S_ = 2048, D_ = 576, H_ = 32, DH_ = 18, F_ = 4608, NQKV_ = 1728;
constexpr int NPH_ = 1152;
constexpr int KPH_ = 40;
constexpr int KPHI_ = H_ * KPH_;
constexpr int SPLITK_ = 4;

typedef short bf16x8 __attribute__((ext_vector_type(8)));
typedef short s16x4 __attribute__((ext_vector_type(4)));
typedef float f32x4 __attribute__((ext_vector_type(4)));
typedef unsigned u32x4 __attribute__((ext_vector_type(4)));
typedef unsigned u32x2 __attribute__((ext_vector_type(2)));
typedef __attribute__((address_space(3))) s16x4* lds_s16x4_p;
typedef unsigned short u16;
template <int V> struct IC { static constexpr int value = V; };

#define STAMP(kid, i) do { } while (0)
#define WAIT_LGKM0() asm volatile("s_waitcnt lgkmcnt(0)" ::: "memory")
#define BAR() __builtin_amdgcn_s_barrier()

template <int N>
__device__ __forceinline__ void wait_vm() {
  static_assert(N >= 0 && N < 64, "vmcnt range");
  asm volatile("s_waitcnt vmcnt(%0)" ::"n"(N) : "memory");
}

__device__ __forceinline__ float bf2f(u16 v) { return __uint_as_float(((unsigned)v) << 16); }
__device__ __forceinline__ u16 f2bf(float f) {
  __hip_bfloat16 b = __float2bfloat16(f);
  return *reinterpret_cast<u16*>(&b);
}
__device__ __forceinline__ unsigned pack2(float lo, float hi) {
  return (unsigned)f2bf(lo) | ((unsigned)f2bf(hi) << 16);
}
__device__ __forceinline__ float rbf(float f) { return bf2f(f2bf(f)); }
__device__ __forceinline__ float lo16(unsigned u) { return __uint_as_float(u << 16); }
__device__ __forceinline__ float hi16(unsigned u) { return __uint_as_float(u & 0xffff0000u); }

__device__ __forceinline__ void glds16(const void* g, void* l) {
  __builtin_amdgcn_global_load_lds((const unsigned*)g, (unsigned*)l, 16, 0, 0);
}

__device__ __forceinline__ bf16x8 swap_halves_if(bf16x8 v, bool odd) {
  bf16x8 s = __builtin_shufflevector(v, v, 4, 5, 6, 7, 0, 1, 2, 3);
  return odd ? s : v;
}

__device__ __forceinline__ bf16x8 wfrag(const char* img, int o0, int o1, int off) {
  s16x4 a = __builtin_amdgcn_ds_read_tr16_b64_v4i16((lds_s16x4_p)(img + o0 + off));
  s16x4 b = __builtin_amdgcn_ds_read_tr16_b64_v4i16((lds_s16x4_p)(img + o1 + off));
  return __builtin_shufflevector(a, b, 0, 1, 2, 3, 4, 5, 6, 7);
}

struct WF { s16x4 lo, hi; };
template <int OFF>
__device__ __forceinline__ void tr_rd(s16x4& r, unsigned addr) {
  asm volatile("ds_read_b64_tr_b16 %0, %1 offset:%2" : "=v"(r) : "v"(addr), "n"(OFF) : "memory");
}
__device__ __forceinline__ void gld16_asm(bf16x8& r, const void* p) {
  asm volatile("global_load_dwordx4 %0, %1, off" : "=v"(r) : "v"(p) : "memory");
}
template <int N>
__device__ __forceinline__ void wait_lgkm() {
  static_assert(N >= 0 && N <= 15, "lgkmcnt range");
  asm volatile("s_waitcnt lgkmcnt(%0)" ::"n"(N) : "memory");
}
__device__ __forceinline__ void pin(WF& w) { asm volatile("" : "+v"(w.lo), "+v"(w.hi)); }
__device__ __forceinline__ void pin(bf16x8& w) { asm volatile("" : "+v"(w)); }
__device__ __forceinline__ bf16x8 cat(const WF& w) { return __builtin_shufflevector(w.lo, w.hi, 0, 1, 2, 3, 4, 5, 6, 7); }
__device__ __forceinline__ unsigned lds_addr(const void* p) {
  return (unsigned)(uintptr_t)(__attribute__((address_space(3))) const char*)p;
}

__global__ __launch_bounds__(256) void k0_prep(const u16* __restrict__ x, const u16* __restrict__ wqkv,
                                               const u16* __restrict__ wphi, u16* __restrict__ xnf,
                                               u16* __restrict__ wfold, unsigned* __restrict__ cnt) {
  __shared__ __attribute__((aligned(16))) float wph[18 * 20];
  const int tid = threadIdx.x, wid = tid >> 6, lane = tid & 63;
  const int bid = blockIdx.x;
  if (bid == 512 && tid < 32) cnt[tid * 32] = 0u;
  if (bid < 512) {
    const int row = bid * 4 + wid;
    const u16* xp = x + (size_t)row * D_;
    u32x4 a = *reinterpret_cast<const u32x4*>(xp + lane * 8);
    u32x4 b = {0u, 0u, 0u, 0u};
    if (lane < 8) b = *reinterpret_cast<const u32x4*>(xp + 512 + lane * 8);
    float v[16];
#pragma unroll
    for (int j = 0; j < 4; ++j) {
      v[2 * j] = lo16(a[j]); v[2 * j + 1] = hi16(a[j]);
      v[8 + 2 * j] = lo16(b[j]); v[8 + 2 * j + 1] = hi16(b[j]);
    }
    float s = 0.f;
#pragma unroll
    for (int j = 0; j < 16; ++j) s += v[j];
#pragma unroll
    for (int o = 32; o >= 1; o >>= 1) s += __shfl_xor(s, o);
    const float mean = s * (1.f / D_);
    float ss = 0.f;
#pragma unroll
    for (int j = 0; j < 8; ++j) { float d = v[j] - mean; ss += d * d; }
    if (lane < 8) {
#pragma unroll
      for (int j = 8; j < 16; ++j) { float d = v[j] - mean; ss += d * d; }
    }
#pragma unroll
    for (int o = 32; o >= 1; o >>= 1) ss += __shfl_xor(ss, o);
    const float rstd = rsqrtf(ss * (1.f / D_) + 1e-5f);
    u32x4 oa, ob;
#pragma unroll
    for (int j = 0; j < 4; ++j) {
      oa[j] = pack2((v[2 * j] - mean) * rstd, (v[2 * j + 1] - mean) * rstd);
      ob[j] = pack2((v[8 + 2 * j] - mean) * rstd, (v[8 + 2 * j + 1] - mean) * rstd);
    }
    {
      const int c = lane;
      u16* dst = xnf + ((size_t)((row >> 4) * 18 + (c >> 2)) * 64 + (c & 3) * 16 + (row & 15)) * 8;
      *reinterpret_cast<u32x4*>(dst) = oa;
    }
    if (lane < 8) {
      const int c = 64 + lane;
      u16* dst = xnf + ((size_t)((row >> 4) * 18 + (c >> 2)) * 64 + (c & 3) * 16 + (row & 15)) * 8;
      *reinterpret_cast<u32x4*>(dst) = ob;
    }
    return;
  }
  {
    const int fb = bid - 512;
    const int hh = fb / 3, k = (fb % 3) * 192 + tid;
    const int h = hh & 31;
    for (int i = tid; i < 18 * 20; i += 256) {
      const int d = i / 20, f = i % 20;
      wph[i] = (f < 18) ? bf2f(wphi[h * 324 + d * 18 + f]) : 0.f;
    }
    unsigned wv[9];
    if (tid < 192) {
      const u16* wp = wqkv + (size_t)k * NQKV_ + hh * 18;
#pragma unroll
      for (int d = 0; d < 9; ++d) wv[d] = *reinterpret_cast<const unsigned*>(wp + 2 * d);
    }
    __syncthreads();
    if (tid < 192) {
      float acc[20];
#pragma unroll
      for (int f = 0; f < 20; ++f) acc[f] = 0.f;
#pragma unroll
      for (int d = 0; d < 18; ++d) {
        const float w = (d & 1) ? hi16(wv[d >> 1]) : lo16(wv[d >> 1]);
#pragma unroll
        for (int f4 = 0; f4 < 5; ++f4) {
          f32x4 pw = *reinterpret_cast<const f32x4*>(wph + d * 20 + f4 * 4);
#pragma unroll
          for (int j = 0; j < 4; ++j) acc[f4 * 4 + j] += w * pw[j];
        }
      }
      u16* op = wfold + (size_t)k * NPH_ + hh * 18;
#pragma unroll
      for (int f = 0; f < 9; ++f) *reinterpret_cast<unsigned*>(op + 2 * f) = pack2(acc[2 * f], acc[2 * f + 1]);
    }
  }
}

constexpr int G1_BUFB = 64 * 128;
constexpr int G1_RING = 8;
constexpr int G1_XOFF = G1_RING * G1_BUFB;
constexpr int G1_BOFF = G1_XOFF + 576 * 32;
constexpr int G1_SMEM = G1_BOFF + 1024;
constexpr int G1_NST = 8;

template <int BASE, int F0, int CNT, int I = 0>
__device__ __forceinline__ void g1_reads(WF (&w)[4], const unsigned (&ad)[4][2]) {
  if constexpr (I < CNT) {
    constexpr int f = F0 + I, ks = f / 4, n = f % 4;
    tr_rd<BASE + ks * 32 * 128>(w[I].lo, ad[n][0]);
    tr_rd<BASE + ks * 32 * 128>(w[I].hi, ad[n][1]);
    g1_reads<BASE, F0, CNT, I + 1>(w, ad);
  }
}
template <int F0, int KC>
__device__ __forceinline__ void g1_mma(WF (&w)[4], const bf16x8 (&xa)[2][18], f32x4 (&acc)[2][4]) {
#pragma unroll
  for (int i = 0; i < 4; ++i) pin(w[i]);
#pragma unroll
  for (int i = 0; i < 4; ++i) {
    const int f = F0 + i, ks = f / 4, n = f % 4;
#pragma unroll
    for (int r = 0; r < 2; ++r)
      acc[r][n] = __builtin_amdgcn_mfma_f32_16x16x32_bf16(cat(w[i]), xa[r][KC * 2 + ks], acc[r][n], 0, 0, 0);
  }
}

__device__ __forceinline__ float gelu_f(float v) {
  float u = 0.7978845608028654f * (v + 0.044715f * v * v * v);
  float e = __expf(-2.f * u);
  return v * __builtin_amdgcn_rcpf(1.f + e);
}

__device__ __forceinline__ void g1_store_tile(const f32x4& a, int tile, int mrow, int g, const float* bias4,
                                              u16* __restrict__ qkv, u16* __restrict__ hf) {
  if (tile < NQKV_ / 16) {
    const int col = tile * 16 + 4 * g;
#pragma unroll
    for (int j = 0; j < 2; ++j) {
      const int c = col + 2 * j;
      const int hh = c / 18, f = c - hh * 18;
      *reinterpret_cast<unsigned*>(qkv + ((size_t)hh * S_ + mrow) * 24 + f) = pack2(a[2 * j], a[2 * j + 1]);
    }
  } else {
    f32x4 bv = *reinterpret_cast<const f32x4*>(bias4);
    float o[4];
#pragma unroll
    for (int j = 0; j < 4; ++j) o[j] = gelu_f(a[j] + bv[j]);
    u32x2 v = {pack2(o[0], o[1]), pack2(o[2], o[3])};
    const int T = tile - NQKV_ / 16;
    u16* dst = hf + ((size_t)((mrow >> 4) * 144 + (T >> 1)) * 64 + ((T & 1) * 2 + (g >> 1)) * 16 + (mrow & 15)) * 8 + (g & 1) * 4;
    *reinterpret_cast<u32x2*>(dst) = v;
  }
}

__device__ __forceinline__ void g1_addr(unsigned (&ad)[4][2], char* smem, int lane) {
  const int g = lane >> 4, q = (lane & 15) >> 2, p = lane & 3;
#pragma unroll
  for (int t = 0; t < 2; ++t) {
    const int row = 8 * g + 4 * t + q;
    const int f = ((row >> 1) & 1) | (((row >> 3) & 1) << 1);
#pragma unroll
    for (int n = 0; n < 4; ++n) ad[n][t] = lds_addr(smem) + row * 128 + ((n ^ f) * 32) + p * 8;
  }
}

template <int U>
__device__ __forceinline__ void g1_unit(const u16*& wsrc, int& wstride, int unit0, const u16* wfold, const u16* wqkv,
                                        const u16* w1, char* smem, const bf16x8 (&xa)[2][18], int m0,
                                        u16* __restrict__ qkv, u16* __restrict__ hf, WF (&wA)[4]) {
  int tz = threadIdx.x;
  asm volatile("" : "+v"(tz));
  const int wid = __builtin_amdgcn_readfirstlane(tz >> 6), lane = tz & 63;
  const int g = lane >> 4;
  unsigned ad[4][2];
  g1_addr(ad, smem, lane);
  f32x4 acc[2][4];
#pragma unroll
  for (int r = 0; r < 2; ++r)
#pragma unroll
    for (int n = 0; n < 4; ++n) acc[r][n] = (f32x4){0.f, 0.f, 0.f, 0.f};

  auto stage = [&](auto KC) {
    constexpr int kc = decltype(KC)::value;
    constexpr int s = U * 9 + kc;
    constexpr int CUR = (s % G1_RING) * G1_BUFB;
    WF wB[4];
    g1_reads<CUR, 4, 4>(wB, ad);
    wait_lgkm<8>();
    g1_mma<0, kc>(wA, xa, acc);
    if constexpr (s < 26) {
      constexpr int nl = (s + 6 <= 26) ? 5 : (26 - s - 1);
      constexpr int nst = (U > 0 && kc <= 5) ? G1_NST : 0;
      wait_vm<nl + nst>();
      BAR();
      if constexpr (s + 7 <= 26) {
        constexpr int sn = s + 7;
        if constexpr (sn % 9 == 0) {
          const int col = (unit0 + sn / 9) * 64;
          const int row = wid * 8 + (lane >> 3);
          const int f = ((row >> 1) & 1) | (((row >> 3) & 1) << 1);
          const int ch = (lane & 7) ^ (f << 1);
          if (col < NPH_) { wsrc = wfold + (size_t)row * NPH_ + col + ch * 8; wstride = 64 * NPH_; }
          else if (col < NQKV_) { wsrc = wqkv + (size_t)row * NQKV_ + col + ch * 8; wstride = 64 * NQKV_; }
          else { wsrc = w1 + (size_t)row * F_ + (col - NQKV_) + ch * 8; wstride = 64 * F_; }
        }
        glds16(wsrc, smem + (sn % G1_RING) * G1_BUFB + wid * 1024 + lane * 16);
        wsrc += wstride;
      }
      WF wN[4];
      g1_reads<((s + 1) % G1_RING) * G1_BUFB, 0, 4>(wN, ad);
      wait_lgkm<8>();
      g1_mma<4, kc>(wB, xa, acc);
#pragma unroll
      for (int i = 0; i < 4; ++i) wA[i] = wN[i];
    } else {
      wait_lgkm<0>();
      g1_mma<4, kc>(wB, xa, acc);
    }
  };
  stage(IC<0>{}); stage(IC<1>{}); stage(IC<2>{}); stage(IC<3>{}); stage(IC<4>{});
  stage(IC<5>{}); stage(IC<6>{}); stage(IC<7>{}); stage(IC<8>{});
  const float* bias_lds = reinterpret_cast<const float*>(smem + G1_BOFF);
#pragma unroll
  for (int n = 0; n < 4; ++n) {
    const int tile = (unit0 + U) * 4 + n;
#pragma unroll
    for (int r = 0; r < 2; ++r)
      g1_store_tile(acc[r][n], tile, m0 + wid * 32 + r * 16 + (lane & 15), g, bias_lds + (U * 4 + n) * 16 + 4 * g, qkv, hf);
  }
}

__global__ __launch_bounds__(512, 2) void k1_gemm1(const u16* __restrict__ xnf, const u16* __restrict__ wfold,
                                                   const u16* __restrict__ wqkv, const u16* __restrict__ w1,
                                                   const u16* __restrict__ b1, u16* __restrict__ qkv,
                                                   u16* __restrict__ hf) {
  __shared__ __attribute__((aligned(16))) char smem[G1_SMEM];
  float* bias_lds = reinterpret_cast<float*>(smem + G1_BOFF);
  const int tid = threadIdx.x, wid = tid >> 6, lane = tid & 63;
  const int bid = blockIdx.x;
  const int mt = bid >> 5, grp = bid & 31;
  const int m0 = mt * 256;
  const int unit0 = grp * 3;
  const bool extra = grp < 12;
  STAMP(1, 0);
  const int xcol = 6144 + grp * 16;
  const u16* wsrc;
  int wstride;
  {
    const int col = unit0 * 64;
    const int row = wid * 8 + (lane >> 3);
    const int f = ((row >> 1) & 1) | (((row >> 3) & 1) << 1);
    const int ch = (lane & 7) ^ (f << 1);
    if (col < NPH_) { wsrc = wfold + (size_t)row * NPH_ + col + ch * 8; wstride = 64 * NPH_; }
    else if (col < NQKV_) { wsrc = wqkv + (size_t)row * NQKV_ + col + ch * 8; wstride = 64 * NQKV_; }
    else { wsrc = w1 + (size_t)row * F_ + (col - NQKV_) + ch * 8; wstride = 64 * F_; }
  }
#pragma unroll
  for (int s = 0; s < 7; ++s) {
    glds16(wsrc, smem + s * G1_BUFB + wid * 1024 + lane * 16);
    wsrc += wstride;
  }
  if (extra) {
#pragma unroll
    for (int i = 0; i < 3; ++i) {
      int ins = wid + 8 * i;
      if (ins >= 18) ins -= 18;
      const int c = ins * 64 + lane;
      const int pr = c >> 1, half = c & 1;
      const int k = (pr & ~0xC) | ((pr & 4) << 1) | ((pr & 8) >> 1);
      glds16(w1 + (size_t)k * F_ + (xcol - NQKV_) + half * 8, smem + G1_XOFF + c * 16);
    }
  }
  if (tid < 208) {
    const int col = (tid < 192) ? (unit0 * 64 + tid) : (xcol + tid - 192);
    bias_lds[tid] = (col >= NQKV_) ? bf2f(b1[col - NQKV_]) : 0.f;
  }
  bf16x8 xa[2][18];
#pragma unroll
  for (int r = 0; r < 2; ++r) {
    const u16* xp = xnf + ((size_t)((m0 >> 4) + wid * 2 + r) * 18 * 64 + lane) * 8;
#pragma unroll
    for (int ks = 0; ks < 18; ++ks) xa[r][ks] = *reinterpret_cast<const bf16x8*>(xp + ks * 512);
  }
  wait_vm<0>();
  WAIT_LGKM0();
  BAR();
  WF wA[4];
  {
    unsigned ad[4][2];
    g1_addr(ad, smem, lane);
    g1_reads<0, 0, 4>(wA, ad);
  }
  STAMP(1, 1);
  g1_unit<0>(wsrc, wstride, unit0, wfold, wqkv, w1, smem, xa, m0, qkv, hf, wA);
  STAMP(1, 2);
  g1_unit<1>(wsrc, wstride, unit0, wfold, wqkv, w1, smem, xa, m0, qkv, hf, wA);
  STAMP(1, 3);
  g1_unit<2>(wsrc, wstride, unit0, wfold, wqkv, w1, smem, xa, m0, qkv, hf, wA);
  STAMP(1, 4);
  if (extra) {
    int tz = threadIdx.x;
    asm volatile("" : "+v"(tz));
    const int wid2 = __builtin_amdgcn_readfirstlane(tz >> 6), lane2 = tz & 63;
    const int g = lane2 >> 4, q = (lane2 & 15) >> 2, p = lane2 & 3;
    unsigned ad[2];
#pragma unroll
    for (int t = 0; t < 2; ++t) {
      const int k = 8 * g + 4 * t + q;
      const int pr = (k & ~0xC) | ((k & 4) << 1) | ((k & 8) >> 1);
      ad[t] = lds_addr(smem) + G1_XOFF + pr * 32 + p * 8;
    }
    f32x4 acc[2] = {(f32x4){0.f, 0.f, 0.f, 0.f}, (f32x4){0.f, 0.f, 0.f, 0.f}};
#pragma unroll
    for (int kb = 0; kb < 18; kb += 6) {
      WF w[6];
      tr_rd<0 * 1024>(w[0].lo, ad[0] + kb * 1024); tr_rd<0 * 1024>(w[0].hi, ad[1] + kb * 1024);
      tr_rd<1 * 1024>(w[1].lo, ad[0] + kb * 1024); tr_rd<1 * 1024>(w[1].hi, ad[1] + kb * 1024);
      tr_rd<2 * 1024>(w[2].lo, ad[0] + kb * 1024); tr_rd<2 * 1024>(w[2].hi, ad[1] + kb * 1024);
      tr_rd<3 * 1024>(w[3].lo, ad[0] + kb * 1024); tr_rd<3 * 1024>(w[3].hi, ad[1] + kb * 1024);
      tr_rd<4 * 1024>(w[4].lo, ad[0] + kb * 1024); tr_rd<4 * 1024>(w[4].hi, ad[1] + kb * 1024);
      tr_rd<5 * 1024>(w[5].lo, ad[0] + kb * 1024); tr_rd<5 * 1024>(w[5].hi, ad[1] + kb * 1024);
      wait_lgkm<0>();
#pragma unroll
      for (int i = 0; i < 6; ++i) pin(w[i]);
#pragma unroll
      for (int i = 0; i < 6; ++i)
#pragma unroll
        for (int r = 0; r < 2; ++r)
          acc[r] = __builtin_amdgcn_mfma_f32_16x16x32_bf16(cat(w[i]), xa[r][kb + i], acc[r], 0, 0, 0);
    }
#pragma unroll
    for (int r = 0; r < 2; ++r)
      g1_store_tile(acc[r], xcol / 16, m0 + wid2 * 32 + r * 16 + (lane2 & 15), g, bias_lds + 192 + 4 * g, qkv, hf);
  }
}

constexpr int G2_NT = 9, G2_RB = G2_NT * 32;
constexpr int G2_BUFB = 64 * G2_RB;
constexpr int G2_RING = 4;
constexpr int G2_STAGES = 18;

__device__ __forceinline__ void softmax_pm(const float (&pk)[18], float (&ep)[18], float (&en)[18]) {
  float mx = pk[0], mn = pk[0];
#pragma unroll
  for (int f = 1; f < 18; ++f) {
    mx = fmaxf(mx, pk[f]);
    mn = fminf(mn, pk[f]);
  }
  float sp = 0.f, sn = 0.f;
#pragma unroll
  for (int f = 0; f < 18; ++f) {
    ep[f] = __expf(pk[f] - mx);
    en[f] = __expf(mn - pk[f]);
    sp += ep[f];
    sn += en[f];
  }
  const float rp = 1.f / sp, rn = 1.f / sn;
#pragma unroll
  for (int f = 0; f < 18; ++f) {
    ep[f] *= rp;
    en[f] *= rn;
  }
}

__device__ void head_block(int hb, const u16* __restrict__ qkv, const u16* __restrict__ wo,
                           u16* __restrict__ mcat, u16* __restrict__ phif, float* pkv, unsigned* cnt, char* smem) {
  u16* phiT = reinterpret_cast<u16*>(smem);
  u16* vT = reinterpret_cast<u16*>(smem + 25344);
  float* kvs = reinterpret_cast<float*>(smem + 25344 + 16896);
  float* zs = reinterpret_cast<float*>(smem + 66816);
  const int h = hb >> 3, c8 = hb & 7;
  const int tid = threadIdx.x, wid = tid >> 6, lane = tid & 63;
  const int row = c8 * 256 + tid;
  STAMP(2, 0);
  u32x4 kr[3], vr[3], qr[3];
  {
    const u32x4* kp = reinterpret_cast<const u32x4*>(qkv + ((size_t)(32 + h) * S_ + row) * 24);
    const u32x4* vp = reinterpret_cast<const u32x4*>(qkv + ((size_t)(64 + h) * S_ + row) * 24);
    const u32x4* qp = reinterpret_cast<const u32x4*>(qkv + ((size_t)h * S_ + row) * 24);
#pragma unroll
    for (int d = 0; d < 3; ++d) { kr[d] = kp[d]; vr[d] = vp[d]; qr[d] = qp[d]; }
  }
  for (int i = tid; i < 12 * 264; i += 256) phiT[36 * 264 + i] = 0;
  for (int i = tid; i < 14 * 264; i += 256) vT[18 * 264 + i] = (i < 264) ? (u16)0x3f80 : (u16)0;
  {
    float pk[18];
#pragma unroll
    for (int d = 0; d < 9; ++d) {
      pk[2 * d] = lo16(kr[d >> 2][d & 3]);
      pk[2 * d + 1] = hi16(kr[d >> 2][d & 3]);
    }
    float ep[18], en[18];
    softmax_pm(pk, ep, en);
#pragma unroll
    for (int f = 0; f < 18; ++f) {
      phiT[f * 264 + tid] = f2bf(ep[f]);
      phiT[(18 + f) * 264 + tid] = f2bf(en[f]);
      const unsigned w = vr[(f >> 1) >> 2][(f >> 1) & 3];
      vT[f * 264 + tid] = (u16)((f & 1) ? (w >> 16) : (w & 0xffff));
    }
  }
  __syncthreads();
  {
    f32x4 acc[3][2];
#pragma unroll
    for (int a = 0; a < 3; ++a)
#pragma unroll
      for (int b = 0; b < 2; ++b) acc[a][b] = (f32x4){0.f, 0.f, 0.f, 0.f};
#pragma unroll
    for (int ks = 0; ks < 2; ++ks) {
      const int s0 = wid * 64 + ks * 32 + 8 * (lane >> 4);
      bf16x8 bv[2];
#pragma unroll
      for (int b = 0; b < 2; ++b) bv[b] = *reinterpret_cast<const bf16x8*>(vT + (b * 16 + (lane & 15)) * 264 + s0);
#pragma unroll
      for (int a = 0; a < 3; ++a) {
        bf16x8 av = *reinterpret_cast<const bf16x8*>(phiT + (a * 16 + (lane & 15)) * 264 + s0);
#pragma unroll
        for (int b = 0; b < 2; ++b) acc[a][b] = __builtin_amdgcn_mfma_f32_16x16x32_bf16(av, bv[b], acc[a][b], 0, 0, 0);
      }
    }
#pragma unroll
    for (int a = 0; a < 3; ++a)
#pragma unroll
      for (int b = 0; b < 2; ++b)
#pragma unroll
        for (int j = 0; j < 4; ++j)
          kvs[(wid * 48 + a * 16 + (lane >> 4) * 4 + j) * 32 + b * 16 + (lane & 15)] = acc[a][b][j];
  }
  __syncthreads();
  float* mypart = pkv + (size_t)(h * 8 + c8) * 1536;
  for (int i = tid; i < 1536; i += 256)
    mypart[i] = kvs[i] + kvs[1536 + i] + kvs[2 * 1536 + i] + kvs[3 * 1536 + i];
  asm volatile("s_waitcnt vmcnt(0)" ::: "memory");
  __syncthreads();
  if (tid == 0) {
    __builtin_amdgcn_fence(__ATOMIC_RELEASE, "agent");
    asm volatile("s_waitcnt vmcnt(0)" ::: "memory");
    __hip_atomic_fetch_add(cnt + h * 32, 1u, __ATOMIC_RELAXED, __HIP_MEMORY_SCOPE_AGENT);
  }
  STAMP(2, 1);
  float ep[18], en[18];
  {
    float pk[18];
#pragma unroll
    for (int d = 0; d < 9; ++d) {
      pk[2 * d] = lo16(qr[d >> 2][d & 3]);
      pk[2 * d + 1] = hi16(qr[d >> 2][d & 3]);
    }
    softmax_pm(pk, ep, en);
#pragma unroll
    for (int f = 0; f < 18; ++f) { ep[f] = rbf(ep[f]); en[f] = rbf(en[f]); }
  }
  if (c8 == 0) {
    u16* woimg = reinterpret_cast<u16*>(smem);
    for (int i = tid; i < 32 * 72; i += 256) {
      const int r = i / 72, c = i % 72;
      u32x4 v = {0u, 0u, 0u, 0u};
      if (r < 18) v = *reinterpret_cast<const u32x4*>(wo + (size_t)(h * 18 + r) * D_ + c * 8);
      *reinterpret_cast<u32x4*>(woimg + r * 576 + c * 8) = v;
    }
  }
  if (tid == 0) {
    unsigned spins = 0;
    while (__hip_atomic_load(cnt + h * 32, __ATOMIC_RELAXED, __HIP_MEMORY_SCOPE_AGENT) < 8u) {
      __builtin_amdgcn_s_sleep(2);
      if (++spins > (1u << 24)) break;
    }
    __builtin_amdgcn_fence(__ATOMIC_ACQUIRE, "agent");
    asm volatile("s_waitcnt vmcnt(0)" ::: "memory");
  }
  __syncthreads();
  STAMP(2, 2);
  const float* hp = pkv + (size_t)h * 8 * 1536;
  if (tid < 36) {
    float z = 0.f;
#pragma unroll
    for (int c = 0; c < 8; ++c) z += hp[c * 1536 + tid * 32 + 18];
    zs[tid] = rbf(z);
  }
  u16* kvb = reinterpret_cast<u16*>(smem + 36864);
  if (c8 == 0) {
    for (int i = tid; i < 1536; i += 256) {
      float v = 0.f;
#pragma unroll
      for (int c = 0; c < 8; ++c) v += hp[c * 1536 + i];
      const int f = i >> 5, d = i & 31;
      kvb[i] = (d < 18 && f < 36) ? f2bf(v) : (u16)0;
    }
  }
  __syncthreads();
  {
    float den = 0.f;
#pragma unroll
    for (int f = 0; f < 18; ++f) den += ep[f] * zs[f] + en[f] * zs[18 + f];
    const float rd = 1.f / (rbf(den) + 1e-6f);
    unsigned o[20];
#pragma unroll
    for (int f = 0; f < 9; ++f) o[f] = pack2(ep[2 * f] * rd, ep[2 * f + 1] * rd);
#pragma unroll
    for (int f = 0; f < 9; ++f) o[9 + f] = pack2(en[2 * f] * rd, en[2 * f + 1] * rd);
    o[18] = 0;
    o[19] = 0;
#pragma unroll
    for (int c = 0; c < 5; ++c) {
      const int cidx = h * 5 + c;
      u16* dst = phif + ((size_t)((row >> 4) * 40 + (cidx >> 2)) * 64 + (cidx & 3) * 16 + (row & 15)) * 8;
      u32x4 v = {o[4 * c], o[4 * c + 1], o[4 * c + 2], o[4 * c + 3]};
      *reinterpret_cast<u32x4*>(dst) = v;
    }
  }
  STAMP(2, 3);
  if (c8 == 0) {
    const int g = lane >> 4, q = (lane & 15) >> 2, p = lane & 3;
    constexpr int RB = 576 * 2;
    const int o0 = (8 * g + q) * RB + p * 8, o1 = (8 * g + 4 + q) * RB + p * 8;
    bf16x8 kb[3];
#pragma unroll
    for (int a = 0; a < 3; ++a) kb[a] = *reinterpret_cast<const bf16x8*>(kvb + (a * 16 + (lane & 15)) * 32 + 8 * g);
    for (int nt = wid; nt < 36; nt += 4) {
      bf16x8 w = wfrag(smem, o0, o1, nt * 32);
#pragma unroll
      for (int a = 0; a < 3; ++a) {
        f32x4 c = __builtin_amdgcn_mfma_f32_16x16x32_bf16(w, kb[a], (f32x4){0.f, 0.f, 0.f, 0.f}, 0, 0, 0);
        const int f = a * 16 + (lane & 15);
        if (f < KPH_) {
          u32x2 v = {pack2(c[0], c[1]), pack2(c[2], c[3])};
          *reinterpret_cast<u32x2*>(mcat + (size_t)(h * KPH_ + f) * D_ + nt * 16 + 4 * g) = v;
        }
      }
    }
  }
  STAMP(2, 4);
}

template <int BASE, int N0, int CNT, int I = 0>
__device__ __forceinline__ void g2_wreads(WF (&w)[5], unsigned a0, unsigned a1) {
  if constexpr (I < CNT) {
    tr_rd<BASE + (N0 + I) * 32>(w[I].lo, a0);
    tr_rd<BASE + (N0 + I) * 32>(w[I].hi, a1);
    g2_wreads<BASE, N0, CNT, I + 1>(w, a0, a1);
  }
}
template <int N0, int CNT>
__device__ __forceinline__ void g2_mma(WF (&w)[5], const bf16x8 (&ac)[2], f32x4 (&acc)[2][G2_NT]) {
#pragma unroll
  for (int i = 0; i < CNT; ++i) pin(w[i]);
#pragma unroll
  for (int i = 0; i < CNT; ++i)
#pragma unroll
    for (int r = 0; r < 2; ++r)
      acc[r][N0 + i] = __builtin_amdgcn_mfma_f32_16x16x32_bf16(cat(w[i]), ac[r], acc[r][N0 + i], 0, 0, 0);
}
constexpr int K2_HEADBLK = 256;

__global__ __launch_bounds__(256, 2) void k2_gemm2(const u16* __restrict__ hf, const u16* __restrict__ w2,
                                                   float* __restrict__ part, const u16* __restrict__ qkv,
                                                   const u16* __restrict__ wo, u16* __restrict__ phif,
                                                   u16* __restrict__ mcat, float* pkv, unsigned* cnt) {
  __shared__ __attribute__((aligned(16))) char smem[G2_RING * G2_BUFB];
  const int tid = threadIdx.x, wid = tid >> 6, lane = tid & 63;
  int bid = blockIdx.x;
  if (bid < K2_HEADBLK) { head_block(bid, qkv, wo, mcat, phif, pkv, cnt, smem); return; }
  bid -= K2_HEADBLK;
  const int xcd = bid & 7, loc = bid >> 3;
  const int sp = xcd >> 1, mt = (xcd & 1) * 8 + (loc >> 2), nt4 = loc & 3;
  const int m0 = mt * 128, n0 = nt4 * 144, k0 = sp * 1152;
  const int g = lane >> 4, q = (lane & 15) >> 2, p = lane & 3;
  const bool odd = g & 1;
  const unsigned wa0 = lds_addr(smem) + (8 * g + 4 * (g & 1) + q) * G2_RB + p * 8;
  const unsigned wa1 = lds_addr(smem) + (8 * g + 4 * (1 - (g & 1)) + q) * G2_RB + p * 8;
  f32x4 acc[2][G2_NT];
#pragma unroll
  for (int r = 0; r < 2; ++r)
#pragma unroll
    for (int n = 0; n < G2_NT; ++n) acc[r][n] = (f32x4){0.f, 0.f, 0.f, 0.f};
  const u16* wp[5];
  int wdst[5];
#pragma unroll
  for (int i = 0; i < 5; ++i) {
    int ins = wid + 4 * i;
    if (ins >= 18) ins -= 18;
    int c = ins * 64 + lane;
    int row = c / 18, cc = c % 18;
    wp[i] = w2 + (size_t)(k0 + row) * D_ + n0 + cc * 8;
    wdst[i] = c * 16;
  }
  const u16* ap[2];
#pragma unroll
  for (int r = 0; r < 2; ++r) ap[r] = hf + ((size_t)((m0 >> 4) + wid * 2 + r) * 144 + (k0 >> 5)) * 512 + lane * 8;
  bf16x8 an[4][2][2];
  auto issue = [&](auto SL) {
    constexpr int slot = decltype(SL)::value;
    char* buf = smem + slot * G2_BUFB;
#pragma unroll
    for (int i = 0; i < 5; ++i) { glds16(wp[i], buf + wdst[i]); wp[i] += 64 * D_; }
#pragma unroll
    for (int r = 0; r < 2; ++r) {
      gld16_asm(an[slot][r][0], ap[r]);
      gld16_asm(an[slot][r][1], ap[r] + 512);
      ap[r] += 1024;
    }
  };
  issue(IC<0>{});
  issue(IC<1>{});
  issue(IC<2>{});
  wait_vm<18>();
  BAR();
  WF wA[5], wB[5];
  g2_wreads<0, 0, 5>(wA, wa0, wa1);
  auto stage = [&](auto U, auto LAST) {
    constexpr int u = decltype(U)::value;
    constexpr int rem = decltype(LAST)::value;
    constexpr int BASE = u * G2_BUFB;
    bf16x8 c0[2], c1[2];
#pragma unroll
    for (int r = 0; r < 2; ++r) {
      pin(an[u][r][0]); pin(an[u][r][1]);
      c0[r] = swap_halves_if(an[u][r][0], odd);
      c1[r] = swap_halves_if(an[u][r][1], odd);
    }
    g2_wreads<BASE, 5, 4>(wB, wa0, wa1);
    wait_lgkm<8>();
    g2_mma<0, 5>(wA, c0, acc);
    g2_wreads<BASE + 32 * G2_RB, 0, 5>(wA, wa0, wa1);
    wait_lgkm<10>();
    g2_mma<5, 4>(wB, c0, acc);
    g2_wreads<BASE + 32 * G2_RB, 5, 4>(wB, wa0, wa1);
    wait_lgkm<8>();
    g2_mma<0, 5>(wA, c1, acc);
    if constexpr (rem >= 1) {
      if constexpr (rem >= 2) wait_vm<9>(); else wait_vm<0>();
      BAR();
      if constexpr (rem >= 3) issue(IC<(u + 3) & 3>{});
      g2_wreads<((u + 1) & 3) * G2_BUFB, 0, 5>(wA, wa0, wa1);
      wait_lgkm<10>();
    } else {
      wait_lgkm<0>();
    }
    g2_mma<5, 4>(wB, c1, acc);
  };
#pragma unroll 1
  for (int it = 0; it < 3; ++it) {
    stage(IC<0>{}, IC<3>{}); stage(IC<1>{}, IC<3>{}); stage(IC<2>{}, IC<3>{}); stage(IC<3>{}, IC<3>{});
  }
  stage(IC<0>{}, IC<3>{}); stage(IC<1>{}, IC<3>{}); stage(IC<2>{}, IC<3>{});
  stage(IC<3>{}, IC<2>{}); stage(IC<0>{}, IC<1>{}); stage(IC<1>{}, IC<0>{});
#pragma unroll
  for (int r = 0; r < 2; ++r) {
    const int m = m0 + wid * 32 + r * 16 + (lane & 15);
    float* pp = part + ((size_t)sp * S_ + m) * D_ + n0 + 4 * g;
#pragma unroll
    for (int n = 0; n < G2_NT; ++n) *reinterpret_cast<f32x4*>(pp + n * 16) = acc[r][n];
  }
}

constexpr int G3_BUFB = 128 * 128;
constexpr int G3_RING = 3;

__global__ __launch_bounds__(256, 2) void k3_final(const u16* __restrict__ phif, const u16* __restrict__ mcat,
                                                   const float* __restrict__ part, const u16* __restrict__ b2,
                                                   const u16* __restrict__ x, u16* __restrict__ out) {
  __shared__ __attribute__((aligned(16))) char smem[G3_RING * G3_BUFB];
  const int tid = threadIdx.x, wid = tid >> 6, lane = tid & 63;
  const int bid = blockIdx.x;
  const int xcd = bid & 7, loc = bid >> 3;
  const int mt = xcd * 4 + loc / 9, nt = loc % 9;
  const int m0 = mt * 64, n0 = nt * 64;
  const int g = lane >> 4, q = (lane & 15) >> 2, p = lane & 3;
  const int wr = wid >> 1, wc = wid & 1;
  STAMP(3, 0);
  const u16* wp[4];
#pragma unroll
  for (int i = 0; i < 4; ++i) {
    const int row = (wid * 4 + i) * 8 + (lane >> 3);
    const int f = ((row >> 1) & 1) | (((row >> 3) & 1) << 1);
    const int ch = (lane & 7) ^ (f << 1);
    wp[i] = mcat + (size_t)row * D_ + n0 + ch * 8;
  }
  const u16* ap[2];
#pragma unroll
  for (int r = 0; r < 2; ++r) ap[r] = phif + ((size_t)((m0 >> 4) + wr * 2 + r) * 40) * 512 + lane * 8;
  bf16x8 an[3][2][4];
  auto issue = [&](auto SL) {
    constexpr int slot = decltype(SL)::value;
    char* buf = smem + slot * G3_BUFB;
#pragma unroll
    for (int i = 0; i < 4; ++i) { glds16(wp[i], buf + (wid * 4 + i) * 1024 + lane * 16); wp[i] += 128 * D_; }
#pragma unroll
    for (int r = 0; r < 2; ++r) {
#pragma unroll
      for (int ks = 0; ks < 4; ++ks) gld16_asm(an[slot][r][ks], ap[r] + ks * 512);
      ap[r] += 2048;
    }
  };
  issue(IC<0>{});
  issue(IC<1>{});
  bf16x8 py[2][2][4];
  u32x2 xx[2][2], bb[2];
#pragma unroll
  for (int r = 0; r < 2; ++r) {
    const int m = m0 + wr * 32 + r * 16 + (lane & 15);
#pragma unroll
    for (int n = 0; n < 2; ++n) {
      const int ncol = n0 + wc * 32 + n * 16 + 4 * g;
#pragma unroll
      for (int sI = 0; sI < SPLITK_; ++sI) gld16_asm(py[r][n][sI], part + ((size_t)sI * S_ + m) * D_ + ncol);
      asm volatile("global_load_dwordx2 %0, %1, off" : "=v"(xx[r][n]) : "v"(x + (size_t)m * D_ + ncol) : "memory");
    }
  }
#pragma unroll
  for (int n = 0; n < 2; ++n)
    asm volatile("global_load_dwordx2 %0, %1, off" : "=v"(bb[n]) : "v"(b2 + n0 + wc * 32 + n * 16 + 4 * g) : "memory");
  unsigned ad[2][2];
#pragma unroll
  for (int t = 0; t < 2; ++t) {
    const int row = 8 * g + 4 * t + q;
    const int f = ((row >> 1) & 1) | (((row >> 3) & 1) << 1);
#pragma unroll
    for (int n = 0; n < 2; ++n) ad[n][t] = lds_addr(smem) + row * 128 + (((wc * 2 + n) ^ f) * 32) + p * 8;
  }
  f32x4 acc[2][2];
#pragma unroll
  for (int r = 0; r < 2; ++r)
#pragma unroll
    for (int n = 0; n < 2; ++n) acc[r][n] = (f32x4){0.f, 0.f, 0.f, 0.f};
  STAMP(3, 1);
  auto stage = [&](auto U, auto WAITN, auto MORE) {
    constexpr int u = decltype(U)::value;
    wait_vm<decltype(WAITN)::value>();
    BAR();
    if constexpr (decltype(MORE)::value) issue(IC<(u + 2) % 3>{});
    constexpr int BASE = u * G3_BUFB;
    WF w[4][2];
#pragma unroll
    for (int n = 0; n < 2; ++n) { tr_rd<BASE + 0 * 4096>(w[0][n].lo, ad[n][0]); tr_rd<BASE + 0 * 4096>(w[0][n].hi, ad[n][1]); }
#pragma unroll
    for (int n = 0; n < 2; ++n) { tr_rd<BASE + 1 * 4096>(w[1][n].lo, ad[n][0]); tr_rd<BASE + 1 * 4096>(w[1][n].hi, ad[n][1]); }
#pragma unroll
    for (int n = 0; n < 2; ++n) { tr_rd<BASE + 2 * 4096>(w[2][n].lo, ad[n][0]); tr_rd<BASE + 2 * 4096>(w[2][n].hi, ad[n][1]); }
#pragma unroll
    for (int n = 0; n < 2; ++n) { tr_rd<BASE + 3 * 4096>(w[3][n].lo, ad[n][0]); tr_rd<BASE + 3 * 4096>(w[3][n].hi, ad[n][1]); }
#pragma unroll
    for (int r = 0; r < 2; ++r)
#pragma unroll
      for (int ks = 0; ks < 4; ++ks) pin(an[u][r][ks]);
    wait_lgkm<8>();
#pragma unroll
    for (int ks = 0; ks < 2; ++ks)
#pragma unroll
      for (int n = 0; n < 2; ++n) pin(w[ks][n]);
#pragma unroll
    for (int ks = 0; ks < 2; ++ks)
#pragma unroll
      for (int n = 0; n < 2; ++n)
#pragma unroll
        for (int r = 0; r < 2; ++r)
          acc[r][n] = __builtin_amdgcn_mfma_f32_16x16x32_bf16(cat(w[ks][n]), an[u][r][ks], acc[r][n], 0, 0, 0);
    wait_lgkm<0>();
#pragma unroll
    for (int ks = 2; ks < 4; ++ks)
#pragma unroll
      for (int n = 0; n < 2; ++n) pin(w[ks][n]);
#pragma unroll
    for (int ks = 2; ks < 4; ++ks)
#pragma unroll
      for (int n = 0; n < 2; ++n)
#pragma unroll
        for (int r = 0; r < 2; ++r)
          acc[r][n] = __builtin_amdgcn_mfma_f32_16x16x32_bf16(cat(w[ks][n]), an[u][r][ks], acc[r][n], 0, 0, 0);
  };
  stage(IC<0>{}, IC<34>{}, IC<1>{});
  stage(IC<1>{}, IC<34>{}, IC<1>{});
  stage(IC<2>{}, IC<12>{}, IC<1>{});
  stage(IC<0>{}, IC<12>{}, IC<1>{});
  stage(IC<1>{}, IC<12>{}, IC<1>{});
  stage(IC<2>{}, IC<12>{}, IC<1>{});
  stage(IC<0>{}, IC<12>{}, IC<1>{});
  stage(IC<1>{}, IC<12>{}, IC<1>{});
  stage(IC<2>{}, IC<12>{}, IC<0>{});
  stage(IC<0>{}, IC<0>{}, IC<0>{});
  STAMP(3, 3);
#pragma unroll
  for (int n = 0; n < 2; ++n) asm volatile("" : "+v"(bb[n]));
#pragma unroll
  for (int r = 0; r < 2; ++r) {
    const int m = m0 + wr * 32 + r * 16 + (lane & 15);
#pragma unroll
    for (int n = 0; n < 2; ++n) {
      const int ncol = n0 + wc * 32 + n * 16 + 4 * g;
      asm volatile("" : "+v"(xx[r][n]));
#pragma unroll
      for (int sI = 0; sI < SPLITK_; ++sI) pin(py[r][n][sI]);
      f32x4 y = *reinterpret_cast<f32x4*>(&py[r][n][0]);
#pragma unroll
      for (int sI = 1; sI < SPLITK_; ++sI) y += *reinterpret_cast<f32x4*>(&py[r][n][sI]);
      float bv[4] = {lo16(bb[n][0]), hi16(bb[n][0]), lo16(bb[n][1]), hi16(bb[n][1])};
      float xv[4] = {lo16(xx[r][n][0]), hi16(xx[r][n][0]), lo16(xx[r][n][1]), hi16(xx[r][n][1])};
      float o[4];
#pragma unroll
      for (int j = 0; j < 4; ++j) {
        float yy = y[j] + bv[j];
        float gg = yy / (1.f + __expf(-yy));
        o[j] = xv[j] + acc[r][n][j] * gg;
      }
      u32x2 v = {pack2(o[0], o[1]), pack2(o[2], o[3])};
      *reinterpret_cast<u32x2*>(out + (size_t)m * D_ + ncol) = v;
    }
  }
  STAMP(3, 4);
}

extern "C" void kernel_launch(void* const* d_in, const int* in_sizes, int n_in, void* d_out, int out_size, void* d_ws,
                              size_t ws_size, hipStream_t stream) {
  const u16* x = (const u16*)d_in[0];
  const u16* wqkv = (const u16*)d_in[1];
  const u16* wphi = (const u16*)d_in[2];
  const u16* wo = (const u16*)d_in[3];
  const u16* w1 = (const u16*)d_in[5];
  const u16* b1 = (const u16*)d_in[6];
  const u16* w2 = (const u16*)d_in[7];
  const u16* b2 = (const u16*)d_in[8];
  u16* out = (u16*)d_out;
  char* ws = (char*)d_ws;
  size_t off = 0;
  u16* xnf = (u16*)(ws + off); off += (size_t)S_ * D_ * 2;
  u16* wfold = (u16*)(ws + off); off += (size_t)D_ * NPH_ * 2;
  u16* qkv = (u16*)(ws + off); off += (size_t)96 * S_ * 24 * 2;
  u16* hf = (u16*)(ws + off); off += (size_t)S_ * F_ * 2;
  float* part = (float*)(ws + off); off += (size_t)SPLITK_ * S_ * D_ * 4;
  u16* phif = (u16*)(ws + off); off += (size_t)S_ * KPHI_ * 2;
  u16* mcat = (u16*)(ws + off); off += (size_t)KPHI_ * D_ * 2;
  float* pkv = (float*)(ws + off); off += (size_t)256 * 1536 * 4;
  unsigned* cnt = (unsigned*)(ws + off); off += 32 * 32 * 4;

  k0_prep<<<512 + 192, 256, 0, stream>>>(x, wqkv, wphi, xnf, wfold, cnt);
  k1_gemm1<<<256, 512, 0, stream>>>(xnf, wfold, wqkv, w1, b1, qkv, hf);
  k2_gemm2<<<K2_HEADBLK + 256, 256, 0, stream>>>(hf, w2, part, qkv, wo, phif, mcat, pkv, cnt);
  k3_final<<<32 * 9, 256, 0, stream>>>(phif, mcat, part, b2, x, out);
}
```

```cpp
#include <hip/hip_runtime.h>
#include <hip/hip_bf16.h>
#include <stdint.h>

constexpr int S_ = 2048, D_ = 576, H_ = 32, DH_ = 18, F_ = 4608, NQKV_ = 1728;
constexpr int NPH_ = 1152;
constexpr int KPH_ = 40;
constexpr int KPHI_ = H_ * KPH_;
constexpr int SPLITK_ = 4;

typedef short bf16x8 __attribute__((ext_vector_type(8)));
typedef short s16x4 __attribute__((ext_vector_type(4)));
typedef float f32x4 __attribute__((ext_vector_type(4)));
typedef unsigned u32x4 __attribute__((ext_vector_type(4)));
typedef unsigned u32x2 __attribute__((ext_vector_type(2)));
typedef __attribute__((address_space(3))) s16x4* lds_s16x4_p;
typedef unsigned short u16;
template <int V> struct IC { static constexpr int value = V; };

#define STAMP(kid, i) do { } while (0)
#define WAIT_LGKM0() asm volatile("s_waitcnt lgkmcnt(0)" ::: "memory")
#define BAR() __builtin_amdgcn_s_barrier()

template <int N>
__device__ __forceinline__ void wait_vm() {
  static_assert(N >= 0 && N < 64, "vmcnt range");
  asm volatile("s_waitcnt vmcnt(%0)" ::"n"(N) : "memory");
}

__device__ __forceinline__ float bf2f(u16 v) { return __uint_as_float(((unsigned)v) << 16); }
__device__ __forceinline__ u16 f2bf(float f) {
  __hip_bfloat16 b = __float2bfloat16(f);
  return *reinterpret_cast<u16*>(&b);
}
__device__ __forceinline__ unsigned pack2(float lo, float hi) {
  return (unsigned)f2bf(lo) | ((unsigned)f2bf(hi) << 16);
}
__device__ __forceinline__ float rbf(float f) { return bf2f(f2bf(f)); }
__device__ __forceinline__ float lo16(unsigned u) { return __uint_as_float(u << 16); }
__device__ __forceinline__ float hi16(unsigned u) { return __uint_as_float(u & 0xffff0000u); }

__device__ __forceinline__ void glds16(const void* g, void* l) {
  __builtin_amdgcn_global_load_lds((const unsigned*)g, (unsigned*)l, 16, 0, 0);
}

__device__ __forceinline__ bf16x8 swap_halves_if(bf16x8 v, bool odd) {
  bf16x8 s = __builtin_shufflevector(v, v, 4, 5, 6, 7, 0, 1, 2, 3);
  return odd ? s : v;
}

__device__ __forceinline__ bf16x8 wfrag(const char* img, int o0, int o1, int off) {
  s16x4 a = __builtin_amdgcn_ds_read_tr16_b64_v4i16((lds_s16x4_p)(img + o0 + off));
  s16x4 b = __builtin_amdgcn_ds_read_tr16_b64_v4i16((lds_s16x4_p)(img + o1 + off));
  return __builtin_shufflevector(a, b, 0, 1, 2, 3, 4, 5, 6, 7);
}

struct WF { s16x4 lo, hi; };
template <int OFF>
__device__ __forceinline__ void tr_rd(s16x4& r, unsigned addr) {
  asm volatile("ds_read_b64_tr_b16 %0, %1 offset:%2" : "=v"(r) : "v"(addr), "n"(OFF) : "memory");
}
__device__ __forceinline__ void gld16_asm(bf16x8& r, const void* p) {
  asm volatile("global_load_dwordx4 %0, %1, off" : "=v"(r) : "v"(p) : "memory");
}
template <int N>
__device__ __forceinline__ void wait_lgkm() {
  static_assert(N >= 0 && N <= 15, "lgkmcnt range");
  asm volatile("s_waitcnt lgkmcnt(%0)" ::"n"(N) : "memory");
}
__device__ __forceinline__ void pin(WF& w) { asm volatile("" : "+v"(w.lo), "+v"(w.hi)); }
__device__ __forceinline__ void pin(bf16x8& w) { asm volatile("" : "+v"(w)); }
__device__ __forceinline__ bf16x8 cat(const WF& w) { return __builtin_shufflevector(w.lo, w.hi, 0, 1, 2, 3, 4, 5, 6, 7); }
__device__ __forceinline__ unsigned lds_addr(const void* p) {
  return (unsigned)(uintptr_t)(__attribute__((address_space(3))) const char*)p;
}

__global__ __launch_bounds__(256) void k0_prep(const u16* __restrict__ x, const u16* __restrict__ wqkv,
                                               const u16* __restrict__ wphi, u16* __restrict__ xnf,
                                               u16* __restrict__ wfold) {
  __shared__ __attribute__((aligned(16))) float wph[18 * 20];
  const int tid = threadIdx.x, wid = tid >> 6, lane = tid & 63;
  const int bid = blockIdx.x;
  if (bid < 512) {
    const int row = bid * 4 + wid;
    const u16* xp = x + (size_t)row * D_;
    u32x4 a = *reinterpret_cast<const u32x4*>(xp + lane * 8);
    u32x4 b = {0u, 0u, 0u, 0u};
    if (lane < 8) b = *reinterpret_cast<const u32x4*>(xp + 512 + lane * 8);
    float v[16];
#pragma unroll
    for (int j = 0; j < 4; ++j) {
      v[2 * j] = lo16(a[j]); v[2 * j + 1] = hi16(a[j]);
      v[8 + 2 * j] = lo16(b[j]); v[8 + 2 * j + 1] = hi16(b[j]);
    }
    float s = 0.f;
#pragma unroll
    for (int j = 0; j < 16; ++j) s += v[j];
#pragma unroll
    for (int o = 32; o >= 1; o >>= 1) s += __shfl_xor(s, o);
    const float mean = s * (1.f / D_);
    float ss = 0.f;
#pragma unroll
    for (int j = 0; j < 8; ++j) { float d = v[j] - mean; ss += d * d; }
    if (lane < 8) {
#pragma unroll
      for (int j = 8; j < 16; ++j) { float d = v[j] - mean; ss += d * d; }
    }
#pragma unroll
    for (int o = 32; o >= 1; o >>= 1) ss += __shfl_xor(ss, o);
    const float rstd = rsqrtf(ss * (1.f / D_) + 1e-5f);
    u32x4 oa, ob;
#pragma unroll
    for (int j = 0; j < 4; ++j) {
      oa[j] = pack2((v[2 * j] - mean) * rstd, (v[2 * j + 1] - mean) * rstd);
      ob[j] = pack2((v[8 + 2 * j] - mean) * rstd, (v[8 + 2 * j + 1] - mean) * rstd);
    }
    {
      const int c = lane;
      u16* dst = xnf + ((size_t)((row >> 4) * 18 + (c >> 2)) * 64 + (c & 3) * 16 + (row & 15)) * 8;
      *reinterpret_cast<u32x4*>(dst) = oa;
    }
    if (lane < 8) {
      const int c = 64 + lane;
      u16* dst = xnf + ((size_t)((row >> 4) * 18 + (c >> 2)) * 64 + (c & 3) * 16 + (row & 15)) * 8;
      *reinterpret_cast<u32x4*>(dst) = ob;
    }
    return;
  }
  {
    const int fb = bid - 512;
    const int hh = fb / 3, k = (fb % 3) * 192 + tid;
    const int h = hh & 31;
    for (int i = tid; i < 18 * 20; i += 256) {
      const int d = i / 20, f = i % 20;
      wph[i] = (f < 18) ? bf2f(wphi[h * 324 + d * 18 + f]) : 0.f;
    }
    unsigned wv[9];
    if (tid < 192) {
      const u16* wp = wqkv + (size_t)k * NQKV_ + hh * 18;
#pragma unroll
      for (int d = 0; d < 9; ++d) wv[d] = *reinterpret_cast<const unsigned*>(wp + 2 * d);
    }
    __syncthreads();
    if (tid < 192) {
      float acc[20];
#pragma unroll
      for (int f = 0; f < 20; ++f) acc[f] = 0.f;
#pragma unroll
      for (int d = 0; d < 18; ++d) {
        const float w = (d & 1) ? hi16(wv[d >> 1]) : lo16(wv[d >> 1]);
#pragma unroll
        for (int f4 = 0; f4 < 5; ++f4) {
          f32x4 pw = *reinterpret_cast<const f32x4*>(wph + d * 20 + f4 * 4);
#pragma unroll
          for (int j = 0; j < 4; ++j) acc[f4 * 4 + j] += w * pw[j];
        }
      }
      u16* op = wfold + (size_t)k * NPH_ + hh * 18;
#pragma unroll
      for (int f = 0; f < 9; ++f) *reinterpret_cast<unsigned*>(op + 2 * f) = pack2(acc[2 * f], acc[2 * f + 1]);
    }
  }
}

constexpr int G1_BUFB = 64 * 128;
constexpr int G1_RING = 8;
constexpr int G1_XOFF = G1_RING * G1_BUFB;
constexpr int G1_BOFF = G1_XOFF + 576 * 32;
constexpr int G1_SMEM = G1_BOFF + 1024;
constexpr int G1_NST = 8;

template <int BASE, int F0, int CNT, int I = 0>
__device__ __forceinline__ void g1_reads(WF (&w)[4], const unsigned (&ad)[4][2]) {
  if constexpr (I < CNT) {
    constexpr int f = F0 + I, ks = f / 4, n = f % 4;
    tr_rd<BASE + ks * 32 * 128>(w[I].lo, ad[n][0]);
    tr_rd<BASE + ks * 32 * 128>(w[I].hi, ad[n][1]);
    g1_reads<BASE, F0, CNT, I + 1>(w, ad);
  }
}
template <int F0, int KC>
__device__ __forceinline__ void g1_mma(WF (&w)[4], const bf16x8 (&xa)[2][18], f32x4 (&acc)[2][4]) {
#pragma unroll
  for (int i = 0; i < 4; ++i) pin(w[i]);
#pragma unroll
  for (int i = 0; i < 4; ++i) {
    const int f = F0 + i, ks = f / 4, n = f % 4;
#pragma unroll
    for (int r = 0; r < 2; ++r)
      acc[r][n] = __builtin_amdgcn_mfma_f32_16x16x32_bf16(cat(w[i]), xa[r][KC * 2 + ks], acc[r][n], 0, 0, 0);
  }
}

__device__ __forceinline__ float gelu_f(float v) {
  float u = 0.7978845608028654f * (v + 0.044715f * v * v * v);
  float e = __expf(-2.f * u);
  return v * __builtin_amdgcn_rcpf(1.f + e);
}

__device__ __forceinline__ void g1_store_tile(const f32x4& a, int tile, int mrow, int g, const float* bias4,
                                              u16* __restrict__ qkv, u16* __restrict__ hf) {
  if (tile < NQKV_ / 16) {
    const int col = tile * 16 + 4 * g;
#pragma unroll
    for (int j = 0; j < 2; ++j) {
      const int c = col + 2 * j;
      const int hh = c / 18, f = c - hh * 18;
      *reinterpret_cast<unsigned*>(qkv + ((size_t)hh * S_ + mrow) * 24 + f) = pack2(a[2 * j], a[2 * j + 1]);
    }
  } else {
    f32x4 bv = *reinterpret_cast<const f32x4*>(bias4);
    float o[4];
#pragma unroll
    for (int j = 0; j < 4; ++j) o[j] = gelu_f(a[j] + bv[j]);
    u32x2 v = {pack2(o[0], o[1]), pack2(o[2], o[3])};
    const int T = tile - NQKV_ / 16;
    u16* dst = hf + ((size_t)((mrow >> 4) * 144 + (T >> 1)) * 64 + ((T & 1) * 2 + (g >> 1)) * 16 + (mrow & 15)) * 8 + (g & 1) * 4;
    *reinterpret_cast<u32x2*>(dst) = v;
  }
}

__device__ __forceinline__ void g1_addr(unsigned (&ad)[4][2], char* smem, int lane) {
  const int g = lane >> 4, q = (lane & 15) >> 2, p = lane & 3;
#pragma unroll
  for (int t = 0; t < 2; ++t) {
    const int row = 8 * g + 4 * t + q;
    const int f = ((row >> 1) & 1) | (((row >> 3) & 1) << 1);
#pragma unroll
    for (int n = 0; n < 4; ++n) ad[n][t] = lds_addr(smem) + row * 128 + ((n ^ f) * 32) + p * 8;
  }
}

template <int U>
__device__ __forceinline__ void g1_unit(const u16*& wsrc, int& wstride, int unit0, const u16* wfold, const u16* wqkv,
                                        const u16* w1, char* smem, const bf16x8 (&xa)[2][18], int m0,
                                        u16* __restrict__ qkv, u16* __restrict__ hf, WF (&wA)[4]) {
  int tz = threadIdx.x;
  asm volatile("" : "+v"(tz));
  const int wid = __builtin_amdgcn_readfirstlane(tz >> 6), lane = tz & 63;
  const int g = lane >> 4;
  unsigned ad[4][2];
  g1_addr(ad, smem, lane);
  f32x4 acc[2][4];
#pragma unroll
  for (int r = 0; r < 2; ++r)
#pragma unroll
    for (int n = 0; n < 4; ++n) acc[r][n] = (f32x4){0.f, 0.f, 0.f, 0.f};

  auto stage = [&](auto KC) {
    constexpr int kc = decltype(KC)::value;
    constexpr int s = U * 9 + kc;
    constexpr int CUR = (s % G1_RING) * G1_BUFB;
    WF wB[4];
    g1_reads<CUR, 4, 4>(wB, ad);
    wait_lgkm<8>();
    g1_mma<0, kc>(wA, xa, acc);
    if constexpr (s < 26) {
      constexpr int nl = (s + 6 <= 26) ? 5 : (26 - s - 1);
      constexpr int nst = (U > 0 && kc <= 5) ? G1_NST : 0;
      wait_vm<nl + nst>();
      BAR();
      if constexpr (s + 7 <= 26) {
        constexpr int sn = s + 7;
        if constexpr (sn % 9 == 0) {
          const int col = (unit0 + sn / 9) * 64;
          const int row = wid * 8 + (lane >> 3);
          const int f = ((row >> 1) & 1) | (((row >> 3) & 1) << 1);
          const int ch = (lane & 7) ^ (f << 1);
          if (col < NPH_) { wsrc = wfold + (size_t)row * NPH_ + col + ch * 8; wstride = 64 * NPH_; }
          else if (col < NQKV_) { wsrc = wqkv + (size_t)row * NQKV_ + col + ch * 8; wstride = 64 * NQKV_; }
          else { wsrc = w1 + (size_t)row * F_ + (col - NQKV_) + ch * 8; wstride = 64 * F_; }
        }
        glds16(wsrc, smem + (sn % G1_RING) * G1_BUFB + wid * 1024 + lane * 16);
        wsrc += wstride;
      }
      WF wN[4];
      g1_reads<((s + 1) % G1_RING) * G1_BUFB, 0, 4>(wN, ad);
      wait_lgkm<8>();
      g1_mma<4, kc>(wB, xa, acc);
#pragma unroll
      for (int i = 0; i < 4; ++i) wA[i] = wN[i];
    } else {
      wait_lgkm<0>();
      g1_mma<4, kc>(wB, xa, acc);
    }
  };
  stage(IC<0>{}); stage(IC<1>{}); stage(IC<2>{}); stage(IC<3>{}); stage(IC<4>{});
  stage(IC<5>{}); stage(IC<6>{}); stage(IC<7>{}); stage(IC<8>{});
  const float* bias_lds = reinterpret_cast<const float*>(smem + G1_BOFF);
#pragma unroll
  for (int n = 0; n < 4; ++n) {
    const int tile = (unit0 + U) * 4 + n;
#pragma unroll
    for (int r = 0; r < 2; ++r)
      g1_store_tile(acc[r][n], tile, m0 + wid * 32 + r * 16 + (lane & 15), g, bias_lds + (U * 4 + n) * 16 + 4 * g, qkv, hf);
  }
}

__global__ __launch_bounds__(512, 2) void k1_gemm1(const u16* __restrict__ xnf, const u16* __restrict__ wfold,
                                                   const u16* __restrict__ wqkv, const u16* __restrict__ w1,
                                                   const u16* __restrict__ b1, u16* __restrict__ qkv,
                                                   u16* __restrict__ hf) {
  __shared__ __attribute__((aligned(16))) char smem[G1_SMEM];
  float* bias_lds = reinterpret_cast<float*>(smem + G1_BOFF);
  const int tid = threadIdx.x, wid = tid >> 6, lane = tid & 63;
  const int bid = blockIdx.x;
  const int mt = bid >> 5, grp = bid & 31;
  const int m0 = mt * 256;
  const int unit0 = grp * 3;
  const bool extra = grp < 12;
  STAMP(1, 0);
  const int xcol = 6144 + grp * 16;
  const u16* wsrc;
  int wstride;
  {
    const int col = unit0 * 64;
    const int row = wid * 8 + (lane >> 3);
    const int f = ((row >> 1) & 1) | (((row >> 3) & 1) << 1);
    const int ch = (lane & 7) ^ (f << 1);
    if (col < NPH_) { wsrc = wfold + (size_t)row * NPH_ + col + ch * 8; wstride = 64 * NPH_; }
    else if (col < NQKV_) { wsrc = wqkv + (size_t)row * NQKV_ + col + ch * 8; wstride = 64 * NQKV_; }
    else { wsrc = w1 + (size_t)row * F_ + (col - NQKV_) + ch * 8; wstride = 64 * F_; }
  }
#pragma unroll
  for (int s = 0; s < 7; ++s) {
    glds16(wsrc, smem + s * G1_BUFB + wid * 1024 + lane * 16);
    wsrc += wstride;
  }
  if (extra) {
#pragma unroll
    for (int i = 0; i < 3; ++i) {
      int ins = wid + 8 * i;
      if (ins >= 18) ins -= 18;
      const int c = ins * 64 + lane;
      const int pr = c >> 1, half = c & 1;
      const int k = (pr & ~0xC) | ((pr & 4) << 1) | ((pr & 8) >> 1);
      glds16(w1 + (size_t)k * F_ + (xcol - NQKV_) + half * 8, smem + G1_XOFF + c * 16);
    }
  }
  if (tid < 208) {
    const int col = (tid < 192) ? (unit0 * 64 + tid) : (xcol + tid - 192);
    bias_lds[tid] = (col >= NQKV_) ? bf2f(b1[col - NQKV_]) : 0.f;
  }
  bf16x8 xa[2][18];
#pragma unroll
  for (int r = 0; r < 2; ++r) {
    const u16* xp = xnf + ((size_t)((m0 >> 4) + wid * 2 + r) * 18 * 64 + lane) * 8;
#pragma unroll
    for (int ks = 0; ks < 18; ++ks) xa[r][ks] = *reinterpret_cast<const bf16x8*>(xp + ks * 512);
  }
  wait_vm<0>();
  WAIT_LGKM0();
  BAR();
  WF wA[4];
  {
    unsigned ad[4][2];
    g1_addr(ad, smem, lane);
    g1_reads<0, 0, 4>(wA, ad);
  }
  STAMP(1, 1);
  g1_unit<0>(wsrc, wstride, unit0, wfold, wqkv, w1, smem, xa, m0, qkv, hf, wA);
  STAMP(1, 2);
  g1_unit<1>(wsrc, wstride, unit0, wfold, wqkv, w1, smem, xa, m0, qkv, hf, wA);
  STAMP(1, 3);
  g1_unit<2>(wsrc, wstride, unit0, wfold, wqkv, w1, smem, xa, m0, qkv, hf, wA);
  STAMP(1, 4);
  if (extra) {
    int tz = threadIdx.x;
    asm volatile("" : "+v"(tz));
    const int wid2 = __builtin_amdgcn_readfirstlane(tz >> 6), lane2 = tz & 63;
    const int g = lane2 >> 4, q = (lane2 & 15) >> 2, p = lane2 & 3;
    unsigned ad[2];
#pragma unroll
    for (int t = 0; t < 2; ++t) {
      const int k = 8 * g + 4 * t + q;
      const int pr = (k & ~0xC) | ((k & 4) << 1) | ((k & 8) >> 1);
      ad[t] = lds_addr(smem) + G1_XOFF + pr * 32 + p * 8;
    }
    f32x4 acc[2] = {(f32x4){0.f, 0.f, 0.f, 0.f}, (f32x4){0.f, 0.f, 0.f, 0.f}};
#pragma unroll
    for (int kb = 0; kb < 18; kb += 6) {
      WF w[6];
      tr_rd<0 * 1024>(w[0].lo, ad[0] + kb * 1024); tr_rd<0 * 1024>(w[0].hi, ad[1] + kb * 1024);
      tr_rd<1 * 1024>(w[1].lo, ad[0] + kb * 1024); tr_rd<1 * 1024>(w[1].hi, ad[1] + kb * 1024);
      tr_rd<2 * 1024>(w[2].lo, ad[0] + kb * 1024); tr_rd<2 * 1024>(w[2].hi, ad[1] + kb * 1024);
      tr_rd<3 * 1024>(w[3].lo, ad[0] + kb * 1024); tr_rd<3 * 1024>(w[3].hi, ad[1] + kb * 1024);
      tr_rd<4 * 1024>(w[4].lo, ad[0] + kb * 1024); tr_rd<4 * 1024>(w[4].hi, ad[1] + kb * 1024);
      tr_rd<5 * 1024>(w[5].lo, ad[0] + kb * 1024); tr_rd<5 * 1024>(w[5].hi, ad[1] + kb * 1024);
      wait_lgkm<0>();
#pragma unroll
      for (int i = 0; i < 6; ++i) pin(w[i]);
#pragma unroll
      for (int i = 0; i < 6; ++i)
#pragma unroll
        for (int r = 0; r < 2; ++r)
          acc[r] = __builtin_amdgcn_mfma_f32_16x16x32_bf16(cat(w[i]), xa[r][kb + i], acc[r], 0, 0, 0);
    }
#pragma unroll
    for (int r = 0; r < 2; ++r)
      g1_store_tile(acc[r], xcol / 16, m0 + wid2 * 32 + r * 16 + (lane2 & 15), g, bias_lds + 192 + 4 * g, qkv, hf);
  }
}

constexpr int G2_NT = 9, G2_RB = G2_NT * 32;
constexpr int G2_BUFB = 64 * G2_RB;
constexpr int G2_RING = 4;
constexpr int G2_STAGES = 18;

__device__ __forceinline__ void softmax_pm(const float (&pk)[18], float (&ep)[18], float (&en)[18]) {
  float mx = pk[0], mn = pk[0];
#pragma unroll
  for (int f = 1; f < 18; ++f) {
    mx = fmaxf(mx, pk[f]);
    mn = fminf(mn, pk[f]);
  }
  float sp = 0.f, sn = 0.f;
#pragma unroll
  for (int f = 0; f < 18; ++f) {
    ep[f] = __expf(pk[f] - mx);
    en[f] = __expf(mn - pk[f]);
    sp += ep[f];
    sn += en[f];
  }
  const float rp = 1.f / sp, rn = 1.f / sn;
#pragma unroll
  for (int f = 0; f < 18; ++f) {
    ep[f] *= rp;
    en[f] *= rn;
  }
}

__device__ void head_part1(int hb, const u16* __restrict__ qkv, float* __restrict__ pkv, char* smem) {
  u16* phiT = reinterpret_cast<u16*>(smem);
  u16* vT = reinterpret_cast<u16*>(smem + 25344);
  float* kvs = reinterpret_cast<float*>(smem + 25344 + 16896);
  const int h = hb >> 3, c8 = hb & 7;
  const int tid = threadIdx.x, wid = tid >> 6, lane = tid & 63;
  const int row = c8 * 256 + tid;
  u32x4 kr[3], vr[3];
  {
    const u32x4* kp = reinterpret_cast<const u32x4*>(qkv + ((size_t)(32 + h) * S_ + row) * 24);
    const u32x4* vp = reinterpret_cast<const u32x4*>(qkv + ((size_t)(64 + h) * S_ + row) * 24);
#pragma unroll
    for (int d = 0; d < 3; ++d) { kr[d] = kp[d]; vr[d] = vp[d]; }
  }
  for (int i = tid; i < 12 * 264; i += 256) phiT[36 * 264 + i] = 0;
  for (int i = tid; i < 14 * 264; i += 256) vT[18 * 264 + i] = (i < 264) ? (u16)0x3f80 : (u16)0;
  {
    float pk[18];
#pragma unroll
    for (int d = 0; d < 9; ++d) {
      pk[2 * d] = lo16(kr[d >> 2][d & 3]);
      pk[2 * d + 1] = hi16(kr[d >> 2][d & 3]);
    }
    float ep[18], en[18];
    softmax_pm(pk, ep, en);
#pragma unroll
    for (int f = 0; f < 18; ++f) {
      phiT[f * 264 + tid] = f2bf(ep[f]);
      phiT[(18 + f) * 264 + tid] = f2bf(en[f]);
      const unsigned w = vr[(f >> 1) >> 2][(f >> 1) & 3];
      vT[f * 264 + tid] = (u16)((f & 1) ? (w >> 16) : (w & 0xffff));
    }
  }
  __syncthreads();
  {
    f32x4 acc[3][2];
#pragma unroll
    for (int a = 0; a < 3; ++a)
#pragma unroll
      for (int b = 0; b < 2; ++b) acc[a][b] = (f32x4){0.f, 0.f, 0.f, 0.f};
#pragma unroll
    for (int ks = 0; ks < 2; ++ks) {
      const int s0 = wid * 64 + ks * 32 + 8 * (lane >> 4);
      bf16x8 bv[2];
#pragma unroll
      for (int b = 0; b < 2; ++b) bv[b] = *reinterpret_cast<const bf16x8*>(vT + (b * 16 + (lane & 15)) * 264 + s0);
#pragma unroll
      for (int a = 0; a < 3; ++a) {
        bf16x8 av = *reinterpret_cast<const bf16x8*>(phiT + (a * 16 + (lane & 15)) * 264 + s0);
#pragma unroll
        for (int b = 0; b < 2; ++b) acc[a][b] = __builtin_amdgcn_mfma_f32_16x16x32_bf16(av, bv[b], acc[a][b], 0, 0, 0);
      }
    }
#pragma unroll
    for (int a = 0; a < 3; ++a)
#pragma unroll
      for (int b = 0; b < 2; ++b)
#pragma unroll
        for (int j = 0; j < 4; ++j)
          kvs[(wid * 48 + a * 16 + (lane >> 4) * 4 + j) * 32 + b * 16 + (lane & 15)] = acc[a][b][j];
  }
  __syncthreads();
  float* mypart = pkv + (size_t)(h * 8 + c8) * 1536;
  for (int i = tid; i < 1536; i += 256)
    mypart[i] = kvs[i] + kvs[1536 + i] + kvs[2 * 1536 + i] + kvs[3 * 1536 + i];
}

__global__ __launch_bounds__(256) void k2b_head(const u16* __restrict__ qkv, const u16* __restrict__ wo,
                                                const float* __restrict__ pkv, u16* __restrict__ mcat,
                                                u16* __restrict__ phif) {
  __shared__ __attribute__((aligned(16))) char smem[36864 + 3072 + 256];
  const int tid = threadIdx.x, wid = tid >> 6, lane = tid & 63;
  const int bid = blockIdx.x;
  if (bid < 256) {
    float* zs = reinterpret_cast<float*>(smem);
    const int h = bid >> 3, c8 = bid & 7;
    const int row = c8 * 256 + tid;
    u32x4 qr[3];
    const u32x4* qp = reinterpret_cast<const u32x4*>(qkv + ((size_t)h * S_ + row) * 24);
#pragma unroll
    for (int d = 0; d < 3; ++d) qr[d] = qp[d];
    const float* hp = pkv + (size_t)h * 8 * 1536;
    if (tid < 36) {
      float z = 0.f;
#pragma unroll
      for (int c = 0; c < 8; ++c) z += hp[c * 1536 + tid * 32 + 18];
      zs[tid] = rbf(z);
    }
    float ep[18], en[18];
    {
      float pk[18];
#pragma unroll
      for (int d = 0; d < 9; ++d) {
        pk[2 * d] = lo16(qr[d >> 2][d & 3]);
        pk[2 * d + 1] = hi16(qr[d >> 2][d & 3]);
      }
      softmax_pm(pk, ep, en);
    }
    __syncthreads();
    float den = 0.f;
#pragma unroll
    for (int f = 0; f < 18; ++f) {
      ep[f] = rbf(ep[f]);
      en[f] = rbf(en[f]);
      den += ep[f] * zs[f] + en[f] * zs[18 + f];
    }
    const float rd = 1.f / (rbf(den) + 1e-6f);
    unsigned o[20];
#pragma unroll
    for (int f = 0; f < 9; ++f) o[f] = pack2(ep[2 * f] * rd, ep[2 * f + 1] * rd);
#pragma unroll
    for (int f = 0; f < 9; ++f) o[9 + f] = pack2(en[2 * f] * rd, en[2 * f + 1] * rd);
    o[18] = 0;
    o[19] = 0;
#pragma unroll
    for (int c = 0; c < 5; ++c) {
      const int cidx = h * 5 + c;
      u16* dst = phif + ((size_t)((row >> 4) * 40 + (cidx >> 2)) * 64 + (cidx & 3) * 16 + (row & 15)) * 8;
      u32x4 v = {o[4 * c], o[4 * c + 1], o[4 * c + 2], o[4 * c + 3]};
      *reinterpret_cast<u32x4*>(dst) = v;
    }
    return;
  }
  const int h = bid - 256;
  u16* woimg = reinterpret_cast<u16*>(smem);
  u16* kvb = reinterpret_cast<u16*>(smem + 36864);
  const float* hp = pkv + (size_t)h * 8 * 1536;
  float kvv[6];
#pragma unroll
  for (int it = 0; it < 6; ++it) {
    const int i = it * 256 + tid;
    float v = 0.f;
#pragma unroll
    for (int c = 0; c < 8; ++c) v += hp[c * 1536 + i];
    kvv[it] = v;
  }
#pragma unroll
  for (int it = 0; it < 9; ++it) {
    const int i = it * 256 + tid;
    const int r = i / 72, c = i % 72;
    u32x4 v = {0u, 0u, 0u, 0u};
    if (r < 18) v = *reinterpret_cast<const u32x4*>(wo + (size_t)(h * 18 + r) * D_ + c * 8);
    *reinterpret_cast<u32x4*>(woimg + r * 576 + c * 8) = v;
  }
#pragma unroll
  for (int it = 0; it < 6; ++it) {
    const int i = it * 256 + tid;
    const int f = i >> 5, d = i & 31;
    kvb[i] = (d < 18 && f < 36) ? f2bf(kvv[it]) : (u16)0;
  }
  __syncthreads();
  {
    const int g = lane >> 4, q = (lane & 15) >> 2, p = lane & 3;
    constexpr int RB = 576 * 2;
    const int o0 = (8 * g + q) * RB + p * 8, o1 = (8 * g + 4 + q) * RB + p * 8;
    bf16x8 kb[3];
#pragma unroll
    for (int a = 0; a < 3; ++a) kb[a] = *reinterpret_cast<const bf16x8*>(kvb + (a * 16 + (lane & 15)) * 32 + 8 * g);
    for (int nt = wid; nt < 36; nt += 4) {
      bf16x8 w = wfrag(smem, o0, o1, nt * 32);
#pragma unroll
      for (int a = 0; a < 3; ++a) {
        f32x4 c = __builtin_amdgcn_mfma_f32_16x16x32_bf16(w, kb[a], (f32x4){0.f, 0.f, 0.f, 0.f}, 0, 0, 0);
        const int f = a * 16 + (lane & 15);
        if (f < KPH_) {
          u32x2 v = {pack2(c[0], c[1]), pack2(c[2], c[3])};
          *reinterpret_cast<u32x2*>(mcat + (size_t)(h * KPH_ + f) * D_ + nt * 16 + 4 * g) = v;
        }
      }
    }
  }
}

template <int BASE, int N0, int CNT, int I = 0>
__device__ __forceinline__ void g2_wreads(WF (&w)[5], unsigned a0, unsigned a1) {
  if constexpr (I < CNT) {
    tr_rd<BASE + (N0 + I) * 32>(w[I].lo, a0);
    tr_rd<BASE + (N0 + I) * 32>(w[I].hi, a1);
    g2_wreads<BASE, N0, CNT, I + 1>(w, a0, a1);
  }
}
template <int N0, int CNT>
__device__ __forceinline__ void g2_mma(WF (&w)[5], const bf16x8 (&ac)[2], f32x4 (&acc)[2][G2_NT]) {
#pragma unroll
  for (int i = 0; i < CNT; ++i) pin(w[i]);
#pragma unroll
  for (int i = 0; i < CNT; ++i)
#pragma unroll
    for (int r = 0; r < 2; ++r)
      acc[r][N0 + i] = __builtin_amdgcn_mfma_f32_16x16x32_bf16(cat(w[i]), ac[r], acc[r][N0 + i], 0, 0, 0);
}
constexpr int K2_HEADBLK = 256;

__global__ __launch_bounds__(256, 2) void k2_gemm2(const u16* __restrict__ hf, const u16* __restrict__ w2,
                                                   float* __restrict__ part, const u16* __restrict__ qkv,
                                                   float* __restrict__ pkv) {
  __shared__ __attribute__((aligned(16))) char smem[G2_RING * G2_BUFB];
  const int tid = threadIdx.x, wid = tid >> 6, lane = tid & 63;
  int bid = blockIdx.x;
  if (bid < K2_HEADBLK) { head_part1(bid, qkv, pkv, smem); return; }
  bid -= K2_HEADBLK;
  const int xcd = bid & 7, loc = bid >> 3;
  const int sp = xcd >> 1, mt = (xcd & 1) * 8 + (loc >> 2), nt4 = loc & 3;
  const int m0 = mt * 128, n0 = nt4 * 144, k0 = sp * 1152;
  const int g = lane >> 4, q = (lane & 15) >> 2, p = lane & 3;
  const bool odd = g & 1;
  const unsigned wa0 = lds_addr(smem) + (8 * g + 4 * (g & 1) + q) * G2_RB + p * 8;
  const unsigned wa1 = lds_addr(smem) + (8 * g + 4 * (1 - (g & 1)) + q) * G2_RB + p * 8;
  f32x4 acc[2][G2_NT];
#pragma unroll
  for (int r = 0; r < 2; ++r)
#pragma unroll
    for (int n = 0; n < G2_NT; ++n) acc[r][n] = (f32x4){0.f, 0.f, 0.f, 0.f};
  const u16* wp[5];
  int wdst[5];
#pragma unroll
  for (int i = 0; i < 5; ++i) {
    int ins = wid + 4 * i;
    if (ins >= 18) ins -= 18;
    int c = ins * 64 + lane;
    int row = c / 18, cc = c % 18;
    wp[i] = w2 + (size_t)(k0 + row) * D_ + n0 + cc * 8;
    wdst[i] = c * 16;
  }
  const u16* ap[2];
#pragma unroll
  for (int r = 0; r < 2; ++r) ap[r] = hf + ((size_t)((m0 >> 4) + wid * 2 + r) * 144 + (k0 >> 5)) * 512 + lane * 8;
  bf16x8 an[4][2][2];
  auto issue = [&](auto SL) {
    constexpr int slot = decltype(SL)::value;
    char* buf = smem + slot * G2_BUFB;
#pragma unroll
    for (int i = 0; i < 5; ++i) { glds16(wp[i], buf + wdst[i]); wp[i] += 64 * D_; }
#pragma unroll
    for (int r = 0; r < 2; ++r) {
      gld16_asm(an[slot][r][0], ap[r]);
      gld16_asm(an[slot][r][1], ap[r] + 512);
      ap[r] += 1024;
    }
  };
  issue(IC<0>{});
  issue(IC<1>{});
  issue(IC<2>{});
  wait_vm<18>();
  BAR();
  WF wA[5], wB[5];
  g2_wreads<0, 0, 5>(wA, wa0, wa1);
  auto stage = [&](auto U, auto LAST) {
    constexpr int u = decltype(U)::value;
    constexpr int rem = decltype(LAST)::value;
    constexpr int BASE = u * G2_BUFB;
    bf16x8 c0[2], c1[2];
#pragma unroll
    for (int r = 0; r < 2; ++r) {
      pin(an[u][r][0]); pin(an[u][r][1]);
      c0[r] = swap_halves_if(an[u][r][0], odd);
      c1[r] = swap_halves_if(an[u][r][1], odd);
    }
    g2_wreads<BASE, 5, 4>(wB, wa0, wa1);
    wait_lgkm<8>();
    g2_mma<0, 5>(wA, c0, acc);
    g2_wreads<BASE + 32 * G2_RB, 0, 5>(wA, wa0, wa1);
    wait_lgkm<10>();
    g2_mma<5, 4>(wB, c0, acc);
    g2_wreads<BASE + 32 * G2_RB, 5, 4>(wB, wa0, wa1);
    wait_lgkm<8>();
    g2_mma<0, 5>(wA, c1, acc);
    if constexpr (rem >= 1) {
      if constexpr (rem >= 2) wait_vm<9>(); else wait_vm<0>();
      BAR();
      if constexpr (rem >= 3) issue(IC<(u + 3) & 3>{});
      g2_wreads<((u + 1) & 3) * G2_BUFB, 0, 5>(wA, wa0, wa1);
      wait_lgkm<10>();
    } else {
      wait_lgkm<0>();
    }
    g2_mma<5, 4>(wB, c1, acc);
  };
#pragma unroll 1
  for (int it = 0; it < 3; ++it) {
    stage(IC<0>{}, IC<3>{}); stage(IC<1>{}, IC<3>{}); stage(IC<2>{}, IC<3>{}); stage(IC<3>{}, IC<3>{});
  }
  stage(IC<0>{}, IC<3>{}); stage(IC<1>{}, IC<3>{}); stage(IC<2>{}, IC<3>{});
  stage(IC<3>{}, IC<2>{}); stage(IC<0>{}, IC<1>{}); stage(IC<1>{}, IC<0>{});
#pragma unroll
  for (int r = 0; r < 2; ++r) {
    const int m = m0 + wid * 32 + r * 16 + (lane & 15);
    float* pp = part + ((size_t)sp * S_ + m) * D_ + n0 + 4 * g;
#pragma unroll
    for (int n = 0; n < G2_NT; ++n) *reinterpret_cast<f32x4*>(pp + n * 16) = acc[r][n];
  }
}

constexpr int G3_BUFB = 128 * 128;
constexpr int G3_RING = 3;

__global__ __launch_bounds__(256, 2) void k3_final(const u16* __restrict__ phif, const u16* __restrict__ mcat,
                                                   const float* __restrict__ part, const u16* __restrict__ b2,
                                                   const u16* __restrict__ x, u16* __restrict__ out) {
  __shared__ __attribute__((aligned(16))) char smem[G3_RING * G3_BUFB];
  const int tid = threadIdx.x, wid = tid >> 6, lane = tid & 63;
  const int bid = blockIdx.x;
  const int xcd = bid & 7, loc = bid >> 3;
  const int mt = xcd * 4 + loc / 9, nt = loc % 9;
  const int m0 = mt * 64, n0 = nt * 64;
  const int g = lane >> 4, q = (lane & 15) >> 2, p = lane & 3;
  const int wr = wid >> 1, wc = wid & 1;
  STAMP(3, 0);
  const u16* wp[4];
#pragma unroll
  for (int i = 0; i < 4; ++i) {
    const int row = (wid * 4 + i) * 8 + (lane >> 3);
    const int f = ((row >> 1) & 1) | (((row >> 3) & 1) << 1);
    const int ch = (lane & 7) ^ (f << 1);
    wp[i] = mcat + (size_t)row * D_ + n0 + ch * 8;
  }
  const u16* ap[2];
#pragma unroll
  for (int r = 0; r < 2; ++r) ap[r] = phif + ((size_t)((m0 >> 4) + wr * 2 + r) * 40) * 512 + lane * 8;
  bf16x8 an[3][2][4];
  auto issue = [&](auto SL) {
    constexpr int slot = decltype(SL)::value;
    char* buf = smem + slot * G3_BUFB;
#pragma unroll
    for (int i = 0; i < 4; ++i) { glds16(wp[i], buf + (wid * 4 + i) * 1024 + lane * 16); wp[i] += 128 * D_; }
#pragma unroll
    for (int r = 0; r < 2; ++r) {
#pragma unroll
      for (int ks = 0; ks < 4; ++ks) gld16_asm(an[slot][r][ks], ap[r] + ks * 512);
      ap[r] += 2048;
    }
  };
  issue(IC<0>{});
  issue(IC<1>{});
  bf16x8 py[2][2][4];
  u32x2 xx[2][2], bb[2];
#pragma unroll
  for (int r = 0; r < 2; ++r) {
    const int m = m0 + wr * 32 + r * 16 + (lane & 15);
#pragma unroll
    for (int n = 0; n < 2; ++n) {
      const int ncol = n0 + wc * 32 + n * 16 + 4 * g;
#pragma unroll
      for (int sI = 0; sI < SPLITK_; ++sI) gld16_asm(py[r][n][sI], part + ((size_t)sI * S_ + m) * D_ + ncol);
      asm volatile("global_load_dwordx2 %0, %1, off" : "=v"(xx[r][n]) : "v"(x + (size_t)m * D_ + ncol) : "memory");
    }
  }
#pragma unroll
  for (int n = 0; n < 2; ++n)
    asm volatile("global_load_dwordx2 %0, %1, off" : "=v"(bb[n]) : "v"(b2 + n0 + wc * 32 + n * 16 + 4 * g) : "memory");
  unsigned ad[2][2];
#pragma unroll
  for (int t = 0; t < 2; ++t) {
    const int row = 8 * g + 4 * t + q;
    const int f = ((row >> 1) & 1) | (((row >> 3) & 1) << 1);
#pragma unroll
    for (int n = 0; n < 2; ++n) ad[n][t] = lds_addr(smem) + row * 128 + (((wc * 2 + n) ^ f) * 32) + p * 8;
  }
  f32x4 acc[2][2];
#pragma unroll
  for (int r = 0; r < 2; ++r)
#pragma unroll
    for (int n = 0; n < 2; ++n) acc[r][n] = (f32x4){0.f, 0.f, 0.f, 0.f};
  STAMP(3, 1);
  auto stage = [&](auto U, auto WAITN, auto MORE) {
    constexpr int u = decltype(U)::value;
    wait_vm<decltype(WAITN)::value>();
    BAR();
    if constexpr (decltype(MORE)::value) issue(IC<(u + 2) % 3>{});
    constexpr int BASE = u * G3_BUFB;
    WF w[4][2];
#pragma unroll
    for (int n = 0; n < 2; ++n) { tr_rd<BASE + 0 * 4096>(w[0][n].lo, ad[n][0]); tr_rd<BASE + 0 * 4096>(w[0][n].hi, ad[n][1]); }
#pragma unroll
    for (int n = 0; n < 2; ++n) { tr_rd<BASE + 1 * 4096>(w[1][n].lo, ad[n][0]); tr_rd<BASE + 1 * 4096>(w[1][n].hi, ad[n][1]); }
#pragma unroll
    for (int n = 0; n < 2; ++n) { tr_rd<BASE + 2 * 4096>(w[2][n].lo, ad[n][0]); tr_rd<BASE + 2 * 4096>(w[2][n].hi, ad[n][1]); }
#pragma unroll
    for (int n = 0; n < 2; ++n) { tr_rd<BASE + 3 * 4096>(w[3][n].lo, ad[n][0]); tr_rd<BASE + 3 * 4096>(w[3][n].hi, ad[n][1]); }
#pragma unroll
    for (int r = 0; r < 2; ++r)
#pragma unroll
      for (int ks = 0; ks < 4; ++ks) pin(an[u][r][ks]);
    wait_lgkm<8>();
#pragma unroll
    for (int ks = 0; ks < 2; ++ks)
#pragma unroll
      for (int n = 0; n < 2; ++n) pin(w[ks][n]);
#pragma unroll
    for (int ks = 0; ks < 2; ++ks)
#pragma unroll
      for (int n = 0; n < 2; ++n)
#pragma unroll
        for (int r = 0; r < 2; ++r)
          acc[r][n] = __builtin_amdgcn_mfma_f32_16x16x32_bf16(cat(w[ks][n]), an[u][r][ks], acc[r][n], 0, 0, 0);
    wait_lgkm<0>();
#pragma unroll
    for (int ks = 2; ks < 4; ++ks)
#pragma unroll
      for (int n = 0; n < 2; ++n) pin(w[ks][n]);
#pragma unroll
    for (int ks = 2; ks < 4; ++ks)
#pragma unroll
      for (int n = 0; n < 2; ++n)
#pragma unroll
        for (int r = 0; r < 2; ++r)
          acc[r][n] = __builtin_amdgcn_mfma_f32_16x16x32_bf16(cat(w[ks][n]), an[u][r][ks], acc[r][n], 0, 0, 0);
  };
  stage(IC<0>{}, IC<34>{}, IC<1>{});
  stage(IC<1>{}, IC<34>{}, IC<1>{});
  stage(IC<2>{}, IC<12>{}, IC<1>{});
  stage(IC<0>{}, IC<12>{}, IC<1>{});
  stage(IC<1>{}, IC<12>{}, IC<1>{});
  stage(IC<2>{}, IC<12>{}, IC<1>{});
  stage(IC<0>{}, IC<12>{}, IC<1>{});
  stage(IC<1>{}, IC<12>{}, IC<1>{});
  stage(IC<2>{}, IC<12>{}, IC<0>{});
  stage(IC<0>{}, IC<0>{}, IC<0>{});
  STAMP(3, 3);
#pragma unroll
  for (int n = 0; n < 2; ++n) asm volatile("" : "+v"(bb[n]));
#pragma unroll
  for (int r = 0; r < 2; ++r) {
    const int m = m0 + wr * 32 + r * 16 + (lane & 15);
#pragma unroll
    for (int n = 0; n < 2; ++n) {
      const int ncol = n0 + wc * 32 + n * 16 + 4 * g;
      asm volatile("" : "+v"(xx[r][n]));
#pragma unroll
      for (int sI = 0; sI < SPLITK_; ++sI) pin(py[r][n][sI]);
      f32x4 y = *reinterpret_cast<f32x4*>(&py[r][n][0]);
#pragma unroll
      for (int sI = 1; sI < SPLITK_; ++sI) y += *reinterpret_cast<f32x4*>(&py[r][n][sI]);
      float bv[4] = {lo16(bb[n][0]), hi16(bb[n][0]), lo16(bb[n][1]), hi16(bb[n][1])};
      float xv[4] = {lo16(xx[r][n][0]), hi16(xx[r][n][0]), lo16(xx[r][n][1]), hi16(xx[r][n][1])};
      float o[4];
#pragma unroll
      for (int j = 0; j < 4; ++j) {
        float yy = y[j] + bv[j];
        float gg = yy / (1.f + __expf(-yy));
        o[j] = xv[j] + acc[r][n][j] * gg;
      }
      u32x2 v = {pack2(o[0], o[1]), pack2(o[2], o[3])};
      *reinterpret_cast<u32x2*>(out + (size_t)m * D_ + ncol) = v;
    }
  }
  STAMP(3, 4);
}

extern "C" void kernel_launch(void* const* d_in, const int* in_sizes, int n_in, void* d_out, int out_size, void* d_ws,
                              size_t ws_size, hipStream_t stream) {
  const u16* x = (const u16*)d_in[0];
  const u16* wqkv = (const u16*)d_in[1];
  const u16* wphi = (const u16*)d_in[2];
  const u16* wo = (const u16*)d_in[3];
  const u16* w1 = (const u16*)d_in[5];
  const u16* b1 = (const u16*)d_in[6];
  const u16* w2 = (const u16*)d_in[7];
  const u16* b2 = (const u16*)d_in[8];
  u16* out = (u16*)d_out;
  char* ws = (char*)d_ws;
  size_t off = 0;
  u16* xnf = (u16*)(ws + off); off += (size_t)S_ * D_ * 2;
  u16* wfold = (u16*)(ws + off); off += (size_t)D_ * NPH_ * 2;
  u16* qkv = (u16*)(ws + off); off += (size_t)96 * S_ * 24 * 2;
  u16* hf = (u16*)(ws + off); off += (size_t)S_ * F_ * 2;
  float* part = (float*)(ws + off); off += (size_t)SPLITK_ * S_ * D_ * 4;
  u16* phif = (u16*)(ws + off); off += (size_t)S_ * KPHI_ * 2;
  u16* mcat = (u16*)(ws + off); off += (size_t)KPHI_ * D_ * 2;
  float* pkv = (float*)(ws + off); off += (size_t)256 * 1536 * 4;

  k0_prep<<<512 + 192, 256, 0, stream>>>(x, wqkv, wphi, xnf, wfold);
  k1_gemm1<<<256, 512, 0, stream>>>(xnf, wfold, wqkv, w1, b1, qkv, hf);
  k2_gemm2<<<K2_HEADBLK + 256, 256, 0, stream>>>(hf, w2, part, qkv, pkv);
  k2b_head<<<256 + 32, 256, 0, stream>>>(qkv, wo, pkv, mcat, phif);
  k3_final<<<32 * 9, 256, 0, stream>>>(phif, mcat, part, b2, x, out);
}
```

```cpp
#include <hip/hip_runtime.h>
#include <hip/hip_bf16.h>
#include <stdint.h>

constexpr int S_ = 2048, D_ = 576, H_ = 32, DH_ = 18, F_ = 4608, NQKV_ = 1728;
constexpr int NPH_ = 1152;
constexpr int KPH_ = 40;
constexpr int KPHI_ = H_ * KPH_;
constexpr int SPLITK_ = 4;

typedef short bf16x8 __attribute__((ext_vector_type(8)));
typedef short s16x4 __attribute__((ext_vector_type(4)));
typedef float f32x4 __attribute__((ext_vector_type(4)));
typedef unsigned u32x4 __attribute__((ext_vector_type(4)));
typedef unsigned u32x2 __attribute__((ext_vector_type(2)));
typedef __attribute__((address_space(3))) s16x4* lds_s16x4_p;
typedef unsigned short u16;
template <int V> struct IC { static constexpr int value = V; };

#define STAMP(kid, i) do { } while (0)
#define WAIT_LGKM0() asm volatile("s_waitcnt lgkmcnt(0)" ::: "memory")
#define BAR() __builtin_amdgcn_s_barrier()

template <int N>
__device__ __forceinline__ void wait_vm() {
  static_assert(N >= 0 && N < 64, "vmcnt range");
  asm volatile("s_waitcnt vmcnt(%0)" ::"n"(N) : "memory");
}

__device__ __forceinline__ float bf2f(u16 v) { return __uint_as_float(((unsigned)v) << 16); }
__device__ __forceinline__ u16 f2bf(float f) {
  __hip_bfloat16 b = __float2bfloat16(f);
  return *reinterpret_cast<u16*>(&b);
}
__device__ __forceinline__ unsigned pack2(float lo, float hi) {
  return (unsigned)f2bf(lo) | ((unsigned)f2bf(hi) << 16);
}
__device__ __forceinline__ float rbf(float f) { return bf2f(f2bf(f)); }
__device__ __forceinline__ float lo16(unsigned u) { return __uint_as_float(u << 16); }
__device__ __forceinline__ float hi16(unsigned u) { return __uint_as_float(u & 0xffff0000u); }

__device__ __forceinline__ void glds16(const void* g, void* l) {
  __builtin_amdgcn_global_load_lds((const unsigned*)g, (unsigned*)l, 16, 0, 0);
}

__device__ __forceinline__ bf16x8 swap_halves_if(bf16x8 v, bool odd) {
  bf16x8 s = __builtin_shufflevector(v, v, 4, 5, 6, 7, 0, 1, 2, 3);
  return odd ? s : v;
}

__device__ __forceinline__ bf16x8 wfrag(const char* img, int o0, int o1, int off) {
  s16x4 a = __builtin_amdgcn_ds_read_tr16_b64_v4i16((lds_s16x4_p)(img + o0 + off));
  s16x4 b = __builtin_amdgcn_ds_read_tr16_b64_v4i16((lds_s16x4_p)(img + o1 + off));
  return __builtin_shufflevector(a, b, 0, 1, 2, 3, 4, 5, 6, 7);
}

struct WF { s16x4 lo, hi; };
template <int OFF>
__device__ __forceinline__ void tr_rd(s16x4& r, unsigned addr) {
  asm volatile("ds_read_b64_tr_b16 %0, %1 offset:%2" : "=v"(r) : "v"(addr), "n"(OFF) : "memory");
}
__device__ __forceinline__ void gld16_asm(bf16x8& r, const void* p) {
  asm volatile("global_load_dwordx4 %0, %1, off" : "=v"(r) : "v"(p) : "memory");
}
template <int N>
__device__ __forceinline__ void wait_lgkm() {
  static_assert(N >= 0 && N <= 15, "lgkmcnt range");
  asm volatile("s_waitcnt lgkmcnt(%0)" ::"n"(N) : "memory");
}
__device__ __forceinline__ void pin(WF& w) { asm volatile("" : "+v"(w.lo), "+v"(w.hi)); }
__device__ __forceinline__ void pin(bf16x8& w) { asm volatile("" : "+v"(w)); }
__device__ __forceinline__ bf16x8 cat(const WF& w) { return __builtin_shufflevector(w.lo, w.hi, 0, 1, 2, 3, 4, 5, 6, 7); }
__device__ __forceinline__ unsigned lds_addr(const void* p) {
  return (unsigned)(uintptr_t)(__attribute__((address_space(3))) const char*)p;
}

__global__ __launch_bounds__(256) void k0_prep(const u16* __restrict__ x, const u16* __restrict__ wqkv,
                                               const u16* __restrict__ wphi, u16* __restrict__ xnf,
                                               u16* __restrict__ wfold) {
  __shared__ __attribute__((aligned(16))) float wph[18 * 20];
  const int tid = threadIdx.x, wid = tid >> 6, lane = tid & 63;
  const int bid = blockIdx.x;
  if (bid < 512) {
    const int row = bid * 4 + wid;
    const u16* xp = x + (size_t)row * D_;
    u32x4 a = *reinterpret_cast<const u32x4*>(xp + lane * 8);
    u32x4 b = {0u, 0u, 0u, 0u};
    if (lane < 8) b = *reinterpret_cast<const u32x4*>(xp + 512 + lane * 8);
    float v[16];
#pragma unroll
    for (int j = 0; j < 4; ++j) {
      v[2 * j] = lo16(a[j]); v[2 * j + 1] = hi16(a[j]);
      v[8 + 2 * j] = lo16(b[j]); v[8 + 2 * j + 1] = hi16(b[j]);
    }
    float s = 0.f;
#pragma unroll
    for (int j = 0; j < 16; ++j) s += v[j];
#pragma unroll
    for (int o = 32; o >= 1; o >>= 1) s += __shfl_xor(s, o);
    const float mean = s * (1.f / D_);
    float ss = 0.f;
#pragma unroll
    for (int j = 0; j < 8; ++j) { float d = v[j] - mean; ss += d * d; }
    if (lane < 8) {
#pragma unroll
      for (int j = 8; j < 16; ++j) { float d = v[j] - mean; ss += d * d; }
    }
#pragma unroll
    for (int o = 32; o >= 1; o >>= 1) ss += __shfl_xor(ss, o);
    const float rstd = rsqrtf(ss * (1.f / D_) + 1e-5f);
    u32x4 oa, ob;
#pragma unroll
    for (int j = 0; j < 4; ++j) {
      oa[j] = pack2((v[2 * j] - mean) * rstd, (v[2 * j + 1] - mean) * rstd);
      ob[j] = pack2((v[8 + 2 * j] - mean) * rstd, (v[8 + 2 * j + 1] - mean) * rstd);
    }
    {
      const int c = lane;
      u16* dst = xnf + ((size_t)((row >> 4) * 18 + (c >> 2)) * 64 + (c & 3) * 16 + (row & 15)) * 8;
      *reinterpret_cast<u32x4*>(dst) = oa;
    }
    if (lane < 8) {
      const int c = 64 + lane;
      u16* dst = xnf + ((size_t)((row >> 4) * 18 + (c >> 2)) * 64 + (c & 3) * 16 + (row & 15)) * 8;
      *reinterpret_cast<u32x4*>(dst) = ob;
    }
    return;
  }
  {
    const int fb = bid - 512;
    const int hh = fb / 3, k = (fb % 3) * 192 + tid;
    const int h = hh & 31;
    for (int i = tid; i < 18 * 20; i += 256) {
      const int d = i / 20, f = i % 20;
      wph[i] = (f < 18) ? bf2f(wphi[h * 324 + d * 18 + f]) : 0.f;
    }
    unsigned wv[9];
    if (tid < 192) {
      const u16* wp = wqkv + (size_t)k * NQKV_ + hh * 18;
#pragma unroll
      for (int d = 0; d < 9; ++d) wv[d] = *reinterpret_cast<const unsigned*>(wp + 2 * d);
    }
    __syncthreads();
    if (tid < 192) {
      float acc[20];
#pragma unroll
      for (int f = 0; f < 20; ++f) acc[f] = 0.f;
#pragma unroll
      for (int d = 0; d < 18; ++d) {
        const float w = (d & 1) ? hi16(wv[d >> 1]) : lo16(wv[d >> 1]);
#pragma unroll
        for (int f4 = 0; f4 < 5; ++f4) {
          f32x4 pw = *reinterpret_cast<const f32x4*>(wph + d * 20 + f4 * 4);
#pragma unroll
          for (int j = 0; j < 4; ++j) acc[f4 * 4 + j] += w * pw[j];
        }
      }
      u16* op = wfold + (size_t)k * NPH_ + hh * 18;
#pragma unroll
      for (int f = 0; f < 9; ++f) *reinterpret_cast<unsigned*>(op + 2 * f) = pack2(acc[2 * f], acc[2 * f + 1]);
    }
  }
}

constexpr int G1_BUFB = 192 * 128;
constexpr int G1_RING = 4;
constexpr int G1_XOFF = G1_RING * G1_BUFB;
constexpr int G1_BOFF = G1_XOFF + 576 * 32;
constexpr int G1_SMEM = G1_BOFF + 1024;
constexpr int G1_NST = 8;

template <int BASE, int I = 0>
__device__ __forceinline__ void g1_reads(WF (&w)[4], const unsigned (&ad)[4][2]) {
  if constexpr (I < 4) {
    if constexpr (BASE >= 49152) {
      tr_rd<BASE - 49152>(w[I].lo, ad[I][0] + 49152u);
      tr_rd<BASE - 49152>(w[I].hi, ad[I][1] + 49152u);
    } else {
      tr_rd<BASE>(w[I].lo, ad[I][0]);
      tr_rd<BASE>(w[I].hi, ad[I][1]);
    }
    g1_reads<BASE, I + 1>(w, ad);
  }
}
template <int KS>
__device__ __forceinline__ void g1_mma(WF (&w)[4], const bf16x8 (&xa)[2][18], f32x4 (&acc)[2][4]) {
#pragma unroll
  for (int i = 0; i < 4; ++i) pin(w[i]);
#pragma unroll
  for (int i = 0; i < 4; ++i)
#pragma unroll
    for (int r = 0; r < 2; ++r)
      acc[r][i] = __builtin_amdgcn_mfma_f32_16x16x32_bf16(cat(w[i]), xa[r][KS], acc[r][i], 0, 0, 0);
}

__device__ __forceinline__ float gelu_f(float v) {
  constexpr float c1 = -2.f * 0.7978845608028654f * 1.4426950408889634f;
  constexpr float c3 = c1 * 0.044715f;
  const float t = v * v;
  const float e = __builtin_amdgcn_exp2f(v * fmaf(t, c3, c1));
  return v * __builtin_amdgcn_rcpf(1.f + e);
}

__device__ __forceinline__ void g1_store_tile(const f32x4& a, int tile, int mrow, int g, const u16* bias4,
                                              u16* __restrict__ qkv, u16* __restrict__ hf) {
  if (tile < NQKV_ / 16) {
    const int col = tile * 16 + 4 * g;
#pragma unroll
    for (int j = 0; j < 2; ++j) {
      const int c = col + 2 * j;
      const int hh = c / 18, f = c - hh * 18;
      *reinterpret_cast<unsigned*>(qkv + ((size_t)hh * S_ + mrow) * 24 + f) = pack2(a[2 * j], a[2 * j + 1]);
    }
  } else {
    u32x2 bb = *reinterpret_cast<const u32x2*>(bias4);
    float o[4];
    o[0] = gelu_f(a[0] + lo16(bb[0]));
    o[1] = gelu_f(a[1] + hi16(bb[0]));
    o[2] = gelu_f(a[2] + lo16(bb[1]));
    o[3] = gelu_f(a[3] + hi16(bb[1]));
    u32x2 v = {pack2(o[0], o[1]), pack2(o[2], o[3])};
    const int T = tile - NQKV_ / 16;
    u16* dst = hf + ((size_t)((mrow >> 4) * 144 + (T >> 1)) * 64 + ((T & 1) * 2 + (g >> 1)) * 16 + (mrow & 15)) * 8 + (g & 1) * 4;
    *reinterpret_cast<u32x2*>(dst) = v;
  }
}

__device__ __forceinline__ void g1_addr(unsigned (&ad)[4][2], char* smem, int lane) {
  const int g = lane >> 4, q = (lane & 15) >> 2, p = lane & 3;
#pragma unroll
  for (int t = 0; t < 2; ++t) {
    const int row = 8 * g + 4 * t + q;
    const int f = ((row >> 1) & 1) | (((row >> 3) & 1) << 1);
#pragma unroll
    for (int n = 0; n < 4; ++n) ad[n][t] = lds_addr(smem) + row * 128 + ((n ^ f) * 32) + p * 8;
  }
}

struct G1W { const u16* src; int stride; };

__device__ __forceinline__ void g1_wsetup(G1W& w, int col, int wid, int lane, const u16* wfold, const u16* wqkv, const u16* w1) {
  const int row = wid * 8 + (lane >> 3);
  const int f = ((row >> 1) & 1) | (((row >> 3) & 1) << 1);
  const int ch = (lane & 7) ^ (f << 1);
  if (col < NPH_) { w.src = wfold + (size_t)row * NPH_ + col + ch * 8; w.stride = 64 * NPH_; }
  else if (col < NQKV_) { w.src = wqkv + (size_t)row * NQKV_ + col + ch * 8; w.stride = 64 * NQKV_; }
  else { w.src = w1 + (size_t)row * F_ + (col - NQKV_) + ch * 8; w.stride = 64 * F_; }
}
__device__ __forceinline__ void g1_wissue(G1W& w, char* buf, int wid, int lane) {
#pragma unroll
  for (int i = 0; i < 3; ++i) {
    glds16(w.src, buf + i * 8192 + wid * 1024 + lane * 16);
    w.src += w.stride;
  }
}

template <int U>
__device__ __forceinline__ void g1_unit(G1W& wst, int unit0, const u16* wfold, const u16* wqkv,
                                        const u16* w1, char* smem, bf16x8 (&xa)[2][18], int m0,
                                        u16* __restrict__ qkv, u16* __restrict__ hf, WF (&wA)[4]) {
  int tz = threadIdx.x;
  asm volatile("" : "+v"(tz));
  const int wid = __builtin_amdgcn_readfirstlane(tz >> 6), lane = tz & 63;
  const int g = lane >> 4;
  unsigned ad[4][2];
  g1_addr(ad, smem, lane);
  f32x4 acc[2][4];
#pragma unroll
  for (int r = 0; r < 2; ++r)
#pragma unroll
    for (int n = 0; n < 4; ++n) acc[r][n] = (f32x4){0.f, 0.f, 0.f, 0.f};

  auto stage = [&](auto KC) {
    constexpr int kc = decltype(KC)::value;
    constexpr int s = U * 3 + kc;
    constexpr int CUR = (s % G1_RING) * G1_BUFB;
    if constexpr (U == 0) {
#pragma unroll
      for (int k = 0; k < 6; ++k) { pin(xa[0][kc * 6 + k]); pin(xa[1][kc * 6 + k]); }
    }
    WF wB[4];
    g1_reads<CUR + 1 * 4096>(wB, ad); wait_lgkm<8>(); g1_mma<kc * 6 + 0>(wA, xa, acc);
    g1_reads<CUR + 2 * 4096>(wA, ad); wait_lgkm<8>(); g1_mma<kc * 6 + 1>(wB, xa, acc);
    g1_reads<CUR + 3 * 4096>(wB, ad); wait_lgkm<8>(); g1_mma<kc * 6 + 2>(wA, xa, acc);
    g1_reads<CUR + 4 * 4096>(wA, ad); wait_lgkm<8>(); g1_mma<kc * 6 + 3>(wB, xa, acc);
    g1_reads<CUR + 5 * 4096>(wB, ad); wait_lgkm<8>(); g1_mma<kc * 6 + 4>(wA, xa, acc);
    if constexpr (s < 8) {
      constexpr int nl = (s == 0) ? 12 : ((s + 2 <= 8) ? 3 : 0);
      constexpr int nst = (U > 0 && kc <= 1) ? G1_NST : 0;
      wait_vm<nl + nst>();
      BAR();
      if constexpr (s + 3 <= 8) {
        constexpr int sn = s + 3;
        if constexpr (sn % 3 == 0) g1_wsetup(wst, (unit0 + sn / 3) * 64, wid, lane, wfold, wqkv, w1);
        g1_wissue(wst, smem + (sn % G1_RING) * G1_BUFB, wid, lane);
      }
      g1_reads<((s + 1) % G1_RING) * G1_BUFB>(wA, ad);
      wait_lgkm<8>();
    } else {
      wait_lgkm<0>();
    }
    g1_mma<kc * 6 + 5>(wB, xa, acc);
  };
  stage(IC<0>{}); stage(IC<1>{}); stage(IC<2>{});
  const u16* bias_lds = reinterpret_cast<const u16*>(smem + G1_BOFF);
#pragma unroll
  for (int n = 0; n < 4; ++n) {
    const int tile = (unit0 + U) * 4 + n;
#pragma unroll
    for (int r = 0; r < 2; ++r)
      g1_store_tile(acc[r][n], tile, m0 + wid * 32 + r * 16 + (lane & 15), g, bias_lds + (U * 4 + n) * 16 + 4 * g, qkv, hf);
  }
}

__global__ __launch_bounds__(512, 2) void k1_gemm1(const u16* __restrict__ xnf, const u16* __restrict__ wfold,
                                                   const u16* __restrict__ wqkv, const u16* __restrict__ w1,
                                                   const u16* __restrict__ b1, u16* __restrict__ qkv,
                                                   u16* __restrict__ hf) {
  __shared__ __attribute__((aligned(16))) char smem[G1_SMEM];
  const int tid = threadIdx.x, wid = tid >> 6, lane = tid & 63;
  const int bid = blockIdx.x;
  const int mt = bid >> 5, grp = bid & 31;
  const int m0 = mt * 256;
  const int unit0 = grp * 3;
  const bool extra = grp < 12;
  const int xcol = 6144 + (extra ? grp : 0) * 16;
  STAMP(1, 0);
  G1W wst;
  g1_wsetup(wst, unit0 * 64, wid, lane, wfold, wqkv, w1);
  g1_wissue(wst, smem + 0 * G1_BUFB, wid, lane);
  g1_wissue(wst, smem + 1 * G1_BUFB, wid, lane);
  g1_wissue(wst, smem + 2 * G1_BUFB, wid, lane);
#pragma unroll
  for (int i = 0; i < 3; ++i) {
    int ins = wid + 8 * i;
    if (ins >= 18) ins -= 18;
    const int c = ins * 64 + lane;
    const int pr = c >> 1, half = c & 1;
    const int k = (pr & ~0xC) | ((pr & 4) << 1) | ((pr & 8) >> 1);
    glds16(w1 + (size_t)k * F_ + (xcol - NQKV_) + half * 8, smem + G1_XOFF + c * 16);
  }
  {
    const int e = (wid & 1) * 64 + lane;
    int col = (e < 96) ? (unit0 * 64 + 2 * e - NQKV_) : (xcol - NQKV_ + 2 * (e - 96));
    if (col < 0 || e >= 104) col = 0;
    __builtin_amdgcn_global_load_lds((const unsigned*)(b1 + col), (unsigned*)(smem + G1_BOFF + e * 4), 4, 0, 0);
  }
  bf16x8 xa[2][18];
#pragma unroll
  for (int ks = 0; ks < 18; ++ks)
#pragma unroll
    for (int r = 0; r < 2; ++r)
      gld16_asm(xa[r][ks], xnf + ((size_t)(((m0 >> 4) + wid * 2 + r) * 18 + ks) * 64 + lane) * 8);
  wait_vm<24>();
  BAR();
  WF wA[4];
  {
    unsigned ad[4][2];
    g1_addr(ad, smem, lane);
    g1_reads<0>(wA, ad);
  }
  STAMP(1, 1);
  g1_unit<0>(wst, unit0, wfold, wqkv, w1, smem, xa, m0, qkv, hf, wA);
  STAMP(1, 2);
  g1_unit<1>(wst, unit0, wfold, wqkv, w1, smem, xa, m0, qkv, hf, wA);
  STAMP(1, 3);
  g1_unit<2>(wst, unit0, wfold, wqkv, w1, smem, xa, m0, qkv, hf, wA);
  STAMP(1, 4);
  if (extra) {
    int tz = threadIdx.x;
    asm volatile("" : "+v"(tz));
    const int wid2 = __builtin_amdgcn_readfirstlane(tz >> 6), lane2 = tz & 63;
    const int g = lane2 >> 4, q = (lane2 & 15) >> 2, p = lane2 & 3;
    unsigned ad[2];
#pragma unroll
    for (int t = 0; t < 2; ++t) {
      const int k = 8 * g + 4 * t + q;
      const int pr = (k & ~0xC) | ((k & 4) << 1) | ((k & 8) >> 1);
      ad[t] = lds_addr(smem) + G1_XOFF + pr * 32 + p * 8;
    }
    f32x4 acc[2] = {(f32x4){0.f, 0.f, 0.f, 0.f}, (f32x4){0.f, 0.f, 0.f, 0.f}};
#pragma unroll
    for (int kb = 0; kb < 18; kb += 6) {
      WF w[6];
      tr_rd<0 * 1024>(w[0].lo, ad[0] + kb * 1024); tr_rd<0 * 1024>(w[0].hi, ad[1] + kb * 1024);
      tr_rd<1 * 1024>(w[1].lo, ad[0] + kb * 1024); tr_rd<1 * 1024>(w[1].hi, ad[1] + kb * 1024);
      tr_rd<2 * 1024>(w[2].lo, ad[0] + kb * 1024); tr_rd<2 * 1024>(w[2].hi, ad[1] + kb * 1024);
      tr_rd<3 * 1024>(w[3].lo, ad[0] + kb * 1024); tr_rd<3 * 1024>(w[3].hi, ad[1] + kb * 1024);
      tr_rd<4 * 1024>(w[4].lo, ad[0] + kb * 1024); tr_rd<4 * 1024>(w[4].hi, ad[1] + kb * 1024);
      tr_rd<5 * 1024>(w[5].lo, ad[0] + kb * 1024); tr_rd<5 * 1024>(w[5].hi, ad[1] + kb * 1024);
      wait_lgkm<0>();
#pragma unroll
      for (int i = 0; i < 6; ++i) pin(w[i]);
#pragma unroll
      for (int i = 0; i < 6; ++i)
#pragma unroll
        for (int r = 0; r < 2; ++r)
          acc[r] = __builtin_amdgcn_mfma_f32_16x16x32_bf16(cat(w[i]), xa[r][kb + i], acc[r], 0, 0, 0);
    }
    const u16* bias_lds = reinterpret_cast<const u16*>(smem + G1_BOFF);
#pragma unroll
    for (int r = 0; r < 2; ++r)
      g1_store_tile(acc[r], xcol / 16, m0 + wid2 * 32 + r * 16 + (lane2 & 15), g, bias_lds + 192 + 4 * g, qkv, hf);
  }
}

constexpr int G2_NT = 9, G2_RB = G2_NT * 32;
constexpr int G2_BUFB = 64 * G2_RB;
constexpr int G2_RING = 4;
constexpr int G2_STAGES = 18;

__device__ __forceinline__ void softmax_pm(const float (&pk)[18], float (&ep)[18], float (&en)[18]) {
  float mx = pk[0], mn = pk[0];
#pragma unroll
  for (int f = 1; f < 18; ++f) {
    mx = fmaxf(mx, pk[f]);
    mn = fminf(mn, pk[f]);
  }
  float sp = 0.f, sn = 0.f;
#pragma unroll
  for (int f = 0; f < 18; ++f) {
    ep[f] = __expf(pk[f] - mx);
    en[f] = __expf(mn - pk[f]);
    sp += ep[f];
    sn += en[f];
  }
  const float rp = 1.f / sp, rn = 1.f / sn;
#pragma unroll
  for (int f = 0; f < 18; ++f) {
    ep[f] *= rp;
    en[f] *= rn;
  }
}

__device__ void head_part1(int hb, const u16* __restrict__ qkv, float* __restrict__ pkv, char* smem) {
  u16* phiT = reinterpret_cast<u16*>(smem);
  u16* vT = reinterpret_cast<u16*>(smem + 25344);
  float* kvs = reinterpret_cast<float*>(smem + 25344 + 16896);
  const int h = hb >> 3, c8 = hb & 7;
  const int tid = threadIdx.x, wid = tid >> 6, lane = tid & 63;
  const int row = c8 * 256 + tid;
  u32x4 kr[3], vr[3];
  {
    const u32x4* kp = reinterpret_cast<const u32x4*>(qkv + ((size_t)(32 + h) * S_ + row) * 24);
    const u32x4* vp = reinterpret_cast<const u32x4*>(qkv + ((size_t)(64 + h) * S_ + row) * 24);
#pragma unroll
    for (int d = 0; d < 3; ++d) { kr[d] = kp[d]; vr[d] = vp[d]; }
  }
  for (int i = tid; i < 12 * 264; i += 256) phiT[36 * 264 + i] = 0;
  for (int i = tid; i < 14 * 264; i += 256) vT[18 * 264 + i] = (i < 264) ? (u16)0x3f80 : (u16)0;
  {
    float pk[18];
#pragma unroll
    for (int d = 0; d < 9; ++d) {
      pk[2 * d] = lo16(kr[d >> 2][d & 3]);
      pk[2 * d + 1] = hi16(kr[d >> 2][d & 3]);
    }
    float ep[18], en[18];
    softmax_pm(pk, ep, en);
#pragma unroll
    for (int f = 0; f < 18; ++f) {
      phiT[f * 264 + tid] = f2bf(ep[f]);
      phiT[(18 + f) * 264 + tid] = f2bf(en[f]);
      const unsigned w = vr[(f >> 1) >> 2][(f >> 1) & 3];
      vT[f * 264 + tid] = (u16)((f & 1) ? (w >> 16) : (w & 0xffff));
    }
  }
  __syncthreads();
  {
    f32x4 acc[3][2];
#pragma unroll
    for (int a = 0; a < 3; ++a)
#pragma unroll
      for (int b = 0; b < 2; ++b) acc[a][b] = (f32x4){0.f, 0.f, 0.f, 0.f};
#pragma unroll
    for (int ks = 0; ks < 2; ++ks) {
      const int s0 = wid * 64 + ks * 32 + 8 * (lane >> 4);
      bf16x8 bv[2];
#pragma unroll
      for (int b = 0; b < 2; ++b) bv[b] = *reinterpret_cast<const bf16x8*>(vT + (b * 16 + (lane & 15)) * 264 + s0);
#pragma unroll
      for (int a = 0; a < 3; ++a) {
        bf16x8 av = *reinterpret_cast<const bf16x8*>(phiT + (a * 16 + (lane & 15)) * 264 + s0);
#pragma unroll
        for (int b = 0; b < 2; ++b) acc[a][b] = __builtin_amdgcn_mfma_f32_16x16x32_bf16(av, bv[b], acc[a][b], 0, 0, 0);
      }
    }
#pragma unroll
    for (int a = 0; a < 3; ++a)
#pragma unroll
      for (int b = 0; b < 2; ++b)
#pragma unroll
        for (int j = 0; j < 4; ++j)
          kvs[(wid * 48 + a * 16 + (lane >> 4) * 4 + j) * 32 + b * 16 + (lane & 15)] = acc[a][b][j];
  }
  __syncthreads();
  float* mypart = pkv + (size_t)(h * 8 + c8) * 1536;
  for (int i = tid; i < 1536; i += 256)
    mypart[i] = kvs[i] + kvs[1536 + i] + kvs[2 * 1536 + i] + kvs[3 * 1536 + i];
}

__global__ __launch_bounds__(256) void k2b_head(const u16* __restrict__ qkv, const u16* __restrict__ wo,
                                                const float* __restrict__ pkv, u16* __restrict__ mcat,
                                                u16* __restrict__ phif) {
  __shared__ __attribute__((aligned(16))) char smem[36864 + 3072 + 256];
  const int tid = threadIdx.x, wid = tid >> 6, lane = tid & 63;
  const int bid = blockIdx.x;
  if (bid < 256) {
    float* zs = reinterpret_cast<float*>(smem);
    const int h = bid >> 3, c8 = bid & 7;
    const int row = c8 * 256 + tid;
    u32x4 qr[3];
    const u32x4* qp = reinterpret_cast<const u32x4*>(qkv + ((size_t)h * S_ + row) * 24);
#pragma unroll
    for (int d = 0; d < 3; ++d) qr[d] = qp[d];
    const float* hp = pkv + (size_t)h * 8 * 1536;
    if (tid < 36) {
      float z = 0.f;
#pragma unroll
      for (int c = 0; c < 8; ++c) z += hp[c * 1536 + tid * 32 + 18];
      zs[tid] = rbf(z);
    }
    float ep[18], en[18];
    {
      float pk[18];
#pragma unroll
      for (int d = 0; d < 9; ++d) {
        pk[2 * d] = lo16(qr[d >> 2][d & 3]);
        pk[2 * d + 1] = hi16(qr[d >> 2][d & 3]);
      }
      softmax_pm(pk, ep, en);
    }
    __syncthreads();
    float den = 0.f;
#pragma unroll
    for (int f = 0; f < 18; ++f) {
      ep[f] = rbf(ep[f]);
      en[f] = rbf(en[f]);
      den += ep[f] * zs[f] + en[f] * zs[18 + f];
    }
    const float rd = 1.f / (rbf(den) + 1e-6f);
    unsigned o[20];
#pragma unroll
    for (int f = 0; f < 9; ++f) o[f] = pack2(ep[2 * f] * rd, ep[2 * f + 1] * rd);
#pragma unroll
    for (int f = 0; f < 9; ++f) o[9 + f] = pack2(en[2 * f] * rd, en[2 * f + 1] * rd);
    o[18] = 0;
    o[19] = 0;
#pragma unroll
    for (int c = 0; c < 5; ++c) {
      const int cidx = h * 5 + c;
      u16* dst = phif + ((size_t)((row >> 4) * 40 + (cidx >> 2)) * 64 + (cidx & 3) * 16 + (row & 15)) * 8;
      u32x4 v = {o[4 * c], o[4 * c + 1], o[4 * c + 2], o[4 * c + 3]};
      *reinterpret_cast<u32x4*>(dst) = v;
    }
    return;
  }
  const int h = bid - 256;
  u16* woimg = reinterpret_cast<u16*>(smem);
  u16* kvb = reinterpret_cast<u16*>(smem + 36864);
  const float* hp = pkv + (size_t)h * 8 * 1536;
  float kvv[6];
#pragma unroll
  for (int it = 0; it < 6; ++it) {
    const int i = it * 256 + tid;
    float v = 0.f;
#pragma unroll
    for (int c = 0; c < 8; ++c) v += hp[c * 1536 + i];
    kvv[it] = v;
  }
#pragma unroll
  for (int it = 0; it < 9; ++it) {
    const int i = it * 256 + tid;
    const int r = i / 72, c = i % 72;
    u32x4 v = {0u, 0u, 0u, 0u};
    if (r < 18) v = *reinterpret_cast<const u32x4*>(wo + (size_t)(h * 18 + r) * D_ + c * 8);
    *reinterpret_cast<u32x4*>(woimg + r * 576 + c * 8) = v;
  }
#pragma unroll
  for (int it = 0; it < 6; ++it) {
    const int i = it * 256 + tid;
    const int f = i >> 5, d = i & 31;
    kvb[i] = (d < 18 && f < 36) ? f2bf(kvv[it]) : (u16)0;
  }
  __syncthreads();
  {
    const int g = lane >> 4, q = (lane & 15) >> 2, p = lane & 3;
    constexpr int RB = 576 * 2;
    const int o0 = (8 * g + q) * RB + p * 8, o1 = (8 * g + 4 + q) * RB + p * 8;
    bf16x8 kb[3];
#pragma unroll
    for (int a = 0; a < 3; ++a) kb[a] = *reinterpret_cast<const bf16x8*>(kvb + (a * 16 + (lane & 15)) * 32 + 8 * g);
    for (int nt = wid; nt < 36; nt += 4) {
      bf16x8 w = wfrag(smem, o0, o1, nt * 32);
#pragma unroll
      for (int a = 0; a < 3; ++a) {
        f32x4 c = __builtin_amdgcn_mfma_f32_16x16x32_bf16(w, kb[a], (f32x4){0.f, 0.f, 0.f, 0.f}, 0, 0, 0);
        const int f = a * 16 + (lane & 15);
        if (f < KPH_) {
          u32x2 v = {pack2(c[0], c[1]), pack2(c[2], c[3])};
          *reinterpret_cast<u32x2*>(mcat + (size_t)(h * KPH_ + f) * D_ + nt * 16 + 4 * g) = v;
        }
      }
    }
  }
}

template <int BASE, int N0, int CNT, int I = 0>
__device__ __forceinline__ void g2_wreads(WF (&w)[5], unsigned a0, unsigned a1) {
  if constexpr (I < CNT) {
    tr_rd<BASE + (N0 + I) * 32>(w[I].lo, a0);
    tr_rd<BASE + (N0 + I) * 32>(w[I].hi, a1);
    g2_wreads<BASE, N0, CNT, I + 1>(w, a0, a1);
  }
}
template <int N0, int CNT>
__device__ __forceinline__ void g2_mma(WF (&w)[5], const bf16x8 (&ac)[2], f32x4 (&acc)[2][G2_NT]) {
#pragma unroll
  for (int i = 0; i < CNT; ++i) pin(w[i]);
#pragma unroll
  for (int i = 0; i < CNT; ++i)
#pragma unroll
    for (int r = 0; r < 2; ++r)
      acc[r][N0 + i] = __builtin_amdgcn_mfma_f32_16x16x32_bf16(cat(w[i]), ac[r], acc[r][N0 + i], 0, 0, 0);
}
constexpr int K2_HEADBLK = 256;

__global__ __launch_bounds__(256, 2) void k2_gemm2(const u16* __restrict__ hf, const u16* __restrict__ w2,
                                                   float* __restrict__ part, const u16* __restrict__ qkv,
                                                   float* __restrict__ pkv) {
  __shared__ __attribute__((aligned(16))) char smem[G2_RING * G2_BUFB];
  const int tid = threadIdx.x, wid = tid >> 6, lane = tid & 63;
  int bid = blockIdx.x;
  if (bid < K2_HEADBLK) { head_part1(bid, qkv, pkv, smem); return; }
  bid -= K2_HEADBLK;
  STAMP(2, 0);
  const int xcd = bid & 7, loc = bid >> 3;
  const int sp = xcd >> 1, mt = (xcd & 1) * 8 + (loc >> 2), nt4 = loc & 3;
  const int m0 = mt * 128, n0 = nt4 * 144, k0 = sp * 1152;
  const int g = lane >> 4, q = (lane & 15) >> 2, p = lane & 3;
  const bool odd = g & 1;
  const unsigned wa0 = lds_addr(smem) + (8 * g + 4 * (g & 1) + q) * G2_RB + p * 8;
  const unsigned wa1 = lds_addr(smem) + (8 * g + 4 * (1 - (g & 1)) + q) * G2_RB + p * 8;
  f32x4 acc[2][G2_NT];
#pragma unroll
  for (int r = 0; r < 2; ++r)
#pragma unroll
    for (int n = 0; n < G2_NT; ++n) acc[r][n] = (f32x4){0.f, 0.f, 0.f, 0.f};
  const u16* wp[5];
  int wdst[5];
#pragma unroll
  for (int i = 0; i < 5; ++i) {
    int ins = wid + 4 * i;
    if (ins >= 18) ins -= 18;
    int c = ins * 64 + lane;
    int row = c / 18, cc = c % 18;
    wp[i] = w2 + (size_t)(k0 + row) * D_ + n0 + cc * 8;
    wdst[i] = c * 16;
  }
  const u16* ap[2];
#pragma unroll
  for (int r = 0; r < 2; ++r) ap[r] = hf + ((size_t)((m0 >> 4) + wid * 2 + r) * 144 + (k0 >> 5)) * 512 + lane * 8;
  bf16x8 an[4][2][2];
  auto issue = [&](auto SL) {
    constexpr int slot = decltype(SL)::value;
    char* buf = smem + slot * G2_BUFB;
#pragma unroll
    for (int i = 0; i < 5; ++i) { glds16(wp[i], buf + wdst[i]); wp[i] += 64 * D_; }
#pragma unroll
    for (int r = 0; r < 2; ++r) {
      gld16_asm(an[slot][r][0], ap[r]);
      gld16_asm(an[slot][r][1], ap[r] + 512);
      ap[r] += 1024;
    }
  };
  issue(IC<0>{});
  issue(IC<1>{});
  issue(IC<2>{});
  wait_vm<18>();
  BAR();
  STAMP(2, 1);
  WF wA[5], wB[5];
  g2_wreads<0, 0, 5>(wA, wa0, wa1);
  auto stage = [&](auto U, auto LAST) {
    constexpr int u = decltype(U)::value;
    constexpr int rem = decltype(LAST)::value;
    constexpr int BASE = u * G2_BUFB;
    bf16x8 c0[2], c1[2];
#pragma unroll
    for (int r = 0; r < 2; ++r) {
      pin(an[u][r][0]); pin(an[u][r][1]);
      c0[r] = swap_halves_if(an[u][r][0], odd);
      c1[r] = swap_halves_if(an[u][r][1], odd);
    }
    g2_wreads<BASE, 5, 4>(wB, wa0, wa1);
    wait_lgkm<8>();
    g2_mma<0, 5>(wA, c0, acc);
    g2_wreads<BASE + 32 * G2_RB, 0, 5>(wA, wa0, wa1);
    wait_lgkm<10>();
    g2_mma<5, 4>(wB, c0, acc);
    g2_wreads<BASE + 32 * G2_RB, 5, 4>(wB, wa0, wa1);
    wait_lgkm<8>();
    g2_mma<0, 5>(wA, c1, acc);
    if constexpr (rem >= 1) {
      if constexpr (rem >= 2) wait_vm<9>(); else wait_vm<0>();
      BAR();
      if constexpr (rem >= 3) issue(IC<(u + 3) & 3>{});
      g2_wreads<((u + 1) & 3) * G2_BUFB, 0, 5>(wA, wa0, wa1);
      wait_lgkm<10>();
    } else {
      wait_lgkm<0>();
    }
    g2_mma<5, 4>(wB, c1, acc);
  };
#pragma unroll 1
  for (int it = 0; it < 3; ++it) {
    stage(IC<0>{}, IC<3>{}); stage(IC<1>{}, IC<3>{}); stage(IC<2>{}, IC<3>{}); stage(IC<3>{}, IC<3>{});
  }
  STAMP(2, 2);
  stage(IC<0>{}, IC<3>{}); stage(IC<1>{}, IC<3>{}); stage(IC<2>{}, IC<3>{});
  stage(IC<3>{}, IC<2>{}); stage(IC<0>{}, IC<1>{}); stage(IC<1>{}, IC<0>{});
  STAMP(2, 3);
#pragma unroll
  for (int r = 0; r < 2; ++r) {
    const int m = m0 + wid * 32 + r * 16 + (lane & 15);
    float* pp = part + ((size_t)sp * S_ + m) * D_ + n0 + 4 * g;
#pragma unroll
    for (int n = 0; n < G2_NT; ++n) *reinterpret_cast<f32x4*>(pp + n * 16) = acc[r][n];
  }
  asm volatile("s_waitcnt vmcnt(0)" ::: "memory");
  STAMP(2, 4);
}

constexpr int G3_BUFB = 128 * 128;
constexpr int G3_RING = 3;

__global__ __launch_bounds__(256, 2) void k3_final(const u16* __restrict__ phif, const u16* __restrict__ mcat,
                                                   const float* __restrict__ part, const u16* __restrict__ b2,
                                                   const u16* __restrict__ x, u16* __restrict__ out) {
  __shared__ __attribute__((aligned(16))) char smem[G3_RING * G3_BUFB];
  const int tid = threadIdx.x, wid = tid >> 6, lane = tid & 63;
  const int bid = blockIdx.x;
  const int xcd = bid & 7, loc = bid >> 3;
  const int mt = xcd * 4 + loc / 9, nt = loc % 9;
  const int m0 = mt * 64, n0 = nt * 64;
  const int g = lane >> 4, q = (lane & 15) >> 2, p = lane & 3;
  const int wr = wid >> 1, wc = wid & 1;
  STAMP(3, 0);
  const u16* wp[4];
#pragma unroll
  for (int i = 0; i < 4; ++i) {
    const int row = (wid * 4 + i) * 8 + (lane >> 3);
    const int f = ((row >> 1) & 1) | (((row >> 3) & 1) << 1);
    const int ch = (lane & 7) ^ (f << 1);
    wp[i] = mcat + (size_t)row * D_ + n0 + ch * 8;
  }
  const u16* ap[2];
#pragma unroll
  for (int r = 0; r < 2; ++r) ap[r] = phif + ((size_t)((m0 >> 4) + wr * 2 + r) * 40) * 512 + lane * 8;
  bf16x8 an[3][2][4];
  auto issue = [&](auto SL) {
    constexpr int slot = decltype(SL)::value;
    char* buf = smem + slot * G3_BUFB;
#pragma unroll
    for (int i = 0; i < 4; ++i) { glds16(wp[i], buf + (wid * 4 + i) * 1024 + lane * 16); wp[i] += 128 * D_; }
#pragma unroll
    for (int r = 0; r < 2; ++r) {
#pragma unroll
      for (int ks = 0; ks < 4; ++ks) gld16_asm(an[slot][r][ks], ap[r] + ks * 512);
      ap[r] += 2048;
    }
  };
  issue(IC<0>{});
  issue(IC<1>{});
  bf16x8 py[2][2][4];
  u32x2 xx[2][2], bb[2];
#pragma unroll
  for (int r = 0; r < 2; ++r) {
    const int m = m0 + wr * 32 + r * 16 + (lane & 15);
#pragma unroll
    for (int n = 0; n < 2; ++n) {
      const int ncol = n0 + wc * 32 + n * 16 + 4 * g;
#pragma unroll
      for (int sI = 0; sI < SPLITK_; ++sI) gld16_asm(py[r][n][sI], part + ((size_t)sI * S_ + m) * D_ + ncol);
      asm volatile("global_load_dwordx2 %0, %1, off" : "=v"(xx[r][n]) : "v"(x + (size_t)m * D_ + ncol) : "memory");
    }
  }
#pragma unroll
  for (int n = 0; n < 2; ++n)
    asm volatile("global_load_dwordx2 %0, %1, off" : "=v"(bb[n]) : "v"(b2 + n0 + wc * 32 + n * 16 + 4 * g) : "memory");
  unsigned ad[2][2];
#pragma unroll
  for (int t = 0; t < 2; ++t) {
    const int row = 8 * g + 4 * t + q;
    const int f = ((row >> 1) & 1) | (((row >> 3) & 1) << 1);
#pragma unroll
    for (int n = 0; n < 2; ++n) ad[n][t] = lds_addr(smem) + row * 128 + (((wc * 2 + n) ^ f) * 32) + p * 8;
  }
  f32x4 acc[2][2];
#pragma unroll
  for (int r = 0; r < 2; ++r)
#pragma unroll
    for (int n = 0; n < 2; ++n) acc[r][n] = (f32x4){0.f, 0.f, 0.f, 0.f};
  STAMP(3, 1);
  auto stage = [&](auto U, auto WAITN, auto MORE) {
    constexpr int u = decltype(U)::value;
    wait_vm<decltype(WAITN)::value>();
    BAR();
    if constexpr (decltype(MORE)::value) issue(IC<(u + 2) % 3>{});
    constexpr int BASE = u * G3_BUFB;
    WF w[4][2];
#pragma unroll
    for (int n = 0; n < 2; ++n) { tr_rd<BASE + 0 * 4096>(w[0][n].lo, ad[n][0]); tr_rd<BASE + 0 * 4096>(w[0][n].hi, ad[n][1]); }
#pragma unroll
    for (int n = 0; n < 2; ++n) { tr_rd<BASE + 1 * 4096>(w[1][n].lo, ad[n][0]); tr_rd<BASE + 1 * 4096>(w[1][n].hi, ad[n][1]); }
#pragma unroll
    for (int n = 0; n < 2; ++n) { tr_rd<BASE + 2 * 4096>(w[2][n].lo, ad[n][0]); tr_rd<BASE + 2 * 4096>(w[2][n].hi, ad[n][1]); }
#pragma unroll
    for (int n = 0; n < 2; ++n) { tr_rd<BASE + 3 * 4096>(w[3][n].lo, ad[n][0]); tr_rd<BASE + 3 * 4096>(w[3][n].hi, ad[n][1]); }
#pragma unroll
    for (int r = 0; r < 2; ++r)
#pragma unroll
      for (int ks = 0; ks < 4; ++ks) pin(an[u][r][ks]);
    wait_lgkm<8>();
#pragma unroll
    for (int ks = 0; ks < 2; ++ks)
#pragma unroll
      for (int n = 0; n < 2; ++n) pin(w[ks][n]);
#pragma unroll
    for (int ks = 0; ks < 2; ++ks)
#pragma unroll
      for (int n = 0; n < 2; ++n)
#pragma unroll
        for (int r = 0; r < 2; ++r)
          acc[r][n] = __builtin_amdgcn_mfma_f32_16x16x32_bf16(cat(w[ks][n]), an[u][r][ks], acc[r][n], 0, 0, 0);
    wait_lgkm<0>();
#pragma unroll
    for (int ks = 2; ks < 4; ++ks)
#pragma unroll
      for (int n = 0; n < 2; ++n) pin(w[ks][n]);
#pragma unroll
    for (int ks = 2; ks < 4; ++ks)
#pragma unroll
      for (int n = 0; n < 2; ++n)
#pragma unroll
        for (int r = 0; r < 2; ++r)
          acc[r][n] = __builtin_amdgcn_mfma_f32_16x16x32_bf16(cat(w[ks][n]), an[u][r][ks], acc[r][n], 0, 0, 0);
  };
  stage(IC<0>{}, IC<34>{}, IC<1>{});
  stage(IC<1>{}, IC<34>{}, IC<1>{});
  stage(IC<2>{}, IC<12>{}, IC<1>{});
  stage(IC<0>{}, IC<12>{}, IC<1>{});
  stage(IC<1>{}, IC<12>{}, IC<1>{});
  stage(IC<2>{}, IC<12>{}, IC<1>{});
  stage(IC<0>{}, IC<12>{}, IC<1>{});
  stage(IC<1>{}, IC<12>{}, IC<1>{});
  stage(IC<2>{}, IC<12>{}, IC<0>{});
  stage(IC<0>{}, IC<0>{}, IC<0>{});
  STAMP(3, 3);
#pragma unroll
  for (int n = 0; n < 2; ++n) asm volatile("" : "+v"(bb[n]));
#pragma unroll
  for (int r = 0; r < 2; ++r) {
    const int m = m0 + wr * 32 + r * 16 + (lane & 15);
#pragma unroll
    for (int n = 0; n < 2; ++n) {
      const int ncol = n0 + wc * 32 + n * 16 + 4 * g;
      asm volatile("" : "+v"(xx[r][n]));
#pragma unroll
      for (int sI = 0; sI < SPLITK_; ++sI) pin(py[r][n][sI]);
      f32x4 y = *reinterpret_cast<f32x4*>(&py[r][n][0]);
#pragma unroll
      for (int sI = 1; sI < SPLITK_; ++sI) y += *reinterpret_cast<f32x4*>(&py[r][n][sI]);
      float bv[4] = {lo16(bb[n][0]), hi16(bb[n][0]), lo16(bb[n][1]), hi16(bb[n][1])};
      float xv[4] = {lo16(xx[r][n][0]), hi16(xx[r][n][0]), lo16(xx[r][n][1]), hi16(xx[r][n][1])};
      float o[4];
#pragma unroll
      for (int j = 0; j < 4; ++j) {
        float yy = y[j] + bv[j];
        float gg = yy / (1.f + __expf(-yy));
        o[j] = xv[j] + acc[r][n][j] * gg;
      }
      u32x2 v = {pack2(o[0], o[1]), pack2(o[2], o[3])};
      *reinterpret_cast<u32x2*>(out + (size_t)m * D_ + ncol) = v;
    }
  }
  STAMP(3, 4);
}

extern "C" void kernel_launch(void* const* d_in, const int* in_sizes, int n_in, void* d_out, int out_size, void* d_ws,
                              size_t ws_size, hipStream_t stream) {
  const u16* x = (const u16*)d_in[0];
  const u16* wqkv = (const u16*)d_in[1];
  const u16* wphi = (const u16*)d_in[2];
  const u16* wo = (const u16*)d_in[3];
  const u16* w1 = (const u16*)d_in[5];
  const u16* b1 = (const u16*)d_in[6];
  const u16* w2 = (const u16*)d_in[7];
  const u16* b2 = (const u16*)d_in[8];
  u16* out = (u16*)d_out;
  char* ws = (char*)d_ws;
  size_t off = 0;
  u16* xnf = (u16*)(ws + off); off += (size_t)S_ * D_ * 2;
  u16* wfold = (u16*)(ws + off); off += (size_t)D_ * NPH_ * 2;
  u16* qkv = (u16*)(ws + off); off += (size_t)96 * S_ * 24 * 2;
  u16* hf = (u16*)(ws + off); off += (size_t)S_ * F_ * 2;
  float* part = (float*)(ws + off); off += (size_t)SPLITK_ * S_ * D_ * 4;
  u16* phif = (u16*)(ws + off); off += (size_t)S_ * KPHI_ * 2;
  u16* mcat = (u16*)(ws + off); off += (size_t)KPHI_ * D_ * 2;
  float* pkv = (float*)(ws + off); off += (size_t)256 * 1536 * 4;

  k0_prep<<<512 + 192, 256, 0, stream>>>(x, wqkv, wphi, xnf, wfold);
  k1_gemm1<<<256, 512, 0, stream>>>(xnf, wfold, wqkv, w1, b1, qkv, hf);
  k2_gemm2<<<K2_HEADBLK + 256, 256, 0, stream>>>(hf, w2, part, qkv, pkv);
  k2b_head<<<256 + 32, 256, 0, stream>>>(qkv, wo, pkv, mcat, phif);
  k3_final<<<32 * 9, 256, 0, stream>>>(phif, mcat, part, b2, x, out);
}
```

```cpp
#include <hip/hip_runtime.h>
#include <hip/hip_bf16.h>
#include <stdint.h>

constexpr int S_ = 2048, D_ = 576, H_ = 32, DH_ = 18, F_ = 4608, NQKV_ = 1728;
constexpr int NPH_ = 1152;
constexpr int KPH_ = 40;
constexpr int KPHI_ = H_ * KPH_;
constexpr int SPLITK_ = 4;

typedef short bf16x8 __attribute__((ext_vector_type(8)));
typedef short s16x4 __attribute__((ext_vector_type(4)));
typedef float f32x4 __attribute__((ext_vector_type(4)));
typedef unsigned u32x4 __attribute__((ext_vector_type(4)));
typedef unsigned u32x2 __attribute__((ext_vector_type(2)));
typedef __attribute__((address_space(3))) s16x4* lds_s16x4_p;
typedef unsigned short u16;
template <int V> struct IC { static constexpr int value = V; };

#define STAMP(kid, i) do { } while (0)
#define WAIT_LGKM0() asm volatile("s_waitcnt lgkmcnt(0)" ::: "memory")
#define BAR() __builtin_amdgcn_s_barrier()

template <int N>
__device__ __forceinline__ void wait_vm() {
  static_assert(N >= 0 && N < 64, "vmcnt range");
  asm volatile("s_waitcnt vmcnt(%0)" ::"n"(N) : "memory");
}

__device__ __forceinline__ float bf2f(u16 v) { return __uint_as_float(((unsigned)v) << 16); }
__device__ __forceinline__ u16 f2bf(float f) {
  __hip_bfloat16 b = __float2bfloat16(f);
  return *reinterpret_cast<u16*>(&b);
}
__device__ __forceinline__ unsigned pack2(float lo, float hi) {
  return (unsigned)f2bf(lo) | ((unsigned)f2bf(hi) << 16);
}
__device__ __forceinline__ float rbf(float f) { return bf2f(f2bf(f)); }
__device__ __forceinline__ float lo16(unsigned u) { return __uint_as_float(u << 16); }
__device__ __forceinline__ float hi16(unsigned u) { return __uint_as_float(u & 0xffff0000u); }

__device__ __forceinline__ void glds16(const void* g, void* l) {
  __builtin_amdgcn_global_load_lds((const unsigned*)g, (unsigned*)l, 16, 0, 0);
}

__device__ __forceinline__ bf16x8 swap_halves_if(bf16x8 v, bool odd) {
  bf16x8 s = __builtin_shufflevector(v, v, 4, 5, 6, 7, 0, 1, 2, 3);
  return odd ? s : v;
}

__device__ __forceinline__ bf16x8 wfrag(const char* img, int o0, int o1, int off) {
  s16x4 a = __builtin_amdgcn_ds_read_tr16_b64_v4i16((lds_s16x4_p)(img + o0 + off));
  s16x4 b = __builtin_amdgcn_ds_read_tr16_b64_v4i16((lds_s16x4_p)(img + o1 + off));
  return __builtin_shufflevector(a, b, 0, 1, 2, 3, 4, 5, 6, 7);
}

struct WF { s16x4 lo, hi; };
template <int OFF>
__device__ __forceinline__ void tr_rd(s16x4& r, unsigned addr) {
  asm volatile("ds_read_b64_tr_b16 %0, %1 offset:%2" : "=v"(r) : "v"(addr), "n"(OFF) : "memory");
}
__device__ __forceinline__ void gld16_asm(bf16x8& r, const void* p) {
  asm volatile("global_load_dwordx4 %0, %1, off" : "=v"(r) : "v"(p) : "memory");
}
template <int N>
__device__ __forceinline__ void wait_lgkm() {
  static_assert(N >= 0 && N <= 15, "lgkmcnt range");
  asm volatile("s_waitcnt lgkmcnt(%0)" ::"n"(N) : "memory");
}
__device__ __forceinline__ void pin(WF& w) { asm volatile("" : "+v"(w.lo), "+v"(w.hi)); }
__device__ __forceinline__ void pin(bf16x8& w) { asm volatile("" : "+v"(w)); }
__device__ __forceinline__ bf16x8 cat(const WF& w) { return __builtin_shufflevector(w.lo, w.hi, 0, 1, 2, 3, 4, 5, 6, 7); }
__device__ __forceinline__ unsigned lds_addr(const void* p) {
  return (unsigned)(uintptr_t)(__attribute__((address_space(3))) const char*)p;
}

__global__ __launch_bounds__(256) void k0_prep(const u16* __restrict__ x, const u16* __restrict__ wqkv,
                                               const u16* __restrict__ wphi, u16* __restrict__ xnf,
                                               u16* __restrict__ wfold, unsigned* __restrict__ cnt) {
  __shared__ __attribute__((aligned(16))) float wph[18 * 20];
  const int tid = threadIdx.x, wid = tid >> 6, lane = tid & 63;
  const int bid = blockIdx.x;
  if (bid == 512 && tid < 32) cnt[tid * 32] = 0u;
  if (bid < 512) {
    const int row = bid * 4 + wid;
    const u16* xp = x + (size_t)row * D_;
    u32x4 a = *reinterpret_cast<const u32x4*>(xp + lane * 8);
    u32x4 b = {0u, 0u, 0u, 0u};
    if (lane < 8) b = *reinterpret_cast<const u32x4*>(xp + 512 + lane * 8);
    float v[16];
#pragma unroll
    for (int j = 0; j < 4; ++j) {
      v[2 * j] = lo16(a[j]); v[2 * j + 1] = hi16(a[j]);
      v[8 + 2 * j] = lo16(b[j]); v[8 + 2 * j + 1] = hi16(b[j]);
    }
    float s = 0.f;
#pragma unroll
    for (int j = 0; j < 16; ++j) s += v[j];
#pragma unroll
    for (int o = 32; o >= 1; o >>= 1) s += __shfl_xor(s, o);
    const float mean = s * (1.f / D_);
    float ss = 0.f;
#pragma unroll
    for (int j = 0; j < 8; ++j) { float d = v[j] - mean; ss += d * d; }
    if (lane < 8) {
#pragma unroll
      for (int j = 8; j < 16; ++j) { float d = v[j] - mean; ss += d * d; }
    }
#pragma unroll
    for (int o = 32; o >= 1; o >>= 1) ss += __shfl_xor(ss, o);
    const float rstd = rsqrtf(ss * (1.f / D_) + 1e-5f);
    u32x4 oa, ob;
#pragma unroll
    for (int j = 0; j < 4; ++j) {
      oa[j] = pack2((v[2 * j] - mean) * rstd, (v[2 * j + 1] - mean) * rstd);
      ob[j] = pack2((v[8 + 2 * j] - mean) * rstd, (v[8 + 2 * j + 1] - mean) * rstd);
    }
    {
      const int c = lane;
      u16* dst = xnf + ((size_t)((row >> 4) * 18 + (c >> 2)) * 64 + (c & 3) * 16 + (row & 15)) * 8;
      *reinterpret_cast<u32x4*>(dst) = oa;
    }
    if (lane < 8) {
      const int c = 64 + lane;
      u16* dst = xnf + ((size_t)((row >> 4) * 18 + (c >> 2)) * 64 + (c & 3) * 16 + (row & 15)) * 8;
      *reinterpret_cast<u32x4*>(dst) = ob;
    }
    return;
  }
  {
    const int fb = bid - 512;
    const int hh = fb / 3, k = (fb % 3) * 192 + tid;
    const int h = hh & 31;
    for (int i = tid; i < 18 * 20; i += 256) {
      const int d = i / 20, f = i % 20;
      wph[i] = (f < 18) ? bf2f(wphi[h * 324 + d * 18 + f]) : 0.f;
    }
    unsigned wv[9];
    if (tid < 192) {
      const u16* wp = wqkv + (size_t)k * NQKV_ + hh * 18;
#pragma unroll
      for (int d = 0; d < 9; ++d) wv[d] = *reinterpret_cast<const unsigned*>(wp + 2 * d);
    }
    __syncthreads();
    if (tid < 192) {
      float acc[20];
#pragma unroll
      for (int f = 0; f < 20; ++f) acc[f] = 0.f;
#pragma unroll
      for (int d = 0; d < 18; ++d) {
        const float w = (d & 1) ? hi16(wv[d >> 1]) : lo16(wv[d >> 1]);
#pragma unroll
        for (int f4 = 0; f4 < 5; ++f4) {
          f32x4 pw = *reinterpret_cast<const f32x4*>(wph + d * 20 + f4 * 4);
#pragma unroll
          for (int j = 0; j < 4; ++j) acc[f4 * 4 + j] += w * pw[j];
        }
      }
      u16* op = wfold + (size_t)k * NPH_ + hh * 18;
#pragma unroll
      for (int f = 0; f < 9; ++f) *reinterpret_cast<unsigned*>(op + 2 * f) = pack2(acc[2 * f], acc[2 * f + 1]);
    }
  }
}

constexpr int G1_BUFB = 192 * 128;
constexpr int G1_RING = 4;
constexpr int G1_XOFF = G1_RING * G1_BUFB;
constexpr int G1_BOFF = G1_XOFF + 576 * 32;
constexpr int G1_SMEM = G1_BOFF + 1024;
constexpr int G1_NST = 8;

template <int BASE, int I = 0>
__device__ __forceinline__ void g1_reads(WF (&w)[4], const unsigned (&ad)[4][2]) {
  if constexpr (I < 4) {
    if constexpr (BASE >= 49152) {
      tr_rd<BASE - 49152>(w[I].lo, ad[I][0] + 49152u);
      tr_rd<BASE - 49152>(w[I].hi, ad[I][1] + 49152u);
    } else {
      tr_rd<BASE>(w[I].lo, ad[I][0]);
      tr_rd<BASE>(w[I].hi, ad[I][1]);
    }
    g1_reads<BASE, I + 1>(w, ad);
  }
}
template <int KS>
__device__ __forceinline__ void g1_mma(WF (&w)[4], const bf16x8 (&xa)[2][18], f32x4 (&acc)[2][4]) {
#pragma unroll
  for (int i = 0; i < 4; ++i) pin(w[i]);
#pragma unroll
  for (int i = 0; i < 4; ++i)
#pragma unroll
    for (int r = 0; r < 2; ++r)
      acc[r][i] = __builtin_amdgcn_mfma_f32_16x16x32_bf16(cat(w[i]), xa[r][KS], acc[r][i], 0, 0, 0);
}

__device__ __forceinline__ float gelu_f(float v) {
  constexpr float c1 = -2.f * 0.7978845608028654f * 1.4426950408889634f;
  constexpr float c3 = c1 * 0.044715f;
  const float t = v * v;
  const float e = __builtin_amdgcn_exp2f(v * fmaf(t, c3, c1));
  return v * __builtin_amdgcn_rcpf(1.f + e);
}

__device__ __forceinline__ void g1_store_tile(const f32x4& a, int tile, int mrow, int g, const u16* bias4,
                                              u16* __restrict__ qkv, u16* __restrict__ hf) {
  if (tile < NQKV_ / 16) {
    const int col = tile * 16 + 4 * g;
#pragma unroll
    for (int j = 0; j < 2; ++j) {
      const int c = col + 2 * j;
      const int hh = c / 18, f = c - hh * 18;
      *reinterpret_cast<unsigned*>(qkv + ((size_t)hh * S_ + mrow) * 24 + f) = pack2(a[2 * j], a[2 * j + 1]);
    }
  } else {
    u32x2 bb = *reinterpret_cast<const u32x2*>(bias4);
    float o[4];
    o[0] = gelu_f(a[0] + lo16(bb[0]));
    o[1] = gelu_f(a[1] + hi16(bb[0]));
    o[2] = gelu_f(a[2] + lo16(bb[1]));
    o[3] = gelu_f(a[3] + hi16(bb[1]));
    u32x2 v = {pack2(o[0], o[1]), pack2(o[2], o[3])};
    const int T = tile - NQKV_ / 16;
    u16* dst = hf + ((size_t)((mrow >> 4) * 144 + (T >> 1)) * 64 + ((T & 1) * 2 + (g >> 1)) * 16 + (mrow & 15)) * 8 + (g & 1) * 4;
    *reinterpret_cast<u32x2*>(dst) = v;
  }
}

__device__ __forceinline__ void g1_addr(unsigned (&ad)[4][2], char* smem, int lane) {
  const int g = lane >> 4, q = (lane & 15) >> 2, p = lane & 3;
#pragma unroll
  for (int t = 0; t < 2; ++t) {
    const int row = 8 * g + 4 * t + q;
    const int f = ((row >> 1) & 1) | (((row >> 3) & 1) << 1);
#pragma unroll
    for (int n = 0; n < 4; ++n) ad[n][t] = lds_addr(smem) + row * 128 + ((n ^ f) * 32) + p * 8;
  }
}

struct G1W { const u16* src; int stride; };

__device__ __forceinline__ void g1_wsetup(G1W& w, int col, int wid, int lane, const u16* wfold, const u16* wqkv, const u16* w1) {
  const int row = wid * 8 + (lane >> 3);
  const int f = ((row >> 1) & 1) | (((row >> 3) & 1) << 1);
  const int ch = (lane & 7) ^ (f << 1);
  if (col < NPH_) { w.src = wfold + (size_t)row * NPH_ + col + ch * 8; w.stride = 64 * NPH_; }
  else if (col < NQKV_) { w.src = wqkv + (size_t)row * NQKV_ + col + ch * 8; w.stride = 64 * NQKV_; }
  else { w.src = w1 + (size_t)row * F_ + (col - NQKV_) + ch * 8; w.stride = 64 * F_; }
}
__device__ __forceinline__ void g1_wissue(G1W& w, char* buf, int wid, int lane) {
#pragma unroll
  for (int i = 0; i < 3; ++i) {
    glds16(w.src, buf + i * 8192 + wid * 1024 + lane * 16);
    w.src += w.stride;
  }
}

template <int U>
__device__ __forceinline__ void g1_unit(G1W& wst, int unit0, const u16* wfold, const u16* wqkv,
                                        const u16* w1, char* smem, bf16x8 (&xa)[2][18], int m0,
                                        u16* __restrict__ qkv, u16* __restrict__ hf, WF (&wA)[4]) {
  int tz = threadIdx.x;
  asm volatile("" : "+v"(tz));
  const int wid = __builtin_amdgcn_readfirstlane(tz >> 6), lane = tz & 63;
  const int g = lane >> 4;
  unsigned ad[4][2];
  g1_addr(ad, smem, lane);
  f32x4 acc[2][4];
#pragma unroll
  for (int r = 0; r < 2; ++r)
#pragma unroll
    for (int n = 0; n < 4; ++n) acc[r][n] = (f32x4){0.f, 0.f, 0.f, 0.f};

  auto stage = [&](auto KC) {
    constexpr int kc = decltype(KC)::value;
    constexpr int s = U * 3 + kc;
    constexpr int CUR = (s % G1_RING) * G1_BUFB;
    if constexpr (U == 0) {
#pragma unroll
      for (int k = 0; k < 6; ++k) { pin(xa[0][kc * 6 + k]); pin(xa[1][kc * 6 + k]); }
    }
    WF wB[4];
    g1_reads<CUR + 1 * 4096>(wB, ad); wait_lgkm<8>(); g1_mma<kc * 6 + 0>(wA, xa, acc);
    g1_reads<CUR + 2 * 4096>(wA, ad); wait_lgkm<8>(); g1_mma<kc * 6 + 1>(wB, xa, acc);
    g1_reads<CUR + 3 * 4096>(wB, ad); wait_lgkm<8>(); g1_mma<kc * 6 + 2>(wA, xa, acc);
    g1_reads<CUR + 4 * 4096>(wA, ad); wait_lgkm<8>(); g1_mma<kc * 6 + 3>(wB, xa, acc);
    g1_reads<CUR + 5 * 4096>(wB, ad); wait_lgkm<8>(); g1_mma<kc * 6 + 4>(wA, xa, acc);
    if constexpr (s < 8) {
      constexpr int nl = (s == 0) ? 12 : ((s + 2 <= 8) ? 3 : 0);
      constexpr int nst = (U > 0 && kc <= 1) ? G1_NST : 0;
      wait_vm<nl + nst>();
      BAR();
      if constexpr (s + 3 <= 8) {
        constexpr int sn = s + 3;
        if constexpr (sn % 3 == 0) g1_wsetup(wst, (unit0 + sn / 3) * 64, wid, lane, wfold, wqkv, w1);
        g1_wissue(wst, smem + (sn % G1_RING) * G1_BUFB, wid, lane);
      }
      g1_reads<((s + 1) % G1_RING) * G1_BUFB>(wA, ad);
      wait_lgkm<8>();
    } else {
      wait_lgkm<0>();
    }
    g1_mma<kc * 6 + 5>(wB, xa, acc);
  };
  stage(IC<0>{}); stage(IC<1>{}); stage(IC<2>{});
  const u16* bias_lds = reinterpret_cast<const u16*>(smem + G1_BOFF);
#pragma unroll
  for (int n = 0; n < 4; ++n) {
    const int tile = (unit0 + U) * 4 + n;
#pragma unroll
    for (int r = 0; r < 2; ++r)
      g1_store_tile(acc[r][n], tile, m0 + wid * 32 + r * 16 + (lane & 15), g, bias_lds + (U * 4 + n) * 16 + 4 * g, qkv, hf);
  }
}

__global__ __launch_bounds__(512, 2) void k1_gemm1(const u16* __restrict__ xnf, const u16* __restrict__ wfold,
                                                   const u16* __restrict__ wqkv, const u16* __restrict__ w1,
                                                   const u16* __restrict__ b1, u16* __restrict__ qkv,
                                                   u16* __restrict__ hf) {
  __shared__ __attribute__((aligned(16))) char smem[G1_SMEM];
  const int tid = threadIdx.x, wid = tid >> 6, lane = tid & 63;
  const int bid = blockIdx.x;
  const int mt = bid >> 5, grp = bid & 31;
  const int m0 = mt * 256;
  const int unit0 = grp * 3;
  const bool extra = grp < 12;
  const int xcol = 6144 + (extra ? grp : 0) * 16;
  STAMP(1, 0);
  G1W wst;
  g1_wsetup(wst, unit0 * 64, wid, lane, wfold, wqkv, w1);
  g1_wissue(wst, smem + 0 * G1_BUFB, wid, lane);
  g1_wissue(wst, smem + 1 * G1_BUFB, wid, lane);
  g1_wissue(wst, smem + 2 * G1_BUFB, wid, lane);
#pragma unroll
  for (int i = 0; i < 3; ++i) {
    int ins = wid + 8 * i;
    if (ins >= 18) ins -= 18;
    const int c = ins * 64 + lane;
    const int pr = c >> 1, half = c & 1;
    const int k = (pr & ~0xC) | ((pr & 4) << 1) | ((pr & 8) >> 1);
    glds16(w1 + (size_t)k * F_ + (xcol - NQKV_) + half * 8, smem + G1_XOFF + c * 16);
  }
  {
    const int e = (wid & 1) * 64 + lane;
    int col = (e < 96) ? (unit0 * 64 + 2 * e - NQKV_) : (xcol - NQKV_ + 2 * (e - 96));
    if (col < 0 || e >= 104) col = 0;
    __builtin_amdgcn_global_load_lds((const unsigned*)(b1 + col), (unsigned*)(smem + G1_BOFF + e * 4), 4, 0, 0);
  }
  bf16x8 xa[2][18];
#pragma unroll
  for (int ks = 0; ks < 18; ++ks)
#pragma unroll
    for (int r = 0; r < 2; ++r)
      gld16_asm(xa[r][ks], xnf + ((size_t)(((m0 >> 4) + wid * 2 + r) * 18 + ks) * 64 + lane) * 8);
  wait_vm<24>();
  BAR();
  WF wA[4];
  {
    unsigned ad[4][2];
    g1_addr(ad, smem, lane);
    g1_reads<0>(wA, ad);
  }
  STAMP(1, 1);
  g1_unit<0>(wst, unit0, wfold, wqkv, w1, smem, xa, m0, qkv, hf, wA);
  STAMP(1, 2);
  g1_unit<1>(wst, unit0, wfold, wqkv, w1, smem, xa, m0, qkv, hf, wA);
  STAMP(1, 3);
  g1_unit<2>(wst, unit0, wfold, wqkv, w1, smem, xa, m0, qkv, hf, wA);
  STAMP(1, 4);
  if (extra) {
    int tz = threadIdx.x;
    asm volatile("" : "+v"(tz));
    const int wid2 = __builtin_amdgcn_readfirstlane(tz >> 6), lane2 = tz & 63;
    const int g = lane2 >> 4, q = (lane2 & 15) >> 2, p = lane2 & 3;
    unsigned ad[2];
#pragma unroll
    for (int t = 0; t < 2; ++t) {
      const int k = 8 * g + 4 * t + q;
      const int pr = (k & ~0xC) | ((k & 4) << 1) | ((k & 8) >> 1);
      ad[t] = lds_addr(smem) + G1_XOFF + pr * 32 + p * 8;
    }
    f32x4 acc[2] = {(f32x4){0.f, 0.f, 0.f, 0.f}, (f32x4){0.f, 0.f, 0.f, 0.f}};
#pragma unroll
    for (int kb = 0; kb < 18; kb += 6) {
      WF w[6];
      tr_rd<0 * 1024>(w[0].lo, ad[0] + kb * 1024); tr_rd<0 * 1024>(w[0].hi, ad[1] + kb * 1024);
      tr_rd<1 * 1024>(w[1].lo, ad[0] + kb * 1024); tr_rd<1 * 1024>(w[1].hi, ad[1] + kb * 1024);
      tr_rd<2 * 1024>(w[2].lo, ad[0] + kb * 1024); tr_rd<2 * 1024>(w[2].hi, ad[1] + kb * 1024);
      tr_rd<3 * 1024>(w[3].lo, ad[0] + kb * 1024); tr_rd<3 * 1024>(w[3].hi, ad[1] + kb * 1024);
      tr_rd<4 * 1024>(w[4].lo, ad[0] + kb * 1024); tr_rd<4 * 1024>(w[4].hi, ad[1] + kb * 1024);
      tr_rd<5 * 1024>(w[5].lo, ad[0] + kb * 1024); tr_rd<5 * 1024>(w[5].hi, ad[1] + kb * 1024);
      wait_lgkm<0>();
#pragma unroll
      for (int i = 0; i < 6; ++i) pin(w[i]);
#pragma unroll
      for (int i = 0; i < 6; ++i)
#pragma unroll
        for (int r = 0; r < 2; ++r)
          acc[r] = __builtin_amdgcn_mfma_f32_16x16x32_bf16(cat(w[i]), xa[r][kb + i], acc[r], 0, 0, 0);
    }
    const u16* bias_lds = reinterpret_cast<const u16*>(smem + G1_BOFF);
#pragma unroll
    for (int r = 0; r < 2; ++r)
      g1_store_tile(acc[r], xcol / 16, m0 + wid2 * 32 + r * 16 + (lane2 & 15), g, bias_lds + 192 + 4 * g, qkv, hf);
  }
}

constexpr int G2_NT = 9, G2_RB = G2_NT * 32;
constexpr int G2_BUFB = 64 * G2_RB;
constexpr int G2_RING = 4;
constexpr int G2_STAGES = 18;

__device__ __forceinline__ void softmax_pm(const float (&pk)[18], float (&ep)[18], float (&en)[18]) {
  float mx = pk[0], mn = pk[0];
#pragma unroll
  for (int f = 1; f < 18; ++f) {
    mx = fmaxf(mx, pk[f]);
    mn = fminf(mn, pk[f]);
  }
  float sp = 0.f, sn = 0.f;
#pragma unroll
  for (int f = 0; f < 18; ++f) {
    ep[f] = __expf(pk[f] - mx);
    en[f] = __expf(mn - pk[f]);
    sp += ep[f];
    sn += en[f];
  }
  const float rp = 1.f / sp, rn = 1.f / sn;
#pragma unroll
  for (int f = 0; f < 18; ++f) {
    ep[f] *= rp;
    en[f] *= rn;
  }
}

__device__ void head_block(int hb, const u16* __restrict__ qkv, const u16* __restrict__ wo,
                           u16* __restrict__ mcat, u16* __restrict__ phif, float* pkv, unsigned* cnt, char* smem) {
  u16* phiT = reinterpret_cast<u16*>(smem);
  u16* vT = reinterpret_cast<u16*>(smem + 25344);
  float* kvs = reinterpret_cast<float*>(smem + 25344 + 16896);
  float* zs = reinterpret_cast<float*>(smem + 66816);
  const int h = hb >> 3, c8 = hb & 7;
  const int tid = threadIdx.x, wid = tid >> 6, lane = tid & 63;
  const int row = c8 * 256 + tid;
  u32x4 kr[3], vr[3], qr[3];
  {
    const u32x4* kp = reinterpret_cast<const u32x4*>(qkv + ((size_t)(32 + h) * S_ + row) * 24);
    const u32x4* vp = reinterpret_cast<const u32x4*>(qkv + ((size_t)(64 + h) * S_ + row) * 24);
    const u32x4* qp = reinterpret_cast<const u32x4*>(qkv + ((size_t)h * S_ + row) * 24);
#pragma unroll
    for (int d = 0; d < 3; ++d) { kr[d] = kp[d]; vr[d] = vp[d]; qr[d] = qp[d]; }
  }
  for (int i = tid; i < 12 * 264; i += 256) phiT[36 * 264 + i] = 0;
  for (int i = tid; i < 14 * 264; i += 256) vT[18 * 264 + i] = (i < 264) ? (u16)0x3f80 : (u16)0;
  {
    float pk[18];
#pragma unroll
    for (int d = 0; d < 9; ++d) {
      pk[2 * d] = lo16(kr[d >> 2][d & 3]);
      pk[2 * d + 1] = hi16(kr[d >> 2][d & 3]);
    }
    float ep[18], en[18];
    softmax_pm(pk, ep, en);
#pragma unroll
    for (int f = 0; f < 18; ++f) {
      phiT[f * 264 + tid] = f2bf(ep[f]);
      phiT[(18 + f) * 264 + tid] = f2bf(en[f]);
      const unsigned w = vr[(f >> 1) >> 2][(f >> 1) & 3];
      vT[f * 264 + tid] = (u16)((f & 1) ? (w >> 16) : (w & 0xffff));
    }
  }
  __syncthreads();
  {
    f32x4 acc[3][2];
#pragma unroll
    for (int a = 0; a < 3; ++a)
#pragma unroll
      for (int b = 0; b < 2; ++b) acc[a][b] = (f32x4){0.f, 0.f, 0.f, 0.f};
#pragma unroll
    for (int ks = 0; ks < 2; ++ks) {
      const int s0 = wid * 64 + ks * 32 + 8 * (lane >> 4);
      bf16x8 bv[2];
#pragma unroll
      for (int b = 0; b < 2; ++b) bv[b] = *reinterpret_cast<const bf16x8*>(vT + (b * 16 + (lane & 15)) * 264 + s0);
#pragma unroll
      for (int a = 0; a < 3; ++a) {
        bf16x8 av = *reinterpret_cast<const bf16x8*>(phiT + (a * 16 + (lane & 15)) * 264 + s0);
#pragma unroll
        for (int b = 0; b < 2; ++b) acc[a][b] = __builtin_amdgcn_mfma_f32_16x16x32_bf16(av, bv[b], acc[a][b], 0, 0, 0);
      }
    }
#pragma unroll
    for (int a = 0; a < 3; ++a)
#pragma unroll
      for (int b = 0; b < 2; ++b)
#pragma unroll
        for (int j = 0; j < 4; ++j)
          kvs[(wid * 48 + a * 16 + (lane >> 4) * 4 + j) * 32 + b * 16 + (lane & 15)] = acc[a][b][j];
  }
  __syncthreads();
  float* mypart = pkv + (size_t)(h * 8 + c8) * 1536;
#pragma unroll
  for (int it = 0; it < 6; ++it) {
    const int i = it * 256 + tid;
    const float v = kvs[i] + kvs[1536 + i] + kvs[2 * 1536 + i] + kvs[3 * 1536 + i];
    __hip_atomic_store(mypart + i, v, __ATOMIC_RELAXED, __HIP_MEMORY_SCOPE_AGENT);
  }
  asm volatile("s_waitcnt vmcnt(0)" ::: "memory");
  __syncthreads();
  if (tid == 0) __hip_atomic_fetch_add(cnt + h * 32, 1u, __ATOMIC_RELAXED, __HIP_MEMORY_SCOPE_AGENT);
  float ep[18], en[18];
  {
    float pk[18];
#pragma unroll
    for (int d = 0; d < 9; ++d) {
      pk[2 * d] = lo16(qr[d >> 2][d & 3]);
      pk[2 * d + 1] = hi16(qr[d >> 2][d & 3]);
    }
    softmax_pm(pk, ep, en);
#pragma unroll
    for (int f = 0; f < 18; ++f) { ep[f] = rbf(ep[f]); en[f] = rbf(en[f]); }
  }
  if (c8 == 0) {
    u16* woimg = reinterpret_cast<u16*>(smem);
#pragma unroll
    for (int it = 0; it < 9; ++it) {
      const int i = it * 256 + tid;
      const int r = i / 72, c = i % 72;
      u32x4 v = {0u, 0u, 0u, 0u};
      if (r < 18) v = *reinterpret_cast<const u32x4*>(wo + (size_t)(h * 18 + r) * D_ + c * 8);
      *reinterpret_cast<u32x4*>(woimg + r * 576 + c * 8) = v;
    }
  }
  if (tid == 0) {
    unsigned spins = 0;
    while (__hip_atomic_load(cnt + h * 32, __ATOMIC_RELAXED, __HIP_MEMORY_SCOPE_AGENT) < 8u) {
      __builtin_amdgcn_s_sleep(1);
      if (++spins > (1u << 24)) break;
    }
  }
  __syncthreads();
  float* hp = pkv + (size_t)h * 8 * 1536;
  if (tid < 36) {
    float zp[8];
#pragma unroll
    for (int c = 0; c < 8; ++c) zp[c] = __hip_atomic_load(hp + c * 1536 + tid * 32 + 18, __ATOMIC_RELAXED, __HIP_MEMORY_SCOPE_AGENT);
    float z = 0.f;
#pragma unroll
    for (int c = 0; c < 8; ++c) z += zp[c];
    zs[tid] = rbf(z);
  }
  u16* kvb = reinterpret_cast<u16*>(smem + 36864);
  if (c8 == 0) {
    float pv[6][8];
#pragma unroll
    for (int it = 0; it < 6; ++it)
#pragma unroll
      for (int c = 0; c < 8; ++c)
        pv[it][c] = __hip_atomic_load(hp + c * 1536 + it * 256 + tid, __ATOMIC_RELAXED, __HIP_MEMORY_SCOPE_AGENT);
#pragma unroll
    for (int it = 0; it < 6; ++it) {
      const int i = it * 256 + tid;
      float v = 0.f;
#pragma unroll
      for (int c = 0; c < 8; ++c) v += pv[it][c];
      const int f = i >> 5, d = i & 31;
      kvb[i] = (d < 18 && f < 36) ? f2bf(v) : (u16)0;
    }
  }
  __syncthreads();
  {
    float den = 0.f;
#pragma unroll
    for (int f = 0; f < 18; ++f) den += ep[f] * zs[f] + en[f] * zs[18 + f];
    const float rd = 1.f / (rbf(den) + 1e-6f);
    unsigned o[20];
#pragma unroll
    for (int f = 0; f < 9; ++f) o[f] = pack2(ep[2 * f] * rd, ep[2 * f + 1] * rd);
#pragma unroll
    for (int f = 0; f < 9; ++f) o[9 + f] = pack2(en[2 * f] * rd, en[2 * f + 1] * rd);
    o[18] = 0;
    o[19] = 0;
#pragma unroll
    for (int c = 0; c < 5; ++c) {
      const int cidx = h * 5 + c;
      u16* dst = phif + ((size_t)((row >> 4) * 40 + (cidx >> 2)) * 64 + (cidx & 3) * 16 + (row & 15)) * 8;
      u32x4 v = {o[4 * c], o[4 * c + 1], o[4 * c + 2], o[4 * c + 3]};
      *reinterpret_cast<u32x4*>(dst) = v;
    }
  }
  if (c8 == 0) {
    const int g = lane >> 4, q = (lane & 15) >> 2, p = lane & 3;
    constexpr int RB = 576 * 2;
    const int o0 = (8 * g + q) * RB + p * 8, o1 = (8 * g + 4 + q) * RB + p * 8;
    bf16x8 kb[3];
#pragma unroll
    for (int a = 0; a < 3; ++a) kb[a] = *reinterpret_cast<const bf16x8*>(kvb + (a * 16 + (lane & 15)) * 32 + 8 * g);
    for (int nt = wid; nt < 36; nt += 4) {
      bf16x8 w = wfrag(smem, o0, o1, nt * 32);
#pragma unroll
      for (int a = 0; a < 3; ++a) {
        f32x4 c = __builtin_amdgcn_mfma_f32_16x16x32_bf16(w, kb[a], (f32x4){0.f, 0.f, 0.f, 0.f}, 0, 0, 0);
        const int f = a * 16 + (lane & 15);
        if (f < KPH_) {
          u32x2 v = {pack2(c[0], c[1]), pack2(c[2], c[3])};
          *reinterpret_cast<u32x2*>(mcat + (size_t)(h * KPH_ + f) * D_ + nt * 16 + 4 * g) = v;
        }
      }
    }
  }
}

template <int BASE, int N0, int CNT, int I = 0>
__device__ __forceinline__ void g2_wreads(WF (&w)[5], unsigned a0, unsigned a1) {
  if constexpr (I < CNT) {
    tr_rd<BASE + (N0 + I) * 32>(w[I].lo, a0);
    tr_rd<BASE + (N0 + I) * 32>(w[I].hi, a1);
    g2_wreads<BASE, N0, CNT, I + 1>(w, a0, a1);
  }
}
template <int N0, int CNT>
__device__ __forceinline__ void g2_mma(WF (&w)[5], const bf16x8 (&ac)[2], f32x4 (&acc)[2][G2_NT]) {
#pragma unroll
  for (int i = 0; i < CNT; ++i) pin(w[i]);
#pragma unroll
  for (int i = 0; i < CNT; ++i)
#pragma unroll
    for (int r = 0; r < 2; ++r)
      acc[r][N0 + i] = __builtin_amdgcn_mfma_f32_16x16x32_bf16(cat(w[i]), ac[r], acc[r][N0 + i], 0, 0, 0);
}
constexpr int K2_HEADBLK = 256;

__global__ __launch_bounds__(256, 2) void k2_gemm2(const u16* __restrict__ hf, const u16* __restrict__ w2,
                                                   float* __restrict__ part, const u16* __restrict__ qkv,
                                                   const u16* __restrict__ wo, u16* __restrict__ phif,
                                                   u16* __restrict__ mcat, float* pkv, unsigned* cnt) {
  __shared__ __attribute__((aligned(16))) char smem[G2_RING * G2_BUFB];
  const int tid = threadIdx.x, wid = tid >> 6, lane = tid & 63;
  int bid = blockIdx.x;
  if (bid < K2_HEADBLK) { head_block(bid, qkv, wo, mcat, phif, pkv, cnt, smem); return; }
  bid -= K2_HEADBLK;
  STAMP(2, 0);
  const int xcd = bid & 7, loc = bid >> 3;
  const int sp = xcd >> 1, mt = (xcd & 1) * 8 + (loc >> 2), nt4 = loc & 3;
  const int m0 = mt * 128, n0 = nt4 * 144, k0 = sp * 1152;
  const int g = lane >> 4, q = (lane & 15) >> 2, p = lane & 3;
  const bool odd = g & 1;
  const unsigned wa0 = lds_addr(smem) + (8 * g + 4 * (g & 1) + q) * G2_RB + p * 8;
  const unsigned wa1 = lds_addr(smem) + (8 * g + 4 * (1 - (g & 1)) + q) * G2_RB + p * 8;
  f32x4 acc[2][G2_NT];
#pragma unroll
  for (int r = 0; r < 2; ++r)
#pragma unroll
    for (int n = 0; n < G2_NT; ++n) acc[r][n] = (f32x4){0.f, 0.f, 0.f, 0.f};
  const u16* wp[5];
  int wdst[5];
#pragma unroll
  for (int i = 0; i < 5; ++i) {
    int ins = wid + 4 * i;
    if (ins >= 18) ins -= 18;
    int c = ins * 64 + lane;
    int row = c / 18, cc = c % 18;
    wp[i] = w2 + (size_t)(k0 + row) * D_ + n0 + cc * 8;
    wdst[i] = c * 16;
  }
  const u16* ap[2];
#pragma unroll
  for (int r = 0; r < 2; ++r) ap[r] = hf + ((size_t)((m0 >> 4) + wid * 2 + r) * 144 + (k0 >> 5)) * 512 + lane * 8;
  bf16x8 an[4][2][2];
  auto issue = [&](auto SL) {
    constexpr int slot = decltype(SL)::value;
    char* buf = smem + slot * G2_BUFB;
#pragma unroll
    for (int i = 0; i < 5; ++i) { glds16(wp[i], buf + wdst[i]); wp[i] += 64 * D_; }
#pragma unroll
    for (int r = 0; r < 2; ++r) {
      gld16_asm(an[slot][r][0], ap[r]);
      gld16_asm(an[slot][r][1], ap[r] + 512);
      ap[r] += 1024;
    }
  };
  issue(IC<0>{});
  issue(IC<1>{});
  issue(IC<2>{});
  wait_vm<18>();
  BAR();
  STAMP(2, 1);
  WF wA[5], wB[5];
  g2_wreads<0, 0, 5>(wA, wa0, wa1);
  auto stage = [&](auto U, auto LAST) {
    constexpr int u = decltype(U)::value;
    constexpr int rem = decltype(LAST)::value;
    constexpr int BASE = u * G2_BUFB;
    bf16x8 c0[2], c1[2];
#pragma unroll
    for (int r = 0; r < 2; ++r) {
      pin(an[u][r][0]); pin(an[u][r][1]);
      c0[r] = swap_halves_if(an[u][r][0], odd);
      c1[r] = swap_halves_if(an[u][r][1], odd);
    }
    g2_wreads<BASE, 5, 4>(wB, wa0, wa1);
    wait_lgkm<8>();
    g2_mma<0, 5>(wA, c0, acc);
    g2_wreads<BASE + 32 * G2_RB, 0, 5>(wA, wa0, wa1);
    wait_lgkm<10>();
    g2_mma<5, 4>(wB, c0, acc);
    g2_wreads<BASE + 32 * G2_RB, 5, 4>(wB, wa0, wa1);
    wait_lgkm<8>();
    g2_mma<0, 5>(wA, c1, acc);
    if constexpr (rem >= 1) {
      if constexpr (rem >= 2) wait_vm<9>(); else wait_vm<0>();
      BAR();
      if constexpr (rem >= 3) issue(IC<(u + 3) & 3>{});
      g2_wreads<((u + 1) & 3) * G2_BUFB, 0, 5>(wA, wa0, wa1);
      wait_lgkm<10>();
    } else {
      wait_lgkm<0>();
    }
    g2_mma<5, 4>(wB, c1, acc);
  };
#pragma unroll 1
  for (int it = 0; it < 3; ++it) {
    stage(IC<0>{}, IC<3>{}); stage(IC<1>{}, IC<3>{}); stage(IC<2>{}, IC<3>{}); stage(IC<3>{}, IC<3>{});
  }
  STAMP(2, 2);
  stage(IC<0>{}, IC<3>{}); stage(IC<1>{}, IC<3>{}); stage(IC<2>{}, IC<3>{});
  stage(IC<3>{}, IC<2>{}); stage(IC<0>{}, IC<1>{}); stage(IC<1>{}, IC<0>{});
  STAMP(2, 3);
#pragma unroll
  for (int r = 0; r < 2; ++r) {
    const int m = m0 + wid * 32 + r * 16 + (lane & 15);
    float* pp = part + ((size_t)sp * S_ + m) * D_ + n0 + 4 * g;
#pragma unroll
    for (int n = 0; n < G2_NT; ++n) *reinterpret_cast<f32x4*>(pp + n * 16) = acc[r][n];
  }
  asm volatile("s_waitcnt vmcnt(0)" ::: "memory");
  STAMP(2, 4);
}

constexpr int G3_BUFB = 128 * 128;
constexpr int G3_RING = 3;

__global__ __launch_bounds__(256, 2) void k3_final(const u16* __restrict__ phif, const u16* __restrict__ mcat,
                                                   const float* __restrict__ part, const u16* __restrict__ b2,
                                                   const u16* __restrict__ x, u16* __restrict__ out) {
  __shared__ __attribute__((aligned(16))) char smem[G3_RING * G3_BUFB];
  const int tid = threadIdx.x, wid = tid >> 6, lane = tid & 63;
  const int bid = blockIdx.x;
  const int xcd = bid & 7, loc = bid >> 3;
  const int mt = xcd * 4 + loc / 9, nt = loc % 9;
  const int m0 = mt * 64, n0 = nt * 64;
  const int g = lane >> 4, q = (lane & 15) >> 2, p = lane & 3;
  const int wr = wid >> 1, wc = wid & 1;
  STAMP(3, 0);
  const u16* wp[4];
#pragma unroll
  for (int i = 0; i < 4; ++i) {
    const int row = (wid * 4 + i) * 8 + (lane >> 3);
    const int f = ((row >> 1) & 1) | (((row >> 3) & 1) << 1);
    const int ch = (lane & 7) ^ (f << 1);
    wp[i] = mcat + (size_t)row * D_ + n0 + ch * 8;
  }
  const u16* ap[2];
#pragma unroll
  for (int r = 0; r < 2; ++r) ap[r] = phif + ((size_t)((m0 >> 4) + wr * 2 + r) * 40) * 512 + lane * 8;
  bf16x8 an[3][2][4];
  auto issue = [&](auto SL) {
    constexpr int slot = decltype(SL)::value;
    char* buf = smem + slot * G3_BUFB;
#pragma unroll
    for (int i = 0; i < 4; ++i) { glds16(wp[i], buf + (wid * 4 + i) * 1024 + lane * 16); wp[i] += 128 * D_; }
#pragma unroll
    for (int r = 0; r < 2; ++r) {
#pragma unroll
      for (int ks = 0; ks < 4; ++ks) gld16_asm(an[slot][r][ks], ap[r] + ks * 512);
      ap[r] += 2048;
    }
  };
  issue(IC<0>{});
  issue(IC<1>{});
  bf16x8 py[2][2][4];
  u32x2 xx[2][2], bb[2];
#pragma unroll
  for (int r = 0; r < 2; ++r) {
    const int m = m0 + wr * 32 + r * 16 + (lane & 15);
#pragma unroll
    for (int n = 0; n < 2; ++n) {
      const int ncol = n0 + wc * 32 + n * 16 + 4 * g;
#pragma unroll
      for (int sI = 0; sI < SPLITK_; ++sI) gld16_asm(py[r][n][sI], part + ((size_t)sI * S_ + m) * D_ + ncol);
      asm volatile("global_load_dwordx2 %0, %1, off" : "=v"(xx[r][n]) : "v"(x + (size_t)m * D_ + ncol) : "memory");
    }
  }
#pragma unroll
  for (int n = 0; n < 2; ++n)
    asm volatile("global_load_dwordx2 %0, %1, off" : "=v"(bb[n]) : "v"(b2 + n0 + wc * 32 + n * 16 + 4 * g) : "memory");
  unsigned ad[2][2];
#pragma unroll
  for (int t = 0; t < 2; ++t) {
    const int row = 8 * g + 4 * t + q;
    const int f = ((row >> 1) & 1) | (((row >> 3) & 1) << 1);
#pragma unroll
    for (int n = 0; n < 2; ++n) ad[n][t] = lds_addr(smem) + row * 128 + (((wc * 2 + n) ^ f) * 32) + p * 8;
  }
  f32x4 acc[2][2];
#pragma unroll
  for (int r = 0; r < 2; ++r)
#pragma unroll
    for (int n = 0; n < 2; ++n) acc[r][n] = (f32x4){0.f, 0.f, 0.f, 0.f};
  STAMP(3, 1);
  auto stage = [&](auto U, auto WAITN, auto MORE) {
    constexpr int u = decltype(U)::value;
    wait_vm<decltype(WAITN)::value>();
    BAR();
    if constexpr (decltype(MORE)::value) issue(IC<(u + 2) % 3>{});
    constexpr int BASE = u * G3_BUFB;
    WF w[4][2];
#pragma unroll
    for (int n = 0; n < 2; ++n) { tr_rd<BASE + 0 * 4096>(w[0][n].lo, ad[n][0]); tr_rd<BASE + 0 * 4096>(w[0][n].hi, ad[n][1]); }
#pragma unroll
    for (int n = 0; n < 2; ++n) { tr_rd<BASE + 1 * 4096>(w[1][n].lo, ad[n][0]); tr_rd<BASE + 1 * 4096>(w[1][n].hi, ad[n][1]); }
#pragma unroll
    for (int n = 0; n < 2; ++n) { tr_rd<BASE + 2 * 4096>(w[2][n].lo, ad[n][0]); tr_rd<BASE + 2 * 4096>(w[2][n].hi, ad[n][1]); }
#pragma unroll
    for (int n = 0; n < 2; ++n) { tr_rd<BASE + 3 * 4096>(w[3][n].lo, ad[n][0]); tr_rd<BASE + 3 * 4096>(w[3][n].hi, ad[n][1]); }
#pragma unroll
    for (int r = 0; r < 2; ++r)
#pragma unroll
      for (int ks = 0; ks < 4; ++ks) pin(an[u][r][ks]);
    wait_lgkm<8>();
#pragma unroll
    for (int ks = 0; ks < 2; ++ks)
#pragma unroll
      for (int n = 0; n < 2; ++n) pin(w[ks][n]);
#pragma unroll
    for (int ks = 0; ks < 2; ++ks)
#pragma unroll
      for (int n = 0; n < 2; ++n)
#pragma unroll
        for (int r = 0; r < 2; ++r)
          acc[r][n] = __builtin_amdgcn_mfma_f32_16x16x32_bf16(cat(w[ks][n]), an[u][r][ks], acc[r][n], 0, 0, 0);
    wait_lgkm<0>();
#pragma unroll
    for (int ks = 2; ks < 4; ++ks)
#pragma unroll
      for (int n = 0; n < 2; ++n) pin(w[ks][n]);
#pragma unroll
    for (int ks = 2; ks < 4; ++ks)
#pragma unroll
      for (int n = 0; n < 2; ++n)
#pragma unroll
        for (int r = 0; r < 2; ++r)
          acc[r][n] = __builtin_amdgcn_mfma_f32_16x16x32_bf16(cat(w[ks][n]), an[u][r][ks], acc[r][n], 0, 0, 0);
  };
  stage(IC<0>{}, IC<34>{}, IC<1>{});
  stage(IC<1>{}, IC<34>{}, IC<1>{});
  stage(IC<2>{}, IC<12>{}, IC<1>{});
  stage(IC<0>{}, IC<12>{}, IC<1>{});
  stage(IC<1>{}, IC<12>{}, IC<1>{});
  stage(IC<2>{}, IC<12>{}, IC<1>{});
  stage(IC<0>{}, IC<12>{}, IC<1>{});
  stage(IC<1>{}, IC<12>{}, IC<1>{});
  stage(IC<2>{}, IC<12>{}, IC<0>{});
  stage(IC<0>{}, IC<0>{}, IC<0>{});
  STAMP(3, 3);
#pragma unroll
  for (int n = 0; n < 2; ++n) asm volatile("" : "+v"(bb[n]));
#pragma unroll
  for (int r = 0; r < 2; ++r) {
    const int m = m0 + wr * 32 + r * 16 + (lane & 15);
#pragma unroll
    for (int n = 0; n < 2; ++n) {
      const int ncol = n0 + wc * 32 + n * 16 + 4 * g;
      asm volatile("" : "+v"(xx[r][n]));
#pragma unroll
      for (int sI = 0; sI < SPLITK_; ++sI) pin(py[r][n][sI]);
      f32x4 y = *reinterpret_cast<f32x4*>(&py[r][n][0]);
#pragma unroll
      for (int sI = 1; sI < SPLITK_; ++sI) y += *reinterpret_cast<f32x4*>(&py[r][n][sI]);
      float bv[4] = {lo16(bb[n][0]), hi16(bb[n][0]), lo16(bb[n][1]), hi16(bb[n][1])};
      float xv[4] = {lo16(xx[r][n][0]), hi16(xx[r][n][0]), lo16(xx[r][n][1]), hi16(xx[r][n][1])};
      float o[4];
#pragma unroll
      for (int j = 0; j < 4; ++j) {
        float yy = y[j] + bv[j];
        float gg = yy / (1.f + __expf(-yy));
        o[j] = xv[j] + acc[r][n][j] * gg;
      }
      u32x2 v = {pack2(o[0], o[1]), pack2(o[2], o[3])};
      *reinterpret_cast<u32x2*>(out + (size_t)m * D_ + ncol) = v;
    }
  }
  STAMP(3, 4);
}

extern "C" void kernel_launch(void* const* d_in, const int* in_sizes, int n_in, void* d_out, int out_size, void* d_ws,
                              size_t ws_size, hipStream_t stream) {
  const u16* x = (const u16*)d_in[0];
  const u16* wqkv = (const u16*)d_in[1];
  const u16* wphi = (const u16*)d_in[2];
  const u16* wo = (const u16*)d_in[3];
  const u16* w1 = (const u16*)d_in[5];
  const u16* b1 = (const u16*)d_in[6];
  const u16* w2 = (const u16*)d_in[7];
  const u16* b2 = (const u16*)d_in[8];
  u16* out = (u16*)d_out;
  char* ws = (char*)d_ws;
  size_t off = 0;
  u16* xnf = (u16*)(ws + off); off += (size_t)S_ * D_ * 2;
  u16* wfold = (u16*)(ws + off); off += (size_t)D_ * NPH_ * 2;
  u16* qkv = (u16*)(ws + off); off += (size_t)96 * S_ * 24 * 2;
  u16* hf = (u16*)(ws + off); off += (size_t)S_ * F_ * 2;
  float* part = (float*)(ws + off); off += (size_t)SPLITK_ * S_ * D_ * 4;
  u16* phif = (u16*)(ws + off); off += (size_t)S_ * KPHI_ * 2;
  u16* mcat = (u16*)(ws + off); off += (size_t)KPHI_ * D_ * 2;
  float* pkv = (float*)(ws + off); off += (size_t)256 * 1536 * 4;
  unsigned* cnt = (unsigned*)(ws + off); off += 32 * 32 * 4;

  k0_prep<<<512 + 192, 256, 0, stream>>>(x, wqkv, wphi, xnf, wfold, cnt);
  k1_gemm1<<<256, 512, 0, stream>>>(xnf, wfold, wqkv, w1, b1, qkv, hf);
  k2_gemm2<<<K2_HEADBLK + 256, 256, 0, stream>>>(hf, w2, part, qkv, wo, phif, mcat, pkv, cnt);
  k3_final<<<32 * 9, 256, 0, stream>>>(phif, mcat, part, b2, x, out);
}
```

```cpp
#include <hip/hip_runtime.h>
#include <hip/hip_bf16.h>
#include <stdint.h>

constexpr int S_ = 2048, D_ = 576, H_ = 32, DH_ = 18, F_ = 4608, NQKV_ = 1728;
constexpr int NPH_ = 1152;
constexpr int KPH_ = 40;
constexpr int KPHI_ = H_ * KPH_;
constexpr int SPLITK_ = 4;

typedef short bf16x8 __attribute__((ext_vector_type(8)));
typedef short s16x4 __attribute__((ext_vector_type(4)));
typedef float f32x4 __attribute__((ext_vector_type(4)));
typedef unsigned u32x4 __attribute__((ext_vector_type(4)));
typedef unsigned u32x2 __attribute__((ext_vector_type(2)));
typedef __attribute__((address_space(3))) s16x4* lds_s16x4_p;
typedef unsigned short u16;
template <int V> struct IC { static constexpr int value = V; };

#define STAMP(kid, i) do { } while (0)
#define WAIT_LGKM0() asm volatile("s_waitcnt lgkmcnt(0)" ::: "memory")
#define BAR() __builtin_amdgcn_s_barrier()

template <int N>
__device__ __forceinline__ void wait_vm() {
  static_assert(N >= 0 && N < 64, "vmcnt range");
  asm volatile("s_waitcnt vmcnt(%0)" ::"n"(N) : "memory");
}

__device__ __forceinline__ float bf2f(u16 v) { return __uint_as_float(((unsigned)v) << 16); }
__device__ __forceinline__ u16 f2bf(float f) {
  __hip_bfloat16 b = __float2bfloat16(f);
  return *reinterpret_cast<u16*>(&b);
}
__device__ __forceinline__ unsigned pack2(float lo, float hi) {
  return (unsigned)f2bf(lo) | ((unsigned)f2bf(hi) << 16);
}
__device__ __forceinline__ float rbf(float f) { return bf2f(f2bf(f)); }
__device__ __forceinline__ float lo16(unsigned u) { return __uint_as_float(u << 16); }
__device__ __forceinline__ float hi16(unsigned u) { return __uint_as_float(u & 0xffff0000u); }

__device__ __forceinline__ void glds16(const void* g, void* l) {
  __builtin_amdgcn_global_load_lds((const unsigned*)g, (unsigned*)l, 16, 0, 0);
}

__device__ __forceinline__ void l2_touch(const void* g, void* ldummy) {
  __builtin_amdgcn_global_load_lds((const unsigned*)g, (unsigned*)ldummy, 4, 0, 0);
}

__device__ __forceinline__ bf16x8 swap_halves_if(bf16x8 v, bool odd) {
  bf16x8 s = __builtin_shufflevector(v, v, 4, 5, 6, 7, 0, 1, 2, 3);
  return odd ? s : v;
}

__device__ __forceinline__ bf16x8 wfrag(const char* img, int o0, int o1, int off) {
  s16x4 a = __builtin_amdgcn_ds_read_tr16_b64_v4i16((lds_s16x4_p)(img + o0 + off));
  s16x4 b = __builtin_amdgcn_ds_read_tr16_b64_v4i16((lds_s16x4_p)(img + o1 + off));
  return __builtin_shufflevector(a, b, 0, 1, 2, 3, 4, 5, 6, 7);
}

struct WF { s16x4 lo, hi; };
template <int OFF>
__device__ __forceinline__ void tr_rd(s16x4& r, unsigned addr) {
  asm volatile("ds_read_b64_tr_b16 %0, %1 offset:%2" : "=v"(r) : "v"(addr), "n"(OFF) : "memory");
}
__device__ __forceinline__ void gld16_asm(bf16x8& r, const void* p) {
  asm volatile("global_load_dwordx4 %0, %1, off" : "=v"(r) : "v"(p) : "memory");
}
template <int N>
__device__ __forceinline__ void wait_lgkm() {
  static_assert(N >= 0 && N <= 15, "lgkmcnt range");
  asm volatile("s_waitcnt lgkmcnt(%0)" ::"n"(N) : "memory");
}
__device__ __forceinline__ void pin(WF& w) { asm volatile("" : "+v"(w.lo), "+v"(w.hi)); }
__device__ __forceinline__ void pin(bf16x8& w) { asm volatile("" : "+v"(w)); }
__device__ __forceinline__ bf16x8 cat(const WF& w) { return __builtin_shufflevector(w.lo, w.hi, 0, 1, 2, 3, 4, 5, 6, 7); }
__device__ __forceinline__ unsigned lds_addr(const void* p) {
  return (unsigned)(uintptr_t)(__attribute__((address_space(3))) const char*)p;
}

__global__ __launch_bounds__(256) void k0_prep(const u16* __restrict__ x, const u16* __restrict__ wqkv,
                                               const u16* __restrict__ wphi, u16* __restrict__ xnf,
                                               u16* __restrict__ wfold, unsigned* __restrict__ cnt) {
  __shared__ __attribute__((aligned(16))) float wph[18 * 20];
  const int tid = threadIdx.x, wid = tid >> 6, lane = tid & 63;
  const int bid = blockIdx.x;
  if (bid == 512 && tid < 32) cnt[tid * 32] = 0u;
  if (bid < 512) {
    const int row = bid * 4 + wid;
    const u16* xp = x + (size_t)row * D_;
    u32x4 a = *reinterpret_cast<const u32x4*>(xp + lane * 8);
    u32x4 b = {0u, 0u, 0u, 0u};
    if (lane < 8) b = *reinterpret_cast<const u32x4*>(xp + 512 + lane * 8);
    float v[16];
#pragma unroll
    for (int j = 0; j < 4; ++j) {
      v[2 * j] = lo16(a[j]); v[2 * j + 1] = hi16(a[j]);
      v[8 + 2 * j] = lo16(b[j]); v[8 + 2 * j + 1] = hi16(b[j]);
    }
    float s = 0.f;
#pragma unroll
    for (int j = 0; j < 16; ++j) s += v[j];
#pragma unroll
    for (int o = 32; o >= 1; o >>= 1) s += __shfl_xor(s, o);
    const float mean = s * (1.f / D_);
    float ss = 0.f;
#pragma unroll
    for (int j = 0; j < 8; ++j) { float d = v[j] - mean; ss += d * d; }
    if (lane < 8) {
#pragma unroll
      for (int j = 8; j < 16; ++j) { float d = v[j] - mean; ss += d * d; }
    }
#pragma unroll
    for (int o = 32; o >= 1; o >>= 1) ss += __shfl_xor(ss, o);
    const float rstd = rsqrtf(ss * (1.f / D_) + 1e-5f);
    u32x4 oa, ob;
#pragma unroll
    for (int j = 0; j < 4; ++j) {
      oa[j] = pack2((v[2 * j] - mean) * rstd, (v[2 * j + 1] - mean) * rstd);
      ob[j] = pack2((v[8 + 2 * j] - mean) * rstd, (v[8 + 2 * j + 1] - mean) * rstd);
    }
    {
      const int c = lane;
      u16* dst = xnf + ((size_t)((row >> 4) * 18 + (c >> 2)) * 64 + (c & 3) * 16 + (row & 15)) * 8;
      *reinterpret_cast<u32x4*>(dst) = oa;
    }
    if (lane < 8) {
      const int c = 64 + lane;
      u16* dst = xnf + ((size_t)((row >> 4) * 18 + (c >> 2)) * 64 + (c & 3) * 16 + (row & 15)) * 8;
      *reinterpret_cast<u32x4*>(dst) = ob;
    }
    return;
  }
  {
    const int fb = bid - 512;
    const int hh = fb / 3, k = (fb % 3) * 192 + tid;
    const int h = hh & 31;
    for (int i = tid; i < 18 * 20; i += 256) {
      const int d = i / 20, f = i % 20;
      wph[i] = (f < 18) ? bf2f(wphi[h * 324 + d * 18 + f]) : 0.f;
    }
    unsigned wv[9];
    if (tid < 192) {
      const u16* wp = wqkv + (size_t)k * NQKV_ + hh * 18;
#pragma unroll
      for (int d = 0; d < 9; ++d) wv[d] = *reinterpret_cast<const unsigned*>(wp + 2 * d);
    }
    __syncthreads();
    if (tid < 192) {
      float acc[20];
#pragma unroll
      for (int f = 0; f < 20; ++f) acc[f] = 0.f;
#pragma unroll
      for (int d = 0; d < 18; ++d) {
        const float w = (d & 1) ? hi16(wv[d >> 1]) : lo16(wv[d >> 1]);
#pragma unroll
        for (int f4 = 0; f4 < 5; ++f4) {
          f32x4 pw = *reinterpret_cast<const f32x4*>(wph + d * 20 + f4 * 4);
#pragma unroll
          for (int j = 0; j < 4; ++j) acc[f4 * 4 + j] += w * pw[j];
        }
      }
      u16* op = wfold + (size_t)k * NPH_ + hh * 18;
#pragma unroll
      for (int f = 0; f < 9; ++f) *reinterpret_cast<unsigned*>(op + 2 * f) = pack2(acc[2 * f], acc[2 * f + 1]);
    }
  }
}

constexpr int G1_BUFB = 192 * 128;
constexpr int G1_RING = 4;
constexpr int G1_XOFF = G1_RING * G1_BUFB;
constexpr int G1_BOFF = G1_XOFF + 576 * 32;
constexpr int G1_SMEM = G1_BOFF + 1024;
constexpr int G1_NST = 8;

template <int BASE, int I = 0>
__device__ __forceinline__ void g1_reads(WF (&w)[4], const unsigned (&ad)[4][2]) {
  if constexpr (I < 4) {
    if constexpr (BASE >= 49152) {
      tr_rd<BASE - 49152>(w[I].lo, ad[I][0] + 49152u);
      tr_rd<BASE - 49152>(w[I].hi, ad[I][1] + 49152u);
    } else {
      tr_rd<BASE>(w[I].lo, ad[I][0]);
      tr_rd<BASE>(w[I].hi, ad[I][1]);
    }
    g1_reads<BASE, I + 1>(w, ad);
  }
}
template <int KS>
__device__ __forceinline__ void g1_mma(WF (&w)[4], const bf16x8 (&xa)[2][18], f32x4 (&acc)[2][4]) {
#pragma unroll
  for (int i = 0; i < 4; ++i) pin(w[i]);
#pragma unroll
  for (int i = 0; i < 4; ++i)
#pragma unroll
    for (int r = 0; r < 2; ++r)
      acc[r][i] = __builtin_amdgcn_mfma_f32_16x16x32_bf16(cat(w[i]), xa[r][KS], acc[r][i], 0, 0, 0);
}

__device__ __forceinline__ float gelu_f(float v) {
  constexpr float c1 = -2.f * 0.7978845608028654f * 1.4426950408889634f;
  constexpr float c3 = c1 * 0.044715f;
  const float t = v * v;
  const float e = __builtin_amdgcn_exp2f(v * fmaf(t, c3, c1));
  return v * __builtin_amdgcn_rcpf(1.f + e);
}

__device__ __forceinline__ void g1_store_tile(const f32x4& a, int tile, int mrow, int g, const u16* bias4,
                                              u16* __restrict__ qkv, u16* __restrict__ hf) {
  if (tile < NQKV_ / 16) {
    const int col = tile * 16 + 4 * g;
#pragma unroll
    for (int j = 0; j < 2; ++j) {
      const int c = col + 2 * j;
      const int hh = c / 18, f = c - hh * 18;
      *reinterpret_cast<unsigned*>(qkv + ((size_t)hh * S_ + mrow) * 24 + f) = pack2(a[2 * j], a[2 * j + 1]);
    }
  } else {
    u32x2 bb = *reinterpret_cast<const u32x2*>(bias4);
    float o[4];
    o[0] = gelu_f(a[0] + lo16(bb[0]));
    o[1] = gelu_f(a[1] + hi16(bb[0]));
    o[2] = gelu_f(a[2] + lo16(bb[1]));
    o[3] = gelu_f(a[3] + hi16(bb[1]));
    u32x2 v = {pack2(o[0], o[1]), pack2(o[2], o[3])};
    const int T = tile - NQKV_ / 16;
    u16* dst = hf + ((size_t)((mrow >> 4) * 144 + (T >> 1)) * 64 + ((T & 1) * 2 + (g >> 1)) * 16 + (mrow & 15)) * 8 + (g & 1) * 4;
    *reinterpret_cast<u32x2*>(dst) = v;
  }
}

__device__ __forceinline__ void g1_addr(unsigned (&ad)[4][2], char* smem, int lane) {
  const int g = lane >> 4, q = (lane & 15) >> 2, p = lane & 3;
#pragma unroll
  for (int t = 0; t < 2; ++t) {
    const int row = 8 * g + 4 * t + q;
    const int f = ((row >> 1) & 1) | (((row >> 3) & 1) << 1);
#pragma unroll
    for (int n = 0; n < 4; ++n) ad[n][t] = lds_addr(smem) + row * 128 + ((n ^ f) * 32) + p * 8;
  }
}

struct G1W { const u16* src; int stride; };

__device__ __forceinline__ void g1_wsetup(G1W& w, int col, int wid, int lane, const u16* wfold, const u16* wqkv, const u16* w1) {
  const int row = wid * 8 + (lane >> 3);
  const int f = ((row >> 1) & 1) | (((row >> 3) & 1) << 1);
  const int ch = (lane & 7) ^ (f << 1);
  if (col < NPH_) { w.src = wfold + (size_t)row * NPH_ + col + ch * 8; w.stride = 64 * NPH_; }
  else if (col < NQKV_) { w.src = wqkv + (size_t)row * NQKV_ + col + ch * 8; w.stride = 64 * NQKV_; }
  else { w.src = w1 + (size_t)row * F_ + (col - NQKV_) + ch * 8; w.stride = 64 * F_; }
}
__device__ __forceinline__ void g1_wissue(G1W& w, char* buf, int wid, int lane) {
#pragma unroll
  for (int i = 0; i < 3; ++i) {
    glds16(w.src, buf + i * 8192 + wid * 1024 + lane * 16);
    w.src += w.stride;
  }
}

template <int U>
__device__ __forceinline__ void g1_unit(G1W& wst, int unit0, const u16* wfold, const u16* wqkv,
                                        const u16* w1, char* smem, bf16x8 (&xa)[2][18], int m0,
                                        u16* __restrict__ qkv, u16* __restrict__ hf, WF (&wA)[4]) {
  int tz = threadIdx.x;
  asm volatile("" : "+v"(tz));
  const int wid = __builtin_amdgcn_readfirstlane(tz >> 6), lane = tz & 63;
  const int g = lane >> 4;
  unsigned ad[4][2];
  g1_addr(ad, smem, lane);
  f32x4 acc[2][4];
#pragma unroll
  for (int r = 0; r < 2; ++r)
#pragma unroll
    for (int n = 0; n < 4; ++n) acc[r][n] = (f32x4){0.f, 0.f, 0.f, 0.f};

  auto stage = [&](auto KC) {
    constexpr int kc = decltype(KC)::value;
    constexpr int s = U * 3 + kc;
    constexpr int CUR = (s % G1_RING) * G1_BUFB;
    if constexpr (U == 0) {
#pragma unroll
      for (int k = 0; k < 6; ++k) { pin(xa[0][kc * 6 + k]); pin(xa[1][kc * 6 + k]); }
    }
    WF wB[4];
    g1_reads<CUR + 1 * 4096>(wB, ad); wait_lgkm<8>(); g1_mma<kc * 6 + 0>(wA, xa, acc);
    g1_reads<CUR + 2 * 4096>(wA, ad); wait_lgkm<8>(); g1_mma<kc * 6 + 1>(wB, xa, acc);
    g1_reads<CUR + 3 * 4096>(wB, ad); wait_lgkm<8>(); g1_mma<kc * 6 + 2>(wA, xa, acc);
    g1_reads<CUR + 4 * 4096>(wA, ad); wait_lgkm<8>(); g1_mma<kc * 6 + 3>(wB, xa, acc);
    g1_reads<CUR + 5 * 4096>(wB, ad); wait_lgkm<8>(); g1_mma<kc * 6 + 4>(wA, xa, acc);
    if constexpr (s < 8) {
      constexpr int nl = (s == 0) ? 12 : ((s + 2 <= 8) ? 3 : 0);
      constexpr int nst = (U > 0 && kc <= 1) ? G1_NST : 0;
      wait_vm<nl + nst>();
      BAR();
      if constexpr (s + 3 <= 8) {
        constexpr int sn = s + 3;
        if constexpr (sn % 3 == 0) g1_wsetup(wst, (unit0 + sn / 3) * 64, wid, lane, wfold, wqkv, w1);
        g1_wissue(wst, smem + (sn % G1_RING) * G1_BUFB, wid, lane);
      }
      g1_reads<((s + 1) % G1_RING) * G1_BUFB>(wA, ad);
      wait_lgkm<8>();
    } else {
      wait_lgkm<0>();
    }
    g1_mma<kc * 6 + 5>(wB, xa, acc);
  };
  stage(IC<0>{}); stage(IC<1>{}); stage(IC<2>{});
  const u16* bias_lds = reinterpret_cast<const u16*>(smem + G1_BOFF);
#pragma unroll
  for (int n = 0; n < 4; ++n) {
    const int tile = (unit0 + U) * 4 + n;
#pragma unroll
    for (int r = 0; r < 2; ++r)
      g1_store_tile(acc[r][n], tile, m0 + wid * 32 + r * 16 + (lane & 15), g, bias_lds + (U * 4 + n) * 16 + 4 * g, qkv, hf);
  }
}

__global__ __launch_bounds__(512, 2) void k1_gemm1(const u16* __restrict__ xnf, const u16* __restrict__ wfold,
                                                   const u16* __restrict__ wqkv, const u16* __restrict__ w1,
                                                   const u16* __restrict__ b1, u16* __restrict__ qkv,
                                                   u16* __restrict__ hf) {
  __shared__ __attribute__((aligned(16))) char smem[G1_SMEM];
  const int tid = threadIdx.x, wid = tid >> 6, lane = tid & 63;
  const int bid = blockIdx.x;
  const int mt = bid >> 5, grp = bid & 31;
  const int m0 = mt * 256;
  const int unit0 = grp * 3;
  const bool extra = grp < 12;
  const int xcol = 6144 + (extra ? grp : 0) * 16;
  STAMP(1, 0);
  G1W wst;
  g1_wsetup(wst, unit0 * 64, wid, lane, wfold, wqkv, w1);
  g1_wissue(wst, smem + 0 * G1_BUFB, wid, lane);
  g1_wissue(wst, smem + 1 * G1_BUFB, wid, lane);
  g1_wissue(wst, smem + 2 * G1_BUFB, wid, lane);
#pragma unroll
  for (int i = 0; i < 3; ++i) {
    int ins = wid + 8 * i;
    if (ins >= 18) ins -= 18;
    const int c = ins * 64 + lane;
    const int pr = c >> 1, half = c & 1;
    const int k = (pr & ~0xC) | ((pr & 4) << 1) | ((pr & 8) >> 1);
    glds16(w1 + (size_t)k * F_ + (xcol - NQKV_) + half * 8, smem + G1_XOFF + c * 16);
  }
  {
    const int e = (wid & 1) * 64 + lane;
    int col = (e < 96) ? (unit0 * 64 + 2 * e - NQKV_) : (xcol - NQKV_ + 2 * (e - 96));
    if (col < 0 || e >= 104) col = 0;
    __builtin_amdgcn_global_load_lds((const unsigned*)(b1 + col), (unsigned*)(smem + G1_BOFF + e * 4), 4, 0, 0);
  }
  bf16x8 xa[2][18];
#pragma unroll
  for (int ks = 0; ks < 18; ++ks)
#pragma unroll
    for (int r = 0; r < 2; ++r)
      gld16_asm(xa[r][ks], xnf + ((size_t)(((m0 >> 4) + wid * 2 + r) * 18 + ks) * 64 + lane) * 8);
  wait_vm<24>();
  BAR();
  WF wA[4];
  {
    unsigned ad[4][2];
    g1_addr(ad, smem, lane);
    g1_reads<0>(wA, ad);
  }
  STAMP(1, 1);
  g1_unit<0>(wst, unit0, wfold, wqkv, w1, smem, xa, m0, qkv, hf, wA);
  STAMP(1, 2);
  g1_unit<1>(wst, unit0, wfold, wqkv, w1, smem, xa, m0, qkv, hf, wA);
  STAMP(1, 3);
  g1_unit<2>(wst, unit0, wfold, wqkv, w1, smem, xa, m0, qkv, hf, wA);
  STAMP(1, 4);
  if (extra) {
    int tz = threadIdx.x;
    asm volatile("" : "+v"(tz));
    const int wid2 = __builtin_amdgcn_readfirstlane(tz >> 6), lane2 = tz & 63;
    const int g = lane2 >> 4, q = (lane2 & 15) >> 2, p = lane2 & 3;
    unsigned ad[2];
#pragma unroll
    for (int t = 0; t < 2; ++t) {
      const int k = 8 * g + 4 * t + q;
      const int pr = (k & ~0xC) | ((k & 4) << 1) | ((k & 8) >> 1);
      ad[t] = lds_addr(smem) + G1_XOFF + pr * 32 + p * 8;
    }
    f32x4 acc[2] = {(f32x4){0.f, 0.f, 0.f, 0.f}, (f32x4){0.f, 0.f, 0.f, 0.f}};
#pragma unroll
    for (int kb = 0; kb < 18; kb += 6) {
      WF w[6];
      tr_rd<0 * 1024>(w[0].lo, ad[0] + kb * 1024); tr_rd<0 * 1024>(w[0].hi, ad[1] + kb * 1024);
      tr_rd<1 * 1024>(w[1].lo, ad[0] + kb * 1024); tr_rd<1 * 1024>(w[1].hi, ad[1] + kb * 1024);
      tr_rd<2 * 1024>(w[2].lo, ad[0] + kb * 1024); tr_rd<2 * 1024>(w[2].hi, ad[1] + kb * 1024);
      tr_rd<3 * 1024>(w[3].lo, ad[0] + kb * 1024); tr_rd<3 * 1024>(w[3].hi, ad[1] + kb * 1024);
      tr_rd<4 * 1024>(w[4].lo, ad[0] + kb * 1024); tr_rd<4 * 1024>(w[4].hi, ad[1] + kb * 1024);
      tr_rd<5 * 1024>(w[5].lo, ad[0] + kb * 1024); tr_rd<5 * 1024>(w[5].hi, ad[1] + kb * 1024);
      wait_lgkm<0>();
#pragma unroll
      for (int i = 0; i < 6; ++i) pin(w[i]);
#pragma unroll
      for (int i = 0; i < 6; ++i)
#pragma unroll
        for (int r = 0; r < 2; ++r)
          acc[r] = __builtin_amdgcn_mfma_f32_16x16x32_bf16(cat(w[i]), xa[r][kb + i], acc[r], 0, 0, 0);
    }
    const u16* bias_lds = reinterpret_cast<const u16*>(smem + G1_BOFF);
#pragma unroll
    for (int r = 0; r < 2; ++r)
      g1_store_tile(acc[r], xcol / 16, m0 + wid2 * 32 + r * 16 + (lane2 & 15), g, bias_lds + 192 + 4 * g, qkv, hf);
  }
}

constexpr int G2_NT = 9, G2_RB = G2_NT * 32;
constexpr int G2_BUFB = 64 * G2_RB;
constexpr int G2_RING = 4;
constexpr int G2_STAGES = 18;

__device__ __forceinline__ void softmax_pm(const float (&pk)[18], float (&ep)[18], float (&en)[18]) {
  float mx = pk[0], mn = pk[0];
#pragma unroll
  for (int f = 1; f < 18; ++f) {
    mx = fmaxf(mx, pk[f]);
    mn = fminf(mn, pk[f]);
  }
  float sp = 0.f, sn = 0.f;
#pragma unroll
  for (int f = 0; f < 18; ++f) {
    ep[f] = __expf(pk[f] - mx);
    en[f] = __expf(mn - pk[f]);
    sp += ep[f];
    sn += en[f];
  }
  const float rp = 1.f / sp, rn = 1.f / sn;
#pragma unroll
  for (int f = 0; f < 18; ++f) {
    ep[f] *= rp;
    en[f] *= rn;
  }
}

__device__ void head_block(int hb, const u16* __restrict__ qkv, const u16* __restrict__ wo,
                           u16* __restrict__ mcat, u16* __restrict__ phif, float* pkv, unsigned* cnt, char* smem) {
  u16* phiT = reinterpret_cast<u16*>(smem);
  u16* vT = reinterpret_cast<u16*>(smem + 25344);
  float* kvs = reinterpret_cast<float*>(smem + 25344 + 16896);
  float* zs = reinterpret_cast<float*>(smem + 66816);
  const int h = hb >> 3, c8 = hb & 7;
  const int tid = threadIdx.x, wid = tid >> 6, lane = tid & 63;
  const int row = c8 * 256 + tid;
  u32x4 kr[3], vr[3], qr[3];
  {
    const u32x4* kp = reinterpret_cast<const u32x4*>(qkv + ((size_t)(32 + h) * S_ + row) * 24);
    const u32x4* vp = reinterpret_cast<const u32x4*>(qkv + ((size_t)(64 + h) * S_ + row) * 24);
    const u32x4* qp = reinterpret_cast<const u32x4*>(qkv + ((size_t)h * S_ + row) * 24);
#pragma unroll
    for (int d = 0; d < 3; ++d) { kr[d] = kp[d]; vr[d] = vp[d]; qr[d] = qp[d]; }
  }
  for (int i = tid; i < 12 * 264; i += 256) phiT[36 * 264 + i] = 0;
  for (int i = tid; i < 14 * 264; i += 256) vT[18 * 264 + i] = (i < 264) ? (u16)0x3f80 : (u16)0;
  {
    float pk[18];
#pragma unroll
    for (int d = 0; d < 9; ++d) {
      pk[2 * d] = lo16(kr[d >> 2][d & 3]);
      pk[2 * d + 1] = hi16(kr[d >> 2][d & 3]);
    }
    float ep[18], en[18];
    softmax_pm(pk, ep, en);
#pragma unroll
    for (int f = 0; f < 18; ++f) {
      phiT[f * 264 + tid] = f2bf(ep[f]);
      phiT[(18 + f) * 264 + tid] = f2bf(en[f]);
      const unsigned w = vr[(f >> 1) >> 2][(f >> 1) & 3];
      vT[f * 264 + tid] = (u16)((f & 1) ? (w >> 16) : (w & 0xffff));
    }
  }
  __syncthreads();
  {
    f32x4 acc[3][2];
#pragma unroll
    for (int a = 0; a < 3; ++a)
#pragma unroll
      for (int b = 0; b < 2; ++b) acc[a][b] = (f32x4){0.f, 0.f, 0.f, 0.f};
#pragma unroll
    for (int ks = 0; ks < 2; ++ks) {
      const int s0 = wid * 64 + ks * 32 + 8 * (lane >> 4);
      bf16x8 bv[2];
#pragma unroll
      for (int b = 0; b < 2; ++b) bv[b] = *reinterpret_cast<const bf16x8*>(vT + (b * 16 + (lane & 15)) * 264 + s0);
#pragma unroll
      for (int a = 0; a < 3; ++a) {
        bf16x8 av = *reinterpret_cast<const bf16x8*>(phiT + (a * 16 + (lane & 15)) * 264 + s0);
#pragma unroll
        for (int b = 0; b < 2; ++b) acc[a][b] = __builtin_amdgcn_mfma_f32_16x16x32_bf16(av, bv[b], acc[a][b], 0, 0, 0);
      }
    }
#pragma unroll
    for (int a = 0; a < 3; ++a)
#pragma unroll
      for (int b = 0; b < 2; ++b)
#pragma unroll
        for (int j = 0; j < 4; ++j)
          kvs[(wid * 48 + a * 16 + (lane >> 4) * 4 + j) * 32 + b * 16 + (lane & 15)] = acc[a][b][j];
  }
  __syncthreads();
  float* mypart = pkv + (size_t)(h * 8 + c8) * 1536;
#pragma unroll
  for (int it = 0; it < 6; ++it) {
    const int i = it * 256 + tid;
    const float v = kvs[i] + kvs[1536 + i] + kvs[2 * 1536 + i] + kvs[3 * 1536 + i];
    __hip_atomic_store(mypart + i, v, __ATOMIC_RELAXED, __HIP_MEMORY_SCOPE_AGENT);
  }
  asm volatile("s_waitcnt vmcnt(0)" ::: "memory");
  __syncthreads();
  if (tid == 0) __hip_atomic_fetch_add(cnt + h * 32, 1u, __ATOMIC_RELAXED, __HIP_MEMORY_SCOPE_AGENT);
  float ep[18], en[18];
  {
    float pk[18];
#pragma unroll
    for (int d = 0; d < 9; ++d) {
      pk[2 * d] = lo16(qr[d >> 2][d & 3]);
      pk[2 * d + 1] = hi16(qr[d >> 2][d & 3]);
    }
    softmax_pm(pk, ep, en);
#pragma unroll
    for (int f = 0; f < 18; ++f) { ep[f] = rbf(ep[f]); en[f] = rbf(en[f]); }
  }
  if (c8 == 0) {
    u16* woimg = reinterpret_cast<u16*>(smem);
#pragma unroll
    for (int it = 0; it < 9; ++it) {
      const int i = it * 256 + tid;
      const int r = i / 72, c = i % 72;
      u32x4 v = {0u, 0u, 0u, 0u};
      if (r < 18) v = *reinterpret_cast<const u32x4*>(wo + (size_t)(h * 18 + r) * D_ + c * 8);
      *reinterpret_cast<u32x4*>(woimg + r * 576 + c * 8) = v;
    }
  }
  if (tid == 0) {
    unsigned spins = 0;
    while (__hip_atomic_load(cnt + h * 32, __ATOMIC_RELAXED, __HIP_MEMORY_SCOPE_AGENT) < 8u) {
      __builtin_amdgcn_s_sleep(1);
      if (++spins > (1u << 24)) break;
    }
  }
  __syncthreads();
  float* hp = pkv + (size_t)h * 8 * 1536;
  if (tid < 36) {
    float zp[8];
#pragma unroll
    for (int c = 0; c < 8; ++c) zp[c] = __hip_atomic_load(hp + c * 1536 + tid * 32 + 18, __ATOMIC_RELAXED, __HIP_MEMORY_SCOPE_AGENT);
    float z = 0.f;
#pragma unroll
    for (int c = 0; c < 8; ++c) z += zp[c];
    zs[tid] = rbf(z);
  }
  u16* kvb = reinterpret_cast<u16*>(smem + 36864);
  if (c8 == 0) {
    float pv[6][8];
#pragma unroll
    for (int it = 0; it < 6; ++it)
#pragma unroll
      for (int c = 0; c < 8; ++c)
        pv[it][c] = __hip_atomic_load(hp + c * 1536 + it * 256 + tid, __ATOMIC_RELAXED, __HIP_MEMORY_SCOPE_AGENT);
#pragma unroll
    for (int it = 0; it < 6; ++it) {
      const int i = it * 256 + tid;
      float v = 0.f;
#pragma unroll
      for (int c = 0; c < 8; ++c) v += pv[it][c];
      const int f = i >> 5, d = i & 31;
      kvb[i] = (d < 18 && f < 36) ? f2bf(v) : (u16)0;
    }
  }
  __syncthreads();
  {
    float den = 0.f;
#pragma unroll
    for (int f = 0; f < 18; ++f) den += ep[f] * zs[f] + en[f] * zs[18 + f];
    const float rd = 1.f / (rbf(den) + 1e-6f);
    unsigned o[20];
#pragma unroll
    for (int f = 0; f < 9; ++f) o[f] = pack2(ep[2 * f] * rd, ep[2 * f + 1] * rd);
#pragma unroll
    for (int f = 0; f < 9; ++f) o[9 + f] = pack2(en[2 * f] * rd, en[2 * f + 1] * rd);
    o[18] = 0;
    o[19] = 0;
#pragma unroll
    for (int c = 0; c < 5; ++c) {
      const int cidx = h * 5 + c;
      u16* dst = phif + ((size_t)((row >> 4) * 40 + (cidx >> 2)) * 64 + (cidx & 3) * 16 + (row & 15)) * 8;
      u32x4 v = {o[4 * c], o[4 * c + 1], o[4 * c + 2], o[4 * c + 3]};
      *reinterpret_cast<u32x4*>(dst) = v;
    }
  }
  if (c8 == 0) {
    const int g = lane >> 4, q = (lane & 15) >> 2, p = lane & 3;
    constexpr int RB = 576 * 2;
    const int o0 = (8 * g + q) * RB + p * 8, o1 = (8 * g + 4 + q) * RB + p * 8;
    bf16x8 kb[3];
#pragma unroll
    for (int a = 0; a < 3; ++a) kb[a] = *reinterpret_cast<const bf16x8*>(kvb + (a * 16 + (lane & 15)) * 32 + 8 * g);
    for (int nt = wid; nt < 36; nt += 4) {
      bf16x8 w = wfrag(smem, o0, o1, nt * 32);
#pragma unroll
      for (int a = 0; a < 3; ++a) {
        f32x4 c = __builtin_amdgcn_mfma_f32_16x16x32_bf16(w, kb[a], (f32x4){0.f, 0.f, 0.f, 0.f}, 0, 0, 0);
        const int f = a * 16 + (lane & 15);
        if (f < KPH_) {
          u32x2 v = {pack2(c[0], c[1]), pack2(c[2], c[3])};
          *reinterpret_cast<u32x2*>(mcat + (size_t)(h * KPH_ + f) * D_ + nt * 16 + 4 * g) = v;
        }
      }
    }
  }
}

template <int BASE, int N0, int CNT, int I = 0>
__device__ __forceinline__ void g2_wreads(WF (&w)[5], unsigned a0, unsigned a1) {
  if constexpr (I < CNT) {
    tr_rd<BASE + (N0 + I) * 32>(w[I].lo, a0);
    tr_rd<BASE + (N0 + I) * 32>(w[I].hi, a1);
    g2_wreads<BASE, N0, CNT, I + 1>(w, a0, a1);
  }
}
template <int N0, int CNT>
__device__ __forceinline__ void g2_mma(WF (&w)[5], const bf16x8 (&ac)[2], f32x4 (&acc)[2][G2_NT]) {
#pragma unroll
  for (int i = 0; i < CNT; ++i) pin(w[i]);
#pragma unroll
  for (int i = 0; i < CNT; ++i)
#pragma unroll
    for (int r = 0; r < 2; ++r)
      acc[r][N0 + i] = __builtin_amdgcn_mfma_f32_16x16x32_bf16(cat(w[i]), ac[r], acc[r][N0 + i], 0, 0, 0);
}
constexpr int K2_HEADBLK = 256;

__global__ __launch_bounds__(256, 2) void k2_gemm2(const u16* __restrict__ hf, const u16* __restrict__ w2,
                                                   float* __restrict__ part, const u16* __restrict__ qkv,
                                                   const u16* __restrict__ wo, u16* __restrict__ phif,
                                                   u16* __restrict__ mcat, float* pkv, unsigned* cnt) {
  __shared__ __attribute__((aligned(16))) char smem[G2_RING * G2_BUFB];
  const int tid = threadIdx.x, wid = tid >> 6, lane = tid & 63;
  int bid = blockIdx.x;
  if (bid < K2_HEADBLK) { head_block(bid, qkv, wo, mcat, phif, pkv, cnt, smem); return; }
  bid -= K2_HEADBLK;
  STAMP(2, 0);
  const int xcd = bid & 7, loc = bid >> 3;
  const int sp = xcd >> 1, mt = (xcd & 1) * 8 + (loc >> 2), nt4 = loc & 3;
  const int m0 = mt * 128, n0 = nt4 * 144, k0 = sp * 1152;
  const int g = lane >> 4, q = (lane & 15) >> 2, p = lane & 3;
  const bool odd = g & 1;
  const unsigned wa0 = lds_addr(smem) + (8 * g + 4 * (g & 1) + q) * G2_RB + p * 8;
  const unsigned wa1 = lds_addr(smem) + (8 * g + 4 * (1 - (g & 1)) + q) * G2_RB + p * 8;
  f32x4 acc[2][G2_NT];
#pragma unroll
  for (int r = 0; r < 2; ++r)
#pragma unroll
    for (int n = 0; n < G2_NT; ++n) acc[r][n] = (f32x4){0.f, 0.f, 0.f, 0.f};
  const u16* wp[5];
  int wdst[5];
#pragma unroll
  for (int i = 0; i < 5; ++i) {
    int ins = wid + 4 * i;
    if (ins >= 18) ins -= 18;
    int c = ins * 64 + lane;
    int row = c / 18, cc = c % 18;
    wp[i] = w2 + (size_t)(k0 + row) * D_ + n0 + cc * 8;
    wdst[i] = c * 16;
  }
  const u16* ap[2];
#pragma unroll
  for (int r = 0; r < 2; ++r) ap[r] = hf + ((size_t)((m0 >> 4) + wid * 2 + r) * 144 + (k0 >> 5)) * 512 + lane * 8;
  bf16x8 an[4][2][2];
  auto issue = [&](auto SL) {
    constexpr int slot = decltype(SL)::value;
    char* buf = smem + slot * G2_BUFB;
#pragma unroll
    for (int i = 0; i < 5; ++i) { glds16(wp[i], buf + wdst[i]); wp[i] += 64 * D_; }
#pragma unroll
    for (int r = 0; r < 2; ++r) {
      gld16_asm(an[slot][r][0], ap[r]);
      gld16_asm(an[slot][r][1], ap[r] + 512);
      ap[r] += 1024;
    }
  };
  issue(IC<0>{});
  issue(IC<1>{});
  issue(IC<2>{});
  wait_vm<18>();
  BAR();
  STAMP(2, 1);
  WF wA[5], wB[5];
  g2_wreads<0, 0, 5>(wA, wa0, wa1);
  auto stage = [&](auto U, auto LAST) {
    constexpr int u = decltype(U)::value;
    constexpr int rem = decltype(LAST)::value;
    constexpr int BASE = u * G2_BUFB;
    bf16x8 c0[2], c1[2];
#pragma unroll
    for (int r = 0; r < 2; ++r) {
      pin(an[u][r][0]); pin(an[u][r][1]);
      c0[r] = swap_halves_if(an[u][r][0], odd);
      c1[r] = swap_halves_if(an[u][r][1], odd);
    }
    g2_wreads<BASE, 5, 4>(wB, wa0, wa1);
    wait_lgkm<8>();
    g2_mma<0, 5>(wA, c0, acc);
    g2_wreads<BASE + 32 * G2_RB, 0, 5>(wA, wa0, wa1);
    wait_lgkm<10>();
    g2_mma<5, 4>(wB, c0, acc);
    g2_wreads<BASE + 32 * G2_RB, 5, 4>(wB, wa0, wa1);
    wait_lgkm<8>();
    g2_mma<0, 5>(wA, c1, acc);
    if constexpr (rem >= 1) {
      if constexpr (rem >= 2) wait_vm<9>(); else wait_vm<0>();
      BAR();
      if constexpr (rem >= 3) issue(IC<(u + 3) & 3>{});
      g2_wreads<((u + 1) & 3) * G2_BUFB, 0, 5>(wA, wa0, wa1);
      wait_lgkm<10>();
    } else {
      wait_lgkm<0>();
    }
    g2_mma<5, 4>(wB, c1, acc);
  };
#pragma unroll 1
  for (int it = 0; it < 3; ++it) {
    stage(IC<0>{}, IC<3>{}); stage(IC<1>{}, IC<3>{}); stage(IC<2>{}, IC<3>{}); stage(IC<3>{}, IC<3>{});
  }
  STAMP(2, 2);
  stage(IC<0>{}, IC<3>{}); stage(IC<1>{}, IC<3>{}); stage(IC<2>{}, IC<3>{});
  stage(IC<3>{}, IC<2>{}); stage(IC<0>{}, IC<1>{}); stage(IC<1>{}, IC<0>{});
  STAMP(2, 3);
#pragma unroll
  for (int r = 0; r < 2; ++r) {
    const int m = m0 + wid * 32 + r * 16 + (lane & 15);
    float* pp = part + ((size_t)sp * S_ + m) * D_ + n0 + 4 * g;
#pragma unroll
    for (int n = 0; n < G2_NT; ++n) *reinterpret_cast<f32x4*>(pp + n * 16) = acc[r][n];
  }
  asm volatile("s_waitcnt vmcnt(0)" ::: "memory");
  STAMP(2, 4);
}

constexpr int G3_BUFB = 128 * 128;
constexpr int G3_RING = 3;

__global__ __launch_bounds__(512, 2) void k3_final(const u16* __restrict__ phif, const u16* __restrict__ mcat,
                                                   const float* __restrict__ part, const u16* __restrict__ b2,
                                                   const u16* __restrict__ x, u16* __restrict__ out) {
  __shared__ __attribute__((aligned(16))) char smem[G3_RING * G3_BUFB + 1024];
  const int tid = threadIdx.x, wid = tid >> 6, lane = tid & 63;
  const int bid = blockIdx.x;
  const int xcd = bid & 7, loc = bid >> 3;
  const int mt = xcd * 2 + loc / 9, nt = loc % 9;
  const int m0 = mt * 128, n0 = nt * 64;
  const int g = lane >> 4, q = (lane & 15) >> 2, p = lane & 3;
  const int wr = wid >> 1, wc = wid & 1;
  STAMP(3, 0);
  const u16* wp[2];
#pragma unroll
  for (int i = 0; i < 2; ++i) {
    const int row = (wid * 2 + i) * 8 + (lane >> 3);
    const int f = ((row >> 1) & 1) | (((row >> 3) & 1) << 1);
    const int ch = (lane & 7) ^ (f << 1);
    wp[i] = mcat + (size_t)row * D_ + n0 + ch * 8;
  }
  const u16* ap[2];
#pragma unroll
  for (int r = 0; r < 2; ++r) ap[r] = phif + ((size_t)((m0 >> 4) + wr * 2 + r) * 40) * 512 + lane * 8;
  bf16x8 an[3][2][4];
  auto issue = [&](auto SL) {
    constexpr int slot = decltype(SL)::value;
    char* buf = smem + slot * G3_BUFB;
#pragma unroll
    for (int i = 0; i < 2; ++i) { glds16(wp[i], buf + (wid * 2 + i) * 1024 + lane * 16); wp[i] += 128 * D_; }
#pragma unroll
    for (int r = 0; r < 2; ++r) {
#pragma unroll
      for (int ks = 0; ks < 4; ++ks) gld16_asm(an[slot][r][ks], ap[r] + ks * 512);
      ap[r] += 2048;
    }
  };
  issue(IC<0>{});
  issue(IC<1>{});
  bf16x8 py[2][2][4];
  u32x2 xx[2][2], bb[2];
#pragma unroll
  for (int r = 0; r < 2; ++r) {
    const int m = m0 + wr * 32 + r * 16 + (lane & 15);
#pragma unroll
    for (int n = 0; n < 2; ++n) {
      const int ncol = n0 + wc * 32 + n * 16 + 4 * g;
#pragma unroll
      for (int sI = 0; sI < SPLITK_; ++sI) gld16_asm(py[r][n][sI], part + ((size_t)sI * S_ + m) * D_ + ncol);
      asm volatile("global_load_dwordx2 %0, %1, off" : "=v"(xx[r][n]) : "v"(x + (size_t)m * D_ + ncol) : "memory");
    }
  }
#pragma unroll
  for (int n = 0; n < 2; ++n)
    asm volatile("global_load_dwordx2 %0, %1, off" : "=v"(bb[n]) : "v"(b2 + n0 + wc * 32 + n * 16 + 4 * g) : "memory");
  unsigned ad[2][2];
#pragma unroll
  for (int t = 0; t < 2; ++t) {
    const int row = 8 * g + 4 * t + q;
    const int f = ((row >> 1) & 1) | (((row >> 3) & 1) << 1);
#pragma unroll
    for (int n = 0; n < 2; ++n) ad[n][t] = lds_addr(smem) + row * 128 + (((wc * 2 + n) ^ f) * 32) + p * 8;
  }
  f32x4 acc[2][2];
#pragma unroll
  for (int r = 0; r < 2; ++r)
#pragma unroll
    for (int n = 0; n < 2; ++n) acc[r][n] = (f32x4){0.f, 0.f, 0.f, 0.f};
  STAMP(3, 1);
  auto stage = [&](auto U, auto WAITN, auto MORE) {
    constexpr int u = decltype(U)::value;
    wait_vm<decltype(WAITN)::value>();
    BAR();
    if constexpr (decltype(MORE)::value) issue(IC<(u + 2) % 3>{});
    constexpr int BASE = u * G3_BUFB;
    WF w[4][2];
#pragma unroll
    for (int n = 0; n < 2; ++n) { tr_rd<BASE + 0 * 4096>(w[0][n].lo, ad[n][0]); tr_rd<BASE + 0 * 4096>(w[0][n].hi, ad[n][1]); }
#pragma unroll
    for (int n = 0; n < 2; ++n) { tr_rd<BASE + 1 * 4096>(w[1][n].lo, ad[n][0]); tr_rd<BASE + 1 * 4096>(w[1][n].hi, ad[n][1]); }
#pragma unroll
    for (int n = 0; n < 2; ++n) { tr_rd<BASE + 2 * 4096>(w[2][n].lo, ad[n][0]); tr_rd<BASE + 2 * 4096>(w[2][n].hi, ad[n][1]); }
#pragma unroll
    for (int n = 0; n < 2; ++n) { tr_rd<BASE + 3 * 4096>(w[3][n].lo, ad[n][0]); tr_rd<BASE + 3 * 4096>(w[3][n].hi, ad[n][1]); }
#pragma unroll
    for (int r = 0; r < 2; ++r)
#pragma unroll
      for (int ks = 0; ks < 4; ++ks) pin(an[u][r][ks]);
    wait_lgkm<8>();
#pragma unroll
    for (int ks = 0; ks < 2; ++ks)
#pragma unroll
      for (int n = 0; n < 2; ++n) pin(w[ks][n]);
#pragma unroll
    for (int ks = 0; ks < 2; ++ks)
#pragma unroll
      for (int n = 0; n < 2; ++n)
#pragma unroll
        for (int r = 0; r < 2; ++r)
          acc[r][n] = __builtin_amdgcn_mfma_f32_16x16x32_bf16(cat(w[ks][n]), an[u][r][ks], acc[r][n], 0, 0, 0);
    wait_lgkm<0>();
#pragma unroll
    for (int ks = 2; ks < 4; ++ks)
#pragma unroll
      for (int n = 0; n < 2; ++n) pin(w[ks][n]);
#pragma unroll
    for (int ks = 2; ks < 4; ++ks)
#pragma unroll
      for (int n = 0; n < 2; ++n)
#pragma unroll
        for (int r = 0; r < 2; ++r)
          acc[r][n] = __builtin_amdgcn_mfma_f32_16x16x32_bf16(cat(w[ks][n]), an[u][r][ks], acc[r][n], 0, 0, 0);
  };
  stage(IC<0>{}, IC<32>{}, IC<1>{});
  stage(IC<1>{}, IC<32>{}, IC<1>{});
  STAMP(3, 2);
  stage(IC<2>{}, IC<10>{}, IC<1>{});
  stage(IC<0>{}, IC<10>{}, IC<1>{});
  stage(IC<1>{}, IC<10>{}, IC<1>{});
  stage(IC<2>{}, IC<10>{}, IC<1>{});
  stage(IC<0>{}, IC<10>{}, IC<1>{});
  stage(IC<1>{}, IC<10>{}, IC<1>{});
  stage(IC<2>{}, IC<10>{}, IC<0>{});
  stage(IC<0>{}, IC<0>{}, IC<0>{});
  STAMP(3, 3);
#pragma unroll
  for (int n = 0; n < 2; ++n) asm volatile("" : "+v"(bb[n]));
#pragma unroll
  for (int r = 0; r < 2; ++r) {
    const int m = m0 + wr * 32 + r * 16 + (lane & 15);
#pragma unroll
    for (int n = 0; n < 2; ++n) {
      const int ncol = n0 + wc * 32 + n * 16 + 4 * g;
      asm volatile("" : "+v"(xx[r][n]));
#pragma unroll
      for (int sI = 0; sI < SPLITK_; ++sI) pin(py[r][n][sI]);
      f32x4 y = *reinterpret_cast<f32x4*>(&py[r][n][0]);
#pragma unroll
      for (int sI = 1; sI < SPLITK_; ++sI) y += *reinterpret_cast<f32x4*>(&py[r][n][sI]);
      float bv[4] = {lo16(bb[n][0]), hi16(bb[n][0]), lo16(bb[n][1]), hi16(bb[n][1])};
      float xv[4] = {lo16(xx[r][n][0]), hi16(xx[r][n][0]), lo16(xx[r][n][1]), hi16(xx[r][n][1])};
      float o[4];
#pragma unroll
      for (int j = 0; j < 4; ++j) {
        float yy = y[j] + bv[j];
        float gg = yy / (1.f + __expf(-yy));
        o[j] = xv[j] + acc[r][n][j] * gg;
      }
      u32x2 v = {pack2(o[0], o[1]), pack2(o[2], o[3])};
      *reinterpret_cast<u32x2*>(out + (size_t)m * D_ + ncol) = v;
    }
  }
  STAMP(3, 4);
}

extern "C" void kernel_launch(void* const* d_in, const int* in_sizes, int n_in, void* d_out, int out_size, void* d_ws,
                              size_t ws_size, hipStream_t stream) {
  const u16* x = (const u16*)d_in[0];
  const u16* wqkv = (const u16*)d_in[1];
  const u16* wphi = (const u16*)d_in[2];
  const u16* wo = (const u16*)d_in[3];
  const u16* w1 = (const u16*)d_in[5];
  const u16* b1 = (const u16*)d_in[6];
  const u16* w2 = (const u16*)d_in[7];
  const u16* b2 = (const u16*)d_in[8];
  u16* out = (u16*)d_out;
  char* ws = (char*)d_ws;
  size_t off = 0;
  u16* xnf = (u16*)(ws + off); off += (size_t)S_ * D_ * 2;
  u16* wfold = (u16*)(ws + off); off += (size_t)D_ * NPH_ * 2;
  u16* qkv = (u16*)(ws + off); off += (size_t)96 * S_ * 24 * 2;
  u16* hf = (u16*)(ws + off); off += (size_t)S_ * F_ * 2;
  float* part = (float*)(ws + off); off += (size_t)SPLITK_ * S_ * D_ * 4;
  u16* phif = (u16*)(ws + off); off += (size_t)S_ * KPHI_ * 2;
  u16* mcat = (u16*)(ws + off); off += (size_t)KPHI_ * D_ * 2;
  float* pkv = (float*)(ws + off); off += (size_t)256 * 1536 * 4;
  unsigned* cnt = (unsigned*)(ws + off); off += 32 * 32 * 4;

  k0_prep<<<512 + 192, 256, 0, stream>>>(x, wqkv, wphi, xnf, wfold, cnt);
  k1_gemm1<<<256, 512, 0, stream>>>(xnf, wfold, wqkv, w1, b1, qkv, hf);
  k2_gemm2<<<K2_HEADBLK + 256, 256, 0, stream>>>(hf, w2, part, qkv, wo, phif, mcat, pkv, cnt);
  k3_final<<<16 * 9, 512, 0, stream>>>(phif, mcat, part, b2, x, out);
}
```

```cpp
#include <hip/hip_runtime.h>
#include <hip/hip_bf16.h>
#include <stdint.h>

constexpr int S_ = 2048, D_ = 576, H_ = 32, DH_ = 18, F_ = 4608, NQKV_ = 1728;
constexpr int NPH_ = 1152;
constexpr int KPH_ = 40;
constexpr int KPHI_ = H_ * KPH_;
constexpr int SPLITK_ = 4;

typedef short bf16x8 __attribute__((ext_vector_type(8)));
typedef short s16x4 __attribute__((ext_vector_type(4)));
typedef float f32x4 __attribute__((ext_vector_type(4)));
typedef unsigned u32x4 __attribute__((ext_vector_type(4)));
typedef unsigned u32x2 __attribute__((ext_vector_type(2)));
typedef __attribute__((address_space(3))) s16x4* lds_s16x4_p;
typedef unsigned short u16;
template <int V> struct IC { static constexpr int value = V; };

#define STAMP(kid, i) do { } while (0)
#define WAIT_LGKM0() asm volatile("s_waitcnt lgkmcnt(0)" ::: "memory")
#define BAR() __builtin_amdgcn_s_barrier()

template <int N>
__device__ __forceinline__ void wait_vm() {
  static_assert(N >= 0 && N < 64, "vmcnt range");
  asm volatile("s_waitcnt vmcnt(%0)" ::"n"(N) : "memory");
}

__device__ __forceinline__ float bf2f(u16 v) { return __uint_as_float(((unsigned)v) << 16); }
__device__ __forceinline__ u16 f2bf(float f) {
  __hip_bfloat16 b = __float2bfloat16(f);
  return *reinterpret_cast<u16*>(&b);
}
__device__ __forceinline__ unsigned pack2(float lo, float hi) {
  return (unsigned)f2bf(lo) | ((unsigned)f2bf(hi) << 16);
}
__device__ __forceinline__ float rbf(float f) { return bf2f(f2bf(f)); }
__device__ __forceinline__ float lo16(unsigned u) { return __uint_as_float(u << 16); }
__device__ __forceinline__ float hi16(unsigned u) { return __uint_as_float(u & 0xffff0000u); }

__device__ __forceinline__ void glds16(const void* g, void* l) {
  __builtin_amdgcn_global_load_lds((const unsigned*)g, (unsigned*)l, 16, 0, 0);
}

__device__ __forceinline__ void l2_touch(const void* g, void* ldummy) {
  __builtin_amdgcn_global_load_lds((const unsigned*)g, (unsigned*)ldummy, 4, 0, 0);
}

__device__ __forceinline__ bf16x8 swap_halves_if(bf16x8 v, bool odd) {
  bf16x8 s = __builtin_shufflevector(v, v, 4, 5, 6, 7, 0, 1, 2, 3);
  return odd ? s : v;
}

__device__ __forceinline__ bf16x8 wfrag(const char* img, int o0, int o1, int off) {
  s16x4 a = __builtin_amdgcn_ds_read_tr16_b64_v4i16((lds_s16x4_p)(img + o0 + off));
  s16x4 b = __builtin_amdgcn_ds_read_tr16_b64_v4i16((lds_s16x4_p)(img + o1 + off));
  return __builtin_shufflevector(a, b, 0, 1, 2, 3, 4, 5, 6, 7);
}

struct WF { s16x4 lo, hi; };
template <int OFF>
__device__ __forceinline__ void tr_rd(s16x4& r, unsigned addr) {
  asm volatile("ds_read_b64_tr_b16 %0, %1 offset:%2" : "=v"(r) : "v"(addr), "n"(OFF) : "memory");
}
__device__ __forceinline__ void gld16_asm(bf16x8& r, const void* p) {
  asm volatile("global_load_dwordx4 %0, %1, off" : "=v"(r) : "v"(p) : "memory");
}
template <int N>
__device__ __forceinline__ void wait_lgkm() {
  static_assert(N >= 0 && N <= 15, "lgkmcnt range");
  asm volatile("s_waitcnt lgkmcnt(%0)" ::"n"(N) : "memory");
}
__device__ __forceinline__ void pin(WF& w) { asm volatile("" : "+v"(w.lo), "+v"(w.hi)); }
__device__ __forceinline__ void pin(bf16x8& w) { asm volatile("" : "+v"(w)); }
__device__ __forceinline__ bf16x8 cat(const WF& w) { return __builtin_shufflevector(w.lo, w.hi, 0, 1, 2, 3, 4, 5, 6, 7); }
__device__ __forceinline__ unsigned lds_addr(const void* p) {
  return (unsigned)(uintptr_t)(__attribute__((address_space(3))) const char*)p;
}

__global__ __launch_bounds__(256) void k0_prep(const u16* __restrict__ x, const u16* __restrict__ wqkv,
                                               const u16* __restrict__ wphi, u16* __restrict__ xnf,
                                               u16* __restrict__ wfold, unsigned* __restrict__ cnt) {
  __shared__ __attribute__((aligned(16))) float wph[18 * 20];
  const int tid = threadIdx.x, wid = tid >> 6, lane = tid & 63;
  const int bid = blockIdx.x;
  if (bid == 512 && tid < 32) cnt[tid * 32] = 0u;
  if (bid < 512) {
    const int row = bid * 4 + wid;
    const u16* xp = x + (size_t)row * D_;
    u32x4 a = *reinterpret_cast<const u32x4*>(xp + lane * 8);
    u32x4 b = {0u, 0u, 0u, 0u};
    if (lane < 8) b = *reinterpret_cast<const u32x4*>(xp + 512 + lane * 8);
    float v[16];
#pragma unroll
    for (int j = 0; j < 4; ++j) {
      v[2 * j] = lo16(a[j]); v[2 * j + 1] = hi16(a[j]);
      v[8 + 2 * j] = lo16(b[j]); v[8 + 2 * j + 1] = hi16(b[j]);
    }
    float s = 0.f;
#pragma unroll
    for (int j = 0; j < 16; ++j) s += v[j];
#pragma unroll
    for (int o = 32; o >= 1; o >>= 1) s += __shfl_xor(s, o);
    const float mean = s * (1.f / D_);
    float ss = 0.f;
#pragma unroll
    for (int j = 0; j < 8; ++j) { float d = v[j] - mean; ss += d * d; }
    if (lane < 8) {
#pragma unroll
      for (int j = 8; j < 16; ++j) { float d = v[j] - mean; ss += d * d; }
    }
#pragma unroll
    for (int o = 32; o >= 1; o >>= 1) ss += __shfl_xor(ss, o);
    const float rstd = rsqrtf(ss * (1.f / D_) + 1e-5f);
    u32x4 oa, ob;
#pragma unroll
    for (int j = 0; j < 4; ++j) {
      oa[j] = pack2((v[2 * j] - mean) * rstd, (v[2 * j + 1] - mean) * rstd);
      ob[j] = pack2((v[8 + 2 * j] - mean) * rstd, (v[8 + 2 * j + 1] - mean) * rstd);
    }
    {
      const int c = lane;
      u16* dst = xnf + ((size_t)((row >> 4) * 18 + (c >> 2)) * 64 + (c & 3) * 16 + (row & 15)) * 8;
      *reinterpret_cast<u32x4*>(dst) = oa;
    }
    if (lane < 8) {
      const int c = 64 + lane;
      u16* dst = xnf + ((size_t)((row >> 4) * 18 + (c >> 2)) * 64 + (c & 3) * 16 + (row & 15)) * 8;
      *reinterpret_cast<u32x4*>(dst) = ob;
    }
    return;
  }
  {
    const int fb = bid - 512;
    const int hh = fb / 3, k = (fb % 3) * 192 + tid;
    const int h = hh & 31;
    for (int i = tid; i < 18 * 20; i += 256) {
      const int d = i / 20, f = i % 20;
      wph[i] = (f < 18) ? bf2f(wphi[h * 324 + d * 18 + f]) : 0.f;
    }
    unsigned wv[9];
    if (tid < 192) {
      const u16* wp = wqkv + (size_t)k * NQKV_ + hh * 18;
#pragma unroll
      for (int d = 0; d < 9; ++d) wv[d] = *reinterpret_cast<const unsigned*>(wp + 2 * d);
    }
    __syncthreads();
    if (tid < 192) {
      float acc[20];
#pragma unroll
      for (int f = 0; f < 20; ++f) acc[f] = 0.f;
#pragma unroll
      for (int d = 0; d < 18; ++d) {
        const float w = (d & 1) ? hi16(wv[d >> 1]) : lo16(wv[d >> 1]);
#pragma unroll
        for (int f4 = 0; f4 < 5; ++f4) {
          f32x4 pw = *reinterpret_cast<const f32x4*>(wph + d * 20 + f4 * 4);
#pragma unroll
          for (int j = 0; j < 4; ++j) acc[f4 * 4 + j] += w * pw[j];
        }
      }
      u16* op = wfold + (size_t)k * NPH_ + hh * 18;
#pragma unroll
      for (int f = 0; f < 9; ++f) *reinterpret_cast<unsigned*>(op + 2 * f) = pack2(acc[2 * f], acc[2 * f + 1]);
    }
  }
}

constexpr int G1_BUFB = 192 * 128;
constexpr int G1_RING = 4;
constexpr int G1_XOFF = G1_RING * G1_BUFB;
constexpr int G1_BOFF = G1_XOFF + 576 * 32;
constexpr int G1_SMEM = G1_BOFF + 1024;
constexpr int G1_NST = 8;

template <int BASE, int I = 0>
__device__ __forceinline__ void g1_reads(WF (&w)[4], const unsigned (&ad)[4][2]) {
  if constexpr (I < 4) {
    if constexpr (BASE >= 49152) {
      tr_rd<BASE - 49152>(w[I].lo, ad[I][0] + 49152u);
      tr_rd<BASE - 49152>(w[I].hi, ad[I][1] + 49152u);
    } else {
      tr_rd<BASE>(w[I].lo, ad[I][0]);
      tr_rd<BASE>(w[I].hi, ad[I][1]);
    }
    g1_reads<BASE, I + 1>(w, ad);
  }
}
template <int KS>
__device__ __forceinline__ void g1_mma(WF (&w)[4], const bf16x8 (&xa)[2][18], f32x4 (&acc)[2][4]) {
#pragma unroll
  for (int i = 0; i < 4; ++i) pin(w[i]);
#pragma unroll
  for (int i = 0; i < 4; ++i)
#pragma unroll
    for (int r = 0; r < 2; ++r)
      acc[r][i] = __builtin_amdgcn_mfma_f32_16x16x32_bf16(cat(w[i]), xa[r][KS], acc[r][i], 0, 0, 0);
}

__device__ __forceinline__ float gelu_f(float v) {
  constexpr float c1 = -2.f * 0.7978845608028654f * 1.4426950408889634f;
  constexpr float c3 = c1 * 0.044715f;
  const float t = v * v;
  const float e = __builtin_amdgcn_exp2f(v * fmaf(t, c3, c1));
  return v * __builtin_amdgcn_rcpf(1.f + e);
}

__device__ __forceinline__ void g1_store_tile(const f32x4& a, int tile, int mrow, int g, const u16* bias4,
                                              u16* __restrict__ qkv, u16* __restrict__ hf) {
  if (tile < NQKV_ / 16) {
    const int col = tile * 16 + 4 * g;
#pragma unroll
    for (int j = 0; j < 2; ++j) {
      const int c = col + 2 * j;
      const int hh = c / 18, f = c - hh * 18;
      *reinterpret_cast<unsigned*>(qkv + ((size_t)hh * S_ + mrow) * 24 + f) = pack2(a[2 * j], a[2 * j + 1]);
    }
  } else {
    u32x2 bb = *reinterpret_cast<const u32x2*>(bias4);
    float o[4];
    o[0] = gelu_f(a[0] + lo16(bb[0]));
    o[1] = gelu_f(a[1] + hi16(bb[0]));
    o[2] = gelu_f(a[2] + lo16(bb[1]));
    o[3] = gelu_f(a[3] + hi16(bb[1]));
    u32x2 v = {pack2(o[0], o[1]), pack2(o[2], o[3])};
    const int T = tile - NQKV_ / 16;
    u16* dst = hf + ((size_t)((mrow >> 4) * 144 + (T >> 1)) * 64 + ((T & 1) * 2 + (g >> 1)) * 16 + (mrow & 15)) * 8 + (g & 1) * 4;
    *reinterpret_cast<u32x2*>(dst) = v;
  }
}

__device__ __forceinline__ void g1_addr(unsigned (&ad)[4][2], char* smem, int lane) {
  const int g = lane >> 4, q = (lane & 15) >> 2, p = lane & 3;
#pragma unroll
  for (int t = 0; t < 2; ++t) {
    const int row = 8 * g + 4 * t + q;
    const int f = ((row >> 1) & 1) | (((row >> 3) & 1) << 1);
#pragma unroll
    for (int n = 0; n < 4; ++n) ad[n][t] = lds_addr(smem) + row * 128 + ((n ^ f) * 32) + p * 8;
  }
}

struct G1W { const u16* src; int stride; };

__device__ __forceinline__ void g1_wsetup(G1W& w, int col, int wid, int lane, const u16* wfold, const u16* wqkv, const u16* w1) {
  const int row = wid * 8 + (lane >> 3);
  const int f = ((row >> 1) & 1) | (((row >> 3) & 1) << 1);
  const int ch = (lane & 7) ^ (f << 1);
  if (col < NPH_) { w.src = wfold + (size_t)row * NPH_ + col + ch * 8; w.stride = 64 * NPH_; }
  else if (col < NQKV_) { w.src = wqkv + (size_t)row * NQKV_ + col + ch * 8; w.stride = 64 * NQKV_; }
  else { w.src = w1 + (size_t)row * F_ + (col - NQKV_) + ch * 8; w.stride = 64 * F_; }
}
__device__ __forceinline__ void g1_wissue(G1W& w, char* buf, int wid, int lane) {
#pragma unroll
  for (int i = 0; i < 3; ++i) {
    glds16(w.src, buf + i * 8192 + wid * 1024 + lane * 16);
    w.src += w.stride;
  }
}

template <int U>
__device__ __forceinline__ void g1_unit(G1W& wst, int unit0, const u16* wfold, const u16* wqkv,
                                        const u16* w1, char* smem, bf16x8 (&xa)[2][18], int m0,
                                        u16* __restrict__ qkv, u16* __restrict__ hf, WF (&wA)[4]) {
  int tz = threadIdx.x;
  asm volatile("" : "+v"(tz));
  const int wid = __builtin_amdgcn_readfirstlane(tz >> 6), lane = tz & 63;
  const int g = lane >> 4;
  unsigned ad[4][2];
  g1_addr(ad, smem, lane);
  f32x4 acc[2][4];
#pragma unroll
  for (int r = 0; r < 2; ++r)
#pragma unroll
    for (int n = 0; n < 4; ++n) acc[r][n] = (f32x4){0.f, 0.f, 0.f, 0.f};

  auto stage = [&](auto KC) {
    constexpr int kc = decltype(KC)::value;
    constexpr int s = U * 3 + kc;
    constexpr int CUR = (s % G1_RING) * G1_BUFB;
    if constexpr (U == 0) {
#pragma unroll
      for (int k = 0; k < 6; ++k) { pin(xa[0][kc * 6 + k]); pin(xa[1][kc * 6 + k]); }
    }
    WF wB[4];
    g1_reads<CUR + 1 * 4096>(wB, ad); wait_lgkm<8>(); g1_mma<kc * 6 + 0>(wA, xa, acc);
    g1_reads<CUR + 2 * 4096>(wA, ad); wait_lgkm<8>(); g1_mma<kc * 6 + 1>(wB, xa, acc);
    g1_reads<CUR + 3 * 4096>(wB, ad); wait_lgkm<8>(); g1_mma<kc * 6 + 2>(wA, xa, acc);
    g1_reads<CUR + 4 * 4096>(wA, ad); wait_lgkm<8>(); g1_mma<kc * 6 + 3>(wB, xa, acc);
    g1_reads<CUR + 5 * 4096>(wB, ad); wait_lgkm<8>(); g1_mma<kc * 6 + 4>(wA, xa, acc);
    if constexpr (s < 8) {
      constexpr int nl = (s == 0) ? 12 : ((s + 2 <= 8) ? 3 : 0);
      constexpr int nst = (U > 0 && kc <= 1) ? G1_NST : 0;
      wait_vm<nl + nst>();
      BAR();
      if constexpr (s + 3 <= 8) {
        constexpr int sn = s + 3;
        if constexpr (sn % 3 == 0) g1_wsetup(wst, (unit0 + sn / 3) * 64, wid, lane, wfold, wqkv, w1);
        g1_wissue(wst, smem + (sn % G1_RING) * G1_BUFB, wid, lane);
      }
      g1_reads<((s + 1) % G1_RING) * G1_BUFB>(wA, ad);
      wait_lgkm<8>();
    } else {
      wait_lgkm<0>();
    }
    g1_mma<kc * 6 + 5>(wB, xa, acc);
  };
  stage(IC<0>{}); stage(IC<1>{}); stage(IC<2>{});
  const u16* bias_lds = reinterpret_cast<const u16*>(smem + G1_BOFF);
#pragma unroll
  for (int n = 0; n < 4; ++n) {
    const int tile = (unit0 + U) * 4 + n;
#pragma unroll
    for (int r = 0; r < 2; ++r)
      g1_store_tile(acc[r][n], tile, m0 + wid * 32 + r * 16 + (lane & 15), g, bias_lds + (U * 4 + n) * 16 + 4 * g, qkv, hf);
  }
}

__global__ __launch_bounds__(512, 2) void k1_gemm1(const u16* __restrict__ xnf, const u16* __restrict__ wfold,
                                                   const u16* __restrict__ wqkv, const u16* __restrict__ w1,
                                                   const u16* __restrict__ b1, u16* __restrict__ qkv,
                                                   u16* __restrict__ hf) {
  __shared__ __attribute__((aligned(16))) char smem[G1_SMEM];
  const int tid = threadIdx.x, wid = tid >> 6, lane = tid & 63;
  const int bid = blockIdx.x;
  const int mt = bid >> 5, grp = bid & 31;
  const int m0 = mt * 256;
  const int unit0 = grp * 3;
  const bool extra = grp < 12;
  const int xcol = 6144 + (extra ? grp : 0) * 16;
  STAMP(1, 0);
  G1W wst;
  g1_wsetup(wst, unit0 * 64, wid, lane, wfold, wqkv, w1);
  g1_wissue(wst, smem + 0 * G1_BUFB, wid, lane);
  g1_wissue(wst, smem + 1 * G1_BUFB, wid, lane);
  g1_wissue(wst, smem + 2 * G1_BUFB, wid, lane);
#pragma unroll
  for (int i = 0; i < 3; ++i) {
    int ins = wid + 8 * i;
    if (ins >= 18) ins -= 18;
    const int c = ins * 64 + lane;
    const int pr = c >> 1, half = c & 1;
    const int k = (pr & ~0xC) | ((pr & 4) << 1) | ((pr & 8) >> 1);
    glds16(w1 + (size_t)k * F_ + (xcol - NQKV_) + half * 8, smem + G1_XOFF + c * 16);
  }
  {
    const int e = (wid & 1) * 64 + lane;
    int col = (e < 96) ? (unit0 * 64 + 2 * e - NQKV_) : (xcol - NQKV_ + 2 * (e - 96));
    if (col < 0 || e >= 104) col = 0;
    __builtin_amdgcn_global_load_lds((const unsigned*)(b1 + col), (unsigned*)(smem + G1_BOFF + e * 4), 4, 0, 0);
  }
  bf16x8 xa[2][18];
#pragma unroll
  for (int ks = 0; ks < 18; ++ks)
#pragma unroll
    for (int r = 0; r < 2; ++r)
      gld16_asm(xa[r][ks], xnf + ((size_t)(((m0 >> 4) + wid * 2 + r) * 18 + ks) * 64 + lane) * 8);
  wait_vm<24>();
  BAR();
  WF wA[4];
  {
    unsigned ad[4][2];
    g1_addr(ad, smem, lane);
    g1_reads<0>(wA, ad);
  }
  STAMP(1, 1);
  g1_unit<0>(wst, unit0, wfold, wqkv, w1, smem, xa, m0, qkv, hf, wA);
  STAMP(1, 2);
  g1_unit<1>(wst, unit0, wfold, wqkv, w1, smem, xa, m0, qkv, hf, wA);
  STAMP(1, 3);
  g1_unit<2>(wst, unit0, wfold, wqkv, w1, smem, xa, m0, qkv, hf, wA);
  STAMP(1, 4);
  if (extra) {
    int tz = threadIdx.x;
    asm volatile("" : "+v"(tz));
    const int wid2 = __builtin_amdgcn_readfirstlane(tz >> 6), lane2 = tz & 63;
    const int g = lane2 >> 4, q = (lane2 & 15) >> 2, p = lane2 & 3;
    unsigned ad[2];
#pragma unroll
    for (int t = 0; t < 2; ++t) {
      const int k = 8 * g + 4 * t + q;
      const int pr = (k & ~0xC) | ((k & 4) << 1) | ((k & 8) >> 1);
      ad[t] = lds_addr(smem) + G1_XOFF + pr * 32 + p * 8;
    }
    f32x4 acc[2] = {(f32x4){0.f, 0.f, 0.f, 0.f}, (f32x4){0.f, 0.f, 0.f, 0.f}};
#pragma unroll
    for (int kb = 0; kb < 18; kb += 6) {
      WF w[6];
      tr_rd<0 * 1024>(w[0].lo, ad[0] + kb * 1024); tr_rd<0 * 1024>(w[0].hi, ad[1] + kb * 1024);
      tr_rd<1 * 1024>(w[1].lo, ad[0] + kb * 1024); tr_rd<1 * 1024>(w[1].hi, ad[1] + kb * 1024);
      tr_rd<2 * 1024>(w[2].lo, ad[0] + kb * 1024); tr_rd<2 * 1024>(w[2].hi, ad[1] + kb * 1024);
      tr_rd<3 * 1024>(w[3].lo, ad[0] + kb * 1024); tr_rd<3 * 1024>(w[3].hi, ad[1] + kb * 1024);
      tr_rd<4 * 1024>(w[4].lo, ad[0] + kb * 1024); tr_rd<4 * 1024>(w[4].hi, ad[1] + kb * 1024);
      tr_rd<5 * 1024>(w[5].lo, ad[0] + kb * 1024); tr_rd<5 * 1024>(w[5].hi, ad[1] + kb * 1024);
      wait_lgkm<0>();
#pragma unroll
      for (int i = 0; i < 6; ++i) pin(w[i]);
#pragma unroll
      for (int i = 0; i < 6; ++i)
#pragma unroll
        for (int r = 0; r < 2; ++r)
          acc[r] = __builtin_amdgcn_mfma_f32_16x16x32_bf16(cat(w[i]), xa[r][kb + i], acc[r], 0, 0, 0);
    }
    const u16* bias_lds = reinterpret_cast<const u16*>(smem + G1_BOFF);
#pragma unroll
    for (int r = 0; r < 2; ++r)
      g1_store_tile(acc[r], xcol / 16, m0 + wid2 * 32 + r * 16 + (lane2 & 15), g, bias_lds + 192 + 4 * g, qkv, hf);
  }
}

constexpr int G2_NT = 9, G2_RB = G2_NT * 32;
constexpr int G2_BUFB = 64 * G2_RB;
constexpr int G2_RING = 4;
constexpr int G2_STAGES = 18;

__device__ __forceinline__ void softmax_pm(const float (&pk)[18], float (&ep)[18], float (&en)[18]) {
  float mx = pk[0], mn = pk[0];
#pragma unroll
  for (int f = 1; f < 18; ++f) {
    mx = fmaxf(mx, pk[f]);
    mn = fminf(mn, pk[f]);
  }
  float sp = 0.f, sn = 0.f;
#pragma unroll
  for (int f = 0; f < 18; ++f) {
    ep[f] = __expf(pk[f] - mx);
    en[f] = __expf(mn - pk[f]);
    sp += ep[f];
    sn += en[f];
  }
  const float rp = 1.f / sp, rn = 1.f / sn;
#pragma unroll
  for (int f = 0; f < 18; ++f) {
    ep[f] *= rp;
    en[f] *= rn;
  }
}

__device__ void head_block(int hb, const u16* __restrict__ qkv, u16* __restrict__ of, float* pkv, unsigned* cnt, char* smem) {
  u16* phiT = reinterpret_cast<u16*>(smem);
  u16* vT = reinterpret_cast<u16*>(smem + 25344);
  float* kvs = reinterpret_cast<float*>(smem + 25344 + 16896);
  const int h = hb >> 3, c8 = hb & 7;
  const int tid = threadIdx.x, wid = tid >> 6, lane = tid & 63;
  const int row = c8 * 256 + tid;
  u32x4 kr[3], vr[3], qr[3];
  {
    const u32x4* kp = reinterpret_cast<const u32x4*>(qkv + ((size_t)(32 + h) * S_ + row) * 24);
    const u32x4* vp = reinterpret_cast<const u32x4*>(qkv + ((size_t)(64 + h) * S_ + row) * 24);
    const u32x4* qp = reinterpret_cast<const u32x4*>(qkv + ((size_t)h * S_ + row) * 24);
#pragma unroll
    for (int d = 0; d < 3; ++d) { kr[d] = kp[d]; vr[d] = vp[d]; qr[d] = qp[d]; }
  }
  for (int i = tid; i < 12 * 264; i += 256) phiT[36 * 264 + i] = 0;
  for (int i = tid; i < 14 * 264; i += 256) vT[18 * 264 + i] = (i < 264) ? (u16)0x3f80 : (u16)0;
  {
    float pk[18];
#pragma unroll
    for (int d = 0; d < 9; ++d) {
      pk[2 * d] = lo16(kr[d >> 2][d & 3]);
      pk[2 * d + 1] = hi16(kr[d >> 2][d & 3]);
    }
    float ep[18], en[18];
    softmax_pm(pk, ep, en);
#pragma unroll
    for (int f = 0; f < 18; ++f) {
      phiT[f * 264 + tid] = f2bf(ep[f]);
      phiT[(18 + f) * 264 + tid] = f2bf(en[f]);
      const unsigned w = vr[(f >> 1) >> 2][(f >> 1) & 3];
      vT[f * 264 + tid] = (u16)((f & 1) ? (w >> 16) : (w & 0xffff));
    }
  }
  __syncthreads();
  {
    f32x4 acc[3][2];
#pragma unroll
    for (int a = 0; a < 3; ++a)
#pragma unroll
      for (int b = 0; b < 2; ++b) acc[a][b] = (f32x4){0.f, 0.f, 0.f, 0.f};
#pragma unroll
    for (int ks = 0; ks < 2; ++ks) {
      const int s0 = wid * 64 + ks * 32 + 8 * (lane >> 4);
      bf16x8 bv[2];
#pragma unroll
      for (int b = 0; b < 2; ++b) bv[b] = *reinterpret_cast<const bf16x8*>(vT + (b * 16 + (lane & 15)) * 264 + s0);
#pragma unroll
      for (int a = 0; a < 3; ++a) {
        bf16x8 av = *reinterpret_cast<const bf16x8*>(phiT + (a * 16 + (lane & 15)) * 264 + s0);
#pragma unroll
        for (int b = 0; b < 2; ++b) acc[a][b] = __builtin_amdgcn_mfma_f32_16x16x32_bf16(av, bv[b], acc[a][b], 0, 0, 0);
      }
    }
#pragma unroll
    for (int a = 0; a < 3; ++a)
#pragma unroll
      for (int b = 0; b < 2; ++b)
#pragma unroll
        for (int j = 0; j < 4; ++j)
          kvs[(wid * 48 + a * 16 + (lane >> 4) * 4 + j) * 32 + b * 16 + (lane & 15)] = acc[a][b][j];
  }
  __syncthreads();
  float* mypart = pkv + (size_t)(h * 8 + c8) * 1536;
#pragma unroll
  for (int it = 0; it < 6; ++it) {
    const int i = it * 256 + tid;
    const float v = kvs[i] + kvs[1536 + i] + kvs[2 * 1536 + i] + kvs[3 * 1536 + i];
    __hip_atomic_store(mypart + i, v, __ATOMIC_RELAXED, __HIP_MEMORY_SCOPE_AGENT);
  }
  asm volatile("s_waitcnt vmcnt(0)" ::: "memory");
  __syncthreads();
  if (tid == 0) __hip_atomic_fetch_add(cnt + h * 32, 1u, __ATOMIC_RELAXED, __HIP_MEMORY_SCOPE_AGENT);
  float ep[18], en[18];
  {
    float pk[18];
#pragma unroll
    for (int d = 0; d < 9; ++d) {
      pk[2 * d] = lo16(qr[d >> 2][d & 3]);
      pk[2 * d + 1] = hi16(qr[d >> 2][d & 3]);
    }
    softmax_pm(pk, ep, en);
#pragma unroll
    for (int f = 0; f < 18; ++f) { ep[f] = rbf(ep[f]); en[f] = rbf(en[f]); }
  }
  if (tid == 0) {
    unsigned spins = 0;
    while (__hip_atomic_load(cnt + h * 32, __ATOMIC_RELAXED, __HIP_MEMORY_SCOPE_AGENT) < 8u) {
      __builtin_amdgcn_s_sleep(1);
      if (++spins > (1u << 24)) break;
    }
  }
  __syncthreads();
  float* kvf = reinterpret_cast<float*>(smem);
  {
    float* hp = pkv + (size_t)h * 8 * 1536;
    float pv[3][8];
#pragma unroll
    for (int it = 0; it < 3; ++it) {
      const int i = it * 256 + tid;
      const int f = i / 20, d = i - f * 20;
      const int src = (i < 720) ? (f * 32 + d) : 0;
#pragma unroll
      for (int c = 0; c < 8; ++c) pv[it][c] = __hip_atomic_load(hp + c * 1536 + src, __ATOMIC_RELAXED, __HIP_MEMORY_SCOPE_AGENT);
    }
#pragma unroll
    for (int it = 0; it < 3; ++it) {
      const int i = it * 256 + tid;
      float v = 0.f;
#pragma unroll
      for (int c = 0; c < 8; ++c) v += pv[it][c];
      if (i < 720) kvf[i] = rbf(v);
    }
  }
  __syncthreads();
  {
    float num[20];
#pragma unroll
    for (int d = 0; d < 20; ++d) num[d] = 0.f;
#pragma unroll
    for (int f = 0; f < 36; ++f) {
      const float ph = (f < 18) ? ep[f] : en[f - 18];
#pragma unroll
      for (int d4 = 0; d4 < 5; ++d4) {
        const f32x4 w = *reinterpret_cast<const f32x4*>(kvf + f * 20 + d4 * 4);
#pragma unroll
        for (int j = 0; j < 4; ++j) num[d4 * 4 + j] += ph * w[j];
      }
    }
    const float rd = 1.f / (rbf(num[18]) + 1e-6f);
#pragma unroll
    for (int d2 = 0; d2 < 9; ++d2) {
      const int k = h * 18 + 2 * d2;
      u16* dst = of + ((size_t)((row >> 4) * 18 + (k >> 5)) * 64 + ((k >> 3) & 3) * 16 + (row & 15)) * 8 + (k & 7);
      *reinterpret_cast<unsigned*>(dst) = pack2(rbf(num[2 * d2]) * rd, rbf(num[2 * d2 + 1]) * rd);
    }
  }
}

template <int BASE, int N0, int CNT, int I = 0>
__device__ __forceinline__ void g2_wreads(WF (&w)[5], unsigned a0, unsigned a1) {
  if constexpr (I < CNT) {
    tr_rd<BASE + (N0 + I) * 32>(w[I].lo, a0);
    tr_rd<BASE + (N0 + I) * 32>(w[I].hi, a1);
    g2_wreads<BASE, N0, CNT, I + 1>(w, a0, a1);
  }
}
template <int N0, int CNT>
__device__ __forceinline__ void g2_mma(WF (&w)[5], const bf16x8 (&ac)[2], f32x4 (&acc)[2][G2_NT]) {
#pragma unroll
  for (int i = 0; i < CNT; ++i) pin(w[i]);
#pragma unroll
  for (int i = 0; i < CNT; ++i)
#pragma unroll
    for (int r = 0; r < 2; ++r)
      acc[r][N0 + i] = __builtin_amdgcn_mfma_f32_16x16x32_bf16(cat(w[i]), ac[r], acc[r][N0 + i], 0, 0, 0);
}
constexpr int K2_HEADBLK = 256;

__global__ __launch_bounds__(256, 2) void k2_gemm2(const u16* __restrict__ hf, const u16* __restrict__ w2,
                                                   u16* __restrict__ part, const u16* __restrict__ qkv,
                                                   u16* __restrict__ of, float* pkv, unsigned* cnt) {
  __shared__ __attribute__((aligned(16))) char smem[G2_RING * G2_BUFB];
  const int tid = threadIdx.x, wid = tid >> 6, lane = tid & 63;
  int bid = blockIdx.x;
  if (bid < K2_HEADBLK) { head_block(bid, qkv, of, pkv, cnt, smem); return; }
  bid -= K2_HEADBLK;
  STAMP(2, 0);
  const int xcd = bid & 7, loc = bid >> 3;
  const int sp = xcd >> 1, mt = (xcd & 1) * 8 + (loc >> 2), nt4 = loc & 3;
  const int m0 = mt * 128, n0 = nt4 * 144, k0 = sp * 1152;
  const int g = lane >> 4, q = (lane & 15) >> 2, p = lane & 3;
  const bool odd = g & 1;
  const unsigned wa0 = lds_addr(smem) + (8 * g + 4 * (g & 1) + q) * G2_RB + p * 8;
  const unsigned wa1 = lds_addr(smem) + (8 * g + 4 * (1 - (g & 1)) + q) * G2_RB + p * 8;
  f32x4 acc[2][G2_NT];
#pragma unroll
  for (int r = 0; r < 2; ++r)
#pragma unroll
    for (int n = 0; n < G2_NT; ++n) acc[r][n] = (f32x4){0.f, 0.f, 0.f, 0.f};
  const u16* wp[5];
  int wdst[5];
#pragma unroll
  for (int i = 0; i < 5; ++i) {
    int ins = wid + 4 * i;
    if (ins >= 18) ins -= 18;
    int c = ins * 64 + lane;
    int row = c / 18, cc = c % 18;
    wp[i] = w2 + (size_t)(k0 + row) * D_ + n0 + cc * 8;
    wdst[i] = c * 16;
  }
  const u16* ap[2];
#pragma unroll
  for (int r = 0; r < 2; ++r) ap[r] = hf + ((size_t)((m0 >> 4) + wid * 2 + r) * 144 + (k0 >> 5)) * 512 + lane * 8;
  bf16x8 an[4][2][2];
  auto issue = [&](auto SL) {
    constexpr int slot = decltype(SL)::value;
    char* buf = smem + slot * G2_BUFB;
#pragma unroll
    for (int i = 0; i < 5; ++i) { glds16(wp[i], buf + wdst[i]); wp[i] += 64 * D_; }
#pragma unroll
    for (int r = 0; r < 2; ++r) {
      gld16_asm(an[slot][r][0], ap[r]);
      gld16_asm(an[slot][r][1], ap[r] + 512);
      ap[r] += 1024;
    }
  };
  issue(IC<0>{});
  issue(IC<1>{});
  issue(IC<2>{});
  wait_vm<18>();
  BAR();
  STAMP(2, 1);
  WF wA[5], wB[5];
  g2_wreads<0, 0, 5>(wA, wa0, wa1);
  auto stage = [&](auto U, auto LAST) {
    constexpr int u = decltype(U)::value;
    constexpr int rem = decltype(LAST)::value;
    constexpr int BASE = u * G2_BUFB;
    bf16x8 c0[2], c1[2];
#pragma unroll
    for (int r = 0; r < 2; ++r) {
      pin(an[u][r][0]); pin(an[u][r][1]);
      c0[r] = swap_halves_if(an[u][r][0], odd);
      c1[r] = swap_halves_if(an[u][r][1], odd);
    }
    g2_wreads<BASE, 5, 4>(wB, wa0, wa1);
    wait_lgkm<8>();
    g2_mma<0, 5>(wA, c0, acc);
    g2_wreads<BASE + 32 * G2_RB, 0, 5>(wA, wa0, wa1);
    wait_lgkm<10>();
    g2_mma<5, 4>(wB, c0, acc);
    g2_wreads<BASE + 32 * G2_RB, 5, 4>(wB, wa0, wa1);
    wait_lgkm<8>();
    g2_mma<0, 5>(wA, c1, acc);
    if constexpr (rem >= 1) {
      if constexpr (rem >= 2) wait_vm<9>(); else wait_vm<0>();
      BAR();
      if constexpr (rem >= 3) issue(IC<(u + 3) & 3>{});
      g2_wreads<((u + 1) & 3) * G2_BUFB, 0, 5>(wA, wa0, wa1);
      wait_lgkm<10>();
    } else {
      wait_lgkm<0>();
    }
    g2_mma<5, 4>(wB, c1, acc);
  };
#pragma unroll 1
  for (int it = 0; it < 3; ++it) {
    stage(IC<0>{}, IC<3>{}); stage(IC<1>{}, IC<3>{}); stage(IC<2>{}, IC<3>{}); stage(IC<3>{}, IC<3>{});
  }
  STAMP(2, 2);
  stage(IC<0>{}, IC<3>{}); stage(IC<1>{}, IC<3>{}); stage(IC<2>{}, IC<3>{});
  stage(IC<3>{}, IC<2>{}); stage(IC<0>{}, IC<1>{}); stage(IC<1>{}, IC<0>{});
  STAMP(2, 3);
#pragma unroll
  for (int r = 0; r < 2; ++r) {
    const int m = m0 + wid * 32 + r * 16 + (lane & 15);
    u16* pp = part + ((size_t)sp * S_ + m) * D_ + n0 + 4 * g;
#pragma unroll
    for (int n = 0; n < G2_NT; ++n) {
      u32x2 v = {pack2(acc[r][n][0], acc[r][n][1]), pack2(acc[r][n][2], acc[r][n][3])};
      *reinterpret_cast<u32x2*>(pp + n * 16) = v;
    }
  }
  asm volatile("s_waitcnt vmcnt(0)" ::: "memory");
  STAMP(2, 4);
}

__global__ __launch_bounds__(512, 2) void k3_final(const u16* __restrict__ of, const u16* __restrict__ wo,
                                                   const u16* __restrict__ part, const u16* __restrict__ b2,
                                                   const u16* __restrict__ x, u16* __restrict__ out) {
  __shared__ __attribute__((aligned(16))) char smem[576 * 128];
  const int tid = threadIdx.x, wid = tid >> 6, lane = tid & 63;
  const int bid = blockIdx.x;
  const int xcd = bid & 7, loc = bid >> 3;
  const int mt = xcd * 2 + loc / 9, nt = loc % 9;
  const int m0 = mt * 128, n0 = nt * 64;
  const int g = lane >> 4, q = (lane & 15) >> 2, p = lane & 3;
  const int wr = wid >> 1, wc = wid & 1;
  STAMP(3, 0);
#pragma unroll
  for (int i = 0; i < 9; ++i) {
    const int row = (wid * 9 + i) * 8 + (lane >> 3);
    const int f = ((row >> 1) & 1) | (((row >> 3) & 1) << 1);
    const int ch = (lane & 7) ^ (f << 1);
    glds16(wo + (size_t)row * D_ + n0 + ch * 8, smem + (wid * 9 + i) * 1024 + lane * 16);
  }
  bf16x8 an[2][18];
#pragma unroll
  for (int ks = 0; ks < 18; ++ks)
#pragma unroll
    for (int r = 0; r < 2; ++r)
      gld16_asm(an[r][ks], of + ((size_t)(((m0 >> 4) + wr * 2 + r) * 18 + ks) * 64 + lane) * 8);
  u32x2 py[2][2][4];
  u32x2 xx[2][2], bb[2];
#pragma unroll
  for (int r = 0; r < 2; ++r) {
    const int m = m0 + wr * 32 + r * 16 + (lane & 15);
#pragma unroll
    for (int n = 0; n < 2; ++n) {
      const int ncol = n0 + wc * 32 + n * 16 + 4 * g;
#pragma unroll
      for (int sI = 0; sI < SPLITK_; ++sI)
        asm volatile("global_load_dwordx2 %0, %1, off" : "=v"(py[r][n][sI]) : "v"(part + ((size_t)sI * S_ + m) * D_ + ncol) : "memory");
      asm volatile("global_load_dwordx2 %0, %1, off" : "=v"(xx[r][n]) : "v"(x + (size_t)m * D_ + ncol) : "memory");
    }
  }
#pragma unroll
  for (int n = 0; n < 2; ++n)
    asm volatile("global_load_dwordx2 %0, %1, off" : "=v"(bb[n]) : "v"(b2 + n0 + wc * 32 + n * 16 + 4 * g) : "memory");
  unsigned ad[2][2];
#pragma unroll
  for (int t = 0; t < 2; ++t) {
    const int row = 8 * g + 4 * t + q;
    const int f = ((row >> 1) & 1) | (((row >> 3) & 1) << 1);
#pragma unroll
    for (int n = 0; n < 2; ++n) ad[n][t] = lds_addr(smem) + row * 128 + (((wc * 2 + n) ^ f) * 32) + p * 8;
  }
  f32x4 acc[2][2];
#pragma unroll
  for (int r = 0; r < 2; ++r)
#pragma unroll
    for (int n = 0; n < 2; ++n) acc[r][n] = (f32x4){0.f, 0.f, 0.f, 0.f};
  STAMP(3, 1);
  wait_vm<22>();
  BAR();
  STAMP(3, 2);
#pragma unroll
  for (int ks = 0; ks < 18; ++ks) { pin(an[0][ks]); pin(an[1][ks]); }
  auto grp = [&](auto KB) {
    constexpr int kb = decltype(KB)::value;
    WF w[6][2];
#pragma unroll
    for (int k = 0; k < 6; ++k)
#pragma unroll
      for (int n = 0; n < 2; ++n) {
        if (k == 0) { tr_rd<0 * 4096>(w[0][n].lo, ad[n][0] + kb * 4096); tr_rd<0 * 4096>(w[0][n].hi, ad[n][1] + kb * 4096); }
        if (k == 1) { tr_rd<1 * 4096>(w[1][n].lo, ad[n][0] + kb * 4096); tr_rd<1 * 4096>(w[1][n].hi, ad[n][1] + kb * 4096); }
        if (k == 2) { tr_rd<2 * 4096>(w[2][n].lo, ad[n][0] + kb * 4096); tr_rd<2 * 4096>(w[2][n].hi, ad[n][1] + kb * 4096); }
        if (k == 3) { tr_rd<3 * 4096>(w[3][n].lo, ad[n][0] + kb * 4096); tr_rd<3 * 4096>(w[3][n].hi, ad[n][1] + kb * 4096); }
        if (k == 4) { tr_rd<4 * 4096>(w[4][n].lo, ad[n][0] + kb * 4096); tr_rd<4 * 4096>(w[4][n].hi, ad[n][1] + kb * 4096); }
        if (k == 5) { tr_rd<5 * 4096>(w[5][n].lo, ad[n][0] + kb * 4096); tr_rd<5 * 4096>(w[5][n].hi, ad[n][1] + kb * 4096); }
      }
    wait_lgkm<12>();
#pragma unroll
    for (int k = 0; k < 3; ++k)
#pragma unroll
      for (int n = 0; n < 2; ++n) pin(w[k][n]);
#pragma unroll
    for (int k = 0; k < 3; ++k)
#pragma unroll
      for (int n = 0; n < 2; ++n)
#pragma unroll
        for (int r = 0; r < 2; ++r)
          acc[r][n] = __builtin_amdgcn_mfma_f32_16x16x32_bf16(cat(w[k][n]), an[r][kb + k], acc[r][n], 0, 0, 0);
    wait_lgkm<0>();
#pragma unroll
    for (int k = 3; k < 6; ++k)
#pragma unroll
      for (int n = 0; n < 2; ++n) pin(w[k][n]);
#pragma unroll
    for (int k = 3; k < 6; ++k)
#pragma unroll
      for (int n = 0; n < 2; ++n)
#pragma unroll
        for (int r = 0; r < 2; ++r)
          acc[r][n] = __builtin_amdgcn_mfma_f32_16x16x32_bf16(cat(w[k][n]), an[r][kb + k], acc[r][n], 0, 0, 0);
  };
  grp(IC<0>{}); grp(IC<6>{}); grp(IC<12>{});
  wait_vm<0>();
  STAMP(3, 3);
#pragma unroll
  for (int n = 0; n < 2; ++n) asm volatile("" : "+v"(bb[n]));
#pragma unroll
  for (int r = 0; r < 2; ++r) {
    const int m = m0 + wr * 32 + r * 16 + (lane & 15);
#pragma unroll
    for (int n = 0; n < 2; ++n) {
      const int ncol = n0 + wc * 32 + n * 16 + 4 * g;
      asm volatile("" : "+v"(xx[r][n]));
#pragma unroll
      for (int sI = 0; sI < SPLITK_; ++sI) asm volatile("" : "+v"(py[r][n][sI]));
      f32x4 y = {0.f, 0.f, 0.f, 0.f};
#pragma unroll
      for (int sI = 0; sI < SPLITK_; ++sI) {
        y[0] += lo16(py[r][n][sI][0]); y[1] += hi16(py[r][n][sI][0]);
        y[2] += lo16(py[r][n][sI][1]); y[3] += hi16(py[r][n][sI][1]);
      }
      float bv[4] = {lo16(bb[n][0]), hi16(bb[n][0]), lo16(bb[n][1]), hi16(bb[n][1])};
      float xv[4] = {lo16(xx[r][n][0]), hi16(xx[r][n][0]), lo16(xx[r][n][1]), hi16(xx[r][n][1])};
      float o[4];
#pragma unroll
      for (int j = 0; j < 4; ++j) {
        float yy = y[j] + bv[j];
        float gg = yy / (1.f + __expf(-yy));
        o[j] = xv[j] + acc[r][n][j] * gg;
      }
      u32x2 v = {pack2(o[0], o[1]), pack2(o[2], o[3])};
      *reinterpret_cast<u32x2*>(out + (size_t)m * D_ + ncol) = v;
    }
  }
  STAMP(3, 4);
}

extern "C" void kernel_launch(void* const* d_in, const int* in_sizes, int n_in, void* d_out, int out_size, void* d_ws,
                              size_t ws_size, hipStream_t stream) {
  const u16* x = (const u16*)d_in[0];
  const u16* wqkv = (const u16*)d_in[1];
  const u16* wphi = (const u16*)d_in[2];
  const u16* wo = (const u16*)d_in[3];
  const u16* w1 = (const u16*)d_in[5];
  const u16* b1 = (const u16*)d_in[6];
  const u16* w2 = (const u16*)d_in[7];
  const u16* b2 = (const u16*)d_in[8];
  u16* out = (u16*)d_out;
  char* ws = (char*)d_ws;
  size_t off = 0;
  u16* xnf = (u16*)(ws + off); off += (size_t)S_ * D_ * 2;
  u16* wfold = (u16*)(ws + off); off += (size_t)D_ * NPH_ * 2;
  u16* qkv = (u16*)(ws + off); off += (size_t)96 * S_ * 24 * 2;
  u16* hf = (u16*)(ws + off); off += (size_t)S_ * F_ * 2;
  u16* part = (u16*)(ws + off); off += (size_t)SPLITK_ * S_ * D_ * 2;
  u16* of = (u16*)(ws + off); off += (size_t)S_ * D_ * 2;
  float* pkv = (float*)(ws + off); off += (size_t)256 * 1536 * 4;
  unsigned* cnt = (unsigned*)(ws + off); off += 32 * 32 * 4;

  k0_prep<<<512 + 192, 256, 0, stream>>>(x, wqkv, wphi, xnf, wfold, cnt);
  k1_gemm1<<<256, 512, 0, stream>>>(xnf, wfold, wqkv, w1, b1, qkv, hf);
  k2_gemm2<<<K2_HEADBLK + 256, 256, 0, stream>>>(hf, w2, part, qkv, of, pkv, cnt);
  k3_final<<<16 * 9, 512, 0, stream>>>(of, wo, part, b2, x, out);
}
```

```cpp
#include <hip/hip_runtime.h>
#include <hip/hip_bf16.h>
#include <stdint.h>

constexpr int S_ = 2048, D_ = 576, H_ = 32, DH_ = 18, F_ = 4608, NQKV_ = 1728;
constexpr int NPH_ = 1152;
constexpr int KPH_ = 40;
constexpr int KPHI_ = H_ * KPH_;
constexpr int SPLITK_ = 4;

typedef short bf16x8 __attribute__((ext_vector_type(8)));
typedef short s16x4 __attribute__((ext_vector_type(4)));
typedef float f32x4 __attribute__((ext_vector_type(4)));
typedef unsigned u32x4 __attribute__((ext_vector_type(4)));
typedef unsigned u32x2 __attribute__((ext_vector_type(2)));
typedef __attribute__((address_space(3))) s16x4* lds_s16x4_p;
typedef unsigned short u16;
template <int V> struct IC { static constexpr int value = V; };

#define STAMP(kid, i) do { } while (0)
#define ST_NT(p, v) (*(p) = (v))
#define WAIT_LGKM0() asm volatile("s_waitcnt lgkmcnt(0)" ::: "memory")
#define BAR() __builtin_amdgcn_s_barrier()

template <int N>
__device__ __forceinline__ void wait_vm() {
  static_assert(N >= 0 && N < 64, "vmcnt range");
  asm volatile("s_waitcnt vmcnt(%0)" ::"n"(N) : "memory");
}

__device__ __forceinline__ float bf2f(u16 v) { return __uint_as_float(((unsigned)v) << 16); }
__device__ __forceinline__ u16 f2bf(float f) {
  __hip_bfloat16 b = __float2bfloat16(f);
  return *reinterpret_cast<u16*>(&b);
}
__device__ __forceinline__ unsigned pack2(float lo, float hi) {
  return (unsigned)f2bf(lo) | ((unsigned)f2bf(hi) << 16);
}
__device__ __forceinline__ float rbf(float f) { return bf2f(f2bf(f)); }
__device__ __forceinline__ float lo16(unsigned u) { return __uint_as_float(u << 16); }
__device__ __forceinline__ float hi16(unsigned u) { return __uint_as_float(u & 0xffff0000u); }

__device__ __forceinline__ void glds16(const void* g, void* l) {
  __builtin_amdgcn_global_load_lds((const unsigned*)g, (unsigned*)l, 16, 0, 0);
}

__device__ __forceinline__ void l2_touch(const void* g, void* ldummy) {
  __builtin_amdgcn_global_load_lds((const unsigned*)g, (unsigned*)ldummy, 4, 0, 0);
}

__device__ __forceinline__ bf16x8 swap_halves_if(bf16x8 v, bool odd) {
  bf16x8 s = __builtin_shufflevector(v, v, 4, 5, 6, 7, 0, 1, 2, 3);
  return odd ? s : v;
}

__device__ __forceinline__ bf16x8 wfrag(const char* img, int o0, int o1, int off) {
  s16x4 a = __builtin_amdgcn_ds_read_tr16_b64_v4i16((lds_s16x4_p)(img + o0 + off));
  s16x4 b = __builtin_amdgcn_ds_read_tr16_b64_v4i16((lds_s16x4_p)(img + o1 + off));
  return __builtin_shufflevector(a, b, 0, 1, 2, 3, 4, 5, 6, 7);
}

struct WF { s16x4 lo, hi; };
template <int OFF>
__device__ __forceinline__ void tr_rd(s16x4& r, unsigned addr) {
  asm volatile("ds_read_b64_tr_b16 %0, %1 offset:%2" : "=v"(r) : "v"(addr), "n"(OFF) : "memory");
}
__device__ __forceinline__ void gld16_asm(bf16x8& r, const void* p) {
  asm volatile("global_load_dwordx4 %0, %1, off" : "=v"(r) : "v"(p) : "memory");
}
template <int N>
__device__ __forceinline__ void wait_lgkm() {
  static_assert(N >= 0 && N <= 15, "lgkmcnt range");
  asm volatile("s_waitcnt lgkmcnt(%0)" ::"n"(N) : "memory");
}
__device__ __forceinline__ void pin(WF& w) { asm volatile("" : "+v"(w.lo), "+v"(w.hi)); }
__device__ __forceinline__ void pin(bf16x8& w) { asm volatile("" : "+v"(w)); }
__device__ __forceinline__ bf16x8 cat(const WF& w) { return __builtin_shufflevector(w.lo, w.hi, 0, 1, 2, 3, 4, 5, 6, 7); }
__device__ __forceinline__ unsigned lds_addr(const void* p) {
  return (unsigned)(uintptr_t)(__attribute__((address_space(3))) const char*)p;
}

__global__ __launch_bounds__(256) void k0_prep(const u16* __restrict__ x, const u16* __restrict__ wqkv,
                                               const u16* __restrict__ wphi, u16* __restrict__ xnf,
                                               u16* __restrict__ wfold, unsigned* __restrict__ cnt) {
  __shared__ __attribute__((aligned(16))) float wph[18 * 20];
  const int tid = threadIdx.x, wid = tid >> 6, lane = tid & 63;
  const int bid = blockIdx.x;
  if (bid == 512 && tid < 32) cnt[tid * 32] = 0u;
  if (bid < 512) {
    const int row = bid * 4 + wid;
    const u16* xp = x + (size_t)row * D_;
    u32x4 a = *reinterpret_cast<const u32x4*>(xp + lane * 8);
    u32x4 b = {0u, 0u, 0u, 0u};
    if (lane < 8) b = *reinterpret_cast<const u32x4*>(xp + 512 + lane * 8);
    float v[16];
#pragma unroll
    for (int j = 0; j < 4; ++j) {
      v[2 * j] = lo16(a[j]); v[2 * j + 1] = hi16(a[j]);
      v[8 + 2 * j] = lo16(b[j]); v[8 + 2 * j + 1] = hi16(b[j]);
    }
    float s = 0.f;
#pragma unroll
    for (int j = 0; j < 16; ++j) s += v[j];
#pragma unroll
    for (int o = 32; o >= 1; o >>= 1) s += __shfl_xor(s, o);
    const float mean = s * (1.f / D_);
    float ss = 0.f;
#pragma unroll
    for (int j = 0; j < 8; ++j) { float d = v[j] - mean; ss += d * d; }
    if (lane < 8) {
#pragma unroll
      for (int j = 8; j < 16; ++j) { float d = v[j] - mean; ss += d * d; }
    }
#pragma unroll
    for (int o = 32; o >= 1; o >>= 1) ss += __shfl_xor(ss, o);
    const float rstd = rsqrtf(ss * (1.f / D_) + 1e-5f);
    u32x4 oa, ob;
#pragma unroll
    for (int j = 0; j < 4; ++j) {
      oa[j] = pack2((v[2 * j] - mean) * rstd, (v[2 * j + 1] - mean) * rstd);
      ob[j] = pack2((v[8 + 2 * j] - mean) * rstd, (v[8 + 2 * j + 1] - mean) * rstd);
    }
    {
      const int c = lane;
      u16* dst = xnf + ((size_t)((row >> 4) * 18 + (c >> 2)) * 64 + (c & 3) * 16 + (row & 15)) * 8;
      *reinterpret_cast<u32x4*>(dst) = oa;
    }
    if (lane < 8) {
      const int c = 64 + lane;
      u16* dst = xnf + ((size_t)((row >> 4) * 18 + (c >> 2)) * 64 + (c & 3) * 16 + (row & 15)) * 8;
      *reinterpret_cast<u32x4*>(dst) = ob;
    }
    return;
  }
  {
    const int fb = bid - 512;
    const int hh = fb / 3, k = (fb % 3) * 192 + tid;
    const int h = hh & 31;
    for (int i = tid; i < 18 * 20; i += 256) {
      const int d = i / 20, f = i % 20;
      wph[i] = (f < 18) ? bf2f(wphi[h * 324 + d * 18 + f]) : 0.f;
    }
    unsigned wv[9];
    if (tid < 192) {
      const u16* wp = wqkv + (size_t)k * NQKV_ + hh * 18;
#pragma unroll
      for (int d = 0; d < 9; ++d) wv[d] = *reinterpret_cast<const unsigned*>(wp + 2 * d);
    }
    __syncthreads();
    if (tid < 192) {
      float acc[20];
#pragma unroll
      for (int f = 0; f < 20; ++f) acc[f] = 0.f;
#pragma unroll
      for (int d = 0; d < 18; ++d) {
        const float w = (d & 1) ? hi16(wv[d >> 1]) : lo16(wv[d >> 1]);
#pragma unroll
        for (int f4 = 0; f4 < 5; ++f4) {
          f32x4 pw = *reinterpret_cast<const f32x4*>(wph + d * 20 + f4 * 4);
#pragma unroll
          for (int j = 0; j < 4; ++j) acc[f4 * 4 + j] += w * pw[j];
        }
      }
      u16* op = wfold + (size_t)k * NPH_ + hh * 18;
#pragma unroll
      for (int f = 0; f < 9; ++f) *reinterpret_cast<unsigned*>(op + 2 * f) = pack2(acc[2 * f], acc[2 * f + 1]);
    }
  }
}

constexpr int G1_BUFB = 192 * 128;
constexpr int G1_RING = 4;
constexpr int G1_XOFF = G1_RING * G1_BUFB;
constexpr int G1_BOFF = G1_XOFF + 576 * 32;
constexpr int G1_SMEM = G1_BOFF + 1024;
constexpr int G1_NST = 8;

template <int BASE, int I = 0>
__device__ __forceinline__ void g1_reads(WF (&w)[4], const unsigned (&ad)[4][2]) {
  if constexpr (I < 4) {
    if constexpr (BASE >= 49152) {
      tr_rd<BASE - 49152>(w[I].lo, ad[I][0] + 49152u);
      tr_rd<BASE - 49152>(w[I].hi, ad[I][1] + 49152u);
    } else {
      tr_rd<BASE>(w[I].lo, ad[I][0]);
      tr_rd<BASE>(w[I].hi, ad[I][1]);
    }
    g1_reads<BASE, I + 1>(w, ad);
  }
}
template <int KS>
__device__ __forceinline__ void g1_mma(WF (&w)[4], const bf16x8 (&xa)[2][18], f32x4 (&acc)[2][4]) {
#pragma unroll
  for (int i = 0; i < 4; ++i) pin(w[i]);
#pragma unroll
  for (int i = 0; i < 4; ++i)
#pragma unroll
    for (int r = 0; r < 2; ++r)
      acc[r][i] = __builtin_amdgcn_mfma_f32_16x16x32_bf16(cat(w[i]), xa[r][KS], acc[r][i], 0, 0, 0);
}

__device__ __forceinline__ float gelu_f(float v) {
  constexpr float c1 = -2.f * 0.7978845608028654f * 1.4426950408889634f;
  constexpr float c3 = c1 * 0.044715f;
  const float t = v * v;
  const float e = __builtin_amdgcn_exp2f(v * fmaf(t, c3, c1));
  return v * __builtin_amdgcn_rcpf(1.f + e);
}

typedef float f32x2 __attribute__((ext_vector_type(2)));
__device__ __forceinline__ unsigned cvt_pk(float lo, float hi) {
  unsigned r;
  asm("v_cvt_pk_bf16_f32 %0, %1, %2" : "=v"(r) : "v"(lo), "v"(hi));
  return r;
}
__device__ __forceinline__ unsigned gelu2_pk(f32x2 v) {
  constexpr float c1 = -2.f * 0.7978845608028654f * 1.4426950408889634f;
  constexpr float c3 = c1 * 0.044715f;
  const f32x2 t = v * v;
  const f32x2 pp = t * (f32x2){c3, c3} + (f32x2){c1, c1};
  const f32x2 u = v * pp;
  f32x2 e = {__builtin_amdgcn_exp2f(u[0]), __builtin_amdgcn_exp2f(u[1])};
  e = e + (f32x2){1.f, 1.f};
  const f32x2 r = {__builtin_amdgcn_rcpf(e[0]), __builtin_amdgcn_rcpf(e[1])};
  const f32x2 o = v * r;
  return cvt_pk(o[0], o[1]);
}

__device__ __forceinline__ void g1_store_tile(const f32x4& a, int tile, int mrow, int g, const u16* bias4,
                                              u16* __restrict__ qkv, u16* __restrict__ hf) {
  if (tile < NQKV_ / 16) {
    const int col = tile * 16 + 4 * g;
#pragma unroll
    for (int j = 0; j < 2; ++j) {
      const int c = col + 2 * j;
      const int hh = c / 18, f = c - hh * 18;
      *reinterpret_cast<unsigned*>(qkv + ((size_t)hh * S_ + mrow) * 24 + f) = pack2(a[2 * j], a[2 * j + 1]);
    }
  } else {
    u32x2 bb = *reinterpret_cast<const u32x2*>(bias4);
    float o[4];
    o[0] = gelu_f(a[0] + lo16(bb[0]));
    o[1] = gelu_f(a[1] + hi16(bb[0]));
    o[2] = gelu_f(a[2] + lo16(bb[1]));
    o[3] = gelu_f(a[3] + hi16(bb[1]));
    u32x2 v = {pack2(o[0], o[1]), pack2(o[2], o[3])};
    const int T = tile - NQKV_ / 16;
    u16* dst = hf + ((size_t)((mrow >> 4) * 144 + (T >> 1)) * 64 + ((T & 1) * 2 + (g >> 1)) * 16 + (mrow & 15)) * 8 + (g & 1) * 4;
    *reinterpret_cast<u32x2*>(dst) = v;
  }
}

__device__ __forceinline__ void g1_addr(unsigned (&ad)[4][2], char* smem, int lane) {
  const int g = lane >> 4, q = (lane & 15) >> 2, p = lane & 3;
#pragma unroll
  for (int t = 0; t < 2; ++t) {
    const int row = 8 * g + 4 * t + q;
    const int f = ((row >> 1) & 1) | (((row >> 3) & 1) << 1);
#pragma unroll
    for (int n = 0; n < 4; ++n) ad[n][t] = lds_addr(smem) + row * 128 + ((n ^ f) * 32) + p * 8;
  }
}

struct G1W { const u16* src; int stride; };

__device__ __forceinline__ void g1_wsetup(G1W& w, int col, int wid, int lane, const u16* wfold, const u16* wqkv, const u16* w1) {
  const int row = wid * 8 + (lane >> 3);
  const int f = ((row >> 1) & 1) | (((row >> 3) & 1) << 1);
  const int ch = (lane & 7) ^ (f << 1);
  if (col < NPH_) { w.src = wfold + (size_t)row * NPH_ + col + ch * 8; w.stride = 64 * NPH_; }
  else if (col < NQKV_) { w.src = wqkv + (size_t)row * NQKV_ + col + ch * 8; w.stride = 64 * NQKV_; }
  else { w.src = w1 + (size_t)row * F_ + (col - NQKV_) + ch * 8; w.stride = 64 * F_; }
}
__device__ __forceinline__ void g1_wissue(G1W& w, char* buf, int wid, int lane) {
#pragma unroll
  for (int i = 0; i < 3; ++i) {
    glds16(w.src, buf + i * 8192 + wid * 1024 + lane * 16);
    w.src += w.stride;
  }
}

template <int U>
__device__ __forceinline__ void g1_unit(G1W& wst, int unit0, const u16* wfold, const u16* wqkv,
                                        const u16* w1, char* smem, bf16x8 (&xa)[2][18], int m0,
                                        u16* __restrict__ qkv, u16* __restrict__ hf, WF (&wA)[4]) {
  int tz = threadIdx.x;
  asm volatile("" : "+v"(tz));
  const int wid = __builtin_amdgcn_readfirstlane(tz >> 6), lane = tz & 63;
  const int g = lane >> 4;
  unsigned ad[4][2];
  g1_addr(ad, smem, lane);
  f32x4 acc[2][4];
#pragma unroll
  for (int r = 0; r < 2; ++r)
#pragma unroll
    for (int n = 0; n < 4; ++n) acc[r][n] = (f32x4){0.f, 0.f, 0.f, 0.f};

  auto stage = [&](auto KC) {
    constexpr int kc = decltype(KC)::value;
    constexpr int s = U * 3 + kc;
    constexpr int CUR = (s % G1_RING) * G1_BUFB;
    if constexpr (U == 0) {
#pragma unroll
      for (int k = 0; k < 6; ++k) { pin(xa[0][kc * 6 + k]); pin(xa[1][kc * 6 + k]); }
    }
    WF wB[4];
    g1_reads<CUR + 1 * 4096>(wB, ad); wait_lgkm<8>(); g1_mma<kc * 6 + 0>(wA, xa, acc);
    g1_reads<CUR + 2 * 4096>(wA, ad); wait_lgkm<8>(); g1_mma<kc * 6 + 1>(wB, xa, acc);
    g1_reads<CUR + 3 * 4096>(wB, ad); wait_lgkm<8>(); g1_mma<kc * 6 + 2>(wA, xa, acc);
    g1_reads<CUR + 4 * 4096>(wA, ad); wait_lgkm<8>(); g1_mma<kc * 6 + 3>(wB, xa, acc);
    g1_reads<CUR + 5 * 4096>(wB, ad); wait_lgkm<8>(); g1_mma<kc * 6 + 4>(wA, xa, acc);
    if constexpr (s < 8) {
      constexpr int nl = (s == 0) ? 12 : ((s + 2 <= 8) ? 3 : 0);
      constexpr int nst = (U > 0 && kc <= 1) ? G1_NST : 0;
      wait_vm<nl + nst>();
      BAR();
      if constexpr (s + 3 <= 8) {
        constexpr int sn = s + 3;
        if constexpr (sn % 3 == 0) g1_wsetup(wst, (unit0 + sn / 3) * 64, wid, lane, wfold, wqkv, w1);
        g1_wissue(wst, smem + (sn % G1_RING) * G1_BUFB, wid, lane);
      }
      g1_reads<((s + 1) % G1_RING) * G1_BUFB>(wA, ad);
      wait_lgkm<8>();
    } else {
      wait_lgkm<0>();
    }
    g1_mma<kc * 6 + 5>(wB, xa, acc);
  };
  stage(IC<0>{}); stage(IC<1>{}); stage(IC<2>{});
  const u16* bias_lds = reinterpret_cast<const u16*>(smem + G1_BOFF);
  if ((unit0 + U) * 64 < NQKV_) {
#pragma unroll
    for (int n = 0; n < 4; ++n) {
      const int tile = (unit0 + U) * 4 + n;
#pragma unroll
      for (int r = 0; r < 2; ++r)
        g1_store_tile(acc[r][n], tile, m0 + wid * 32 + r * 16 + (lane & 15), g, bias_lds, qkv, hf);
    }
  } else {
    u32x2 bb[4];
#pragma unroll
    for (int n = 0; n < 4; ++n) bb[n] = *reinterpret_cast<const u32x2*>(bias_lds + (U * 4 + n) * 16 + 4 * g);
    const int T0 = (unit0 + U) * 4 - NQKV_ / 16;
#pragma unroll
    for (int r = 0; r < 2; ++r) {
      const int mrow = m0 + wid * 32 + r * 16 + (lane & 15);
      u16* dst = hf + ((size_t)((mrow >> 4) * 144 + (T0 >> 1)) * 64 + (g >> 1) * 16 + (mrow & 15)) * 8 + (g & 1) * 4;
#pragma unroll
      for (int n = 0; n < 4; ++n) {
        const f32x2 v0 = (f32x2){acc[r][n][0], acc[r][n][1]} + (f32x2){lo16(bb[n][0]), hi16(bb[n][0])};
        const f32x2 v1 = (f32x2){acc[r][n][2], acc[r][n][3]} + (f32x2){lo16(bb[n][1]), hi16(bb[n][1])};
        u32x2 v = {gelu2_pk(v0), gelu2_pk(v1)};
        ST_NT(reinterpret_cast<u32x2*>(dst + (n >> 1) * 512 + (n & 1) * 256), v);
      }
    }
  }
}

__global__ __launch_bounds__(512, 2) void k1_gemm1(const u16* __restrict__ xnf, const u16* __restrict__ wfold,
                                                   const u16* __restrict__ wqkv, const u16* __restrict__ w1,
                                                   const u16* __restrict__ b1, u16* __restrict__ qkv,
                                                   u16* __restrict__ hf) {
  __shared__ __attribute__((aligned(16))) char smem[G1_SMEM];
  const int tid = threadIdx.x, wid = tid >> 6, lane = tid & 63;
  const int bid = blockIdx.x;
  const int mt = bid >> 5, grp = bid & 31;
  const int m0 = mt * 256;
  const int unit0 = grp * 3;
  const bool extra = grp < 12;
  const int xcol = 6144 + (extra ? grp : 0) * 16;
  STAMP(1, 0);
  G1W wst;
  g1_wsetup(wst, unit0 * 64, wid, lane, wfold, wqkv, w1);
  g1_wissue(wst, smem + 0 * G1_BUFB, wid, lane);
  g1_wissue(wst, smem + 1 * G1_BUFB, wid, lane);
  g1_wissue(wst, smem + 2 * G1_BUFB, wid, lane);
#pragma unroll
  for (int i = 0; i < 3; ++i) {
    int ins = wid + 8 * i;
    if (ins >= 18) ins -= 18;
    const int c = ins * 64 + lane;
    const int pr = c >> 1, half = c & 1;
    const int k = (pr & ~0xC) | ((pr & 4) << 1) | ((pr & 8) >> 1);
    glds16(w1 + (size_t)k * F_ + (xcol - NQKV_) + half * 8, smem + G1_XOFF + c * 16);
  }
  {
    const int e = (wid & 1) * 64 + lane;
    int col = (e < 96) ? (unit0 * 64 + 2 * e - NQKV_) : (xcol - NQKV_ + 2 * (e - 96));
    if (col < 0 || e >= 104) col = 0;
    __builtin_amdgcn_global_load_lds((const unsigned*)(b1 + col), (unsigned*)(smem + G1_BOFF + e * 4), 4, 0, 0);
  }
  bf16x8 xa[2][18];
#pragma unroll
  for (int ks = 0; ks < 18; ++ks)
#pragma unroll
    for (int r = 0; r < 2; ++r)
      gld16_asm(xa[r][ks], xnf + ((size_t)(((m0 >> 4) + wid * 2 + r) * 18 + ks) * 64 + lane) * 8);
  wait_vm<24>();
  BAR();
  WF wA[4];
  {
    unsigned ad[4][2];
    g1_addr(ad, smem, lane);
    g1_reads<0>(wA, ad);
  }
  STAMP(1, 1);
  g1_unit<0>(wst, unit0, wfold, wqkv, w1, smem, xa, m0, qkv, hf, wA);
  STAMP(1, 2);
  g1_unit<1>(wst, unit0, wfold, wqkv, w1, smem, xa, m0, qkv, hf, wA);
  STAMP(1, 3);
  g1_unit<2>(wst, unit0, wfold, wqkv, w1, smem, xa, m0, qkv, hf, wA);
  STAMP(1, 4);
  if (extra) {
    int tz = threadIdx.x;
    asm volatile("" : "+v"(tz));
    const int wid2 = __builtin_amdgcn_readfirstlane(tz >> 6), lane2 = tz & 63;
    const int g = lane2 >> 4, q = (lane2 & 15) >> 2, p = lane2 & 3;
    unsigned ad[2];
#pragma unroll
    for (int t = 0; t < 2; ++t) {
      const int k = 8 * g + 4 * t + q;
      const int pr = (k & ~0xC) | ((k & 4) << 1) | ((k & 8) >> 1);
      ad[t] = lds_addr(smem) + G1_XOFF + pr * 32 + p * 8;
    }
    f32x4 acc[2] = {(f32x4){0.f, 0.f, 0.f, 0.f}, (f32x4){0.f, 0.f, 0.f, 0.f}};
#pragma unroll
    for (int kb = 0; kb < 18; kb += 6) {
      WF w[6];
      tr_rd<0 * 1024>(w[0].lo, ad[0] + kb * 1024); tr_rd<0 * 1024>(w[0].hi, ad[1] + kb * 1024);
      tr_rd<1 * 1024>(w[1].lo, ad[0] + kb * 1024); tr_rd<1 * 1024>(w[1].hi, ad[1] + kb * 1024);
      tr_rd<2 * 1024>(w[2].lo, ad[0] + kb * 1024); tr_rd<2 * 1024>(w[2].hi, ad[1] + kb * 1024);
      tr_rd<3 * 1024>(w[3].lo, ad[0] + kb * 1024); tr_rd<3 * 1024>(w[3].hi, ad[1] + kb * 1024);
      tr_rd<4 * 1024>(w[4].lo, ad[0] + kb * 1024); tr_rd<4 * 1024>(w[4].hi, ad[1] + kb * 1024);
      tr_rd<5 * 1024>(w[5].lo, ad[0] + kb * 1024); tr_rd<5 * 1024>(w[5].hi, ad[1] + kb * 1024);
      wait_lgkm<0>();
#pragma unroll
      for (int i = 0; i < 6; ++i) pin(w[i]);
#pragma unroll
      for (int i = 0; i < 6; ++i)
#pragma unroll
        for (int r = 0; r < 2; ++r)
          acc[r] = __builtin_amdgcn_mfma_f32_16x16x32_bf16(cat(w[i]), xa[r][kb + i], acc[r], 0, 0, 0);
    }
    const u16* bias_lds = reinterpret_cast<const u16*>(smem + G1_BOFF);
#pragma unroll
    for (int r = 0; r < 2; ++r)
      g1_store_tile(acc[r], xcol / 16, m0 + wid2 * 32 + r * 16 + (lane2 & 15), g, bias_lds + 192 + 4 * g, qkv, hf);
  }
}

constexpr int G2_NT = 9, G2_RB = G2_NT * 32;
constexpr int G2_BUFB = 64 * G2_RB;
constexpr int G2_RING = 4;
constexpr int G2_STAGES = 18;

__device__ __forceinline__ void softmax_pm(const float (&pk)[18], float (&ep)[18], float (&en)[18]) {
  float mx = pk[0], mn = pk[0];
#pragma unroll
  for (int f = 1; f < 18; ++f) {
    mx = fmaxf(mx, pk[f]);
    mn = fminf(mn, pk[f]);
  }
  float sp = 0.f, sn = 0.f;
#pragma unroll
  for (int f = 0; f < 18; ++f) {
    ep[f] = __expf(pk[f] - mx);
    en[f] = __expf(mn - pk[f]);
    sp += ep[f];
    sn += en[f];
  }
  const float rp = 1.f / sp, rn = 1.f / sn;
#pragma unroll
  for (int f = 0; f < 18; ++f) {
    ep[f] *= rp;
    en[f] *= rn;
  }
}

__device__ __forceinline__ void head_part1(int hb, const u16* __restrict__ qkv, float* pkv, unsigned* cnt, char* smem) {
  u16* phiT = reinterpret_cast<u16*>(smem);
  u16* vT = reinterpret_cast<u16*>(smem + 19536);
  float* kvs = reinterpret_cast<float*>(smem);
  const int h = hb >> 3, c8 = hb & 7;
  const int tid = threadIdx.x, wid = tid >> 6, lane = tid & 63;
  if (tid < 256) {
    const int row = c8 * 256 + tid;
    u32x4 kr[3], vr[3];
    const u32x4* kp = reinterpret_cast<const u32x4*>(qkv + ((size_t)(32 + h) * S_ + row) * 24);
    const u32x4* vp = reinterpret_cast<const u32x4*>(qkv + ((size_t)(64 + h) * S_ + row) * 24);
#pragma unroll
    for (int d = 0; d < 3; ++d) { kr[d] = kp[d]; vr[d] = vp[d]; }
    phiT[36 * 264 + tid] = 0;
    vT[18 * 264 + tid] = (u16)0x3f80;
    vT[19 * 264 + tid] = 0;
    float pk[18];
#pragma unroll
    for (int d = 0; d < 9; ++d) {
      pk[2 * d] = lo16(kr[d >> 2][d & 3]);
      pk[2 * d + 1] = hi16(kr[d >> 2][d & 3]);
    }
    float ep[18], en[18];
    softmax_pm(pk, ep, en);
#pragma unroll
    for (int f = 0; f < 18; ++f) {
      phiT[f * 264 + tid] = f2bf(ep[f]);
      phiT[(18 + f) * 264 + tid] = f2bf(en[f]);
      const unsigned w = vr[(f >> 1) >> 2][(f >> 1) & 3];
      vT[f * 264 + tid] = (u16)((f & 1) ? (w >> 16) : (w & 0xffff));
    }
  }
  __syncthreads();
  f32x4 acc[3][2];
  if (tid < 256) {
#pragma unroll
    for (int a = 0; a < 3; ++a)
#pragma unroll
      for (int b = 0; b < 2; ++b) acc[a][b] = (f32x4){0.f, 0.f, 0.f, 0.f};
    int frow[3], drow[2];
#pragma unroll
    for (int a = 0; a < 3; ++a) { frow[a] = a * 16 + (lane & 15); if (frow[a] > 36) frow[a] = 36; }
#pragma unroll
    for (int b = 0; b < 2; ++b) { drow[b] = b * 16 + (lane & 15); if (drow[b] > 19) drow[b] = 19; }
#pragma unroll
    for (int ks = 0; ks < 2; ++ks) {
      const int s0 = wid * 64 + ks * 32 + 8 * (lane >> 4);
      bf16x8 bv[2];
#pragma unroll
      for (int b = 0; b < 2; ++b) bv[b] = *reinterpret_cast<const bf16x8*>(vT + drow[b] * 264 + s0);
#pragma unroll
      for (int a = 0; a < 3; ++a) {
        bf16x8 av = *reinterpret_cast<const bf16x8*>(phiT + frow[a] * 264 + s0);
#pragma unroll
        for (int b = 0; b < 2; ++b) acc[a][b] = __builtin_amdgcn_mfma_f32_16x16x32_bf16(av, bv[b], acc[a][b], 0, 0, 0);
      }
    }
  }
  __syncthreads();
  if (tid < 256) {
#pragma unroll
    for (int a = 0; a < 3; ++a)
#pragma unroll
      for (int b = 0; b < 2; ++b)
#pragma unroll
        for (int j = 0; j < 4; ++j)
          kvs[(wid * 48 + a * 16 + (lane >> 4) * 4 + j) * 32 + b * 16 + (lane & 15)] = acc[a][b][j];
  }
  __syncthreads();
  float* mypart = pkv + (size_t)(h * 8 + c8) * 1536;
#pragma unroll
  for (int it = 0; it < 3; ++it) {
    const int i = it * 512 + tid;
    const float v = kvs[i] + kvs[1536 + i] + kvs[2 * 1536 + i] + kvs[3 * 1536 + i];
    __hip_atomic_store(mypart + i, v, __ATOMIC_RELAXED, __HIP_MEMORY_SCOPE_AGENT);
  }
  asm volatile("s_waitcnt vmcnt(0)" ::: "memory");
  __syncthreads();
  if (tid == 0) __hip_atomic_fetch_add(cnt + h * 32, 1u, __ATOMIC_RELAXED, __HIP_MEMORY_SCOPE_AGENT);
}

__device__ __forceinline__ void head_part2(int hb, const u16* __restrict__ qkv, u16* __restrict__ of, float* pkv,
                                           unsigned* cnt, char* smem) {
  const int h = hb >> 3, c8 = hb & 7;
  const int tid = threadIdx.x;
  const int row = c8 * 256 + (tid & 255);
  u32x4 qr[3];
  {
    const u32x4* qp = reinterpret_cast<const u32x4*>(qkv + ((size_t)h * S_ + row) * 24);
#pragma unroll
    for (int d = 0; d < 3; ++d) qr[d] = qp[d];
  }
  if (tid == 0) {
    unsigned spins = 0;
    while (__hip_atomic_load(cnt + h * 32, __ATOMIC_RELAXED, __HIP_MEMORY_SCOPE_AGENT) < 8u) {
      __builtin_amdgcn_s_sleep(1);
      if (++spins > (1u << 24)) break;
    }
  }
  __syncthreads();
  float* kvf = reinterpret_cast<float*>(smem);
  {
    float* hp = pkv + (size_t)h * 8 * 1536;
    float pv[2][8];
#pragma unroll
    for (int it = 0; it < 2; ++it) {
      const int i = it * 512 + tid;
      const int f = i / 20, d = i - f * 20;
      const int src = (i < 720) ? (f * 32 + d) : 0;
#pragma unroll
      for (int c = 0; c < 8; ++c) pv[it][c] = __hip_atomic_load(hp + c * 1536 + src, __ATOMIC_RELAXED, __HIP_MEMORY_SCOPE_AGENT);
    }
#pragma unroll
    for (int it = 0; it < 2; ++it) {
      const int i = it * 512 + tid;
      float v = 0.f;
#pragma unroll
      for (int c = 0; c < 8; ++c) v += pv[it][c];
      if (i < 720) kvf[i] = rbf(v);
    }
  }
  float ep[18], en[18];
  {
    float pk[18];
#pragma unroll
    for (int d = 0; d < 9; ++d) {
      pk[2 * d] = lo16(qr[d >> 2][d & 3]);
      pk[2 * d + 1] = hi16(qr[d >> 2][d & 3]);
    }
    softmax_pm(pk, ep, en);
#pragma unroll
    for (int f = 0; f < 18; ++f) { ep[f] = rbf(ep[f]); en[f] = rbf(en[f]); }
  }
  __syncthreads();
  {
    const int hi = tid >> 8;
    float num[12], den = 0.f;
#pragma unroll
    for (int d = 0; d < 12; ++d) num[d] = 0.f;
#pragma unroll
    for (int f = 0; f < 36; ++f) {
      const float ph = (f < 18) ? ep[f] : en[f - 18];
      const float* kp = kvf + f * 20 + hi * 8;
#pragma unroll
      for (int d4 = 0; d4 < 3; ++d4) {
        const f32x4 w = *reinterpret_cast<const f32x4*>(kp + d4 * 4);
#pragma unroll
        for (int j = 0; j < 4; ++j) num[d4 * 4 + j] += ph * w[j];
      }
      den += ph * kvf[f * 20 + 18];
    }
    const float rd = 1.f / (rbf(den) + 1e-6f);
    const int np = hi ? 5 : 4;
#pragma unroll
    for (int d2 = 0; d2 < 5; ++d2) {
      if (d2 < np) {
        const int k = h * 18 + hi * 8 + 2 * d2;
        u16* dst = of + ((size_t)((row >> 4) * 18 + (k >> 5)) * 64 + ((k >> 3) & 3) * 16 + (row & 15)) * 8 + (k & 7);
        *reinterpret_cast<unsigned*>(dst) = pack2(rbf(num[2 * d2]) * rd, rbf(num[2 * d2 + 1]) * rd);
      }
    }
  }
}

template <int BASE, int N0, int CNT, int I = 0>
__device__ __forceinline__ void g2_wreads(WF (&w)[5], unsigned a0, unsigned a1) {
  if constexpr (I < CNT) {
    tr_rd<BASE + (N0 + I) * 32>(w[I].lo, a0);
    tr_rd<BASE + (N0 + I) * 32>(w[I].hi, a1);
    g2_wreads<BASE, N0, CNT, I + 1>(w, a0, a1);
  }
}
template <int N0, int CNT>
__device__ __forceinline__ void g2_mma(WF (&w)[5], const bf16x8 (&ac)[2], f32x4 (&acc)[2][G2_NT]) {
#pragma unroll
  for (int i = 0; i < CNT; ++i) pin(w[i]);
#pragma unroll
  for (int i = 0; i < CNT; ++i)
#pragma unroll
    for (int r = 0; r < 2; ++r)
      acc[r][N0 + i] = __builtin_amdgcn_mfma_f32_16x16x32_bf16(cat(w[i]), ac[r], acc[r][N0 + i], 0, 0, 0);
}

__global__ __launch_bounds__(512, 2) void k2_gemm2(const u16* __restrict__ hf, const u16* __restrict__ w2,
                                                   u16* __restrict__ part, const u16* __restrict__ qkv,
                                                   u16* __restrict__ of, float* pkv, unsigned* cnt) {
  __shared__ __attribute__((aligned(16))) char smem[G2_RING * G2_BUFB + 30720];
  const int tid = threadIdx.x, wid8 = tid >> 6, lane = tid & 63;
  const int bid = blockIdx.x;
  STAMP(2, 0);
  const int wid = wid8 & 3, kh = wid8 >> 2;
  const int xcd = bid & 7, loc = bid >> 3;
  const int sp = xcd >> 1, mt = (xcd & 1) * 8 + (loc >> 2), nt4 = loc & 3;
  const int m0 = mt * 128, n0 = nt4 * 144, k0 = sp * 1152;
  const int g = lane >> 4, q = (lane & 15) >> 2, p = lane & 3;
  const bool odd = g & 1;
  const unsigned wa0 = lds_addr(smem) + (kh * 32 + 8 * g + 4 * (g & 1) + q) * G2_RB + p * 8;
  const unsigned wa1 = lds_addr(smem) + (kh * 32 + 8 * g + 4 * (1 - (g & 1)) + q) * G2_RB + p * 8;
  f32x4 acc[2][G2_NT];
#pragma unroll
  for (int r = 0; r < 2; ++r)
#pragma unroll
    for (int n = 0; n < G2_NT; ++n) acc[r][n] = (f32x4){0.f, 0.f, 0.f, 0.f};
  const u16* wp[3];
  int wdst[3];
#pragma unroll
  for (int i = 0; i < 3; ++i) {
    int ins = wid8 + 8 * i;
    if (ins >= 18) ins -= 18;
    int c = ins * 64 + lane;
    int row = c / 18, cc = c % 18;
    wp[i] = w2 + (size_t)(k0 + row) * D_ + n0 + cc * 8;
    wdst[i] = c * 16;
  }
  const u16* ap[2];
#pragma unroll
  for (int r = 0; r < 2; ++r) ap[r] = hf + ((size_t)((m0 >> 4) + wid * 2 + r) * 144 + (k0 >> 5) + kh) * 512 + lane * 8;
  bf16x8 an[4][2];
  auto issue = [&](auto SL) {
    constexpr int slot = decltype(SL)::value;
    char* buf = smem + slot * G2_BUFB;
#pragma unroll
    for (int i = 0; i < 3; ++i) { glds16(wp[i], buf + wdst[i]); wp[i] += 64 * D_; }
#pragma unroll
    for (int r = 0; r < 2; ++r) { gld16_asm(an[slot][r], ap[r]); ap[r] += 1024; }
  };
  issue(IC<0>{});
  issue(IC<1>{});
  issue(IC<2>{});
  head_part1(bid, qkv, pkv, cnt, smem + G2_RING * G2_BUFB);
  wait_vm<10>();
  BAR();
  STAMP(2, 1);
  WF wA[5], wB[5];
  g2_wreads<0, 0, 5>(wA, wa0, wa1);
  auto stage = [&](auto U, auto LAST) {
    constexpr int u = decltype(U)::value;
    constexpr int rem = decltype(LAST)::value;
    constexpr int BASE = u * G2_BUFB;
    bf16x8 c0[2];
#pragma unroll
    for (int r = 0; r < 2; ++r) { pin(an[u][r]); c0[r] = swap_halves_if(an[u][r], odd); }
    g2_wreads<BASE, 5, 4>(wB, wa0, wa1);
    wait_lgkm<8>();
    g2_mma<0, 5>(wA, c0, acc);
    if constexpr (rem >= 1) {
      if constexpr (rem >= 2) wait_vm<5>(); else wait_vm<0>();
      BAR();
      if constexpr (rem >= 3) issue(IC<(u + 3) & 3>{});
      g2_wreads<((u + 1) & 3) * G2_BUFB, 0, 5>(wA, wa0, wa1);
      wait_lgkm<10>();
    } else {
      wait_lgkm<0>();
    }
    g2_mma<5, 4>(wB, c0, acc);
  };
#pragma unroll 1
  for (int it = 0; it < 3; ++it) {
    stage(IC<0>{}, IC<3>{}); stage(IC<1>{}, IC<3>{}); stage(IC<2>{}, IC<3>{}); stage(IC<3>{}, IC<3>{});
  }
  STAMP(2, 2);
  stage(IC<0>{}, IC<3>{}); stage(IC<1>{}, IC<3>{}); stage(IC<2>{}, IC<3>{});
  stage(IC<3>{}, IC<2>{}); stage(IC<0>{}, IC<1>{}); stage(IC<1>{}, IC<0>{});
  STAMP(2, 3);
  __syncthreads();
  f32x4* red = reinterpret_cast<f32x4*>(smem) + (size_t)wid * 18 * 64 + lane;
  if (kh == 1) {
#pragma unroll
    for (int r = 0; r < 2; ++r)
#pragma unroll
      for (int n = 0; n < G2_NT; ++n) red[(r * G2_NT + n) * 64] = acc[r][n];
  }
  __syncthreads();
  if (kh == 0) {
#pragma unroll
    for (int r = 0; r < 2; ++r) {
      const int m = m0 + wid * 32 + r * 16 + (lane & 15);
      u16* pp = part + ((size_t)sp * S_ + m) * D_ + n0 + 4 * g;
#pragma unroll
      for (int n = 0; n < G2_NT; ++n) {
        const f32x4 t = acc[r][n] + red[(r * G2_NT + n) * 64];
        u32x2 v = {cvt_pk(t[0], t[1]), cvt_pk(t[2], t[3])};
        *reinterpret_cast<u32x2*>(pp + n * 16) = v;
      }
    }
  }
  head_part2(bid, qkv, of, pkv, cnt, smem + G2_RING * G2_BUFB);
  STAMP(2, 4);
}

__global__ __launch_bounds__(512, 2) void k3_final(const u16* __restrict__ of, const u16* __restrict__ wo,
                                                   const u16* __restrict__ part, const u16* __restrict__ b2,
                                                   const u16* __restrict__ x, u16* __restrict__ out) {
  __shared__ __attribute__((aligned(16))) char smem[576 * 128];
  const int tid = threadIdx.x, wid = tid >> 6, lane = tid & 63;
  const int bid = blockIdx.x;
  const int xcd = bid & 7, loc = bid >> 3;
  const int mt = xcd * 2 + loc / 9, nt = loc % 9;
  const int m0 = mt * 128, n0 = nt * 64;
  const int g = lane >> 4, q = (lane & 15) >> 2, p = lane & 3;
  const int wr = wid >> 1, wc = wid & 1;
  STAMP(3, 0);
#pragma unroll
  for (int i = 0; i < 9; ++i) {
    const int row = (wid * 9 + i) * 8 + (lane >> 3);
    const int f = ((row >> 1) & 1) | (((row >> 3) & 1) << 1);
    const int ch = (lane & 7) ^ (f << 1);
    glds16(wo + (size_t)row * D_ + n0 + ch * 8, smem + (wid * 9 + i) * 1024 + lane * 16);
  }
  bf16x8 an[2][18];
#pragma unroll
  for (int ks = 0; ks < 18; ++ks)
#pragma unroll
    for (int r = 0; r < 2; ++r)
      gld16_asm(an[r][ks], of + ((size_t)(((m0 >> 4) + wr * 2 + r) * 18 + ks) * 64 + lane) * 8);
  u32x2 py[2][2][4];
  u32x2 xx[2][2], bb[2];
#pragma unroll
  for (int r = 0; r < 2; ++r) {
    const int m = m0 + wr * 32 + r * 16 + (lane & 15);
#pragma unroll
    for (int n = 0; n < 2; ++n) {
      const int ncol = n0 + wc * 32 + n * 16 + 4 * g;
#pragma unroll
      for (int sI = 0; sI < SPLITK_; ++sI)
        asm volatile("global_load_dwordx2 %0, %1, off" : "=v"(py[r][n][sI]) : "v"(part + ((size_t)sI * S_ + m) * D_ + ncol) : "memory");
      asm volatile("global_load_dwordx2 %0, %1, off" : "=v"(xx[r][n]) : "v"(x + (size_t)m * D_ + ncol) : "memory");
    }
  }
#pragma unroll
  for (int n = 0; n < 2; ++n)
    asm volatile("global_load_dwordx2 %0, %1, off" : "=v"(bb[n]) : "v"(b2 + n0 + wc * 32 + n * 16 + 4 * g) : "memory");
  unsigned ad[2][2];
#pragma unroll
  for (int t = 0; t < 2; ++t) {
    const int row = 8 * g + 4 * t + q;
    const int f = ((row >> 1) & 1) | (((row >> 3) & 1) << 1);
#pragma unroll
    for (int n = 0; n < 2; ++n) ad[n][t] = lds_addr(smem) + row * 128 + (((wc * 2 + n) ^ f) * 32) + p * 8;
  }
  f32x4 acc[2][2];
#pragma unroll
  for (int r = 0; r < 2; ++r)
#pragma unroll
    for (int n = 0; n < 2; ++n) acc[r][n] = (f32x4){0.f, 0.f, 0.f, 0.f};
  STAMP(3, 1);
  wait_vm<22>();
  BAR();
  STAMP(3, 2);
#pragma unroll
  for (int ks = 0; ks < 18; ++ks) { pin(an[0][ks]); pin(an[1][ks]); }
  auto grp = [&](auto KB) {
    constexpr int kb = decltype(KB)::value;
    WF w[6][2];
#pragma unroll
    for (int k = 0; k < 6; ++k)
#pragma unroll
      for (int n = 0; n < 2; ++n) {
        if (k == 0) { tr_rd<0 * 4096>(w[0][n].lo, ad[n][0] + kb * 4096); tr_rd<0 * 4096>(w[0][n].hi, ad[n][1] + kb * 4096); }
        if (k == 1) { tr_rd<1 * 4096>(w[1][n].lo, ad[n][0] + kb * 4096); tr_rd<1 * 4096>(w[1][n].hi, ad[n][1] + kb * 4096); }
        if (k == 2) { tr_rd<2 * 4096>(w[2][n].lo, ad[n][0] + kb * 4096); tr_rd<2 * 4096>(w[2][n].hi, ad[n][1] + kb * 4096); }
        if (k == 3) { tr_rd<3 * 4096>(w[3][n].lo, ad[n][0] + kb * 4096); tr_rd<3 * 4096>(w[3][n].hi, ad[n][1] + kb * 4096); }
        if (k == 4) { tr_rd<4 * 4096>(w[4][n].lo, ad[n][0] + kb * 4096); tr_rd<4 * 4096>(w[4][n].hi, ad[n][1] + kb * 4096); }
        if (k == 5) { tr_rd<5 * 4096>(w[5][n].lo, ad[n][0] + kb * 4096); tr_rd<5 * 4096>(w[5][n].hi, ad[n][1] + kb * 4096); }
      }
    wait_lgkm<12>();
#pragma unroll
    for (int k = 0; k < 3; ++k)
#pragma unroll
      for (int n = 0; n < 2; ++n) pin(w[k][n]);
#pragma unroll
    for (int k = 0; k < 3; ++k)
#pragma unroll
      for (int n = 0; n < 2; ++n)
#pragma unroll
        for (int r = 0; r < 2; ++r)
          acc[r][n] = __builtin_amdgcn_mfma_f32_16x16x32_bf16(cat(w[k][n]), an[r][kb + k], acc[r][n], 0, 0, 0);
    wait_lgkm<0>();
#pragma unroll
    for (int k = 3; k < 6; ++k)
#pragma unroll
      for (int n = 0; n < 2; ++n) pin(w[k][n]);
#pragma unroll
    for (int k = 3; k < 6; ++k)
#pragma unroll
      for (int n = 0; n < 2; ++n)
#pragma unroll
        for (int r = 0; r < 2; ++r)
          acc[r][n] = __builtin_amdgcn_mfma_f32_16x16x32_bf16(cat(w[k][n]), an[r][kb + k], acc[r][n], 0, 0, 0);
  };
  grp(IC<0>{}); grp(IC<6>{}); grp(IC<12>{});
  wait_vm<0>();
  STAMP(3, 3);
#pragma unroll
  for (int n = 0; n < 2; ++n) asm volatile("" : "+v"(bb[n]));
#pragma unroll
  for (int r = 0; r < 2; ++r) {
    const int m = m0 + wr * 32 + r * 16 + (lane & 15);
#pragma unroll
    for (int n = 0; n < 2; ++n) {
      const int ncol = n0 + wc * 32 + n * 16 + 4 * g;
      asm volatile("" : "+v"(xx[r][n]));
#pragma unroll
      for (int sI = 0; sI < SPLITK_; ++sI) asm volatile("" : "+v"(py[r][n][sI]));
      f32x4 y = {0.f, 0.f, 0.f, 0.f};
#pragma unroll
      for (int sI = 0; sI < SPLITK_; ++sI) {
        y[0] += lo16(py[r][n][sI][0]); y[1] += hi16(py[r][n][sI][0]);
        y[2] += lo16(py[r][n][sI][1]); y[3] += hi16(py[r][n][sI][1]);
      }
      float bv[4] = {lo16(bb[n][0]), hi16(bb[n][0]), lo16(bb[n][1]), hi16(bb[n][1])};
      float xv[4] = {lo16(xx[r][n][0]), hi16(xx[r][n][0]), lo16(xx[r][n][1]), hi16(xx[r][n][1])};
      float o[4];
#pragma unroll
      for (int j = 0; j < 4; ++j) {
        float yy = y[j] + bv[j];
        float gg = yy / (1.f + __expf(-yy));
        o[j] = xv[j] + acc[r][n][j] * gg;
      }
      u32x2 v = {pack2(o[0], o[1]), pack2(o[2], o[3])};
      *reinterpret_cast<u32x2*>(out + (size_t)m * D_ + ncol) = v;
    }
  }
  STAMP(3, 4);
}

extern "C" void kernel_launch(void* const* d_in, const int* in_sizes, int n_in, void* d_out, int out_size, void* d_ws,
                              size_t ws_size, hipStream_t stream) {
  const u16* x = (const u16*)d_in[0];
  const u16* wqkv = (const u16*)d_in[1];
  const u16* wphi = (const u16*)d_in[2];
  const u16* wo = (const u16*)d_in[3];
  const u16* w1 = (const u16*)d_in[5];
  const u16* b1 = (const u16*)d_in[6];
  const u16* w2 = (const u16*)d_in[7];
  const u16* b2 = (const u16*)d_in[8];
  u16* out = (u16*)d_out;
  char* ws = (char*)d_ws;
  size_t off = 0;
  u16* xnf = (u16*)(ws + off); off += (size_t)S_ * D_ * 2;
  u16* wfold = (u16*)(ws + off); off += (size_t)D_ * NPH_ * 2;
  u16* qkv = (u16*)(ws + off); off += (size_t)96 * S_ * 24 * 2;
  u16* hf = (u16*)(ws + off); off += (size_t)S_ * F_ * 2;
  u16* part = (u16*)(ws + off); off += (size_t)SPLITK_ * S_ * D_ * 2;
  u16* of = (u16*)(ws + off); off += (size_t)S_ * D_ * 2;
  float* pkv = (float*)(ws + off); off += (size_t)256 * 1536 * 4;
  unsigned* cnt = (unsigned*)(ws + off); off += 32 * 32 * 4;

  k0_prep<<<512 + 192, 256, 0, stream>>>(x, wqkv, wphi, xnf, wfold, cnt);
  k1_gemm1<<<256, 512, 0, stream>>>(xnf, wfold, wqkv, w1, b1, qkv, hf);
  k2_gemm2<<<256, 512, 0, stream>>>(hf, w2, part, qkv, of, pkv, cnt);
  k3_final<<<16 * 9, 512, 0, stream>>>(of, wo, part, b2, x, out);
}
```

```cpp
#include <hip/hip_runtime.h>
#include <hip/hip_bf16.h>
#include <stdint.h>

constexpr int S_ = 2048, D_ = 576, H_ = 32, DH_ = 18, F_ = 4608, NQKV_ = 1728;
constexpr int NPH_ = 1152;
constexpr int KPH_ = 40;
constexpr int KPHI_ = H_ * KPH_;
constexpr int SPLITK_ = 4;

typedef short bf16x8 __attribute__((ext_vector_type(8)));
typedef short s16x4 __attribute__((ext_vector_type(4)));
typedef float f32x4 __attribute__((ext_vector_type(4)));
typedef unsigned u32x4 __attribute__((ext_vector_type(4)));
typedef unsigned u32x2 __attribute__((ext_vector_type(2)));
typedef __attribute__((address_space(3))) s16x4* lds_s16x4_p;
typedef unsigned short u16;
template <int V> struct IC { static constexpr int value = V; };

#define STAMP(kid, i) do { } while (0)
#define ST_NT(p, v) (*(p) = (v))
#define WAIT_LGKM0() asm volatile("s_waitcnt lgkmcnt(0)" ::: "memory")
#define BAR() __builtin_amdgcn_s_barrier()

template <int N>
__device__ __forceinline__ void wait_vm() {
  static_assert(N >= 0 && N < 64, "vmcnt range");
  asm volatile("s_waitcnt vmcnt(%0)" ::"n"(N) : "memory");
}

__device__ __forceinline__ float bf2f(u16 v) { return __uint_as_float(((unsigned)v) << 16); }
__device__ __forceinline__ u16 f2bf(float f) {
  __hip_bfloat16 b = __float2bfloat16(f);
  return *reinterpret_cast<u16*>(&b);
}
__device__ __forceinline__ unsigned pack2(float lo, float hi) {
  return (unsigned)f2bf(lo) | ((unsigned)f2bf(hi) << 16);
}
__device__ __forceinline__ float rbf(float f) { return bf2f(f2bf(f)); }
__device__ __forceinline__ float lo16(unsigned u) { return __uint_as_float(u << 16); }
__device__ __forceinline__ float hi16(unsigned u) { return __uint_as_float(u & 0xffff0000u); }

__device__ __forceinline__ void glds16(const void* g, void* l) {
  __builtin_amdgcn_global_load_lds((const unsigned*)g, (unsigned*)l, 16, 0, 0);
}

__device__ __forceinline__ void l2_touch(const void* g, void* ldummy) {
  __builtin_amdgcn_global_load_lds((const unsigned*)g, (unsigned*)ldummy, 4, 0, 0);
}

__device__ __forceinline__ bf16x8 swap_halves_if(bf16x8 v, bool odd) {
  bf16x8 s = __builtin_shufflevector(v, v, 4, 5, 6, 7, 0, 1, 2, 3);
  return odd ? s : v;
}

__device__ __forceinline__ bf16x8 wfrag(const char* img, int o0, int o1, int off) {
  s16x4 a = __builtin_amdgcn_ds_read_tr16_b64_v4i16((lds_s16x4_p)(img + o0 + off));
  s16x4 b = __builtin_amdgcn_ds_read_tr16_b64_v4i16((lds_s16x4_p)(img + o1 + off));
  return __builtin_shufflevector(a, b, 0, 1, 2, 3, 4, 5, 6, 7);
}

struct WF { s16x4 lo, hi; };
template <int OFF>
__device__ __forceinline__ void tr_rd(s16x4& r, unsigned addr) {
  asm volatile("ds_read_b64_tr_b16 %0, %1 offset:%2" : "=v"(r) : "v"(addr), "n"(OFF) : "memory");
}
__device__ __forceinline__ void gld16_asm(bf16x8& r, const void* p) {
  asm volatile("global_load_dwordx4 %0, %1, off" : "=v"(r) : "v"(p) : "memory");
}
template <int N>
__device__ __forceinline__ void wait_lgkm() {
  static_assert(N >= 0 && N <= 15, "lgkmcnt range");
  asm volatile("s_waitcnt lgkmcnt(%0)" ::"n"(N) : "memory");
}
__device__ __forceinline__ void pin(WF& w) { asm volatile("" : "+v"(w.lo), "+v"(w.hi)); }
__device__ __forceinline__ void pin(bf16x8& w) { asm volatile("" : "+v"(w)); }
__device__ __forceinline__ bf16x8 cat(const WF& w) { return __builtin_shufflevector(w.lo, w.hi, 0, 1, 2, 3, 4, 5, 6, 7); }
__device__ __forceinline__ unsigned lds_addr(const void* p) {
  return (unsigned)(uintptr_t)(__attribute__((address_space(3))) const char*)p;
}

__global__ __launch_bounds__(256) void k0_prep(const u16* __restrict__ x, const u16* __restrict__ wqkv,
                                               const u16* __restrict__ wphi, u16* __restrict__ xnf,
                                               u16* __restrict__ wfold, unsigned* __restrict__ cnt) {
  __shared__ __attribute__((aligned(16))) float wph[18 * 20];
  const int tid = threadIdx.x, wid = tid >> 6, lane = tid & 63;
  const int bid = blockIdx.x;
  if (bid == 512 && tid < 32) cnt[tid * 32] = 0u;
  if (bid < 512) {
    const int row = bid * 4 + wid;
    const u16* xp = x + (size_t)row * D_;
    u32x4 a = *reinterpret_cast<const u32x4*>(xp + lane * 8);
    u32x4 b = {0u, 0u, 0u, 0u};
    if (lane < 8) b = *reinterpret_cast<const u32x4*>(xp + 512 + lane * 8);
    float v[16];
#pragma unroll
    for (int j = 0; j < 4; ++j) {
      v[2 * j] = lo16(a[j]); v[2 * j + 1] = hi16(a[j]);
      v[8 + 2 * j] = lo16(b[j]); v[8 + 2 * j + 1] = hi16(b[j]);
    }
    float s = 0.f;
#pragma unroll
    for (int j = 0; j < 16; ++j) s += v[j];
#pragma unroll
    for (int o = 32; o >= 1; o >>= 1) s += __shfl_xor(s, o);
    const float mean = s * (1.f / D_);
    float ss = 0.f;
#pragma unroll
    for (int j = 0; j < 8; ++j) { float d = v[j] - mean; ss += d * d; }
    if (lane < 8) {
#pragma unroll
      for (int j = 8; j < 16; ++j) { float d = v[j] - mean; ss += d * d; }
    }
#pragma unroll
    for (int o = 32; o >= 1; o >>= 1) ss += __shfl_xor(ss, o);
    const float rstd = rsqrtf(ss * (1.f / D_) + 1e-5f);
    u32x4 oa, ob;
#pragma unroll
    for (int j = 0; j < 4; ++j) {
      oa[j] = pack2((v[2 * j] - mean) * rstd, (v[2 * j + 1] - mean) * rstd);
      ob[j] = pack2((v[8 + 2 * j] - mean) * rstd, (v[8 + 2 * j + 1] - mean) * rstd);
    }
    {
      const int c = lane;
      u16* dst = xnf + ((size_t)((row >> 4) * 18 + (c >> 2)) * 64 + (c & 3) * 16 + (row & 15)) * 8;
      *reinterpret_cast<u32x4*>(dst) = oa;
    }
    if (lane < 8) {
      const int c = 64 + lane;
      u16* dst = xnf + ((size_t)((row >> 4) * 18 + (c >> 2)) * 64 + (c & 3) * 16 + (row & 15)) * 8;
      *reinterpret_cast<u32x4*>(dst) = ob;
    }
    return;
  }
  {
    const int fb = bid - 512;
    const int hh = fb / 3, k = (fb % 3) * 192 + tid;
    const int h = hh & 31;
    for (int i = tid; i < 18 * 20; i += 256) {
      const int d = i / 20, f = i % 20;
      wph[i] = (f < 18) ? bf2f(wphi[h * 324 + d * 18 + f]) : 0.f;
    }
    unsigned wv[9];
    if (tid < 192) {
      const u16* wp = wqkv + (size_t)k * NQKV_ + hh * 18;
#pragma unroll
      for (int d = 0; d < 9; ++d) wv[d] = *reinterpret_cast<const unsigned*>(wp + 2 * d);
    }
    __syncthreads();
    if (tid < 192) {
      float acc[20];
#pragma unroll
      for (int f = 0; f < 20; ++f) acc[f] = 0.f;
#pragma unroll
      for (int d = 0; d < 18; ++d) {
        const float w = (d & 1) ? hi16(wv[d >> 1]) : lo16(wv[d >> 1]);
#pragma unroll
        for (int f4 = 0; f4 < 5; ++f4) {
          f32x4 pw = *reinterpret_cast<const f32x4*>(wph + d * 20 + f4 * 4);
#pragma unroll
          for (int j = 0; j < 4; ++j) acc[f4 * 4 + j] += w * pw[j];
        }
      }
      u16* op = wfold + (size_t)k * NPH_ + hh * 18;
#pragma unroll
      for (int f = 0; f < 9; ++f) *reinterpret_cast<unsigned*>(op + 2 * f) = pack2(acc[2 * f], acc[2 * f + 1]);
    }
  }
}

constexpr int G1_BUFB = 96 * 128;
constexpr int G1_RING = 4;
constexpr int G1_XOFF = G1_RING * G1_BUFB;
constexpr int G1_BOFF = G1_XOFF + 576 * 32;
constexpr int G1_SMEM = G1_BOFF + 1024;
constexpr int G1_NST = 8;

template <int BASE, int I = 0>
__device__ __forceinline__ void g1_reads(WF (&w)[4], const unsigned (&ad)[4][2]) {
  if constexpr (I < 4) {
    if constexpr (BASE >= 49152) {
      tr_rd<BASE - 49152>(w[I].lo, ad[I][0] + 49152u);
      tr_rd<BASE - 49152>(w[I].hi, ad[I][1] + 49152u);
    } else {
      tr_rd<BASE>(w[I].lo, ad[I][0]);
      tr_rd<BASE>(w[I].hi, ad[I][1]);
    }
    g1_reads<BASE, I + 1>(w, ad);
  }
}
template <int KS>
__device__ __forceinline__ void g1_mma(WF (&w)[4], const bf16x8 (&xa)[2][18], f32x4 (&acc)[2][4]) {
#pragma unroll
  for (int i = 0; i < 4; ++i) pin(w[i]);
#pragma unroll
  for (int i = 0; i < 4; ++i)
#pragma unroll
    for (int r = 0; r < 2; ++r)
      acc[r][i] = __builtin_amdgcn_mfma_f32_16x16x32_bf16(cat(w[i]), xa[r][KS], acc[r][i], 0, 0, 0);
}

__device__ __forceinline__ float gelu_f(float v) {
  constexpr float c1 = -2.f * 0.7978845608028654f * 1.4426950408889634f;
  constexpr float c3 = c1 * 0.044715f;
  const float t = v * v;
  const float e = __builtin_amdgcn_exp2f(v * fmaf(t, c3, c1));
  return v * __builtin_amdgcn_rcpf(1.f + e);
}

typedef float f32x2 __attribute__((ext_vector_type(2)));
__device__ __forceinline__ unsigned cvt_pk(float lo, float hi) {
  unsigned r;
  asm("v_cvt_pk_bf16_f32 %0, %1, %2" : "=v"(r) : "v"(lo), "v"(hi));
  return r;
}
__device__ __forceinline__ unsigned gelu2_pk(f32x2 v) {
  constexpr float c1 = -2.f * 0.7978845608028654f * 1.4426950408889634f;
  constexpr float c3 = c1 * 0.044715f;
  const f32x2 t = v * v;
  const f32x2 pp = t * (f32x2){c3, c3} + (f32x2){c1, c1};
  const f32x2 u = v * pp;
  f32x2 e = {__builtin_amdgcn_exp2f(u[0]), __builtin_amdgcn_exp2f(u[1])};
  e = e + (f32x2){1.f, 1.f};
  const f32x2 r = {__builtin_amdgcn_rcpf(e[0]), __builtin_amdgcn_rcpf(e[1])};
  const f32x2 o = v * r;
  return cvt_pk(o[0], o[1]);
}

__device__ __forceinline__ void g1_store_tile(const f32x4& a, int tile, int mrow, int g, const u16* bias4,
                                              u16* __restrict__ qkv, u16* __restrict__ hf) {
  if (tile < NQKV_ / 16) {
    const int col = tile * 16 + 4 * g;
#pragma unroll
    for (int j = 0; j < 2; ++j) {
      const int c = col + 2 * j;
      const int hh = c / 18, f = c - hh * 18;
      *reinterpret_cast<unsigned*>(qkv + ((size_t)hh * S_ + mrow) * 24 + f) = pack2(a[2 * j], a[2 * j + 1]);
    }
  } else {
    u32x2 bb = *reinterpret_cast<const u32x2*>(bias4);
    float o[4];
    o[0] = gelu_f(a[0] + lo16(bb[0]));
    o[1] = gelu_f(a[1] + hi16(bb[0]));
    o[2] = gelu_f(a[2] + lo16(bb[1]));
    o[3] = gelu_f(a[3] + hi16(bb[1]));
    u32x2 v = {pack2(o[0], o[1]), pack2(o[2], o[3])};
    const int T = tile - NQKV_ / 16;
    u16* dst = hf + ((size_t)((mrow >> 4) * 144 + (T >> 1)) * 64 + ((T & 1) * 2 + (g >> 1)) * 16 + (mrow & 15)) * 8 + (g & 1) * 4;
    *reinterpret_cast<u32x2*>(dst) = v;
  }
}

__device__ __forceinline__ void g1_addr(unsigned (&ad)[4][2], char* smem, int lane) {
  const int g = lane >> 4, q = (lane & 15) >> 2, p = lane & 3;
#pragma unroll
  for (int t = 0; t < 2; ++t) {
    const int row = 8 * g + 4 * t + q;
    const int f = ((row >> 1) & 1) | (((row >> 3) & 1) << 1);
#pragma unroll
    for (int n = 0; n < 4; ++n) ad[n][t] = lds_addr(smem) + row * 128 + ((n ^ f) * 32) + p * 8;
  }
}

struct G1W { const u16* src; int stride; };

__device__ __forceinline__ void g1_wsetup(G1W& w, int col, int wid, int lane, const u16* wfold, const u16* wqkv, const u16* w1) {
  const int row = wid * 8 + (lane >> 3);
  const int f = ((row >> 1) & 1) | (((row >> 3) & 1) << 1);
  const int ch = (lane & 7) ^ (f << 1);
  if (col < NPH_) { w.src = wfold + (size_t)row * NPH_ + col + ch * 8; w.stride = 32 * NPH_; }
  else if (col < NQKV_) { w.src = wqkv + (size_t)row * NQKV_ + col + ch * 8; w.stride = 32 * NQKV_; }
  else { w.src = w1 + (size_t)row * F_ + (col - NQKV_) + ch * 8; w.stride = 32 * F_; }
}
__device__ __forceinline__ void g1_wissue(G1W& w, char* buf, int wid, int lane) {
#pragma unroll
  for (int i = 0; i < 3; ++i) {
    glds16(w.src, buf + i * 4096 + wid * 1024 + lane * 16);
    w.src += w.stride;
  }
}

template <int U>
__device__ __forceinline__ void g1_unit(G1W& wst, int unit0, const u16* wfold, const u16* wqkv,
                                        const u16* w1, char* smem, bf16x8 (&xa)[2][18], int m0,
                                        u16* __restrict__ qkv, u16* __restrict__ hf, WF (&wA)[4]) {
  int tz = threadIdx.x;
  asm volatile("" : "+v"(tz));
  const int wid = __builtin_amdgcn_readfirstlane(tz >> 6), lane = tz & 63;
  const int g = lane >> 4;
  unsigned ad[4][2];
  g1_addr(ad, smem, lane);
  f32x4 acc[2][4];
#pragma unroll
  for (int r = 0; r < 2; ++r)
#pragma unroll
    for (int n = 0; n < 4; ++n) acc[r][n] = (f32x4){0.f, 0.f, 0.f, 0.f};

  auto stage = [&](auto KC) {
    constexpr int kc = decltype(KC)::value;
    constexpr int s = U * 6 + kc;
    constexpr int CUR = (s % G1_RING) * G1_BUFB;
    if constexpr (U == 0) {
#pragma unroll
      for (int k = 0; k < 3; ++k) { pin(xa[0][kc * 3 + k]); pin(xa[1][kc * 3 + k]); }
    }
    WF wB[4];
    g1_reads<CUR + 1 * 4096>(wB, ad); wait_lgkm<8>(); g1_mma<kc * 3 + 0>(wA, xa, acc);
    g1_reads<CUR + 2 * 4096>(wA, ad); wait_lgkm<8>(); g1_mma<kc * 3 + 1>(wB, xa, acc);
    if constexpr (s < 17) {
      constexpr int nxa = (U == 0 && kc < 2) ? (36 - 6 * (kc + 2)) : 0;
      constexpr int nwl = (U == 0 && kc < 2) ? 3 * kc : ((s + 2 <= 17) ? 3 : 0);
      constexpr int nst = (U > 0 && kc <= 1) ? G1_NST : 0;
      wait_vm<nxa + nwl + nst>();
      BAR();
      if constexpr (s + 3 <= 17) {
        constexpr int sn = s + 3;
        if constexpr (sn % 6 == 0) g1_wsetup(wst, (unit0 + sn / 6) * 64, wid, lane, wfold, wqkv, w1);
        g1_wissue(wst, smem + (sn % G1_RING) * G1_BUFB, wid, lane);
      }
      g1_reads<((s + 1) % G1_RING) * G1_BUFB>(wB, ad);
      wait_lgkm<8>();
      g1_mma<kc * 3 + 2>(wA, xa, acc);
#pragma unroll
      for (int i = 0; i < 4; ++i) wA[i] = wB[i];
    } else {
      wait_lgkm<0>();
      g1_mma<kc * 3 + 2>(wA, xa, acc);
    }
  };
  stage(IC<0>{}); stage(IC<1>{}); stage(IC<2>{}); stage(IC<3>{}); stage(IC<4>{}); stage(IC<5>{});
  const u16* bias_lds = reinterpret_cast<const u16*>(smem + G1_BOFF);
  if ((unit0 + U) * 64 < NQKV_) {
#pragma unroll
    for (int n = 0; n < 4; ++n) {
      const int tile = (unit0 + U) * 4 + n;
#pragma unroll
      for (int r = 0; r < 2; ++r)
        g1_store_tile(acc[r][n], tile, m0 + wid * 32 + r * 16 + (lane & 15), g, bias_lds, qkv, hf);
    }
  } else {
    u32x2 bb[4];
#pragma unroll
    for (int n = 0; n < 4; ++n) bb[n] = *reinterpret_cast<const u32x2*>(bias_lds + (U * 4 + n) * 16 + 4 * g);
    const int T0 = (unit0 + U) * 4 - NQKV_ / 16;
#pragma unroll
    for (int r = 0; r < 2; ++r) {
      const int mrow = m0 + wid * 32 + r * 16 + (lane & 15);
      u16* dst = hf + ((size_t)((mrow >> 4) * 144 + (T0 >> 1)) * 64 + (g >> 1) * 16 + (mrow & 15)) * 8 + (g & 1) * 4;
#pragma unroll
      for (int n = 0; n < 4; ++n) {
        const f32x2 v0 = (f32x2){acc[r][n][0], acc[r][n][1]} + (f32x2){lo16(bb[n][0]), hi16(bb[n][0])};
        const f32x2 v1 = (f32x2){acc[r][n][2], acc[r][n][3]} + (f32x2){lo16(bb[n][1]), hi16(bb[n][1])};
        u32x2 v = {gelu2_pk(v0), gelu2_pk(v1)};
        ST_NT(reinterpret_cast<u32x2*>(dst + (n >> 1) * 512 + (n & 1) * 256), v);
      }
    }
  }
}

__global__ __launch_bounds__(256, 2) void k1_gemm1(const u16* __restrict__ xnf, const u16* __restrict__ wfold,
                                                   const u16* __restrict__ wqkv, const u16* __restrict__ w1,
                                                   const u16* __restrict__ b1, u16* __restrict__ qkv,
                                                   u16* __restrict__ hf) {
  __shared__ __attribute__((aligned(16))) char smem[G1_SMEM];
  const int tid = threadIdx.x, wid = tid >> 6, lane = tid & 63;
  const int bid = blockIdx.x;
  const int mt = bid >> 5, grp = bid & 31;
  const int m0 = mt * 128;
  const int unit0 = grp * 3;
  const bool extra = grp < 12;
  const int xcol = 6144 + (extra ? grp : 0) * 16;
  STAMP(1, 0);
  G1W wst;
  g1_wsetup(wst, unit0 * 64, wid, lane, wfold, wqkv, w1);
  g1_wissue(wst, smem + 0 * G1_BUFB, wid, lane);
  g1_wissue(wst, smem + 1 * G1_BUFB, wid, lane);
  g1_wissue(wst, smem + 2 * G1_BUFB, wid, lane);
#pragma unroll
  for (int i = 0; i < 5; ++i) {
    int ins = wid + 4 * i;
    if (ins >= 18) ins -= 18;
    const int c = ins * 64 + lane;
    const int pr = c >> 1, half = c & 1;
    const int k = (pr & ~0xC) | ((pr & 4) << 1) | ((pr & 8) >> 1);
    glds16(w1 + (size_t)k * F_ + (xcol - NQKV_) + half * 8, smem + G1_XOFF + c * 16);
  }
  {
    const int e = (wid & 1) * 64 + lane;
    int col = (e < 96) ? (unit0 * 64 + 2 * e - NQKV_) : (xcol - NQKV_ + 2 * (e - 96));
    if (col < 0 || e >= 104) col = 0;
    __builtin_amdgcn_global_load_lds((const unsigned*)(b1 + col), (unsigned*)(smem + G1_BOFF + e * 4), 4, 0, 0);
  }
  bf16x8 xa[2][18];
#pragma unroll
  for (int ks = 0; ks < 18; ++ks)
#pragma unroll
    for (int r = 0; r < 2; ++r)
      gld16_asm(xa[r][ks], xnf + ((size_t)(((m0 >> 4) + wid * 2 + r) * 18 + ks) * 64 + lane) * 8);
  wait_vm<30>();
  BAR();
  WF wA[4];
  {
    unsigned ad[4][2];
    g1_addr(ad, smem, lane);
    g1_reads<0>(wA, ad);
  }
  STAMP(1, 1);
  g1_unit<0>(wst, unit0, wfold, wqkv, w1, smem, xa, m0, qkv, hf, wA);
  STAMP(1, 2);
  g1_unit<1>(wst, unit0, wfold, wqkv, w1, smem, xa, m0, qkv, hf, wA);
  STAMP(1, 3);
  g1_unit<2>(wst, unit0, wfold, wqkv, w1, smem, xa, m0, qkv, hf, wA);
  STAMP(1, 4);
  if (extra) {
    int tz = threadIdx.x;
    asm volatile("" : "+v"(tz));
    const int wid2 = __builtin_amdgcn_readfirstlane(tz >> 6), lane2 = tz & 63;
    const int g = lane2 >> 4, q = (lane2 & 15) >> 2, p = lane2 & 3;
    unsigned ad[2];
#pragma unroll
    for (int t = 0; t < 2; ++t) {
      const int k = 8 * g + 4 * t + q;
      const int pr = (k & ~0xC) | ((k & 4) << 1) | ((k & 8) >> 1);
      ad[t] = lds_addr(smem) + G1_XOFF + pr * 32 + p * 8;
    }
    f32x4 acc[2] = {(f32x4){0.f, 0.f, 0.f, 0.f}, (f32x4){0.f, 0.f, 0.f, 0.f}};
#pragma unroll
    for (int kb = 0; kb < 18; kb += 6) {
      WF w[6];
      tr_rd<0 * 1024>(w[0].lo, ad[0] + kb * 1024); tr_rd<0 * 1024>(w[0].hi, ad[1] + kb * 1024);
      tr_rd<1 * 1024>(w[1].lo, ad[0] + kb * 1024); tr_rd<1 * 1024>(w[1].hi, ad[1] + kb * 1024);
      tr_rd<2 * 1024>(w[2].lo, ad[0] + kb * 1024); tr_rd<2 * 1024>(w[2].hi, ad[1] + kb * 1024);
      tr_rd<3 * 1024>(w[3].lo, ad[0] + kb * 1024); tr_rd<3 * 1024>(w[3].hi, ad[1] + kb * 1024);
      tr_rd<4 * 1024>(w[4].lo, ad[0] + kb * 1024); tr_rd<4 * 1024>(w[4].hi, ad[1] + kb * 1024);
      tr_rd<5 * 1024>(w[5].lo, ad[0] + kb * 1024); tr_rd<5 * 1024>(w[5].hi, ad[1] + kb * 1024);
      wait_lgkm<0>();
#pragma unroll
      for (int i = 0; i < 6; ++i) pin(w[i]);
#pragma unroll
      for (int i = 0; i < 6; ++i)
#pragma unroll
        for (int r = 0; r < 2; ++r)
          acc[r] = __builtin_amdgcn_mfma_f32_16x16x32_bf16(cat(w[i]), xa[r][kb + i], acc[r], 0, 0, 0);
    }
    const u16* bias_lds = reinterpret_cast<const u16*>(smem + G1_BOFF);
#pragma unroll
    for (int r = 0; r < 2; ++r)
      g1_store_tile(acc[r], xcol / 16, m0 + wid2 * 32 + r * 16 + (lane2 & 15), g, bias_lds + 192 + 4 * g, qkv, hf);
  }
}

constexpr int G2_NT = 9, G2_RB = G2_NT * 32;
constexpr int G2_BUFB = 64 * G2_RB;
constexpr int G2_RING = 4;
constexpr int G2_STAGES = 18;

__device__ __forceinline__ void softmax_pm(const float (&pk)[18], float (&ep)[18], float (&en)[18]) {
  float mx = pk[0], mn = pk[0];
#pragma unroll
  for (int f = 1; f < 18; ++f) {
    mx = fmaxf(mx, pk[f]);
    mn = fminf(mn, pk[f]);
  }
  float sp = 0.f, sn = 0.f;
#pragma unroll
  for (int f = 0; f < 18; ++f) {
    ep[f] = __expf(pk[f] - mx);
    en[f] = __expf(mn - pk[f]);
    sp += ep[f];
    sn += en[f];
  }
  const float rp = 1.f / sp, rn = 1.f / sn;
#pragma unroll
  for (int f = 0; f < 18; ++f) {
    ep[f] *= rp;
    en[f] *= rn;
  }
}

__device__ void head_block(int hb, const u16* __restrict__ qkv, u16* __restrict__ of, float* pkv, unsigned* cnt, char* smem) {
  u16* phiT = reinterpret_cast<u16*>(smem);
  u16* vT = reinterpret_cast<u16*>(smem + 25344);
  float* kvs = reinterpret_cast<float*>(smem + 25344 + 16896);
  const int h = hb >> 3, c8 = hb & 7;
  const int tid = threadIdx.x, wid = tid >> 6, lane = tid & 63;
  const int row = c8 * 256 + tid;
  u32x4 kr[3], vr[3], qr[3];
  {
    const u32x4* kp = reinterpret_cast<const u32x4*>(qkv + ((size_t)(32 + h) * S_ + row) * 24);
    const u32x4* vp = reinterpret_cast<const u32x4*>(qkv + ((size_t)(64 + h) * S_ + row) * 24);
    const u32x4* qp = reinterpret_cast<const u32x4*>(qkv + ((size_t)h * S_ + row) * 24);
#pragma unroll
    for (int d = 0; d < 3; ++d) { kr[d] = kp[d]; vr[d] = vp[d]; qr[d] = qp[d]; }
  }
  for (int i = tid; i < 12 * 264; i += 256) phiT[36 * 264 + i] = 0;
  for (int i = tid; i < 14 * 264; i += 256) vT[18 * 264 + i] = (i < 264) ? (u16)0x3f80 : (u16)0;
  {
    float pk[18];
#pragma unroll
    for (int d = 0; d < 9; ++d) {
      pk[2 * d] = lo16(kr[d >> 2][d & 3]);
      pk[2 * d + 1] = hi16(kr[d >> 2][d & 3]);
    }
    float ep[18], en[18];
    softmax_pm(pk, ep, en);
#pragma unroll
    for (int f = 0; f < 18; ++f) {
      phiT[f * 264 + tid] = f2bf(ep[f]);
      phiT[(18 + f) * 264 + tid] = f2bf(en[f]);
      const unsigned w = vr[(f >> 1) >> 2][(f >> 1) & 3];
      vT[f * 264 + tid] = (u16)((f & 1) ? (w >> 16) : (w & 0xffff));
    }
  }
  __syncthreads();
  {
    f32x4 acc[3][2];
#pragma unroll
    for (int a = 0; a < 3; ++a)
#pragma unroll
      for (int b = 0; b < 2; ++b) acc[a][b] = (f32x4){0.f, 0.f, 0.f, 0.f};
#pragma unroll
    for (int ks = 0; ks < 2; ++ks) {
      const int s0 = wid * 64 + ks * 32 + 8 * (lane >> 4);
      bf16x8 bv[2];
#pragma unroll
      for (int b = 0; b < 2; ++b) bv[b] = *reinterpret_cast<const bf16x8*>(vT + (b * 16 + (lane & 15)) * 264 + s0);
#pragma unroll
      for (int a = 0; a < 3; ++a) {
        bf16x8 av = *reinterpret_cast<const bf16x8*>(phiT + (a * 16 + (lane & 15)) * 264 + s0);
#pragma unroll
        for (int b = 0; b < 2; ++b) acc[a][b] = __builtin_amdgcn_mfma_f32_16x16x32_bf16(av, bv[b], acc[a][b], 0, 0, 0);
      }
    }
#pragma unroll
    for (int a = 0; a < 3; ++a)
#pragma unroll
      for (int b = 0; b < 2; ++b)
#pragma unroll
        for (int j = 0; j < 4; ++j)
          kvs[(wid * 48 + a * 16 + (lane >> 4) * 4 + j) * 32 + b * 16 + (lane & 15)] = acc[a][b][j];
  }
  __syncthreads();
  float* mypart = pkv + (size_t)(h * 8 + c8) * 1536;
#pragma unroll
  for (int it = 0; it < 6; ++it) {
    const int i = it * 256 + tid;
    const float v = kvs[i] + kvs[1536 + i] + kvs[2 * 1536 + i] + kvs[3 * 1536 + i];
    __hip_atomic_store(mypart + i, v, __ATOMIC_RELAXED, __HIP_MEMORY_SCOPE_AGENT);
  }
  asm volatile("s_waitcnt vmcnt(0)" ::: "memory");
  __syncthreads();
  if (tid == 0) __hip_atomic_fetch_add(cnt + h * 32, 1u, __ATOMIC_RELAXED, __HIP_MEMORY_SCOPE_AGENT);
  float ep[18], en[18];
  {
    float pk[18];
#pragma unroll
    for (int d = 0; d < 9; ++d) {
      pk[2 * d] = lo16(qr[d >> 2][d & 3]);
      pk[2 * d + 1] = hi16(qr[d >> 2][d & 3]);
    }
    softmax_pm(pk, ep, en);
#pragma unroll
    for (int f = 0; f < 18; ++f) { ep[f] = rbf(ep[f]); en[f] = rbf(en[f]); }
  }
  if (tid == 0) {
    unsigned spins = 0;
    while (__hip_atomic_load(cnt + h * 32, __ATOMIC_RELAXED, __HIP_MEMORY_SCOPE_AGENT) < 8u) {
      __builtin_amdgcn_s_sleep(1);
      if (++spins > (1u << 24)) break;
    }
  }
  __syncthreads();
  float* kvf = reinterpret_cast<float*>(smem);
  {
    float* hp = pkv + (size_t)h * 8 * 1536;
    float pv[3][8];
#pragma unroll
    for (int it = 0; it < 3; ++it) {
      const int i = it * 256 + tid;
      const int f = i / 20, d = i - f * 20;
      const int src = (i < 720) ? (f * 32 + d) : 0;
#pragma unroll
      for (int c = 0; c < 8; ++c) pv[it][c] = __hip_atomic_load(hp + c * 1536 + src, __ATOMIC_RELAXED, __HIP_MEMORY_SCOPE_AGENT);
    }
#pragma unroll
    for (int it = 0; it < 3; ++it) {
      const int i = it * 256 + tid;
      float v = 0.f;
#pragma unroll
      for (int c = 0; c < 8; ++c) v += pv[it][c];
      if (i < 720) kvf[i] = rbf(v);
    }
  }
  __syncthreads();
  {
    float num[20];
#pragma unroll
    for (int d = 0; d < 20; ++d) num[d] = 0.f;
#pragma unroll
    for (int f = 0; f < 36; ++f) {
      const float ph = (f < 18) ? ep[f] : en[f - 18];
#pragma unroll
      for (int d4 = 0; d4 < 5; ++d4) {
        const f32x4 w = *reinterpret_cast<const f32x4*>(kvf + f * 20 + d4 * 4);
#pragma unroll
        for (int j = 0; j < 4; ++j) num[d4 * 4 + j] += ph * w[j];
      }
    }
    const float rd = 1.f / (rbf(num[18]) + 1e-6f);
#pragma unroll
    for (int d2 = 0; d2 < 9; ++d2) {
      const int k = h * 18 + 2 * d2;
      u16* dst = of + ((size_t)((row >> 4) * 18 + (k >> 5)) * 64 + ((k >> 3) & 3) * 16 + (row & 15)) * 8 + (k & 7);
      *reinterpret_cast<unsigned*>(dst) = pack2(rbf(num[2 * d2]) * rd, rbf(num[2 * d2 + 1]) * rd);
    }
  }
}

template <int BASE, int N0, int CNT, int I = 0>
__device__ __forceinline__ void g2_wreads(WF (&w)[5], unsigned a0, unsigned a1) {
  if constexpr (I < CNT) {
    tr_rd<BASE + (N0 + I) * 32>(w[I].lo, a0);
    tr_rd<BASE + (N0 + I) * 32>(w[I].hi, a1);
    g2_wreads<BASE, N0, CNT, I + 1>(w, a0, a1);
  }
}
template <int N0, int CNT>
__device__ __forceinline__ void g2_mma(WF (&w)[5], const bf16x8 (&ac)[2], f32x4 (&acc)[2][G2_NT]) {
#pragma unroll
  for (int i = 0; i < CNT; ++i) pin(w[i]);
#pragma unroll
  for (int i = 0; i < CNT; ++i)
#pragma unroll
    for (int r = 0; r < 2; ++r)
      acc[r][N0 + i] = __builtin_amdgcn_mfma_f32_16x16x32_bf16(cat(w[i]), ac[r], acc[r][N0 + i], 0, 0, 0);
}
constexpr int K2_HEADBLK = 256;

__global__ __launch_bounds__(256, 2) void k2_gemm2(const u16* __restrict__ hf, const u16* __restrict__ w2,
                                                   u16* __restrict__ part, const u16* __restrict__ qkv,
                                                   u16* __restrict__ of, float* pkv, unsigned* cnt) {
  __shared__ __attribute__((aligned(16))) char smem[G2_RING * G2_BUFB];
  const int tid = threadIdx.x, wid = tid >> 6, lane = tid & 63;
  int bid = blockIdx.x;
  if (bid < K2_HEADBLK) { head_block(bid, qkv, of, pkv, cnt, smem); return; }
  bid -= K2_HEADBLK;
  STAMP(2, 0);
  const int xcd = bid & 7, loc = bid >> 3;
  const int sp = xcd >> 1, mt = (xcd & 1) * 8 + (loc >> 2), nt4 = loc & 3;
  const int m0 = mt * 128, n0 = nt4 * 144, k0 = sp * 1152;
  const int g = lane >> 4, q = (lane & 15) >> 2, p = lane & 3;
  const bool odd = g & 1;
  const unsigned wa0 = lds_addr(smem) + (8 * g + 4 * (g & 1) + q) * G2_RB + p * 8;
  const unsigned wa1 = lds_addr(smem) + (8 * g + 4 * (1 - (g & 1)) + q) * G2_RB + p * 8;
  f32x4 acc[2][G2_NT];
#pragma unroll
  for (int r = 0; r < 2; ++r)
#pragma unroll
    for (int n = 0; n < G2_NT; ++n) acc[r][n] = (f32x4){0.f, 0.f, 0.f, 0.f};
  const u16* wp[5];
  int wdst[5];
#pragma unroll
  for (int i = 0; i < 5; ++i) {
    int ins = wid + 4 * i;
    if (ins >= 18) ins -= 18;
    int c = ins * 64 + lane;
    int row = c / 18, cc = c % 18;
    wp[i] = w2 + (size_t)(k0 + row) * D_ + n0 + cc * 8;
    wdst[i] = c * 16;
  }
  const u16* ap[2];
#pragma unroll
  for (int r = 0; r < 2; ++r) ap[r] = hf + ((size_t)((m0 >> 4) + wid * 2 + r) * 144 + (k0 >> 5)) * 512 + lane * 8;
  bf16x8 an[4][2][2];
  auto issue = [&](auto SL) {
    constexpr int slot = decltype(SL)::value;
    char* buf = smem + slot * G2_BUFB;
#pragma unroll
    for (int i = 0; i < 5; ++i) { glds16(wp[i], buf + wdst[i]); wp[i] += 64 * D_; }
#pragma unroll
    for (int r = 0; r < 2; ++r) {
      gld16_asm(an[slot][r][0], ap[r]);
      gld16_asm(an[slot][r][1], ap[r] + 512);
      ap[r] += 1024;
    }
  };
  issue(IC<0>{});
  issue(IC<1>{});
  issue(IC<2>{});
  wait_vm<18>();
  BAR();
  STAMP(2, 1);
  WF wA[5], wB[5];
  g2_wreads<0, 0, 5>(wA, wa0, wa1);
  auto stage = [&](auto U, auto LAST) {
    constexpr int u = decltype(U)::value;
    constexpr int rem = decltype(LAST)::value;
    constexpr int BASE = u * G2_BUFB;
    bf16x8 c0[2], c1[2];
#pragma unroll
    for (int r = 0; r < 2; ++r) {
      pin(an[u][r][0]); pin(an[u][r][1]);
      c0[r] = swap_halves_if(an[u][r][0], odd);
      c1[r] = swap_halves_if(an[u][r][1], odd);
    }
    g2_wreads<BASE, 5, 4>(wB, wa0, wa1);
    wait_lgkm<8>();
    g2_mma<0, 5>(wA, c0, acc);
    g2_wreads<BASE + 32 * G2_RB, 0, 5>(wA, wa0, wa1);
    wait_lgkm<10>();
    g2_mma<5, 4>(wB, c0, acc);
    g2_wreads<BASE + 32 * G2_RB, 5, 4>(wB, wa0, wa1);
    wait_lgkm<8>();
    g2_mma<0, 5>(wA, c1, acc);
    if constexpr (rem >= 1) {
      if constexpr (rem >= 2) wait_vm<9>(); else wait_vm<0>();
      BAR();
      if constexpr (rem >= 3) issue(IC<(u + 3) & 3>{});
      g2_wreads<((u + 1) & 3) * G2_BUFB, 0, 5>(wA, wa0, wa1);
      wait_lgkm<10>();
    } else {
      wait_lgkm<0>();
    }
    g2_mma<5, 4>(wB, c1, acc);
  };
#pragma unroll 1
  for (int it = 0; it < 3; ++it) {
    stage(IC<0>{}, IC<3>{}); stage(IC<1>{}, IC<3>{}); stage(IC<2>{}, IC<3>{}); stage(IC<3>{}, IC<3>{});
  }
  STAMP(2, 2);
  stage(IC<0>{}, IC<3>{}); stage(IC<1>{}, IC<3>{}); stage(IC<2>{}, IC<3>{});
  stage(IC<3>{}, IC<2>{}); stage(IC<0>{}, IC<1>{}); stage(IC<1>{}, IC<0>{});
  STAMP(2, 3);
#pragma unroll
  for (int r = 0; r < 2; ++r) {
    const int m = m0 + wid * 32 + r * 16 + (lane & 15);
    u16* pp = part + ((size_t)sp * S_ + m) * D_ + n0 + 4 * g;
#pragma unroll
    for (int n = 0; n < G2_NT; ++n) {
      u32x2 v = {pack2(acc[r][n][0], acc[r][n][1]), pack2(acc[r][n][2], acc[r][n][3])};
      ST_NT(reinterpret_cast<u32x2*>(pp + n * 16), v);
    }
  }
  asm volatile("s_waitcnt vmcnt(0)" ::: "memory");
  STAMP(2, 4);
}

__global__ __launch_bounds__(512, 2) void k3_final(const u16* __restrict__ of, const u16* __restrict__ wo,
                                                   const u16* __restrict__ part, const u16* __restrict__ b2,
                                                   const u16* __restrict__ x, u16* __restrict__ out) {
  __shared__ __attribute__((aligned(16))) char smem[576 * 128];
  const int tid = threadIdx.x, wid = tid >> 6, lane = tid & 63;
  const int bid = blockIdx.x;
  const int xcd = bid & 7, loc = bid >> 3;
  const int mt = xcd * 2 + loc / 9, nt = loc % 9;
  const int m0 = mt * 128, n0 = nt * 64;
  const int g = lane >> 4, q = (lane & 15) >> 2, p = lane & 3;
  const int wr = wid >> 1, wc = wid & 1;
  STAMP(3, 0);
#pragma unroll
  for (int i = 0; i < 9; ++i) {
    const int row = (wid * 9 + i) * 8 + (lane >> 3);
    const int f = ((row >> 1) & 1) | (((row >> 3) & 1) << 1);
    const int ch = (lane & 7) ^ (f << 1);
    glds16(wo + (size_t)row * D_ + n0 + ch * 8, smem + (wid * 9 + i) * 1024 + lane * 16);
  }
  bf16x8 an[2][18];
#pragma unroll
  for (int ks = 0; ks < 18; ++ks)
#pragma unroll
    for (int r = 0; r < 2; ++r)
      gld16_asm(an[r][ks], of + ((size_t)(((m0 >> 4) + wr * 2 + r) * 18 + ks) * 64 + lane) * 8);
  u32x2 py[2][2][4];
  u32x2 xx[2][2], bb[2];
#pragma unroll
  for (int r = 0; r < 2; ++r) {
    const int m = m0 + wr * 32 + r * 16 + (lane & 15);
#pragma unroll
    for (int n = 0; n < 2; ++n) {
      const int ncol = n0 + wc * 32 + n * 16 + 4 * g;
#pragma unroll
      for (int sI = 0; sI < SPLITK_; ++sI)
        asm volatile("global_load_dwordx2 %0, %1, off" : "=v"(py[r][n][sI]) : "v"(part + ((size_t)sI * S_ + m) * D_ + ncol) : "memory");
      asm volatile("global_load_dwordx2 %0, %1, off" : "=v"(xx[r][n]) : "v"(x + (size_t)m * D_ + ncol) : "memory");
    }
  }
#pragma unroll
  for (int n = 0; n < 2; ++n)
    asm volatile("global_load_dwordx2 %0, %1, off" : "=v"(bb[n]) : "v"(b2 + n0 + wc * 32 + n * 16 + 4 * g) : "memory");
  unsigned ad[2][2];
#pragma unroll
  for (int t = 0; t < 2; ++t) {
    const int row = 8 * g + 4 * t + q;
    const int f = ((row >> 1) & 1) | (((row >> 3) & 1) << 1);
#pragma unroll
    for (int n = 0; n < 2; ++n) ad[n][t] = lds_addr(smem) + row * 128 + (((wc * 2 + n) ^ f) * 32) + p * 8;
  }
  f32x4 acc[2][2];
#pragma unroll
  for (int r = 0; r < 2; ++r)
#pragma unroll
    for (int n = 0; n < 2; ++n) acc[r][n] = (f32x4){0.f, 0.f, 0.f, 0.f};
  STAMP(3, 1);
  wait_vm<22>();
  BAR();
  STAMP(3, 2);
#pragma unroll
  for (int ks = 0; ks < 18; ++ks) { pin(an[0][ks]); pin(an[1][ks]); }
  auto grp = [&](auto KB) {
    constexpr int kb = decltype(KB)::value;
    WF w[6][2];
#pragma unroll
    for (int k = 0; k < 6; ++k)
#pragma unroll
      for (int n = 0; n < 2; ++n) {
        if (k == 0) { tr_rd<0 * 4096>(w[0][n].lo, ad[n][0] + kb * 4096); tr_rd<0 * 4096>(w[0][n].hi, ad[n][1] + kb * 4096); }
        if (k == 1) { tr_rd<1 * 4096>(w[1][n].lo, ad[n][0] + kb * 4096); tr_rd<1 * 4096>(w[1][n].hi, ad[n][1] + kb * 4096); }
        if (k == 2) { tr_rd<2 * 4096>(w[2][n].lo, ad[n][0] + kb * 4096); tr_rd<2 * 4096>(w[2][n].hi, ad[n][1] + kb * 4096); }
        if (k == 3) { tr_rd<3 * 4096>(w[3][n].lo, ad[n][0] + kb * 4096); tr_rd<3 * 4096>(w[3][n].hi, ad[n][1] + kb * 4096); }
        if (k == 4) { tr_rd<4 * 4096>(w[4][n].lo, ad[n][0] + kb * 4096); tr_rd<4 * 4096>(w[4][n].hi, ad[n][1] + kb * 4096); }
        if (k == 5) { tr_rd<5 * 4096>(w[5][n].lo, ad[n][0] + kb * 4096); tr_rd<5 * 4096>(w[5][n].hi, ad[n][1] + kb * 4096); }
      }
    wait_lgkm<12>();
#pragma unroll
    for (int k = 0; k < 3; ++k)
#pragma unroll
      for (int n = 0; n < 2; ++n) pin(w[k][n]);
#pragma unroll
    for (int k = 0; k < 3; ++k)
#pragma unroll
      for (int n = 0; n < 2; ++n)
#pragma unroll
        for (int r = 0; r < 2; ++r)
          acc[r][n] = __builtin_amdgcn_mfma_f32_16x16x32_bf16(cat(w[k][n]), an[r][kb + k], acc[r][n], 0, 0, 0);
    wait_lgkm<0>();
#pragma unroll
    for (int k = 3; k < 6; ++k)
#pragma unroll
      for (int n = 0; n < 2; ++n) pin(w[k][n]);
#pragma unroll
    for (int k = 3; k < 6; ++k)
#pragma unroll
      for (int n = 0; n < 2; ++n)
#pragma unroll
        for (int r = 0; r < 2; ++r)
          acc[r][n] = __builtin_amdgcn_mfma_f32_16x16x32_bf16(cat(w[k][n]), an[r][kb + k], acc[r][n], 0, 0, 0);
  };
  grp(IC<0>{}); grp(IC<6>{}); grp(IC<12>{});
  wait_vm<0>();
  STAMP(3, 3);
#pragma unroll
  for (int n = 0; n < 2; ++n) asm volatile("" : "+v"(bb[n]));
#pragma unroll
  for (int r = 0; r < 2; ++r) {
    const int m = m0 + wr * 32 + r * 16 + (lane & 15);
#pragma unroll
    for (int n = 0; n < 2; ++n) {
      const int ncol = n0 + wc * 32 + n * 16 + 4 * g;
      asm volatile("" : "+v"(xx[r][n]));
#pragma unroll
      for (int sI = 0; sI < SPLITK_; ++sI) asm volatile("" : "+v"(py[r][n][sI]));
      f32x4 y = {0.f, 0.f, 0.f, 0.f};
#pragma unroll
      for (int sI = 0; sI < SPLITK_; ++sI) {
        y[0] += lo16(py[r][n][sI][0]); y[1] += hi16(py[r][n][sI][0]);
        y[2] += lo16(py[r][n][sI][1]); y[3] += hi16(py[r][n][sI][1]);
      }
      float bv[4] = {lo16(bb[n][0]), hi16(bb[n][0]), lo16(bb[n][1]), hi16(bb[n][1])};
      float xv[4] = {lo16(xx[r][n][0]), hi16(xx[r][n][0]), lo16(xx[r][n][1]), hi16(xx[r][n][1])};
      float o[4];
#pragma unroll
      for (int j = 0; j < 4; ++j) {
        float yy = y[j] + bv[j];
        float gg = yy / (1.f + __expf(-yy));
        o[j] = xv[j] + acc[r][n][j] * gg;
      }
      u32x2 v = {pack2(o[0], o[1]), pack2(o[2], o[3])};
      *reinterpret_cast<u32x2*>(out + (size_t)m * D_ + ncol) = v;
    }
  }
  STAMP(3, 4);
}

extern "C" void kernel_launch(void* const* d_in, const int* in_sizes, int n_in, void* d_out, int out_size, void* d_ws,
                              size_t ws_size, hipStream_t stream) {
  const u16* x = (const u16*)d_in[0];
  const u16* wqkv = (const u16*)d_in[1];
  const u16* wphi = (const u16*)d_in[2];
  const u16* wo = (const u16*)d_in[3];
  const u16* w1 = (const u16*)d_in[5];
  const u16* b1 = (const u16*)d_in[6];
  const u16* w2 = (const u16*)d_in[7];
  const u16* b2 = (const u16*)d_in[8];
  u16* out = (u16*)d_out;
  char* ws = (char*)d_ws;
  size_t off = 0;
  u16* xnf = (u16*)(ws + off); off += (size_t)S_ * D_ * 2;
  u16* wfold = (u16*)(ws + off); off += (size_t)D_ * NPH_ * 2;
  u16* qkv = (u16*)(ws + off); off += (size_t)96 * S_ * 24 * 2;
  u16* hf = (u16*)(ws + off); off += (size_t)S_ * F_ * 2;
  u16* part = (u16*)(ws + off); off += (size_t)SPLITK_ * S_ * D_ * 2;
  u16* of = (u16*)(ws + off); off += (size_t)S_ * D_ * 2;
  float* pkv = (float*)(ws + off); off += (size_t)256 * 1536 * 4;
  unsigned* cnt = (unsigned*)(ws + off); off += 32 * 32 * 4;

  k0_prep<<<512 + 192, 256, 0, stream>>>(x, wqkv, wphi, xnf, wfold, cnt);
  k1_gemm1<<<512, 256, 0, stream>>>(xnf, wfold, wqkv, w1, b1, qkv, hf);
  k2_gemm2<<<K2_HEADBLK + 256, 256, 0, stream>>>(hf, w2, part, qkv, of, pkv, cnt);
  k3_final<<<16 * 9, 512, 0, stream>>>(of, wo, part, b2, x, out);
}
```

```cpp
#include <hip/hip_runtime.h>
#include <hip/hip_bf16.h>
#include <stdint.h>

constexpr int S_ = 2048, D_ = 576, H_ = 32, DH_ = 18, F_ = 4608, NQKV_ = 1728;
constexpr int NPH_ = 1152;
constexpr int KPH_ = 40;
constexpr int KPHI_ = H_ * KPH_;
constexpr int SPLITK_ = 4;

typedef short bf16x8 __attribute__((ext_vector_type(8)));
typedef short s16x4 __attribute__((ext_vector_type(4)));
typedef float f32x4 __attribute__((ext_vector_type(4)));
typedef unsigned u32x4 __attribute__((ext_vector_type(4)));
typedef unsigned u32x2 __attribute__((ext_vector_type(2)));
typedef __attribute__((address_space(3))) s16x4* lds_s16x4_p;
typedef unsigned short u16;
template <int V> struct IC { static constexpr int value = V; };

#define STAMP(kid, i) do { } while (0)
#define ST_NT(p, v) (*(p) = (v))
#define WAIT_LGKM0() asm volatile("s_waitcnt lgkmcnt(0)" ::: "memory")
#define BAR() __builtin_amdgcn_s_barrier()

template <int N>
__device__ __forceinline__ void wait_vm() {
  static_assert(N >= 0 && N < 64, "vmcnt range");
  asm volatile("s_waitcnt vmcnt(%0)" ::"n"(N) : "memory");
}

__device__ __forceinline__ float bf2f(u16 v) { return __uint_as_float(((unsigned)v) << 16); }
__device__ __forceinline__ u16 f2bf(float f) {
  __hip_bfloat16 b = __float2bfloat16(f);
  return *reinterpret_cast<u16*>(&b);
}
__device__ __forceinline__ unsigned pack2(float lo, float hi) {
  return (unsigned)f2bf(lo) | ((unsigned)f2bf(hi) << 16);
}
__device__ __forceinline__ float rbf(float f) { return bf2f(f2bf(f)); }
__device__ __forceinline__ float lo16(unsigned u) { return __uint_as_float(u << 16); }
__device__ __forceinline__ float hi16(unsigned u) { return __uint_as_float(u & 0xffff0000u); }

__device__ __forceinline__ void glds16(const void* g, void* l) {
  __builtin_amdgcn_global_load_lds((const unsigned*)g, (unsigned*)l, 16, 0, 0);
}

__device__ __forceinline__ void l2_touch(const void* g, void* ldummy) {
  __builtin_amdgcn_global_load_lds((const unsigned*)g, (unsigned*)ldummy, 4, 0, 0);
}

__device__ __forceinline__ bf16x8 swap_halves_if(bf16x8 v, bool odd) {
  bf16x8 s = __builtin_shufflevector(v, v, 4, 5, 6, 7, 0, 1, 2, 3);
  return odd ? s : v;
}

__device__ __forceinline__ bf16x8 wfrag(const char* img, int o0, int o1, int off) {
  s16x4 a = __builtin_amdgcn_ds_read_tr16_b64_v4i16((lds_s16x4_p)(img + o0 + off));
  s16x4 b = __builtin_amdgcn_ds_read_tr16_b64_v4i16((lds_s16x4_p)(img + o1 + off));
  return __builtin_shufflevector(a, b, 0, 1, 2, 3, 4, 5, 6, 7);
}

struct WF { s16x4 lo, hi; };
template <int OFF>
__device__ __forceinline__ void tr_rd(s16x4& r, unsigned addr) {
  asm volatile("ds_read_b64_tr_b16 %0, %1 offset:%2" : "=v"(r) : "v"(addr), "n"(OFF) : "memory");
}
__device__ __forceinline__ void gld16_asm(bf16x8& r, const void* p) {
  asm volatile("global_load_dwordx4 %0, %1, off" : "=v"(r) : "v"(p) : "memory");
}
template <int N>
__device__ __forceinline__ void wait_lgkm() {
  static_assert(N >= 0 && N <= 15, "lgkmcnt range");
  asm volatile("s_waitcnt lgkmcnt(%0)" ::"n"(N) : "memory");
}
__device__ __forceinline__ void pin(WF& w) { asm volatile("" : "+v"(w.lo), "+v"(w.hi)); }
__device__ __forceinline__ void pin(bf16x8& w) { asm volatile("" : "+v"(w)); }
__device__ __forceinline__ bf16x8 cat(const WF& w) { return __builtin_shufflevector(w.lo, w.hi, 0, 1, 2, 3, 4, 5, 6, 7); }
__device__ __forceinline__ unsigned lds_addr(const void* p) {
  return (unsigned)(uintptr_t)(__attribute__((address_space(3))) const char*)p;
}

__global__ __launch_bounds__(256) void k0_prep(const u16* __restrict__ x, u16* __restrict__ xnf,
                                               unsigned* __restrict__ cnt) {
  const int tid = threadIdx.x, wid = tid >> 6, lane = tid & 63;
  const int bid = blockIdx.x;
  if (bid == 0 && tid < 64) cnt[tid * 32] = 0u;
  {
    const int row = bid * 4 + wid;
    const u16* xp = x + (size_t)row * D_;
    u32x4 a = *reinterpret_cast<const u32x4*>(xp + lane * 8);
    u32x4 b = {0u, 0u, 0u, 0u};
    if (lane < 8) b = *reinterpret_cast<const u32x4*>(xp + 512 + lane * 8);
    float v[16];
#pragma unroll
    for (int j = 0; j < 4; ++j) {
      v[2 * j] = lo16(a[j]); v[2 * j + 1] = hi16(a[j]);
      v[8 + 2 * j] = lo16(b[j]); v[8 + 2 * j + 1] = hi16(b[j]);
    }
    float s = 0.f;
#pragma unroll
    for (int j = 0; j < 16; ++j) s += v[j];
#pragma unroll
    for (int o = 32; o >= 1; o >>= 1) s += __shfl_xor(s, o);
    const float mean = s * (1.f / D_);
    float ss = 0.f;
#pragma unroll
    for (int j = 0; j < 8; ++j) { float d = v[j] - mean; ss += d * d; }
    if (lane < 8) {
#pragma unroll
      for (int j = 8; j < 16; ++j) { float d = v[j] - mean; ss += d * d; }
    }
#pragma unroll
    for (int o = 32; o >= 1; o >>= 1) ss += __shfl_xor(ss, o);
    const float rstd = rsqrtf(ss * (1.f / D_) + 1e-5f);
    u32x4 oa, ob;
#pragma unroll
    for (int j = 0; j < 4; ++j) {
      oa[j] = pack2((v[2 * j] - mean) * rstd, (v[2 * j + 1] - mean) * rstd);
      ob[j] = pack2((v[8 + 2 * j] - mean) * rstd, (v[8 + 2 * j + 1] - mean) * rstd);
    }
    {
      const int c = lane;
      u16* dst = xnf + ((size_t)((row >> 4) * 18 + (c >> 2)) * 64 + (c & 3) * 16 + (row & 15)) * 8;
      *reinterpret_cast<u32x4*>(dst) = oa;
    }
    if (lane < 8) {
      const int c = 64 + lane;
      u16* dst = xnf + ((size_t)((row >> 4) * 18 + (c >> 2)) * 64 + (c & 3) * 16 + (row & 15)) * 8;
      *reinterpret_cast<u32x4*>(dst) = ob;
    }
  }
}

constexpr int G1_BUFB = 192 * 128;
constexpr int G1_RING = 4;
constexpr int G1_XOFF = G1_RING * G1_BUFB;
constexpr int G1_BOFF = G1_XOFF + 576 * 32;
constexpr int G1_SMEM = G1_BOFF + 1024;
constexpr int G1_NST = 8;

template <int BASE, int I = 0>
__device__ __forceinline__ void g1_reads(WF (&w)[4], const unsigned (&ad)[4][2]) {
  if constexpr (I < 4) {
    if constexpr (BASE >= 49152) {
      tr_rd<BASE - 49152>(w[I].lo, ad[I][0] + 49152u);
      tr_rd<BASE - 49152>(w[I].hi, ad[I][1] + 49152u);
    } else {
      tr_rd<BASE>(w[I].lo, ad[I][0]);
      tr_rd<BASE>(w[I].hi, ad[I][1]);
    }
    g1_reads<BASE, I + 1>(w, ad);
  }
}
template <int KS>
__device__ __forceinline__ void g1_mma(WF (&w)[4], const bf16x8 (&xa)[2][18], f32x4 (&acc)[2][4]) {
#pragma unroll
  for (int i = 0; i < 4; ++i) pin(w[i]);
#pragma unroll
  for (int i = 0; i < 4; ++i)
#pragma unroll
    for (int r = 0; r < 2; ++r)
      acc[r][i] = __builtin_amdgcn_mfma_f32_16x16x32_bf16(cat(w[i]), xa[r][KS], acc[r][i], 0, 0, 0);
}

__device__ __forceinline__ float gelu_f(float v) {
  constexpr float c1 = -2.f * 0.7978845608028654f * 1.4426950408889634f;
  constexpr float c3 = c1 * 0.044715f;
  const float t = v * v;
  const float e = __builtin_amdgcn_exp2f(v * fmaf(t, c3, c1));
  return v * __builtin_amdgcn_rcpf(1.f + e);
}

typedef float f32x2 __attribute__((ext_vector_type(2)));
__device__ __forceinline__ unsigned cvt_pk(float lo, float hi) {
  unsigned r;
  asm("v_cvt_pk_bf16_f32 %0, %1, %2" : "=v"(r) : "v"(lo), "v"(hi));
  return r;
}
__device__ __forceinline__ unsigned gelu2_pk(f32x2 v) {
  constexpr float c1 = -2.f * 0.7978845608028654f * 1.4426950408889634f;
  constexpr float c3 = c1 * 0.044715f;
  const f32x2 t = v * v;
  const f32x2 pp = t * (f32x2){c3, c3} + (f32x2){c1, c1};
  const f32x2 u = v * pp;
  f32x2 e = {__builtin_amdgcn_exp2f(u[0]), __builtin_amdgcn_exp2f(u[1])};
  e = e + (f32x2){1.f, 1.f};
  const f32x2 r = {__builtin_amdgcn_rcpf(e[0]), __builtin_amdgcn_rcpf(e[1])};
  const f32x2 o = v * r;
  return cvt_pk(o[0], o[1]);
}

__device__ __forceinline__ void g1_store_tile(const f32x4& a, int tile, int mrow, int g, const u16* bias4,
                                              u16* __restrict__ qkv, u16* __restrict__ hf) {
  if (tile < NQKV_ / 16) {
    const int col = tile * 16 + 4 * g;
#pragma unroll
    for (int j = 0; j < 2; ++j) {
      const int c = col + 2 * j;
      const int hh = c / 18, f = c - hh * 18;
      *reinterpret_cast<unsigned*>(qkv + ((size_t)hh * S_ + mrow) * 24 + f) = pack2(a[2 * j], a[2 * j + 1]);
    }
  } else {
    u32x2 bb = *reinterpret_cast<const u32x2*>(bias4);
    float o[4];
    o[0] = gelu_f(a[0] + lo16(bb[0]));
    o[1] = gelu_f(a[1] + hi16(bb[0]));
    o[2] = gelu_f(a[2] + lo16(bb[1]));
    o[3] = gelu_f(a[3] + hi16(bb[1]));
    u32x2 v = {pack2(o[0], o[1]), pack2(o[2], o[3])};
    const int T = tile - NQKV_ / 16;
    u16* dst = hf + ((size_t)((mrow >> 4) * 144 + (T >> 1)) * 64 + ((T & 1) * 2 + (g >> 1)) * 16 + (mrow & 15)) * 8 + (g & 1) * 4;
    *reinterpret_cast<u32x2*>(dst) = v;
  }
}

__device__ __forceinline__ void g1_addr(unsigned (&ad)[4][2], char* smem, int lane) {
  const int g = lane >> 4, q = (lane & 15) >> 2, p = lane & 3;
#pragma unroll
  for (int t = 0; t < 2; ++t) {
    const int row = 8 * g + 4 * t + q;
    const int f = ((row >> 1) & 1) | (((row >> 3) & 1) << 1);
#pragma unroll
    for (int n = 0; n < 4; ++n) ad[n][t] = lds_addr(smem) + row * 128 + ((n ^ f) * 32) + p * 8;
  }
}

struct G1W { const u16* src; int stride; };

__device__ __forceinline__ void g1_wsetup(G1W& w, int col, int wid, int lane, const u16* wqkv, const u16* w1) {
  const int row = wid * 8 + (lane >> 3);
  const int f = ((row >> 1) & 1) | (((row >> 3) & 1) << 1);
  const int ch = (lane & 7) ^ (f << 1);
  if (col < NQKV_) { w.src = wqkv + (size_t)row * NQKV_ + col + ch * 8; w.stride = 64 * NQKV_; }
  else { w.src = w1 + (size_t)row * F_ + (col - NQKV_) + ch * 8; w.stride = 64 * F_; }
}
__device__ __forceinline__ void g1_wissue(G1W& w, char* buf, int wid, int lane) {
#pragma unroll
  for (int i = 0; i < 3; ++i) {
    glds16(w.src, buf + i * 8192 + wid * 1024 + lane * 16);
    w.src += w.stride;
  }
}

template <int U>
__device__ __forceinline__ void g1_unit(G1W& wst, int unit0, const u16* wqkv,
                                        const u16* w1, char* smem, bf16x8 (&xa)[2][18], int m0,
                                        u16* __restrict__ qkv, u16* __restrict__ hf, WF (&wA)[4]) {
  int tz = threadIdx.x;
  asm volatile("" : "+v"(tz));
  const int wid = __builtin_amdgcn_readfirstlane(tz >> 6), lane = tz & 63;
  const int g = lane >> 4;
  unsigned ad[4][2];
  g1_addr(ad, smem, lane);
  f32x4 acc[2][4];
#pragma unroll
  for (int r = 0; r < 2; ++r)
#pragma unroll
    for (int n = 0; n < 4; ++n) acc[r][n] = (f32x4){0.f, 0.f, 0.f, 0.f};

  auto stage = [&](auto KC) {
    constexpr int kc = decltype(KC)::value;
    constexpr int s = U * 3 + kc;
    constexpr int CUR = (s % G1_RING) * G1_BUFB;
    if constexpr (U == 0) {
#pragma unroll
      for (int k = 0; k < 6; ++k) { pin(xa[0][kc * 6 + k]); pin(xa[1][kc * 6 + k]); }
    }
    WF wB[4];
    g1_reads<CUR + 1 * 4096>(wB, ad); wait_lgkm<8>(); g1_mma<kc * 6 + 0>(wA, xa, acc);
    g1_reads<CUR + 2 * 4096>(wA, ad); wait_lgkm<8>(); g1_mma<kc * 6 + 1>(wB, xa, acc);
    g1_reads<CUR + 3 * 4096>(wB, ad); wait_lgkm<8>(); g1_mma<kc * 6 + 2>(wA, xa, acc);
    g1_reads<CUR + 4 * 4096>(wA, ad); wait_lgkm<8>(); g1_mma<kc * 6 + 3>(wB, xa, acc);
    g1_reads<CUR + 5 * 4096>(wB, ad); wait_lgkm<8>(); g1_mma<kc * 6 + 4>(wA, xa, acc);
    if constexpr (s < 8) {
      constexpr int nl = (s == 0) ? 12 : ((s + 2 <= 8) ? 3 : 0);
      constexpr int nst = (U > 0 && kc <= 1) ? G1_NST : 0;
      wait_vm<nl + nst>();
      BAR();
      if constexpr (s + 3 <= 8) {
        constexpr int sn = s + 3;
        if constexpr (sn % 3 == 0) g1_wsetup(wst, (unit0 + sn / 3) * 64, wid, lane, wqkv, w1);
        g1_wissue(wst, smem + (sn % G1_RING) * G1_BUFB, wid, lane);
      }
      g1_reads<((s + 1) % G1_RING) * G1_BUFB>(wA, ad);
      wait_lgkm<8>();
    } else {
      wait_lgkm<0>();
    }
    g1_mma<kc * 6 + 5>(wB, xa, acc);
  };
  stage(IC<0>{}); stage(IC<1>{}); stage(IC<2>{});
  const u16* bias_lds = reinterpret_cast<const u16*>(smem + G1_BOFF);
  if ((unit0 + U) * 64 < NQKV_) {
#pragma unroll
    for (int n = 0; n < 4; ++n) {
      const int tile = (unit0 + U) * 4 + n;
#pragma unroll
      for (int r = 0; r < 2; ++r)
        g1_store_tile(acc[r][n], tile, m0 + wid * 32 + r * 16 + (lane & 15), g, bias_lds, qkv, hf);
    }
  } else {
    u32x2 bb[4];
#pragma unroll
    for (int n = 0; n < 4; ++n) bb[n] = *reinterpret_cast<const u32x2*>(bias_lds + (U * 4 + n) * 16 + 4 * g);
    const int T0 = (unit0 + U) * 4 - NQKV_ / 16;
#pragma unroll
    for (int r = 0; r < 2; ++r) {
      const int mrow = m0 + wid * 32 + r * 16 + (lane & 15);
      u16* dst = hf + ((size_t)((mrow >> 4) * 144 + (T0 >> 1)) * 64 + (g >> 1) * 16 + (mrow & 15)) * 8 + (g & 1) * 4;
#pragma unroll
      for (int n = 0; n < 4; ++n) {
        const f32x2 v0 = (f32x2){acc[r][n][0], acc[r][n][1]} + (f32x2){lo16(bb[n][0]), hi16(bb[n][0])};
        const f32x2 v1 = (f32x2){acc[r][n][2], acc[r][n][3]} + (f32x2){lo16(bb[n][1]), hi16(bb[n][1])};
        u32x2 v = {gelu2_pk(v0), gelu2_pk(v1)};
        ST_NT(reinterpret_cast<u32x2*>(dst + (n >> 1) * 512 + (n & 1) * 256), v);
      }
    }
  }
}

__global__ __launch_bounds__(512, 2) void k1_gemm1(const u16* __restrict__ xnf,
                                                   const u16* __restrict__ wqkv, const u16* __restrict__ w1,
                                                   const u16* __restrict__ b1, u16* __restrict__ qkv,
                                                   u16* __restrict__ hf) {
  __shared__ __attribute__((aligned(16))) char smem[G1_SMEM];
  const int tid = threadIdx.x, wid = tid >> 6, lane = tid & 63;
  const int bid = blockIdx.x;
  const int mt = bid >> 5, grp = bid & 31;
  const int m0 = mt * 256;
  const int unit0 = grp * 3;
  const bool extra = grp < 12;
  const int xcol = 6144 + (extra ? grp : 0) * 16;
  STAMP(1, 0);
  G1W wst;
  g1_wsetup(wst, unit0 * 64, wid, lane, wqkv, w1);
  g1_wissue(wst, smem + 0 * G1_BUFB, wid, lane);
  g1_wissue(wst, smem + 1 * G1_BUFB, wid, lane);
  g1_wissue(wst, smem + 2 * G1_BUFB, wid, lane);
#pragma unroll
  for (int i = 0; i < 3; ++i) {
    int ins = wid + 8 * i;
    if (ins >= 18) ins -= 18;
    const int c = ins * 64 + lane;
    const int pr = c >> 1, half = c & 1;
    const int k = (pr & ~0xC) | ((pr & 4) << 1) | ((pr & 8) >> 1);
    glds16(w1 + (size_t)k * F_ + (xcol - NQKV_) + half * 8, smem + G1_XOFF + c * 16);
  }
  {
    const int e = (wid & 1) * 64 + lane;
    int col = (e < 96) ? (unit0 * 64 + 2 * e - NQKV_) : (xcol - NQKV_ + 2 * (e - 96));
    if (col < 0 || e >= 104) col = 0;
    __builtin_amdgcn_global_load_lds((const unsigned*)(b1 + col), (unsigned*)(smem + G1_BOFF + e * 4), 4, 0, 0);
  }
  bf16x8 xa[2][18];
#pragma unroll
  for (int ks = 0; ks < 18; ++ks)
#pragma unroll
    for (int r = 0; r < 2; ++r)
      gld16_asm(xa[r][ks], xnf + ((size_t)(((m0 >> 4) + wid * 2 + r) * 18 + ks) * 64 + lane) * 8);
  wait_vm<24>();
  BAR();
  WF wA[4];
  {
    unsigned ad[4][2];
    g1_addr(ad, smem, lane);
    g1_reads<0>(wA, ad);
  }
  STAMP(1, 1);
  g1_unit<0>(wst, unit0, wqkv, w1, smem, xa, m0, qkv, hf, wA);
  STAMP(1, 2);
  g1_unit<1>(wst, unit0, wqkv, w1, smem, xa, m0, qkv, hf, wA);
  STAMP(1, 3);
  g1_unit<2>(wst, unit0, wqkv, w1, smem, xa, m0, qkv, hf, wA);
  STAMP(1, 4);
  if (extra) {
    int tz = threadIdx.x;
    asm volatile("" : "+v"(tz));
    const int wid2 = __builtin_amdgcn_readfirstlane(tz >> 6), lane2 = tz & 63;
    const int g = lane2 >> 4, q = (lane2 & 15) >> 2, p = lane2 & 3;
    unsigned ad[2];
#pragma unroll
    for (int t = 0; t < 2; ++t) {
      const int k = 8 * g + 4 * t + q;
      const int pr = (k & ~0xC) | ((k & 4) << 1) | ((k & 8) >> 1);
      ad[t] = lds_addr(smem) + G1_XOFF + pr * 32 + p * 8;
    }
    f32x4 acc[2] = {(f32x4){0.f, 0.f, 0.f, 0.f}, (f32x4){0.f, 0.f, 0.f, 0.f}};
#pragma unroll
    for (int kb = 0; kb < 18; kb += 6) {
      WF w[6];
      tr_rd<0 * 1024>(w[0].lo, ad[0] + kb * 1024); tr_rd<0 * 1024>(w[0].hi, ad[1] + kb * 1024);
      tr_rd<1 * 1024>(w[1].lo, ad[0] + kb * 1024); tr_rd<1 * 1024>(w[1].hi, ad[1] + kb * 1024);
      tr_rd<2 * 1024>(w[2].lo, ad[0] + kb * 1024); tr_rd<2 * 1024>(w[2].hi, ad[1] + kb * 1024);
      tr_rd<3 * 1024>(w[3].lo, ad[0] + kb * 1024); tr_rd<3 * 1024>(w[3].hi, ad[1] + kb * 1024);
      tr_rd<4 * 1024>(w[4].lo, ad[0] + kb * 1024); tr_rd<4 * 1024>(w[4].hi, ad[1] + kb * 1024);
      tr_rd<5 * 1024>(w[5].lo, ad[0] + kb * 1024); tr_rd<5 * 1024>(w[5].hi, ad[1] + kb * 1024);
      wait_lgkm<0>();
#pragma unroll
      for (int i = 0; i < 6; ++i) pin(w[i]);
#pragma unroll
      for (int i = 0; i < 6; ++i)
#pragma unroll
        for (int r = 0; r < 2; ++r)
          acc[r] = __builtin_amdgcn_mfma_f32_16x16x32_bf16(cat(w[i]), xa[r][kb + i], acc[r], 0, 0, 0);
    }
    const u16* bias_lds = reinterpret_cast<const u16*>(smem + G1_BOFF);
#pragma unroll
    for (int r = 0; r < 2; ++r)
      g1_store_tile(acc[r], xcol / 16, m0 + wid2 * 32 + r * 16 + (lane2 & 15), g, bias_lds + 192 + 4 * g, qkv, hf);
  }
}

constexpr int G2_NT = 9, G2_RB = G2_NT * 32;
constexpr int G2_BUFB = 64 * G2_RB;
constexpr int G2_RING = 4;
constexpr int G2_STAGES = 18;

__device__ __forceinline__ void softmax_pm(const float (&pk)[18], float (&ep)[18], float (&en)[18]) {
  float mx = pk[0], mn = pk[0];
#pragma unroll
  for (int f = 1; f < 18; ++f) {
    mx = fmaxf(mx, pk[f]);
    mn = fminf(mn, pk[f]);
  }
  float sp = 0.f, sn = 0.f;
#pragma unroll
  for (int f = 0; f < 18; ++f) {
    ep[f] = __expf(pk[f] - mx);
    en[f] = __expf(mn - pk[f]);
    sp += ep[f];
    sn += en[f];
  }
  const float rp = 1.f / sp, rn = 1.f / sn;
#pragma unroll
  for (int f = 0; f < 18; ++f) {
    ep[f] *= rp;
    en[f] *= rn;
  }
}

__device__ __forceinline__ void phi_proj(const u32x4 (&xr)[3], const float* wph, float (&pk)[18]) {
  float xv[18];
#pragma unroll
  for (int d = 0; d < 18; ++d) {
    const unsigned u = xr[(d >> 1) >> 2][(d >> 1) & 3];
    xv[d] = (d & 1) ? hi16(u) : lo16(u);
  }
#pragma unroll 1
  for (int f = 0; f < 18; ++f) {
    float a = 0.f;
#pragma unroll
    for (int d4 = 0; d4 < 5; ++d4) {
      const f32x4 pw = *reinterpret_cast<const f32x4*>(wph + f * 20 + d4 * 4);
#pragma unroll
      for (int j = 0; j < 4; ++j)
        if (d4 * 4 + j < 18) a += xv[d4 * 4 + j] * pw[j];
    }
    float* dst = &pk[0];
#pragma unroll
    for (int t = 0; t < 18; ++t) if (t == f) dst[t] = rbf(a);
  }
}

__device__ void head_block(int hb, const u16* __restrict__ qkv, const u16* __restrict__ wphi, u16* __restrict__ of, float* pkv, unsigned* cnt, char* smem) {
  u16* phiT = reinterpret_cast<u16*>(smem);
  u16* vT = reinterpret_cast<u16*>(smem + 25344);
  float* kvs = reinterpret_cast<float*>(smem + 25344 + 16896);
  const int h = hb >> 3, c8 = hb & 7;
  const int tid = threadIdx.x, wid = tid >> 6, lane = tid & 63;
  const int row = c8 * 256 + tid;
  u32x4 kr[3], vr[3], qr[3];
  {
    const u32x4* kp = reinterpret_cast<const u32x4*>(qkv + ((size_t)(32 + h) * S_ + row) * 24);
    const u32x4* vp = reinterpret_cast<const u32x4*>(qkv + ((size_t)(64 + h) * S_ + row) * 24);
    const u32x4* qp = reinterpret_cast<const u32x4*>(qkv + ((size_t)h * S_ + row) * 24);
#pragma unroll
    for (int d = 0; d < 3; ++d) { kr[d] = kp[d]; vr[d] = vp[d]; qr[d] = qp[d]; }
  }
  float* wph = reinterpret_cast<float*>(smem + 66816);
  for (int i = tid; i < 18 * 20; i += 256) {
    const int f = i / 20, d = i - f * 20;
    wph[i] = (d < 18) ? bf2f(wphi[h * 324 + d * 18 + f]) : 0.f;
  }
  for (int i = tid; i < 12 * 264; i += 256) phiT[36 * 264 + i] = 0;
  for (int i = tid; i < 14 * 264; i += 256) vT[18 * 264 + i] = (i < 264) ? (u16)0x3f80 : (u16)0;
  __syncthreads();
  {
    float pk[18];
    phi_proj(kr, wph, pk);
    float ep[18], en[18];
    softmax_pm(pk, ep, en);
#pragma unroll
    for (int f = 0; f < 18; ++f) {
      phiT[f * 264 + tid] = f2bf(ep[f]);
      phiT[(18 + f) * 264 + tid] = f2bf(en[f]);
      const unsigned w = vr[(f >> 1) >> 2][(f >> 1) & 3];
      vT[f * 264 + tid] = (u16)((f & 1) ? (w >> 16) : (w & 0xffff));
    }
  }
  __syncthreads();
  {
    f32x4 acc[3][2];
#pragma unroll
    for (int a = 0; a < 3; ++a)
#pragma unroll
      for (int b = 0; b < 2; ++b) acc[a][b] = (f32x4){0.f, 0.f, 0.f, 0.f};
#pragma unroll
    for (int ks = 0; ks < 2; ++ks) {
      const int s0 = wid * 64 + ks * 32 + 8 * (lane >> 4);
      bf16x8 bv[2];
#pragma unroll
      for (int b = 0; b < 2; ++b) bv[b] = *reinterpret_cast<const bf16x8*>(vT + (b * 16 + (lane & 15)) * 264 + s0);
#pragma unroll
      for (int a = 0; a < 3; ++a) {
        bf16x8 av = *reinterpret_cast<const bf16x8*>(phiT + (a * 16 + (lane & 15)) * 264 + s0);
#pragma unroll
        for (int b = 0; b < 2; ++b) acc[a][b] = __builtin_amdgcn_mfma_f32_16x16x32_bf16(av, bv[b], acc[a][b], 0, 0, 0);
      }
    }
#pragma unroll
    for (int a = 0; a < 3; ++a)
#pragma unroll
      for (int b = 0; b < 2; ++b)
#pragma unroll
        for (int j = 0; j < 4; ++j)
          kvs[(wid * 48 + a * 16 + (lane >> 4) * 4 + j) * 32 + b * 16 + (lane & 15)] = acc[a][b][j];
  }
  __syncthreads();
  float* mypart = pkv + (size_t)(h * 8 + c8) * 1536;
#pragma unroll
  for (int it = 0; it < 6; ++it) {
    const int i = it * 256 + tid;
    const float v = kvs[i] + kvs[1536 + i] + kvs[2 * 1536 + i] + kvs[3 * 1536 + i];
    __hip_atomic_store(mypart + i, v, __ATOMIC_RELAXED, __HIP_MEMORY_SCOPE_AGENT);
  }
  asm volatile("s_waitcnt vmcnt(0)" ::: "memory");
  __syncthreads();
  if (tid == 0) __hip_atomic_fetch_add(cnt + h * 32, 1u, __ATOMIC_RELAXED, __HIP_MEMORY_SCOPE_AGENT);
  float ep[18], en[18];
  {
    float pk[18];
    phi_proj(qr, wph, pk);
    softmax_pm(pk, ep, en);
#pragma unroll
    for (int f = 0; f < 18; ++f) { ep[f] = rbf(ep[f]); en[f] = rbf(en[f]); }
  }
  if (tid == 0) {
    unsigned spins = 0;
    while (__hip_atomic_load(cnt + h * 32, __ATOMIC_RELAXED, __HIP_MEMORY_SCOPE_AGENT) < 8u) {
      __builtin_amdgcn_s_sleep(1);
      if (++spins > (1u << 24)) break;
    }
  }
  __syncthreads();
  float* kvf = reinterpret_cast<float*>(smem);
  {
    float* hp = pkv + (size_t)h * 8 * 1536;
    float pv[3][8];
#pragma unroll
    for (int it = 0; it < 3; ++it) {
      const int i = it * 256 + tid;
      const int f = i / 20, d = i - f * 20;
      const int src = (i < 720) ? (f * 32 + d) : 0;
#pragma unroll
      for (int c = 0; c < 8; ++c) pv[it][c] = __hip_atomic_load(hp + c * 1536 + src, __ATOMIC_RELAXED, __HIP_MEMORY_SCOPE_AGENT);
    }
#pragma unroll
    for (int it = 0; it < 3; ++it) {
      const int i = it * 256 + tid;
      float v = 0.f;
#pragma unroll
      for (int c = 0; c < 8; ++c) v += pv[it][c];
      if (i < 720) kvf[i] = rbf(v);
    }
  }
  __syncthreads();
  {
    float num[20];
#pragma unroll
    for (int d = 0; d < 20; ++d) num[d] = 0.f;
#pragma unroll
    for (int f = 0; f < 36; ++f) {
      const float ph = (f < 18) ? ep[f] : en[f - 18];
#pragma unroll
      for (int d4 = 0; d4 < 5; ++d4) {
        const f32x4 w = *reinterpret_cast<const f32x4*>(kvf + f * 20 + d4 * 4);
#pragma unroll
        for (int j = 0; j < 4; ++j) num[d4 * 4 + j] += ph * w[j];
      }
    }
    const float rd = 1.f / (rbf(num[18]) + 1e-6f);
#pragma unroll
    for (int d2 = 0; d2 < 9; ++d2) {
      const int k = h * 18 + 2 * d2;
      u16* dst = of + ((size_t)((row >> 4) * 18 + (k >> 5)) * 64 + ((k >> 3) & 3) * 16 + (row & 15)) * 8 + (k & 7);
      *reinterpret_cast<unsigned*>(dst) = pack2(rbf(num[2 * d2]) * rd, rbf(num[2 * d2 + 1]) * rd);
    }
  }
}

template <int BASE, int N0, int CNT, int I = 0>
__device__ __forceinline__ void g2_wreads(WF (&w)[5], unsigned a0, unsigned a1) {
  if constexpr (I < CNT) {
    tr_rd<BASE + (N0 + I) * 32>(w[I].lo, a0);
    tr_rd<BASE + (N0 + I) * 32>(w[I].hi, a1);
    g2_wreads<BASE, N0, CNT, I + 1>(w, a0, a1);
  }
}
template <int N0, int CNT>
__device__ __forceinline__ void g2_mma(WF (&w)[5], const bf16x8 (&ac)[2], f32x4 (&acc)[2][G2_NT]) {
#pragma unroll
  for (int i = 0; i < CNT; ++i) pin(w[i]);
#pragma unroll
  for (int i = 0; i < CNT; ++i)
#pragma unroll
    for (int r = 0; r < 2; ++r)
      acc[r][N0 + i] = __builtin_amdgcn_mfma_f32_16x16x32_bf16(cat(w[i]), ac[r], acc[r][N0 + i], 0, 0, 0);
}
constexpr int K2_HEADBLK = 256;

__global__ __launch_bounds__(256, 2) void k2_gemm2(const u16* __restrict__ hf, const u16* __restrict__ w2,
                                                   u16* __restrict__ part, const u16* __restrict__ qkv,
                                                   const u16* __restrict__ wphi,
                                                   u16* __restrict__ of, float* pkv, unsigned* cnt) {
  __shared__ __attribute__((aligned(16))) char smem[G2_RING * G2_BUFB];
  const int tid = threadIdx.x, wid = tid >> 6, lane = tid & 63;
  int bid = blockIdx.x;
  if (bid < K2_HEADBLK) { head_block(bid, qkv, wphi, of, pkv, cnt, smem); return; }
  bid -= K2_HEADBLK;
  STAMP(2, 0);
  const int xcd = bid & 7, loc = bid >> 3;
  const int sp = xcd >> 1, mt = (xcd & 1) * 8 + (loc >> 2), nt4 = loc & 3;
  const int m0 = mt * 128, n0 = nt4 * 144, k0 = sp * 1152;
  const int g = lane >> 4, q = (lane & 15) >> 2, p = lane & 3;
  const bool odd = g & 1;
  const unsigned wa0 = lds_addr(smem) + (8 * g + 4 * (g & 1) + q) * G2_RB + p * 8;
  const unsigned wa1 = lds_addr(smem) + (8 * g + 4 * (1 - (g & 1)) + q) * G2_RB + p * 8;
  f32x4 acc[2][G2_NT];
#pragma unroll
  for (int r = 0; r < 2; ++r)
#pragma unroll
    for (int n = 0; n < G2_NT; ++n) acc[r][n] = (f32x4){0.f, 0.f, 0.f, 0.f};
  const u16* wp[5];
  int wdst[5];
#pragma unroll
  for (int i = 0; i < 5; ++i) {
    int ins = wid + 4 * i;
    if (ins >= 18) ins -= 18;
    int c = ins * 64 + lane;
    int row = c / 18, cc = c % 18;
    wp[i] = w2 + (size_t)(k0 + row) * D_ + n0 + cc * 8;
    wdst[i] = c * 16;
  }
  const u16* ap[2];
#pragma unroll
  for (int r = 0; r < 2; ++r) ap[r] = hf + ((size_t)((m0 >> 4) + wid * 2 + r) * 144 + (k0 >> 5)) * 512 + lane * 8;
  bf16x8 an[4][2][2];
  auto issue = [&](auto SL) {
    constexpr int slot = decltype(SL)::value;
    char* buf = smem + slot * G2_BUFB;
#pragma unroll
    for (int i = 0; i < 5; ++i) { glds16(wp[i], buf + wdst[i]); wp[i] += 64 * D_; }
#pragma unroll
    for (int r = 0; r < 2; ++r) {
      gld16_asm(an[slot][r][0], ap[r]);
      gld16_asm(an[slot][r][1], ap[r] + 512);
      ap[r] += 1024;
    }
  };
  issue(IC<0>{});
  issue(IC<1>{});
  issue(IC<2>{});
  wait_vm<18>();
  BAR();
  STAMP(2, 1);
  WF wA[5], wB[5];
  g2_wreads<0, 0, 5>(wA, wa0, wa1);
  auto stage = [&](auto U, auto LAST) {
    constexpr int u = decltype(U)::value;
    constexpr int rem = decltype(LAST)::value;
    constexpr int BASE = u * G2_BUFB;
    bf16x8 c0[2], c1[2];
#pragma unroll
    for (int r = 0; r < 2; ++r) {
      pin(an[u][r][0]); pin(an[u][r][1]);
      c0[r] = swap_halves_if(an[u][r][0], odd);
      c1[r] = swap_halves_if(an[u][r][1], odd);
    }
    g2_wreads<BASE, 5, 4>(wB, wa0, wa1);
    wait_lgkm<8>();
    g2_mma<0, 5>(wA, c0, acc);
    g2_wreads<BASE + 32 * G2_RB, 0, 5>(wA, wa0, wa1);
    wait_lgkm<10>();
    g2_mma<5, 4>(wB, c0, acc);
    g2_wreads<BASE + 32 * G2_RB, 5, 4>(wB, wa0, wa1);
    wait_lgkm<8>();
    g2_mma<0, 5>(wA, c1, acc);
    if constexpr (rem >= 1) {
      if constexpr (rem >= 2) wait_vm<9>(); else wait_vm<0>();
      BAR();
      if constexpr (rem >= 3) issue(IC<(u + 3) & 3>{});
      g2_wreads<((u + 1) & 3) * G2_BUFB, 0, 5>(wA, wa0, wa1);
      wait_lgkm<10>();
    } else {
      wait_lgkm<0>();
    }
    g2_mma<5, 4>(wB, c1, acc);
  };
#pragma unroll 1
  for (int it = 0; it < 3; ++it) {
    stage(IC<0>{}, IC<3>{}); stage(IC<1>{}, IC<3>{}); stage(IC<2>{}, IC<3>{}); stage(IC<3>{}, IC<3>{});
  }
  STAMP(2, 2);
  stage(IC<0>{}, IC<3>{}); stage(IC<1>{}, IC<3>{}); stage(IC<2>{}, IC<3>{});
  stage(IC<3>{}, IC<2>{}); stage(IC<0>{}, IC<1>{}); stage(IC<1>{}, IC<0>{});
  STAMP(2, 3);
#pragma unroll
  for (int r = 0; r < 2; ++r) {
    const int m = m0 + wid * 32 + r * 16 + (lane & 15);
    u16* pp = part + ((size_t)sp * S_ + m) * D_ + n0 + 4 * g;
#pragma unroll
    for (int n = 0; n < G2_NT; ++n) {
      u32x2 v = {pack2(acc[r][n][0], acc[r][n][1]), pack2(acc[r][n][2], acc[r][n][3])};
      ST_NT(reinterpret_cast<u32x2*>(pp + n * 16), v);
    }
  }
  asm volatile("s_waitcnt vmcnt(0)" ::: "memory");
  STAMP(2, 4);
}

__global__ __launch_bounds__(512, 2) void k3_final(const u16* __restrict__ of, const u16* __restrict__ wo,
                                                   const u16* __restrict__ part, const u16* __restrict__ b2,
                                                   const u16* __restrict__ x, u16* __restrict__ out) {
  __shared__ __attribute__((aligned(16))) char smem[576 * 128];
  const int tid = threadIdx.x, wid = tid >> 6, lane = tid & 63;
  const int bid = blockIdx.x;
  const int xcd = bid & 7, loc = bid >> 3;
  const int mt = xcd * 2 + loc / 9, nt = loc % 9;
  const int m0 = mt * 128, n0 = nt * 64;
  const int g = lane >> 4, q = (lane & 15) >> 2, p = lane & 3;
  const int wr = wid >> 1, wc = wid & 1;
  STAMP(3, 0);
#pragma unroll
  for (int i = 0; i < 9; ++i) {
    const int row = (wid * 9 + i) * 8 + (lane >> 3);
    const int f = ((row >> 1) & 1) | (((row >> 3) & 1) << 1);
    const int ch = (lane & 7) ^ (f << 1);
    glds16(wo + (size_t)row * D_ + n0 + ch * 8, smem + (wid * 9 + i) * 1024 + lane * 16);
  }
  bf16x8 an[2][18];
#pragma unroll
  for (int ks = 0; ks < 18; ++ks)
#pragma unroll
    for (int r = 0; r < 2; ++r)
      gld16_asm(an[r][ks], of + ((size_t)(((m0 >> 4) + wr * 2 + r) * 18 + ks) * 64 + lane) * 8);
  u32x2 py[2][2][4];
  u32x2 xx[2][2], bb[2];
#pragma unroll
  for (int r = 0; r < 2; ++r) {
    const int m = m0 + wr * 32 + r * 16 + (lane & 15);
#pragma unroll
    for (int n = 0; n < 2; ++n) {
      const int ncol = n0 + wc * 32 + n * 16 + 4 * g;
#pragma unroll
      for (int sI = 0; sI < SPLITK_; ++sI)
        asm volatile("global_load_dwordx2 %0, %1, off" : "=v"(py[r][n][sI]) : "v"(part + ((size_t)sI * S_ + m) * D_ + ncol) : "memory");
      asm volatile("global_load_dwordx2 %0, %1, off" : "=v"(xx[r][n]) : "v"(x + (size_t)m * D_ + ncol) : "memory");
    }
  }
#pragma unroll
  for (int n = 0; n < 2; ++n)
    asm volatile("global_load_dwordx2 %0, %1, off" : "=v"(bb[n]) : "v"(b2 + n0 + wc * 32 + n * 16 + 4 * g) : "memory");
  unsigned ad[2][2];
#pragma unroll
  for (int t = 0; t < 2; ++t) {
    const int row = 8 * g + 4 * t + q;
    const int f = ((row >> 1) & 1) | (((row >> 3) & 1) << 1);
#pragma unroll
    for (int n = 0; n < 2; ++n) ad[n][t] = lds_addr(smem) + row * 128 + (((wc * 2 + n) ^ f) * 32) + p * 8;
  }
  f32x4 acc[2][2];
#pragma unroll
  for (int r = 0; r < 2; ++r)
#pragma unroll
    for (int n = 0; n < 2; ++n) acc[r][n] = (f32x4){0.f, 0.f, 0.f, 0.f};
  STAMP(3, 1);
  wait_vm<22>();
  BAR();
  STAMP(3, 2);
#pragma unroll
  for (int ks = 0; ks < 18; ++ks) { pin(an[0][ks]); pin(an[1][ks]); }
  auto grp = [&](auto KB) {
    constexpr int kb = decltype(KB)::value;
    WF w[6][2];
#pragma unroll
    for (int k = 0; k < 6; ++k)
#pragma unroll
      for (int n = 0; n < 2; ++n) {
        if (k == 0) { tr_rd<0 * 4096>(w[0][n].lo, ad[n][0] + kb * 4096); tr_rd<0 * 4096>(w[0][n].hi, ad[n][1] + kb * 4096); }
        if (k == 1) { tr_rd<1 * 4096>(w[1][n].lo, ad[n][0] + kb * 4096); tr_rd<1 * 4096>(w[1][n].hi, ad[n][1] + kb * 4096); }
        if (k == 2) { tr_rd<2 * 4096>(w[2][n].lo, ad[n][0] + kb * 4096); tr_rd<2 * 4096>(w[2][n].hi, ad[n][1] + kb * 4096); }
        if (k == 3) { tr_rd<3 * 4096>(w[3][n].lo, ad[n][0] + kb * 4096); tr_rd<3 * 4096>(w[3][n].hi, ad[n][1] + kb * 4096); }
        if (k == 4) { tr_rd<4 * 4096>(w[4][n].lo, ad[n][0] + kb * 4096); tr_rd<4 * 4096>(w[4][n].hi, ad[n][1] + kb * 4096); }
        if (k == 5) { tr_rd<5 * 4096>(w[5][n].lo, ad[n][0] + kb * 4096); tr_rd<5 * 4096>(w[5][n].hi, ad[n][1] + kb * 4096); }
      }
    wait_lgkm<12>();
#pragma unroll
    for (int k = 0; k < 3; ++k)
#pragma unroll
      for (int n = 0; n < 2; ++n) pin(w[k][n]);
#pragma unroll
    for (int k = 0; k < 3; ++k)
#pragma unroll
      for (int n = 0; n < 2; ++n)
#pragma unroll
        for (int r = 0; r < 2; ++r)
          acc[r][n] = __builtin_amdgcn_mfma_f32_16x16x32_bf16(cat(w[k][n]), an[r][kb + k], acc[r][n], 0, 0, 0);
    wait_lgkm<0>();
#pragma unroll
    for (int k = 3; k < 6; ++k)
#pragma unroll
      for (int n = 0; n < 2; ++n) pin(w[k][n]);
#pragma unroll
    for (int k = 3; k < 6; ++k)
#pragma unroll
      for (int n = 0; n < 2; ++n)
#pragma unroll
        for (int r = 0; r < 2; ++r)
          acc[r][n] = __builtin_amdgcn_mfma_f32_16x16x32_bf16(cat(w[k][n]), an[r][kb + k], acc[r][n], 0, 0, 0);
  };
  grp(IC<0>{}); grp(IC<6>{}); grp(IC<12>{});
  wait_vm<0>();
  STAMP(3, 3);
#pragma unroll
  for (int n = 0; n < 2; ++n) asm volatile("" : "+v"(bb[n]));
#pragma unroll
  for (int r = 0; r < 2; ++r) {
    const int m = m0 + wr * 32 + r * 16 + (lane & 15);
#pragma unroll
    for (int n = 0; n < 2; ++n) {
      const int ncol = n0 + wc * 32 + n * 16 + 4 * g;
      asm volatile("" : "+v"(xx[r][n]));
#pragma unroll
      for (int sI = 0; sI < SPLITK_; ++sI) asm volatile("" : "+v"(py[r][n][sI]));
      f32x4 y = {0.f, 0.f, 0.f, 0.f};
#pragma unroll
      for (int sI = 0; sI < SPLITK_; ++sI) {
        y[0] += lo16(py[r][n][sI][0]); y[1] += hi16(py[r][n][sI][0]);
        y[2] += lo16(py[r][n][sI][1]); y[3] += hi16(py[r][n][sI][1]);
      }
      float bv[4] = {lo16(bb[n][0]), hi16(bb[n][0]), lo16(bb[n][1]), hi16(bb[n][1])};
      float xv[4] = {lo16(xx[r][n][0]), hi16(xx[r][n][0]), lo16(xx[r][n][1]), hi16(xx[r][n][1])};
      float o[4];
#pragma unroll
      for (int j = 0; j < 4; ++j) {
        float yy = y[j] + bv[j];
        float gg = yy / (1.f + __expf(-yy));
        o[j] = xv[j] + acc[r][n][j] * gg;
      }
      u32x2 v = {pack2(o[0], o[1]), pack2(o[2], o[3])};
      *reinterpret_cast<u32x2*>(out + (size_t)m * D_ + ncol) = v;
    }
  }
  STAMP(3, 4);
}

extern "C" void kernel_launch(void* const* d_in, const int* in_sizes, int n_in, void* d_out, int out_size, void* d_ws,
                              size_t ws_size, hipStream_t stream) {
  const u16* x = (const u16*)d_in[0];
  const u16* wqkv = (const u16*)d_in[1];
  const u16* wphi = (const u16*)d_in[2];
  const u16* wo = (const u16*)d_in[3];
  const u16* w1 = (const u16*)d_in[5];
  const u16* b1 = (const u16*)d_in[6];
  const u16* w2 = (const u16*)d_in[7];
  const u16* b2 = (const u16*)d_in[8];
  u16* out = (u16*)d_out;
  char* ws = (char*)d_ws;
  size_t off = 0;
  u16* xnf = (u16*)(ws + off); off += (size_t)S_ * D_ * 2;
  u16* qkv = (u16*)(ws + off); off += (size_t)96 * S_ * 24 * 2;
  u16* hf = (u16*)(ws + off); off += (size_t)S_ * F_ * 2;
  u16* part = (u16*)(ws + off); off += (size_t)SPLITK_ * S_ * D_ * 2;
  u16* of = (u16*)(ws + off); off += (size_t)S_ * D_ * 2;
  float* pkv = (float*)(ws + off); off += (size_t)256 * 1536 * 4;
  unsigned* cnt = (unsigned*)(ws + off); off += 64 * 32 * 4;

  k0_prep<<<512, 256, 0, stream>>>(x, xnf, cnt);
  k1_gemm1<<<256, 512, 0, stream>>>(xnf, wqkv, w1, b1, qkv, hf);
  k2_gemm2<<<K2_HEADBLK + 256, 256, 0, stream>>>(hf, w2, part, qkv, wphi, of, pkv, cnt);
  k3_final<<<16 * 9, 512, 0, stream>>>(of, wo, part, b2, x, out);
}
```

```cpp
#include <hip/hip_runtime.h>
#include <hip/hip_bf16.h>
#include <stdint.h>

constexpr int S_ = 2048, D_ = 576, H_ = 32, DH_ = 18, F_ = 4608, NQKV_ = 1728;
constexpr int NPH_ = 1152;
constexpr int KPH_ = 40;
constexpr int KPHI_ = H_ * KPH_;
constexpr int SPLITK_ = 4;

typedef short bf16x8 __attribute__((ext_vector_type(8)));
typedef short s16x4 __attribute__((ext_vector_type(4)));
typedef float f32x4 __attribute__((ext_vector_type(4)));
typedef unsigned u32x4 __attribute__((ext_vector_type(4)));
typedef unsigned u32x2 __attribute__((ext_vector_type(2)));
typedef __attribute__((address_space(3))) s16x4* lds_s16x4_p;
typedef unsigned short u16;
template <int V> struct IC { static constexpr int value = V; };

#define STAMP(kid, i) do { } while (0)
#define ST_NT(p, v) (*(p) = (v))
#define WAIT_LGKM0() asm volatile("s_waitcnt lgkmcnt(0)" ::: "memory")
#define BAR() __builtin_amdgcn_s_barrier()

template <int N>
__device__ __forceinline__ void wait_vm() {
  static_assert(N >= 0 && N < 64, "vmcnt range");
  asm volatile("s_waitcnt vmcnt(%0)" ::"n"(N) : "memory");
}

__device__ __forceinline__ float bf2f(u16 v) { return __uint_as_float(((unsigned)v) << 16); }
__device__ __forceinline__ u16 f2bf(float f) {
  __hip_bfloat16 b = __float2bfloat16(f);
  return *reinterpret_cast<u16*>(&b);
}
__device__ __forceinline__ unsigned pack2(float lo, float hi) {
  return (unsigned)f2bf(lo) | ((unsigned)f2bf(hi) << 16);
}
__device__ __forceinline__ float rbf(float f) { return bf2f(f2bf(f)); }
__device__ __forceinline__ float lo16(unsigned u) { return __uint_as_float(u << 16); }
__device__ __forceinline__ float hi16(unsigned u) { return __uint_as_float(u & 0xffff0000u); }

__device__ __forceinline__ void glds16(const void* g, void* l) {
  __builtin_amdgcn_global_load_lds((const unsigned*)g, (unsigned*)l, 16, 0, 0);
}

__device__ __forceinline__ void l2_touch(const void* g, void* ldummy) {
  __builtin_amdgcn_global_load_lds((const unsigned*)g, (unsigned*)ldummy, 4, 0, 0);
}

__device__ __forceinline__ bf16x8 swap_halves_if(bf16x8 v, bool odd) {
  bf16x8 s = __builtin_shufflevector(v, v, 4, 5, 6, 7, 0, 1, 2, 3);
  return odd ? s : v;
}

__device__ __forceinline__ bf16x8 wfrag(const char* img, int o0, int o1, int off) {
  s16x4 a = __builtin_amdgcn_ds_read_tr16_b64_v4i16((lds_s16x4_p)(img + o0 + off));
  s16x4 b = __builtin_amdgcn_ds_read_tr16_b64_v4i16((lds_s16x4_p)(img + o1 + off));
  return __builtin_shufflevector(a, b, 0, 1, 2, 3, 4, 5, 6, 7);
}

struct WF { s16x4 lo, hi; };
template <int OFF>
__device__ __forceinline__ void tr_rd(s16x4& r, unsigned addr) {
  asm volatile("ds_read_b64_tr_b16 %0, %1 offset:%2" : "=v"(r) : "v"(addr), "n"(OFF) : "memory");
}
__device__ __forceinline__ void gld16_asm(bf16x8& r, const void* p) {
  asm volatile("global_load_dwordx4 %0, %1, off" : "=v"(r) : "v"(p) : "memory");
}
template <int N>
__device__ __forceinline__ void wait_lgkm() {
  static_assert(N >= 0 && N <= 15, "lgkmcnt range");
  asm volatile("s_waitcnt lgkmcnt(%0)" ::"n"(N) : "memory");
}
__device__ __forceinline__ void pin(WF& w) { asm volatile("" : "+v"(w.lo), "+v"(w.hi)); }
__device__ __forceinline__ void pin(bf16x8& w) { asm volatile("" : "+v"(w)); }
__device__ __forceinline__ bf16x8 cat(const WF& w) { return __builtin_shufflevector(w.lo, w.hi, 0, 1, 2, 3, 4, 5, 6, 7); }
__device__ __forceinline__ unsigned lds_addr(const void* p) {
  return (unsigned)(uintptr_t)(__attribute__((address_space(3))) const char*)p;
}

__global__ __launch_bounds__(256) void k0_prep(const u16* __restrict__ x, const u16* __restrict__ wqkv,
                                               const u16* __restrict__ wphi, u16* __restrict__ xnf,
                                               u16* __restrict__ wfold, unsigned* __restrict__ cnt) {
  __shared__ __attribute__((aligned(16))) float wph[18 * 20];
  const int tid = threadIdx.x, wid = tid >> 6, lane = tid & 63;
  const int bid = blockIdx.x;
  if (bid == 512 && tid < 32) cnt[tid * 32] = 0u;
  if (bid < 512) {
    const int row = bid * 4 + wid;
    const u16* xp = x + (size_t)row * D_;
    u32x4 a = *reinterpret_cast<const u32x4*>(xp + lane * 8);
    u32x4 b = {0u, 0u, 0u, 0u};
    if (lane < 8) b = *reinterpret_cast<const u32x4*>(xp + 512 + lane * 8);
    float v[16];
#pragma unroll
    for (int j = 0; j < 4; ++j) {
      v[2 * j] = lo16(a[j]); v[2 * j + 1] = hi16(a[j]);
      v[8 + 2 * j] = lo16(b[j]); v[8 + 2 * j + 1] = hi16(b[j]);
    }
    float s = 0.f;
#pragma unroll
    for (int j = 0; j < 16; ++j) s += v[j];
#pragma unroll
    for (int o = 32; o >= 1; o >>= 1) s += __shfl_xor(s, o);
    const float mean = s * (1.f / D_);
    float ss = 0.f;
#pragma unroll
    for (int j = 0; j < 8; ++j) { float d = v[j] - mean; ss += d * d; }
    if (lane < 8) {
#pragma unroll
      for (int j = 8; j < 16; ++j) { float d = v[j] - mean; ss += d * d; }
    }
#pragma unroll
    for (int o = 32; o >= 1; o >>= 1) ss += __shfl_xor(ss, o);
    const float rstd = rsqrtf(ss * (1.f / D_) + 1e-5f);
    u32x4 oa, ob;
#pragma unroll
    for (int j = 0; j < 4; ++j) {
      oa[j] = pack2((v[2 * j] - mean) * rstd, (v[2 * j + 1] - mean) * rstd);
      ob[j] = pack2((v[8 + 2 * j] - mean) * rstd, (v[8 + 2 * j + 1] - mean) * rstd);
    }
    {
      const int c = lane;
      u16* dst = xnf + ((size_t)((row >> 4) * 18 + (c >> 2)) * 64 + (c & 3) * 16 + (row & 15)) * 8;
      *reinterpret_cast<u32x4*>(dst) = oa;
    }
    if (lane < 8) {
      const int c = 64 + lane;
      u16* dst = xnf + ((size_t)((row >> 4) * 18 + (c >> 2)) * 64 + (c & 3) * 16 + (row & 15)) * 8;
      *reinterpret_cast<u32x4*>(dst) = ob;
    }
    return;
  }
  {
    const int fb = bid - 512;
    const int hh = fb / 3, k = (fb % 3) * 192 + tid;
    const int h = hh & 31;
    for (int i = tid; i < 18 * 20; i += 256) {
      const int d = i / 20, f = i % 20;
      wph[i] = (f < 18) ? bf2f(wphi[h * 324 + d * 18 + f]) : 0.f;
    }
    unsigned wv[9];
    if (tid < 192) {
      const u16* wp = wqkv + (size_t)k * NQKV_ + hh * 18;
#pragma unroll
      for (int d = 0; d < 9; ++d) wv[d] = *reinterpret_cast<const unsigned*>(wp + 2 * d);
    }
    __syncthreads();
    if (tid < 192) {
      float acc[20];
#pragma unroll
      for (int f = 0; f < 20; ++f) acc[f] = 0.f;
#pragma unroll
      for (int d = 0; d < 18; ++d) {
        const float w = (d & 1) ? hi16(wv[d >> 1]) : lo16(wv[d >> 1]);
#pragma unroll
        for (int f4 = 0; f4 < 5; ++f4) {
          f32x4 pw = *reinterpret_cast<const f32x4*>(wph + d * 20 + f4 * 4);
#pragma unroll
          for (int j = 0; j < 4; ++j) acc[f4 * 4 + j] += w * pw[j];
        }
      }
      u16* op = wfold + (size_t)k * NPH_ + hh * 18;
#pragma unroll
      for (int f = 0; f < 9; ++f) *reinterpret_cast<unsigned*>(op + 2 * f) = pack2(acc[2 * f], acc[2 * f + 1]);
    }
  }
}

constexpr int G1_BUFB = 192 * 128;
constexpr int G1_RING = 5;
constexpr int G1_XOFF = G1_RING * G1_BUFB;
constexpr int G1_BOFF = G1_XOFF + 576 * 32;
constexpr int G1_SMEM = G1_BOFF + 1024;
constexpr int G1_NST = 8;

template <int BASE, int I = 0>
__device__ __forceinline__ void g1_reads(WF (&w)[4], const unsigned (&ad)[4][2]) {
  if constexpr (I < 4) {
    if constexpr (BASE >= 98304) {
      tr_rd<BASE - 98304>(w[I].lo, ad[I][0] + 98304u);
      tr_rd<BASE - 98304>(w[I].hi, ad[I][1] + 98304u);
    } else if constexpr (BASE >= 49152) {
      tr_rd<BASE - 49152>(w[I].lo, ad[I][0] + 49152u);
      tr_rd<BASE - 49152>(w[I].hi, ad[I][1] + 49152u);
    } else {
      tr_rd<BASE>(w[I].lo, ad[I][0]);
      tr_rd<BASE>(w[I].hi, ad[I][1]);
    }
    g1_reads<BASE, I + 1>(w, ad);
  }
}
template <int KS>
__device__ __forceinline__ void g1_mma(WF (&w)[4], const bf16x8 (&xa)[2][18], f32x4 (&acc)[2][4]) {
#pragma unroll
  for (int i = 0; i < 4; ++i) pin(w[i]);
#pragma unroll
  for (int i = 0; i < 4; ++i)
#pragma unroll
    for (int r = 0; r < 2; ++r)
      acc[r][i] = __builtin_amdgcn_mfma_f32_16x16x32_bf16(cat(w[i]), xa[r][KS], acc[r][i], 0, 0, 0);
}

__device__ __forceinline__ float gelu_f(float v) {
  constexpr float c1 = -2.f * 0.7978845608028654f * 1.4426950408889634f;
  constexpr float c3 = c1 * 0.044715f;
  const float t = v * v;
  const float e = __builtin_amdgcn_exp2f(v * fmaf(t, c3, c1));
  return v * __builtin_amdgcn_rcpf(1.f + e);
}

typedef float f32x2 __attribute__((ext_vector_type(2)));
__device__ __forceinline__ unsigned cvt_pk(float lo, float hi) {
  unsigned r;
  asm("v_cvt_pk_bf16_f32 %0, %1, %2" : "=v"(r) : "v"(lo), "v"(hi));
  return r;
}
__device__ __forceinline__ unsigned gelu2_pk(f32x2 v) {
  constexpr float c1 = -2.f * 0.7978845608028654f * 1.4426950408889634f;
  constexpr float c3 = c1 * 0.044715f;
  const f32x2 t = v * v;
  const f32x2 pp = t * (f32x2){c3, c3} + (f32x2){c1, c1};
  const f32x2 u = v * pp;
  f32x2 e = {__builtin_amdgcn_exp2f(u[0]), __builtin_amdgcn_exp2f(u[1])};
  e = e + (f32x2){1.f, 1.f};
  const f32x2 r = {__builtin_amdgcn_rcpf(e[0]), __builtin_amdgcn_rcpf(e[1])};
  const f32x2 o = v * r;
  return cvt_pk(o[0], o[1]);
}

__device__ __forceinline__ void g1_store_tile(const f32x4& a, int tile, int mrow, int g, const u16* bias4,
                                              u16* __restrict__ qkv, u16* __restrict__ hf) {
  if (tile < NQKV_ / 16) {
    const int col = tile * 16 + 4 * g;
#pragma unroll
    for (int j = 0; j < 2; ++j) {
      const int c = col + 2 * j;
      const int hh = c / 18, f = c - hh * 18;
      *reinterpret_cast<unsigned*>(qkv + ((size_t)hh * S_ + mrow) * 24 + f) = pack2(a[2 * j], a[2 * j + 1]);
    }
  } else {
    u32x2 bb = *reinterpret_cast<const u32x2*>(bias4);
    float o[4];
    o[0] = gelu_f(a[0] + lo16(bb[0]));
    o[1] = gelu_f(a[1] + hi16(bb[0]));
    o[2] = gelu_f(a[2] + lo16(bb[1]));
    o[3] = gelu_f(a[3] + hi16(bb[1]));
    u32x2 v = {pack2(o[0], o[1]), pack2(o[2], o[3])};
    const int T = tile - NQKV_ / 16;
    u16* dst = hf + ((size_t)((mrow >> 4) * 144 + (T >> 1)) * 64 + ((T & 1) * 2 + (g >> 1)) * 16 + (mrow & 15)) * 8 + (g & 1) * 4;
    *reinterpret_cast<u32x2*>(dst) = v;
  }
}

__device__ __forceinline__ void g1_addr(unsigned (&ad)[4][2], char* smem, int lane) {
  const int g = lane >> 4, q = (lane & 15) >> 2, p = lane & 3;
#pragma unroll
  for (int t = 0; t < 2; ++t) {
    const int row = 8 * g + 4 * t + q;
    const int f = ((row >> 1) & 1) | (((row >> 3) & 1) << 1);
#pragma unroll
    for (int n = 0; n < 4; ++n) ad[n][t] = lds_addr(smem) + row * 128 + ((n ^ f) * 32) + p * 8;
  }
}

struct G1W { const u16* src; int stride; };

__device__ __forceinline__ void g1_wsetup(G1W& w, int col, int wid, int lane, const u16* wfold, const u16* wqkv, const u16* w1) {
  const int row = wid * 8 + (lane >> 3);
  const int f = ((row >> 1) & 1) | (((row >> 3) & 1) << 1);
  const int ch = (lane & 7) ^ (f << 1);
  if (col < NPH_) { w.src = wfold + (size_t)row * NPH_ + col + ch * 8; w.stride = 64 * NPH_; }
  else if (col < NQKV_) { w.src = wqkv + (size_t)row * NQKV_ + col + ch * 8; w.stride = 64 * NQKV_; }
  else { w.src = w1 + (size_t)row * F_ + (col - NQKV_) + ch * 8; w.stride = 64 * F_; }
}
__device__ __forceinline__ void g1_wissue(G1W& w, char* buf, int wid, int lane) {
#pragma unroll
  for (int i = 0; i < 3; ++i) {
    glds16(w.src, buf + i * 8192 + wid * 1024 + lane * 16);
    w.src += w.stride;
  }
}

template <int U>
__device__ __forceinline__ void g1_unit(G1W& wst, int unit0, const u16* wfold, const u16* wqkv,
                                        const u16* w1, char* smem, bf16x8 (&xa)[2][18], int m0,
                                        u16* __restrict__ qkv, u16* __restrict__ hf, WF (&wA)[4]) {
  int tz = threadIdx.x;
  asm volatile("" : "+v"(tz));
  const int wid = __builtin_amdgcn_readfirstlane(tz >> 6), lane = tz & 63;
  const int g = lane >> 4;
  unsigned ad[4][2];
  g1_addr(ad, smem, lane);
  f32x4 acc[2][4];
#pragma unroll
  for (int r = 0; r < 2; ++r)
#pragma unroll
    for (int n = 0; n < 4; ++n) acc[r][n] = (f32x4){0.f, 0.f, 0.f, 0.f};

  auto stage = [&](auto KC) {
    constexpr int kc = decltype(KC)::value;
    constexpr int s = U * 3 + kc;
    constexpr int CUR = (s % G1_RING) * G1_BUFB;
    if constexpr (U == 0) {
#pragma unroll
      for (int k = 0; k < 6; ++k) { pin(xa[0][kc * 6 + k]); pin(xa[1][kc * 6 + k]); }
    }
    WF wB[4];
    g1_reads<CUR + 1 * 4096>(wB, ad); wait_lgkm<8>(); g1_mma<kc * 6 + 0>(wA, xa, acc);
    g1_reads<CUR + 2 * 4096>(wA, ad); wait_lgkm<8>(); g1_mma<kc * 6 + 1>(wB, xa, acc);
    g1_reads<CUR + 3 * 4096>(wB, ad); wait_lgkm<8>(); g1_mma<kc * 6 + 2>(wA, xa, acc);
    g1_reads<CUR + 4 * 4096>(wA, ad); wait_lgkm<8>(); g1_mma<kc * 6 + 3>(wB, xa, acc);
    g1_reads<CUR + 5 * 4096>(wB, ad); wait_lgkm<8>(); g1_mma<kc * 6 + 4>(wA, xa, acc);
    if constexpr (s < 8) {
      constexpr int nl0 = (s == 0) ? 12 : ((s + 2 <= 8) ? 3 : 0);
      constexpr int nl1 = (s == 0) ? 15 : ((s + 3 <= 8) ? 3 : 0);
      constexpr int nst = (U > 0 && kc <= 1) ? G1_NST : 0;
      const int lag = wid >> 2;
      if (lag) wait_vm<nl1 + nst>(); else wait_vm<nl0 + nst>();
      BAR();
      {
        const int sn = s + 3 + lag;
        if (sn <= 8) {
          if (sn % 3 == 0) g1_wsetup(wst, (unit0 + sn / 3) * 64, wid, lane, wfold, wqkv, w1);
          g1_wissue(wst, smem + (sn % G1_RING) * G1_BUFB, wid, lane);
        }
      }
      g1_reads<((s + 1) % G1_RING) * G1_BUFB>(wA, ad);
      wait_lgkm<8>();
    } else {
      wait_lgkm<0>();
    }
    g1_mma<kc * 6 + 5>(wB, xa, acc);
  };
  stage(IC<0>{}); stage(IC<1>{}); stage(IC<2>{});
  const u16* bias_lds = reinterpret_cast<const u16*>(smem + G1_BOFF);
  if ((unit0 + U) * 64 < NQKV_) {
#pragma unroll
    for (int n = 0; n < 4; ++n) {
      const int tile = (unit0 + U) * 4 + n;
#pragma unroll
      for (int r = 0; r < 2; ++r)
        g1_store_tile(acc[r][n], tile, m0 + wid * 32 + r * 16 + (lane & 15), g, bias_lds, qkv, hf);
    }
  } else {
    u32x2 bb[4];
#pragma unroll
    for (int n = 0; n < 4; ++n) bb[n] = *reinterpret_cast<const u32x2*>(bias_lds + (U * 4 + n) * 16 + 4 * g);
    const int T0 = (unit0 + U) * 4 - NQKV_ / 16;
#pragma unroll
    for (int r = 0; r < 2; ++r) {
      const int mrow = m0 + wid * 32 + r * 16 + (lane & 15);
      u16* dst = hf + ((size_t)((mrow >> 4) * 144 + (T0 >> 1)) * 64 + (g >> 1) * 16 + (mrow & 15)) * 8 + (g & 1) * 4;
#pragma unroll
      for (int n = 0; n < 4; ++n) {
        const f32x2 v0 = (f32x2){acc[r][n][0], acc[r][n][1]} + (f32x2){lo16(bb[n][0]), hi16(bb[n][0])};
        const f32x2 v1 = (f32x2){acc[r][n][2], acc[r][n][3]} + (f32x2){lo16(bb[n][1]), hi16(bb[n][1])};
        u32x2 v = {gelu2_pk(v0), gelu2_pk(v1)};
        ST_NT(reinterpret_cast<u32x2*>(dst + (n >> 1) * 512 + (n & 1) * 256), v);
      }
    }
  }
}

__global__ __launch_bounds__(512, 2) void k1_gemm1(const u16* __restrict__ xnf, const u16* __restrict__ wfold,
                                                   const u16* __restrict__ wqkv, const u16* __restrict__ w1,
                                                   const u16* __restrict__ b1, u16* __restrict__ qkv,
                                                   u16* __restrict__ hf) {
  __shared__ __attribute__((aligned(16))) char smem[G1_SMEM];
  const int tid = threadIdx.x, wid = tid >> 6, lane = tid & 63;
  const int bid = blockIdx.x;
  const int mt = bid >> 5, grp = bid & 31;
  const int m0 = mt * 256;
  const int unit0 = grp * 3;
  const bool extra = grp < 12;
  const int xcol = 6144 + (extra ? grp : 0) * 16;
  STAMP(1, 0);
  G1W wst;
  g1_wsetup(wst, unit0 * 64, wid, lane, wfold, wqkv, w1);
  g1_wissue(wst, smem + 0 * G1_BUFB, wid, lane);
  g1_wissue(wst, smem + 1 * G1_BUFB, wid, lane);
  g1_wissue(wst, smem + 2 * G1_BUFB, wid, lane);
#pragma unroll
  for (int i = 0; i < 3; ++i) {
    int ins = wid + 8 * i;
    if (ins >= 18) ins -= 18;
    const int c = ins * 64 + lane;
    const int pr = c >> 1, half = c & 1;
    const int k = (pr & ~0xC) | ((pr & 4) << 1) | ((pr & 8) >> 1);
    glds16(w1 + (size_t)k * F_ + (xcol - NQKV_) + half * 8, smem + G1_XOFF + c * 16);
  }
  {
    const int e = (wid & 1) * 64 + lane;
    int col = (e < 96) ? (unit0 * 64 + 2 * e - NQKV_) : (xcol - NQKV_ + 2 * (e - 96));
    if (col < 0 || e >= 104) col = 0;
    __builtin_amdgcn_global_load_lds((const unsigned*)(b1 + col), (unsigned*)(smem + G1_BOFF + e * 4), 4, 0, 0);
  }
  bf16x8 xa[2][18];
#pragma unroll
  for (int ks = 0; ks < 18; ++ks)
#pragma unroll
    for (int r = 0; r < 2; ++r)
      gld16_asm(xa[r][ks], xnf + ((size_t)(((m0 >> 4) + wid * 2 + r) * 18 + ks) * 64 + lane) * 8);
  wait_vm<24>();
  BAR();
  WF wA[4];
  {
    unsigned ad[4][2];
    g1_addr(ad, smem, lane);
    g1_reads<0>(wA, ad);
  }
  STAMP(1, 1);
  const bool grpB = __builtin_amdgcn_readfirstlane(wid) >= 4;
  if (grpB) {
    BAR();
    g1_wsetup(wst, (unit0 + 1) * 64, wid, lane, wfold, wqkv, w1);
    g1_wissue(wst, smem + 3 * G1_BUFB, wid, lane);
  }
  g1_unit<0>(wst, unit0, wfold, wqkv, w1, smem, xa, m0, qkv, hf, wA);
  STAMP(1, 2);
  g1_unit<1>(wst, unit0, wfold, wqkv, w1, smem, xa, m0, qkv, hf, wA);
  STAMP(1, 3);
  g1_unit<2>(wst, unit0, wfold, wqkv, w1, smem, xa, m0, qkv, hf, wA);
  if (!grpB) BAR();
  STAMP(1, 4);
  if (extra) {
    int tz = threadIdx.x;
    asm volatile("" : "+v"(tz));
    const int wid2 = __builtin_amdgcn_readfirstlane(tz >> 6), lane2 = tz & 63;
    const int g = lane2 >> 4, q = (lane2 & 15) >> 2, p = lane2 & 3;
    unsigned ad[2];
#pragma unroll
    for (int t = 0; t < 2; ++t) {
      const int k = 8 * g + 4 * t + q;
      const int pr = (k & ~0xC) | ((k & 4) << 1) | ((k & 8) >> 1);
      ad[t] = lds_addr(smem) + G1_XOFF + pr * 32 + p * 8;
    }
    f32x4 acc[2] = {(f32x4){0.f, 0.f, 0.f, 0.f}, (f32x4){0.f, 0.f, 0.f, 0.f}};
#pragma unroll
    for (int kb = 0; kb < 18; kb += 6) {
      WF w[6];
      tr_rd<0 * 1024>(w[0].lo, ad[0] + kb * 1024); tr_rd<0 * 1024>(w[0].hi, ad[1] + kb * 1024);
      tr_rd<1 * 1024>(w[1].lo, ad[0] + kb * 1024); tr_rd<1 * 1024>(w[1].hi, ad[1] + kb * 1024);
      tr_rd<2 * 1024>(w[2].lo, ad[0] + kb * 1024); tr_rd<2 * 1024>(w[2].hi, ad[1] + kb * 1024);
      tr_rd<3 * 1024>(w[3].lo, ad[0] + kb * 1024); tr_rd<3 * 1024>(w[3].hi, ad[1] + kb * 1024);
      tr_rd<4 * 1024>(w[4].lo, ad[0] + kb * 1024); tr_rd<4 * 1024>(w[4].hi, ad[1] + kb * 1024);
      tr_rd<5 * 1024>(w[5].lo, ad[0] + kb * 1024); tr_rd<5 * 1024>(w[5].hi, ad[1] + kb * 1024);
      wait_lgkm<0>();
#pragma unroll
      for (int i = 0; i < 6; ++i) pin(w[i]);
#pragma unroll
      for (int i = 0; i < 6; ++i)
#pragma unroll
        for (int r = 0; r < 2; ++r)
          acc[r] = __builtin_amdgcn_mfma_f32_16x16x32_bf16(cat(w[i]), xa[r][kb + i], acc[r], 0, 0, 0);
    }
    const u16* bias_lds = reinterpret_cast<const u16*>(smem + G1_BOFF);
#pragma unroll
    for (int r = 0; r < 2; ++r)
      g1_store_tile(acc[r], xcol / 16, m0 + wid2 * 32 + r * 16 + (lane2 & 15), g, bias_lds + 192 + 4 * g, qkv, hf);
  }
}

constexpr int G2_NT = 9, G2_RB = G2_NT * 32;
constexpr int G2_BUFB = 64 * G2_RB;
constexpr int G2_RING = 4;
constexpr int G2_STAGES = 18;

__device__ __forceinline__ void softmax_pm(const float (&pk)[18], float (&ep)[18], float (&en)[18]) {
  float mx = pk[0], mn = pk[0];
#pragma unroll
  for (int f = 1; f < 18; ++f) {
    mx = fmaxf(mx, pk[f]);
    mn = fminf(mn, pk[f]);
  }
  float sp = 0.f, sn = 0.f;
#pragma unroll
  for (int f = 0; f < 18; ++f) {
    ep[f] = __expf(pk[f] - mx);
    en[f] = __expf(mn - pk[f]);
    sp += ep[f];
    sn += en[f];
  }
  const float rp = 1.f / sp, rn = 1.f / sn;
#pragma unroll
  for (int f = 0; f < 18; ++f) {
    ep[f] *= rp;
    en[f] *= rn;
  }
}

__device__ void head_block(int hb, const u16* __restrict__ qkv, u16* __restrict__ of, float* pkv, unsigned* cnt, char* smem) {
  u16* phiT = reinterpret_cast<u16*>(smem);
  u16* vT = reinterpret_cast<u16*>(smem + 25344);
  float* kvs = reinterpret_cast<float*>(smem + 25344 + 16896);
  const int h = hb >> 3, c8 = hb & 7;
  const int tid = threadIdx.x, wid = tid >> 6, lane = tid & 63;
  const int row = c8 * 256 + tid;
  u32x4 kr[3], vr[3], qr[3];
  {
    const u32x4* kp = reinterpret_cast<const u32x4*>(qkv + ((size_t)(32 + h) * S_ + row) * 24);
    const u32x4* vp = reinterpret_cast<const u32x4*>(qkv + ((size_t)(64 + h) * S_ + row) * 24);
    const u32x4* qp = reinterpret_cast<const u32x4*>(qkv + ((size_t)h * S_ + row) * 24);
#pragma unroll
    for (int d = 0; d < 3; ++d) { kr[d] = kp[d]; vr[d] = vp[d]; qr[d] = qp[d]; }
  }
  for (int i = tid; i < 12 * 264; i += 256) phiT[36 * 264 + i] = 0;
  for (int i = tid; i < 14 * 264; i += 256) vT[18 * 264 + i] = (i < 264) ? (u16)0x3f80 : (u16)0;
  {
    float pk[18];
#pragma unroll
    for (int d = 0; d < 9; ++d) {
      pk[2 * d] = lo16(kr[d >> 2][d & 3]);
      pk[2 * d + 1] = hi16(kr[d >> 2][d & 3]);
    }
    float ep[18], en[18];
    softmax_pm(pk, ep, en);
#pragma unroll
    for (int f = 0; f < 18; ++f) {
      phiT[f * 264 + tid] = f2bf(ep[f]);
      phiT[(18 + f) * 264 + tid] = f2bf(en[f]);
      const unsigned w = vr[(f >> 1) >> 2][(f >> 1) & 3];
      vT[f * 264 + tid] = (u16)((f & 1) ? (w >> 16) : (w & 0xffff));
    }
  }
  __syncthreads();
  {
    f32x4 acc[3][2];
#pragma unroll
    for (int a = 0; a < 3; ++a)
#pragma unroll
      for (int b = 0; b < 2; ++b) acc[a][b] = (f32x4){0.f, 0.f, 0.f, 0.f};
#pragma unroll
    for (int ks = 0; ks < 2; ++ks) {
      const int s0 = wid * 64 + ks * 32 + 8 * (lane >> 4);
      bf16x8 bv[2];
#pragma unroll
      for (int b = 0; b < 2; ++b) bv[b] = *reinterpret_cast<const bf16x8*>(vT + (b * 16 + (lane & 15)) * 264 + s0);
#pragma unroll
      for (int a = 0; a < 3; ++a) {
        bf16x8 av = *reinterpret_cast<const bf16x8*>(phiT + (a * 16 + (lane & 15)) * 264 + s0);
#pragma unroll
        for (int b = 0; b < 2; ++b) acc[a][b] = __builtin_amdgcn_mfma_f32_16x16x32_bf16(av, bv[b], acc[a][b], 0, 0, 0);
      }
    }
#pragma unroll
    for (int a = 0; a < 3; ++a)
#pragma unroll
      for (int b = 0; b < 2; ++b)
#pragma unroll
        for (int j = 0; j < 4; ++j)
          kvs[(wid * 48 + a * 16 + (lane >> 4) * 4 + j) * 32 + b * 16 + (lane & 15)] = acc[a][b][j];
  }
  __syncthreads();
  float* mypart = pkv + (size_t)(h * 8 + c8) * 1536;
#pragma unroll
  for (int it = 0; it < 6; ++it) {
    const int i = it * 256 + tid;
    const float v = kvs[i] + kvs[1536 + i] + kvs[2 * 1536 + i] + kvs[3 * 1536 + i];
    __hip_atomic_store(mypart + i, v, __ATOMIC_RELAXED, __HIP_MEMORY_SCOPE_AGENT);
  }
  asm volatile("s_waitcnt vmcnt(0)" ::: "memory");
  __syncthreads();
  if (tid == 0) __hip_atomic_fetch_add(cnt + h * 32, 1u, __ATOMIC_RELAXED, __HIP_MEMORY_SCOPE_AGENT);
  float ep[18], en[18];
  {
    float pk[18];
#pragma unroll
    for (int d = 0; d < 9; ++d) {
      pk[2 * d] = lo16(qr[d >> 2][d & 3]);
      pk[2 * d + 1] = hi16(qr[d >> 2][d & 3]);
    }
    softmax_pm(pk, ep, en);
#pragma unroll
    for (int f = 0; f < 18; ++f) { ep[f] = rbf(ep[f]); en[f] = rbf(en[f]); }
  }
  if (tid == 0) {
    unsigned spins = 0;
    while (__hip_atomic_load(cnt + h * 32, __ATOMIC_RELAXED, __HIP_MEMORY_SCOPE_AGENT) < 8u) {
      __builtin_amdgcn_s_sleep(1);
      if (++spins > (1u << 24)) break;
    }
  }
  __syncthreads();
  float* kvf = reinterpret_cast<float*>(smem);
  {
    float* hp = pkv + (size_t)h * 8 * 1536;
    float pv[3][8];
#pragma unroll
    for (int it = 0; it < 3; ++it) {
      const int i = it * 256 + tid;
      const int f = i / 20, d = i - f * 20;
      const int src = (i < 720) ? (f * 32 + d) : 0;
#pragma unroll
      for (int c = 0; c < 8; ++c) pv[it][c] = __hip_atomic_load(hp + c * 1536 + src, __ATOMIC_RELAXED, __HIP_MEMORY_SCOPE_AGENT);
    }
#pragma unroll
    for (int it = 0; it < 3; ++it) {
      const int i = it * 256 + tid;
      float v = 0.f;
#pragma unroll
      for (int c = 0; c < 8; ++c) v += pv[it][c];
      if (i < 720) kvf[i] = rbf(v);
    }
  }
  __syncthreads();
  {
    float num[20];
#pragma unroll
    for (int d = 0; d < 20; ++d) num[d] = 0.f;
#pragma unroll
    for (int f = 0; f < 36; ++f) {
      const float ph = (f < 18) ? ep[f] : en[f - 18];
#pragma unroll
      for (int d4 = 0; d4 < 5; ++d4) {
        const f32x4 w = *reinterpret_cast<const f32x4*>(kvf + f * 20 + d4 * 4);
#pragma unroll
        for (int j = 0; j < 4; ++j) num[d4 * 4 + j] += ph * w[j];
      }
    }
    const float rd = 1.f / (rbf(num[18]) + 1e-6f);
#pragma unroll
    for (int d2 = 0; d2 < 9; ++d2) {
      const int k = h * 18 + 2 * d2;
      u16* dst = of + ((size_t)((row >> 4) * 18 + (k >> 5)) * 64 + ((k >> 3) & 3) * 16 + (row & 15)) * 8 + (k & 7);
      *reinterpret_cast<unsigned*>(dst) = pack2(rbf(num[2 * d2]) * rd, rbf(num[2 * d2 + 1]) * rd);
    }
  }
}

template <int BASE, int N0, int CNT, int I = 0>
__device__ __forceinline__ void g2_wreads(WF (&w)[5], unsigned a0, unsigned a1) {
  if constexpr (I < CNT) {
    tr_rd<BASE + (N0 + I) * 32>(w[I].lo, a0);
    tr_rd<BASE + (N0 + I) * 32>(w[I].hi, a1);
    g2_wreads<BASE, N0, CNT, I + 1>(w, a0, a1);
  }
}
template <int N0, int CNT>
__device__ __forceinline__ void g2_mma(WF (&w)[5], const bf16x8 (&ac)[2], f32x4 (&acc)[2][G2_NT]) {
#pragma unroll
  for (int i = 0; i < CNT; ++i) pin(w[i]);
#pragma unroll
  for (int i = 0; i < CNT; ++i)
#pragma unroll
    for (int r = 0; r < 2; ++r)
      acc[r][N0 + i] = __builtin_amdgcn_mfma_f32_16x16x32_bf16(cat(w[i]), ac[r], acc[r][N0 + i], 0, 0, 0);
}
constexpr int K2_HEADBLK = 256;

__global__ __launch_bounds__(256, 2) void k2_gemm2(const u16* __restrict__ hf, const u16* __restrict__ w2,
                                                   u16* __restrict__ part, const u16* __restrict__ qkv,
                                                   u16* __restrict__ of, float* pkv, unsigned* cnt) {
  __shared__ __attribute__((aligned(16))) char smem[G2_RING * G2_BUFB];
  const int tid = threadIdx.x, wid = tid >> 6, lane = tid & 63;
  int bid = blockIdx.x;
  if (bid < K2_HEADBLK) { head_block(bid, qkv, of, pkv, cnt, smem); return; }
  bid -= K2_HEADBLK;
  STAMP(2, 0);
  const int xcd = bid & 7, loc = bid >> 3;
  const int sp = xcd >> 1, mt = (xcd & 1) * 8 + (loc >> 2), nt4 = loc & 3;
  const int m0 = mt * 128, n0 = nt4 * 144, k0 = sp * 1152;
  const int g = lane >> 4, q = (lane & 15) >> 2, p = lane & 3;
  const bool odd = g & 1;
  const unsigned wa0 = lds_addr(smem) + (8 * g + 4 * (g & 1) + q) * G2_RB + p * 8;
  const unsigned wa1 = lds_addr(smem) + (8 * g + 4 * (1 - (g & 1)) + q) * G2_RB + p * 8;
  f32x4 acc[2][G2_NT];
#pragma unroll
  for (int r = 0; r < 2; ++r)
#pragma unroll
    for (int n = 0; n < G2_NT; ++n) acc[r][n] = (f32x4){0.f, 0.f, 0.f, 0.f};
  const u16* wp[5];
  int wdst[5];
#pragma unroll
  for (int i = 0; i < 5; ++i) {
    int ins = wid + 4 * i;
    if (ins >= 18) ins -= 18;
    int c = ins * 64 + lane;
    int row = c / 18, cc = c % 18;
    wp[i] = w2 + (size_t)(k0 + row) * D_ + n0 + cc * 8;
    wdst[i] = c * 16;
  }
  const u16* ap[2];
#pragma unroll
  for (int r = 0; r < 2; ++r) ap[r] = hf + ((size_t)((m0 >> 4) + wid * 2 + r) * 144 + (k0 >> 5)) * 512 + lane * 8;
  bf16x8 an[4][2][2];
  auto issue = [&](auto SL) {
    constexpr int slot = decltype(SL)::value;
    char* buf = smem + slot * G2_BUFB;
#pragma unroll
    for (int i = 0; i < 5; ++i) { glds16(wp[i], buf + wdst[i]); wp[i] += 64 * D_; }
#pragma unroll
    for (int r = 0; r < 2; ++r) {
      gld16_asm(an[slot][r][0], ap[r]);
      gld16_asm(an[slot][r][1], ap[r] + 512);
      ap[r] += 1024;
    }
  };
  issue(IC<0>{});
  issue(IC<1>{});
  issue(IC<2>{});
  wait_vm<18>();
  BAR();
  STAMP(2, 1);
  WF wA[5], wB[5];
  g2_wreads<0, 0, 5>(wA, wa0, wa1);
  auto stage = [&](auto U, auto LAST) {
    constexpr int u = decltype(U)::value;
    constexpr int rem = decltype(LAST)::value;
    constexpr int BASE = u * G2_BUFB;
    bf16x8 c0[2], c1[2];
#pragma unroll
    for (int r = 0; r < 2; ++r) {
      pin(an[u][r][0]); pin(an[u][r][1]);
      c0[r] = swap_halves_if(an[u][r][0], odd);
      c1[r] = swap_halves_if(an[u][r][1], odd);
    }
    g2_wreads<BASE, 5, 4>(wB, wa0, wa1);
    wait_lgkm<8>();
    g2_mma<0, 5>(wA, c0, acc);
    g2_wreads<BASE + 32 * G2_RB, 0, 5>(wA, wa0, wa1);
    wait_lgkm<10>();
    g2_mma<5, 4>(wB, c0, acc);
    g2_wreads<BASE + 32 * G2_RB, 5, 4>(wB, wa0, wa1);
    wait_lgkm<8>();
    g2_mma<0, 5>(wA, c1, acc);
    if constexpr (rem >= 1) {
      if constexpr (rem >= 2) wait_vm<9>(); else wait_vm<0>();
      BAR();
      if constexpr (rem >= 3) issue(IC<(u + 3) & 3>{});
      g2_wreads<((u + 1) & 3) * G2_BUFB, 0, 5>(wA, wa0, wa1);
      wait_lgkm<10>();
    } else {
      wait_lgkm<0>();
    }
    g2_mma<5, 4>(wB, c1, acc);
  };
#pragma unroll 1
  for (int it = 0; it < 3; ++it) {
    stage(IC<0>{}, IC<3>{}); stage(IC<1>{}, IC<3>{}); stage(IC<2>{}, IC<3>{}); stage(IC<3>{}, IC<3>{});
  }
  STAMP(2, 2);
  stage(IC<0>{}, IC<3>{}); stage(IC<1>{}, IC<3>{}); stage(IC<2>{}, IC<3>{});
  stage(IC<3>{}, IC<2>{}); stage(IC<0>{}, IC<1>{}); stage(IC<1>{}, IC<0>{});
  STAMP(2, 3);
#pragma unroll
  for (int r = 0; r < 2; ++r) {
    const int m = m0 + wid * 32 + r * 16 + (lane & 15);
    u16* pp = part + ((size_t)sp * S_ + m) * D_ + n0 + 4 * g;
#pragma unroll
    for (int n = 0; n < G2_NT; ++n) {
      u32x2 v = {pack2(acc[r][n][0], acc[r][n][1]), pack2(acc[r][n][2], acc[r][n][3])};
      ST_NT(reinterpret_cast<u32x2*>(pp + n * 16), v);
    }
  }
  asm volatile("s_waitcnt vmcnt(0)" ::: "memory");
  STAMP(2, 4);
}

__global__ __launch_bounds__(512, 2) void k3_final(const u16* __restrict__ of, const u16* __restrict__ wo,
                                                   const u16* __restrict__ part, const u16* __restrict__ b2,
                                                   const u16* __restrict__ x, u16* __restrict__ out) {
  __shared__ __attribute__((aligned(16))) char smem[576 * 128];
  const int tid = threadIdx.x, wid = tid >> 6, lane = tid & 63;
  const int bid = blockIdx.x;
  const int xcd = bid & 7, loc = bid >> 3;
  const int mt = xcd * 2 + loc / 9, nt = loc % 9;
  const int m0 = mt * 128, n0 = nt * 64;
  const int g = lane >> 4, q = (lane & 15) >> 2, p = lane & 3;
  const int wr = wid >> 1, wc = wid & 1;
  STAMP(3, 0);
#pragma unroll
  for (int i = 0; i < 9; ++i) {
    const int row = (wid * 9 + i) * 8 + (lane >> 3);
    const int f = ((row >> 1) & 1) | (((row >> 3) & 1) << 1);
    const int ch = (lane & 7) ^ (f << 1);
    glds16(wo + (size_t)row * D_ + n0 + ch * 8, smem + (wid * 9 + i) * 1024 + lane * 16);
  }
  bf16x8 an[2][18];
#pragma unroll
  for (int ks = 0; ks < 18; ++ks)
#pragma unroll
    for (int r = 0; r < 2; ++r)
      gld16_asm(an[r][ks], of + ((size_t)(((m0 >> 4) + wr * 2 + r) * 18 + ks) * 64 + lane) * 8);
  u32x2 py[2][2][4];
  u32x2 xx[2][2], bb[2];
#pragma unroll
  for (int r = 0; r < 2; ++r) {
    const int m = m0 + wr * 32 + r * 16 + (lane & 15);
#pragma unroll
    for (int n = 0; n < 2; ++n) {
      const int ncol = n0 + wc * 32 + n * 16 + 4 * g;
#pragma unroll
      for (int sI = 0; sI < SPLITK_; ++sI)
        asm volatile("global_load_dwordx2 %0, %1, off" : "=v"(py[r][n][sI]) : "v"(part + ((size_t)sI * S_ + m) * D_ + ncol) : "memory");
      asm volatile("global_load_dwordx2 %0, %1, off" : "=v"(xx[r][n]) : "v"(x + (size_t)m * D_ + ncol) : "memory");
    }
  }
#pragma unroll
  for (int n = 0; n < 2; ++n)
    asm volatile("global_load_dwordx2 %0, %1, off" : "=v"(bb[n]) : "v"(b2 + n0 + wc * 32 + n * 16 + 4 * g) : "memory");
  unsigned ad[2][2];
#pragma unroll
  for (int t = 0; t < 2; ++t) {
    const int row = 8 * g + 4 * t + q;
    const int f = ((row >> 1) & 1) | (((row >> 3) & 1) << 1);
#pragma unroll
    for (int n = 0; n < 2; ++n) ad[n][t] = lds_addr(smem) + row * 128 + (((wc * 2 + n) ^ f) * 32) + p * 8;
  }
  f32x4 acc[2][2];
#pragma unroll
  for (int r = 0; r < 2; ++r)
#pragma unroll
    for (int n = 0; n < 2; ++n) acc[r][n] = (f32x4){0.f, 0.f, 0.f, 0.f};
  STAMP(3, 1);
  wait_vm<22>();
  BAR();
  STAMP(3, 2);
#pragma unroll
  for (int ks = 0; ks < 18; ++ks) { pin(an[0][ks]); pin(an[1][ks]); }
  auto grp = [&](auto KB) {
    constexpr int kb = decltype(KB)::value;
    WF w[6][2];
#pragma unroll
    for (int k = 0; k < 6; ++k)
#pragma unroll
      for (int n = 0; n < 2; ++n) {
        if (k == 0) { tr_rd<0 * 4096>(w[0][n].lo, ad[n][0] + kb * 4096); tr_rd<0 * 4096>(w[0][n].hi, ad[n][1] + kb * 4096); }
        if (k == 1) { tr_rd<1 * 4096>(w[1][n].lo, ad[n][0] + kb * 4096); tr_rd<1 * 4096>(w[1][n].hi, ad[n][1] + kb * 4096); }
        if (k == 2) { tr_rd<2 * 4096>(w[2][n].lo, ad[n][0] + kb * 4096); tr_rd<2 * 4096>(w[2][n].hi, ad[n][1] + kb * 4096); }
        if (k == 3) { tr_rd<3 * 4096>(w[3][n].lo, ad[n][0] + kb * 4096); tr_rd<3 * 4096>(w[3][n].hi, ad[n][1] + kb * 4096); }
        if (k == 4) { tr_rd<4 * 4096>(w[4][n].lo, ad[n][0] + kb * 4096); tr_rd<4 * 4096>(w[4][n].hi, ad[n][1] + kb * 4096); }
        if (k == 5) { tr_rd<5 * 4096>(w[5][n].lo, ad[n][0] + kb * 4096); tr_rd<5 * 4096>(w[5][n].hi, ad[n][1] + kb * 4096); }
      }
    wait_lgkm<12>();
#pragma unroll
    for (int k = 0; k < 3; ++k)
#pragma unroll
      for (int n = 0; n < 2; ++n) pin(w[k][n]);
#pragma unroll
    for (int k = 0; k < 3; ++k)
#pragma unroll
      for (int n = 0; n < 2; ++n)
#pragma unroll
        for (int r = 0; r < 2; ++r)
          acc[r][n] = __builtin_amdgcn_mfma_f32_16x16x32_bf16(cat(w[k][n]), an[r][kb + k], acc[r][n], 0, 0, 0);
    wait_lgkm<0>();
#pragma unroll
    for (int k = 3; k < 6; ++k)
#pragma unroll
      for (int n = 0; n < 2; ++n) pin(w[k][n]);
#pragma unroll
    for (int k = 3; k < 6; ++k)
#pragma unroll
      for (int n = 0; n < 2; ++n)
#pragma unroll
        for (int r = 0; r < 2; ++r)
          acc[r][n] = __builtin_amdgcn_mfma_f32_16x16x32_bf16(cat(w[k][n]), an[r][kb + k], acc[r][n], 0, 0, 0);
  };
  grp(IC<0>{}); grp(IC<6>{}); grp(IC<12>{});
  wait_vm<0>();
  STAMP(3, 3);
#pragma unroll
  for (int n = 0; n < 2; ++n) asm volatile("" : "+v"(bb[n]));
#pragma unroll
  for (int r = 0; r < 2; ++r) {
    const int m = m0 + wr * 32 + r * 16 + (lane & 15);
#pragma unroll
    for (int n = 0; n < 2; ++n) {
      const int ncol = n0 + wc * 32 + n * 16 + 4 * g;
      asm volatile("" : "+v"(xx[r][n]));
#pragma unroll
      for (int sI = 0; sI < SPLITK_; ++sI) asm volatile("" : "+v"(py[r][n][sI]));
      f32x4 y = {0.f, 0.f, 0.f, 0.f};
#pragma unroll
      for (int sI = 0; sI < SPLITK_; ++sI) {
        y[0] += lo16(py[r][n][sI][0]); y[1] += hi16(py[r][n][sI][0]);
        y[2] += lo16(py[r][n][sI][1]); y[3] += hi16(py[r][n][sI][1]);
      }
      float bv[4] = {lo16(bb[n][0]), hi16(bb[n][0]), lo16(bb[n][1]), hi16(bb[n][1])};
      float xv[4] = {lo16(xx[r][n][0]), hi16(xx[r][n][0]), lo16(xx[r][n][1]), hi16(xx[r][n][1])};
      float o[4];
#pragma unroll
      for (int j = 0; j < 4; ++j) {
        float yy = y[j] + bv[j];
        float gg = yy / (1.f + __expf(-yy));
        o[j] = xv[j] + acc[r][n][j] * gg;
      }
      u32x2 v = {pack2(o[0], o[1]), pack2(o[2], o[3])};
      *reinterpret_cast<u32x2*>(out + (size_t)m * D_ + ncol) = v;
    }
  }
  STAMP(3, 4);
}

extern "C" void kernel_launch(void* const* d_in, const int* in_sizes, int n_in, void* d_out, int out_size, void* d_ws,
                              size_t ws_size, hipStream_t stream) {
  const u16* x = (const u16*)d_in[0];
  const u16* wqkv = (const u16*)d_in[1];
  const u16* wphi = (const u16*)d_in[2];
  const u16* wo = (const u16*)d_in[3];
  const u16* w1 = (const u16*)d_in[5];
  const u16* b1 = (const u16*)d_in[6];
  const u16* w2 = (const u16*)d_in[7];
  const u16* b2 = (const u16*)d_in[8];
  u16* out = (u16*)d_out;
  char* ws = (char*)d_ws;
  size_t off = 0;
  u16* xnf = (u16*)(ws + off); off += (size_t)S_ * D_ * 2;
  u16* wfold = (u16*)(ws + off); off += (size_t)D_ * NPH_ * 2;
  u16* qkv = (u16*)(ws + off); off += (size_t)96 * S_ * 24 * 2;
  u16* hf = (u16*)(ws + off); off += (size_t)S_ * F_ * 2;
  u16* part = (u16*)(ws + off); off += (size_t)SPLITK_ * S_ * D_ * 2;
  u16* of = (u16*)(ws + off); off += (size_t)S_ * D_ * 2;
  float* pkv = (float*)(ws + off); off += (size_t)256 * 1536 * 4;
  unsigned* cnt = (unsigned*)(ws + off); off += 32 * 32 * 4;

  k0_prep<<<512 + 192, 256, 0, stream>>>(x, wqkv, wphi, xnf, wfold, cnt);
  k1_gemm1<<<256, 512, 0, stream>>>(xnf, wfold, wqkv, w1, b1, qkv, hf);
  k2_gemm2<<<K2_HEADBLK + 256, 256, 0, stream>>>(hf, w2, part, qkv, of, pkv, cnt);
  k3_final<<<16 * 9, 512, 0, stream>>>(of, wo, part, b2, x, out);
}
```

```cpp
#include <hip/hip_runtime.h>
#include <hip/hip_bf16.h>
#include <stdint.h>

constexpr int S_ = 2048, D_ = 576, H_ = 32, DH_ = 18, F_ = 4608, NQKV_ = 1728;
constexpr int NPH_ = 1152;
constexpr int KPH_ = 40;
constexpr int KPHI_ = H_ * KPH_;
constexpr int SPLITK_ = 4;

typedef short bf16x8 __attribute__((ext_vector_type(8)));
typedef short s16x4 __attribute__((ext_vector_type(4)));
typedef float f32x4 __attribute__((ext_vector_type(4)));
typedef unsigned u32x4 __attribute__((ext_vector_type(4)));
typedef unsigned u32x2 __attribute__((ext_vector_type(2)));
typedef __attribute__((address_space(3))) s16x4* lds_s16x4_p;
typedef unsigned short u16;
template <int V> struct IC { static constexpr int value = V; };

#define STAMP(kid, i) do { } while (0)
#define ST_NT(p, v) (*(p) = (v))
#define WAIT_LGKM0() asm volatile("s_waitcnt lgkmcnt(0)" ::: "memory")
#define BAR() __builtin_amdgcn_s_barrier()

template <int N>
__device__ __forceinline__ void wait_vm() {
  static_assert(N >= 0 && N < 64, "vmcnt range");
  asm volatile("s_waitcnt vmcnt(%0)" ::"n"(N) : "memory");
}

__device__ __forceinline__ float bf2f(u16 v) { return __uint_as_float(((unsigned)v) << 16); }
__device__ __forceinline__ u16 f2bf(float f) {
  __hip_bfloat16 b = __float2bfloat16(f);
  return *reinterpret_cast<u16*>(&b);
}
__device__ __forceinline__ unsigned pack2(float lo, float hi) {
  return (unsigned)f2bf(lo) | ((unsigned)f2bf(hi) << 16);
}
__device__ __forceinline__ float rbf(float f) { return bf2f(f2bf(f)); }
__device__ __forceinline__ float lo16(unsigned u) { return __uint_as_float(u << 16); }
__device__ __forceinline__ float hi16(unsigned u) { return __uint_as_float(u & 0xffff0000u); }

__device__ __forceinline__ void glds16(const void* g, void* l) {
  __builtin_amdgcn_global_load_lds((const unsigned*)g, (unsigned*)l, 16, 0, 0);
}

__device__ __forceinline__ void l2_touch(const void* g, void* ldummy) {
  __builtin_amdgcn_global_load_lds((const unsigned*)g, (unsigned*)ldummy, 4, 0, 0);
}

__device__ __forceinline__ bf16x8 swap_halves_if(bf16x8 v, bool odd) {
  bf16x8 s = __builtin_shufflevector(v, v, 4, 5, 6, 7, 0, 1, 2, 3);
  return odd ? s : v;
}

__device__ __forceinline__ bf16x8 wfrag(const char* img, int o0, int o1, int off) {
  s16x4 a = __builtin_amdgcn_ds_read_tr16_b64_v4i16((lds_s16x4_p)(img + o0 + off));
  s16x4 b = __builtin_amdgcn_ds_read_tr16_b64_v4i16((lds_s16x4_p)(img + o1 + off));
  return __builtin_shufflevector(a, b, 0, 1, 2, 3, 4, 5, 6, 7);
}

struct WF { s16x4 lo, hi; };
template <int OFF>
__device__ __forceinline__ void tr_rd(s16x4& r, unsigned addr) {
  asm volatile("ds_read_b64_tr_b16 %0, %1 offset:%2" : "=v"(r) : "v"(addr), "n"(OFF) : "memory");
}
__device__ __forceinline__ void gld16_asm(bf16x8& r, const void* p) {
  asm volatile("global_load_dwordx4 %0, %1, off" : "=v"(r) : "v"(p) : "memory");
}
template <int N>
__device__ __forceinline__ void wait_lgkm() {
  static_assert(N >= 0 && N <= 15, "lgkmcnt range");
  asm volatile("s_waitcnt lgkmcnt(%0)" ::"n"(N) : "memory");
}
__device__ __forceinline__ void pin(WF& w) { asm volatile("" : "+v"(w.lo), "+v"(w.hi)); }
__device__ __forceinline__ void pin(bf16x8& w) { asm volatile("" : "+v"(w)); }
__device__ __forceinline__ bf16x8 cat(const WF& w) { return __builtin_shufflevector(w.lo, w.hi, 0, 1, 2, 3, 4, 5, 6, 7); }
__device__ __forceinline__ unsigned lds_addr(const void* p) {
  return (unsigned)(uintptr_t)(__attribute__((address_space(3))) const char*)p;
}

__global__ __launch_bounds__(256) void k0_prep(const u16* __restrict__ x, const u16* __restrict__ wqkv,
                                               const u16* __restrict__ wphi, u16* __restrict__ xnf,
                                               u16* __restrict__ wfold, unsigned* __restrict__ cnt) {
  __shared__ __attribute__((aligned(16))) float wph[18 * 20];
  const int tid = threadIdx.x, wid = tid >> 6, lane = tid & 63;
  const int bid = blockIdx.x;
  if (bid == 512 && tid < 32) cnt[tid * 32] = 0u;
  if (bid < 512) {
    const int row = bid * 4 + wid;
    const u16* xp = x + (size_t)row * D_;
    u32x4 a = *reinterpret_cast<const u32x4*>(xp + lane * 8);
    u32x4 b = {0u, 0u, 0u, 0u};
    if (lane < 8) b = *reinterpret_cast<const u32x4*>(xp + 512 + lane * 8);
    float v[16];
#pragma unroll
    for (int j = 0; j < 4; ++j) {
      v[2 * j] = lo16(a[j]); v[2 * j + 1] = hi16(a[j]);
      v[8 + 2 * j] = lo16(b[j]); v[8 + 2 * j + 1] = hi16(b[j]);
    }
    float s = 0.f;
#pragma unroll
    for (int j = 0; j < 16; ++j) s += v[j];
#pragma unroll
    for (int o = 32; o >= 1; o >>= 1) s += __shfl_xor(s, o);
    const float mean = s * (1.f / D_);
    float ss = 0.f;
#pragma unroll
    for (int j = 0; j < 8; ++j) { float d = v[j] - mean; ss += d * d; }
    if (lane < 8) {
#pragma unroll
      for (int j = 8; j < 16; ++j) { float d = v[j] - mean; ss += d * d; }
    }
#pragma unroll
    for (int o = 32; o >= 1; o >>= 1) ss += __shfl_xor(ss, o);
    const float rstd = rsqrtf(ss * (1.f / D_) + 1e-5f);
    u32x4 oa, ob;
#pragma unroll
    for (int j = 0; j < 4; ++j) {
      oa[j] = pack2((v[2 * j] - mean) * rstd, (v[2 * j + 1] - mean) * rstd);
      ob[j] = pack2((v[8 + 2 * j] - mean) * rstd, (v[8 + 2 * j + 1] - mean) * rstd);
    }
    {
      const int c = lane;
      u16* dst = xnf + ((size_t)((row >> 4) * 18 + (c >> 2)) * 64 + (c & 3) * 16 + (row & 15)) * 8;
      *reinterpret_cast<u32x4*>(dst) = oa;
    }
    if (lane < 8) {
      const int c = 64 + lane;
      u16* dst = xnf + ((size_t)((row >> 4) * 18 + (c >> 2)) * 64 + (c & 3) * 16 + (row & 15)) * 8;
      *reinterpret_cast<u32x4*>(dst) = ob;
    }
    return;
  }
  {
    const int fb = bid - 512;
    const int hh = fb / 3, k = (fb % 3) * 192 + tid;
    const int h = hh & 31;
    for (int i = tid; i < 18 * 20; i += 256) {
      const int d = i / 20, f = i % 20;
      wph[i] = (f < 18) ? bf2f(wphi[h * 324 + d * 18 + f]) : 0.f;
    }
    unsigned wv[9];
    if (tid < 192) {
      const u16* wp = wqkv + (size_t)k * NQKV_ + hh * 18;
#pragma unroll
      for (int d = 0; d < 9; ++d) wv[d] = *reinterpret_cast<const unsigned*>(wp + 2 * d);
    }
    __syncthreads();
    if (tid < 192) {
      float acc[20];
#pragma unroll
      for (int f = 0; f < 20; ++f) acc[f] = 0.f;
#pragma unroll
      for (int d = 0; d < 18; ++d) {
        const float w = (d & 1) ? hi16(wv[d >> 1]) : lo16(wv[d >> 1]);
#pragma unroll
        for (int f4 = 0; f4 < 5; ++f4) {
          f32x4 pw = *reinterpret_cast<const f32x4*>(wph + d * 20 + f4 * 4);
#pragma unroll
          for (int j = 0; j < 4; ++j) acc[f4 * 4 + j] += w * pw[j];
        }
      }
      u16* op = wfold + (size_t)k * NPH_ + hh * 18;
#pragma unroll
      for (int f = 0; f < 9; ++f) *reinterpret_cast<unsigned*>(op + 2 * f) = pack2(acc[2 * f], acc[2 * f + 1]);
    }
  }
}

constexpr int G1_BUFB = 192 * 128;
constexpr int G1_RING = 4;
constexpr int G1_XOFF = G1_RING * G1_BUFB;
constexpr int G1_BOFF = G1_XOFF + 576 * 32;
constexpr int G1_SMEM = G1_BOFF + 1024;
constexpr int G1_NST = 8;

template <int BASE, int I = 0>
__device__ __forceinline__ void g1_reads(WF (&w)[4], const unsigned (&ad)[4][2]) {
  if constexpr (I < 4) {
    if constexpr (BASE >= 49152) {
      tr_rd<BASE - 49152>(w[I].lo, ad[I][0] + 49152u);
      tr_rd<BASE - 49152>(w[I].hi, ad[I][1] + 49152u);
    } else {
      tr_rd<BASE>(w[I].lo, ad[I][0]);
      tr_rd<BASE>(w[I].hi, ad[I][1]);
    }
    g1_reads<BASE, I + 1>(w, ad);
  }
}
template <int KS>
__device__ __forceinline__ void g1_mma(WF (&w)[4], const bf16x8 (&xa)[2][18], f32x4 (&acc)[2][4]) {
#pragma unroll
  for (int i = 0; i < 4; ++i) pin(w[i]);
#pragma unroll
  for (int i = 0; i < 4; ++i)
#pragma unroll
    for (int r = 0; r < 2; ++r)
      acc[r][i] = __builtin_amdgcn_mfma_f32_16x16x32_bf16(cat(w[i]), xa[r][KS], acc[r][i], 0, 0, 0);
}

__device__ __forceinline__ float gelu_f(float v) {
  constexpr float c1 = -2.f * 0.7978845608028654f * 1.4426950408889634f;
  constexpr float c3 = c1 * 0.044715f;
  const float t = v * v;
  const float e = __builtin_amdgcn_exp2f(v * fmaf(t, c3, c1));
  return v * __builtin_amdgcn_rcpf(1.f + e);
}

typedef float f32x2 __attribute__((ext_vector_type(2)));
__device__ __forceinline__ unsigned cvt_pk(float lo, float hi) {
  unsigned r;
  asm("v_cvt_pk_bf16_f32 %0, %1, %2" : "=v"(r) : "v"(lo), "v"(hi));
  return r;
}
__device__ __forceinline__ unsigned gelu2_pk(f32x2 v) {
  constexpr float c1 = -2.f * 0.7978845608028654f * 1.4426950408889634f;
  constexpr float c3 = c1 * 0.044715f;
  const f32x2 t = v * v;
  const f32x2 pp = t * (f32x2){c3, c3} + (f32x2){c1, c1};
  const f32x2 u = v * pp;
  f32x2 e = {__builtin_amdgcn_exp2f(u[0]), __builtin_amdgcn_exp2f(u[1])};
  e = e + (f32x2){1.f, 1.f};
  const f32x2 r = {__builtin_amdgcn_rcpf(e[0]), __builtin_amdgcn_rcpf(e[1])};
  const f32x2 o = v * r;
  return cvt_pk(o[0], o[1]);
}

__device__ __forceinline__ void g1_store_tile(const f32x4& a, int tile, int mrow, int g, const u16* bias4,
                                              u16* __restrict__ qkv, u16* __restrict__ hf) {
  if (tile < NQKV_ / 16) {
    const int col = tile * 16 + 4 * g;
    const int hh = col / 18, f = col - hh * 18;
    u16* dst = qkv + ((size_t)hh * S_ + mrow) * 24 + f;
    const unsigned v0 = cvt_pk(a[0], a[1]), v1 = cvt_pk(a[2], a[3]);
    if (f != 16) {
      u32x2 v = {v0, v1};
      *reinterpret_cast<u32x2*>(dst) = v;
    } else {
      *reinterpret_cast<unsigned*>(dst) = v0;
      *reinterpret_cast<unsigned*>(qkv + ((size_t)(hh + 1) * S_ + mrow) * 24) = v1;
    }
  } else {
    u32x2 bb = *reinterpret_cast<const u32x2*>(bias4);
    float o[4];
    o[0] = gelu_f(a[0] + lo16(bb[0]));
    o[1] = gelu_f(a[1] + hi16(bb[0]));
    o[2] = gelu_f(a[2] + lo16(bb[1]));
    o[3] = gelu_f(a[3] + hi16(bb[1]));
    u32x2 v = {pack2(o[0], o[1]), pack2(o[2], o[3])};
    const int T = tile - NQKV_ / 16;
    u16* dst = hf + ((size_t)((mrow >> 4) * 144 + (T >> 1)) * 64 + ((T & 1) * 2 + (g >> 1)) * 16 + (mrow & 15)) * 8 + (g & 1) * 4;
    *reinterpret_cast<u32x2*>(dst) = v;
  }
}

__device__ __forceinline__ void g1_addr(unsigned (&ad)[4][2], char* smem, int lane) {
  const int g = lane >> 4, q = (lane & 15) >> 2, p = lane & 3;
#pragma unroll
  for (int t = 0; t < 2; ++t) {
    const int row = 8 * g + 4 * t + q;
    const int f = ((row >> 1) & 1) | (((row >> 3) & 1) << 1);
#pragma unroll
    for (int n = 0; n < 4; ++n) ad[n][t] = lds_addr(smem) + row * 128 + ((n ^ f) * 32) + p * 8;
  }
}

struct G1W { const u16* src; int stride; };

__device__ __forceinline__ void g1_wsetup(G1W& w, int col, int wid, int lane, const u16* wfold, const u16* wqkv, const u16* w1) {
  const int row = wid * 8 + (lane >> 3);
  const int f = ((row >> 1) & 1) | (((row >> 3) & 1) << 1);
  const int ch = (lane & 7) ^ (f << 1);
  if (col < NPH_) { w.src = wfold + (size_t)row * NPH_ + col + ch * 8; w.stride = 64 * NPH_; }
  else if (col < NQKV_) { w.src = wqkv + (size_t)row * NQKV_ + col + ch * 8; w.stride = 64 * NQKV_; }
  else { w.src = w1 + (size_t)row * F_ + (col - NQKV_) + ch * 8; w.stride = 64 * F_; }
}
__device__ __forceinline__ void g1_wissue(G1W& w, char* buf, int wid, int lane) {
#pragma unroll
  for (int i = 0; i < 3; ++i) {
    glds16(w.src, buf + i * 8192 + wid * 1024 + lane * 16);
    w.src += w.stride;
  }
}

template <int U>
__device__ __forceinline__ void g1_unit(G1W& wst, int unit0, const u16* wfold, const u16* wqkv,
                                        const u16* w1, char* smem, bf16x8 (&xa)[2][18], int m0,
                                        u16* __restrict__ qkv, u16* __restrict__ hf, WF (&wA)[4]) {
  int tz = threadIdx.x;
  asm volatile("" : "+v"(tz));
  const int wid = __builtin_amdgcn_readfirstlane(tz >> 6), lane = tz & 63;
  const int g = lane >> 4;
  unsigned ad[4][2];
  g1_addr(ad, smem, lane);
  f32x4 acc[2][4];
#pragma unroll
  for (int r = 0; r < 2; ++r)
#pragma unroll
    for (int n = 0; n < 4; ++n) acc[r][n] = (f32x4){0.f, 0.f, 0.f, 0.f};

  auto stage = [&](auto KC) {
    constexpr int kc = decltype(KC)::value;
    constexpr int s = U * 3 + kc;
    constexpr int CUR = (s % G1_RING) * G1_BUFB;
    if constexpr (U == 0) {
#pragma unroll
      for (int k = 0; k < 6; ++k) { pin(xa[0][kc * 6 + k]); pin(xa[1][kc * 6 + k]); }
    }
    WF wB[4];
    g1_reads<CUR + 1 * 4096>(wB, ad); wait_lgkm<8>(); g1_mma<kc * 6 + 0>(wA, xa, acc);
    g1_reads<CUR + 2 * 4096>(wA, ad); wait_lgkm<8>(); g1_mma<kc * 6 + 1>(wB, xa, acc);
    g1_reads<CUR + 3 * 4096>(wB, ad); wait_lgkm<8>(); g1_mma<kc * 6 + 2>(wA, xa, acc);
    g1_reads<CUR + 4 * 4096>(wA, ad); wait_lgkm<8>(); g1_mma<kc * 6 + 3>(wB, xa, acc);
    g1_reads<CUR + 5 * 4096>(wB, ad); wait_lgkm<8>(); g1_mma<kc * 6 + 4>(wA, xa, acc);
    if constexpr (s < 8) {
      constexpr int nl = (s == 0) ? 12 : ((s + 2 <= 8) ? 3 : 0);
      constexpr int nst = (U > 0 && kc <= 1) ? G1_NST : 0;
      wait_vm<nl + nst>();
      BAR();
      if constexpr (s + 3 <= 8) {
        constexpr int sn = s + 3;
        if constexpr (sn % 3 == 0) g1_wsetup(wst, (unit0 + sn / 3) * 64, wid, lane, wfold, wqkv, w1);
        g1_wissue(wst, smem + (sn % G1_RING) * G1_BUFB, wid, lane);
      }
      g1_reads<((s + 1) % G1_RING) * G1_BUFB>(wA, ad);
      wait_lgkm<8>();
    } else {
      wait_lgkm<0>();
    }
    g1_mma<kc * 6 + 5>(wB, xa, acc);
  };
  stage(IC<0>{}); stage(IC<1>{}); stage(IC<2>{});
  const u16* bias_lds = reinterpret_cast<const u16*>(smem + G1_BOFF);
  if ((unit0 + U) * 64 < NQKV_) {
#pragma unroll
    for (int n = 0; n < 4; ++n) {
      const int tile = (unit0 + U) * 4 + n;
#pragma unroll
      for (int r = 0; r < 2; ++r)
        g1_store_tile(acc[r][n], tile, m0 + wid * 32 + r * 16 + (lane & 15), g, bias_lds, qkv, hf);
    }
  } else {
    u32x2 bb[4];
#pragma unroll
    for (int n = 0; n < 4; ++n) bb[n] = *reinterpret_cast<const u32x2*>(bias_lds + (U * 4 + n) * 16 + 4 * g);
    const int T0 = (unit0 + U) * 4 - NQKV_ / 16;
#pragma unroll
    for (int r = 0; r < 2; ++r) {
      const int mrow = m0 + wid * 32 + r * 16 + (lane & 15);
      u16* dst = hf + ((size_t)((mrow >> 4) * 144 + (T0 >> 1)) * 64 + (g >> 1) * 16 + (mrow & 15)) * 8 + (g & 1) * 4;
#pragma unroll
      for (int n = 0; n < 4; ++n) {
        const f32x2 v0 = (f32x2){acc[r][n][0], acc[r][n][1]} + (f32x2){lo16(bb[n][0]), hi16(bb[n][0])};
        const f32x2 v1 = (f32x2){acc[r][n][2], acc[r][n][3]} + (f32x2){lo16(bb[n][1]), hi16(bb[n][1])};
        u32x2 v = {gelu2_pk(v0), gelu2_pk(v1)};
        ST_NT(reinterpret_cast<u32x2*>(dst + (n >> 1) * 512 + (n & 1) * 256), v);
      }
    }
  }
}

__global__ __launch_bounds__(512, 2) void k1_gemm1(const u16* __restrict__ xnf, const u16* __restrict__ wfold,
                                                   const u16* __restrict__ wqkv, const u16* __restrict__ w1,
                                                   const u16* __restrict__ b1, u16* __restrict__ qkv,
                                                   u16* __restrict__ hf) {
  __shared__ __attribute__((aligned(16))) char smem[G1_SMEM];
  const int tid = threadIdx.x, wid = tid >> 6, lane = tid & 63;
  const int bid = blockIdx.x;
  const int mt = bid >> 5, grp = bid & 31;
  const int m0 = mt * 256;
  const int unit0 = grp * 3;
  const bool extra = grp >= 20;
  const int xcol = 6144 + (extra ? (grp - 20) : 0) * 16;
  STAMP(1, 0);
  G1W wst;
  g1_wsetup(wst, unit0 * 64, wid, lane, wfold, wqkv, w1);
  g1_wissue(wst, smem + 0 * G1_BUFB, wid, lane);
  g1_wissue(wst, smem + 1 * G1_BUFB, wid, lane);
  g1_wissue(wst, smem + 2 * G1_BUFB, wid, lane);
#pragma unroll
  for (int i = 0; i < 3; ++i) {
    int ins = wid + 8 * i;
    if (ins >= 18) ins -= 18;
    const int c = ins * 64 + lane;
    const int pr = c >> 1, half = c & 1;
    const int k = (pr & ~0xC) | ((pr & 4) << 1) | ((pr & 8) >> 1);
    glds16(w1 + (size_t)k * F_ + (xcol - NQKV_) + half * 8, smem + G1_XOFF + c * 16);
  }
  {
    const int e = (wid & 1) * 64 + lane;
    int col = (e < 96) ? (unit0 * 64 + 2 * e - NQKV_) : (xcol - NQKV_ + 2 * (e - 96));
    if (col < 0 || e >= 104) col = 0;
    __builtin_amdgcn_global_load_lds((const unsigned*)(b1 + col), (unsigned*)(smem + G1_BOFF + e * 4), 4, 0, 0);
  }
  bf16x8 xa[2][18];
#pragma unroll
  for (int ks = 0; ks < 18; ++ks)
#pragma unroll
    for (int r = 0; r < 2; ++r)
      gld16_asm(xa[r][ks], xnf + ((size_t)(((m0 >> 4) + wid * 2 + r) * 18 + ks) * 64 + lane) * 8);
  wait_vm<24>();
  BAR();
  WF wA[4];
  {
    unsigned ad[4][2];
    g1_addr(ad, smem, lane);
    g1_reads<0>(wA, ad);
  }
  STAMP(1, 1);
  g1_unit<0>(wst, unit0, wfold, wqkv, w1, smem, xa, m0, qkv, hf, wA);
  STAMP(1, 2);
  g1_unit<1>(wst, unit0, wfold, wqkv, w1, smem, xa, m0, qkv, hf, wA);
  STAMP(1, 3);
  g1_unit<2>(wst, unit0, wfold, wqkv, w1, smem, xa, m0, qkv, hf, wA);
  STAMP(1, 4);
  if (extra) {
    int tz = threadIdx.x;
    asm volatile("" : "+v"(tz));
    const int wid2 = __builtin_amdgcn_readfirstlane(tz >> 6), lane2 = tz & 63;
    const int g = lane2 >> 4, q = (lane2 & 15) >> 2, p = lane2 & 3;
    unsigned ad[2];
#pragma unroll
    for (int t = 0; t < 2; ++t) {
      const int k = 8 * g + 4 * t + q;
      const int pr = (k & ~0xC) | ((k & 4) << 1) | ((k & 8) >> 1);
      ad[t] = lds_addr(smem) + G1_XOFF + pr * 32 + p * 8;
    }
    f32x4 acc[2] = {(f32x4){0.f, 0.f, 0.f, 0.f}, (f32x4){0.f, 0.f, 0.f, 0.f}};
#pragma unroll
    for (int kb = 0; kb < 18; kb += 6) {
      WF w[6];
      tr_rd<0 * 1024>(w[0].lo, ad[0] + kb * 1024); tr_rd<0 * 1024>(w[0].hi, ad[1] + kb * 1024);
      tr_rd<1 * 1024>(w[1].lo, ad[0] + kb * 1024); tr_rd<1 * 1024>(w[1].hi, ad[1] + kb * 1024);
      tr_rd<2 * 1024>(w[2].lo, ad[0] + kb * 1024); tr_rd<2 * 1024>(w[2].hi, ad[1] + kb * 1024);
      tr_rd<3 * 1024>(w[3].lo, ad[0] + kb * 1024); tr_rd<3 * 1024>(w[3].hi, ad[1] + kb * 1024);
      tr_rd<4 * 1024>(w[4].lo, ad[0] + kb * 1024); tr_rd<4 * 1024>(w[4].hi, ad[1] + kb * 1024);
      tr_rd<5 * 1024>(w[5].lo, ad[0] + kb * 1024); tr_rd<5 * 1024>(w[5].hi, ad[1] + kb * 1024);
      wait_lgkm<0>();
#pragma unroll
      for (int i = 0; i < 6; ++i) pin(w[i]);
#pragma unroll
      for (int i = 0; i < 6; ++i)
#pragma unroll
        for (int r = 0; r < 2; ++r)
          acc[r] = __builtin_amdgcn_mfma_f32_16x16x32_bf16(cat(w[i]), xa[r][kb + i], acc[r], 0, 0, 0);
    }
    const u16* bias_lds = reinterpret_cast<const u16*>(smem + G1_BOFF);
#pragma unroll
    for (int r = 0; r < 2; ++r)
      g1_store_tile(acc[r], xcol / 16, m0 + wid2 * 32 + r * 16 + (lane2 & 15), g, bias_lds + 192 + 4 * g, qkv, hf);
  }
}

constexpr int G2_NT = 9, G2_RB = G2_NT * 32;
constexpr int G2_BUFB = 64 * G2_RB;
constexpr int G2_RING = 4;
constexpr int G2_STAGES = 18;

__device__ __forceinline__ void softmax_pm(const float (&pk)[18], float (&ep)[18], float (&en)[18]) {
  float mx = pk[0], mn = pk[0];
#pragma unroll
  for (int f = 1; f < 18; ++f) {
    mx = fmaxf(mx, pk[f]);
    mn = fminf(mn, pk[f]);
  }
  float sp = 0.f, sn = 0.f;
#pragma unroll
  for (int f = 0; f < 18; ++f) {
    ep[f] = __expf(pk[f] - mx);
    en[f] = __expf(mn - pk[f]);
    sp += ep[f];
    sn += en[f];
  }
  const float rp = 1.f / sp, rn = 1.f / sn;
#pragma unroll
  for (int f = 0; f < 18; ++f) {
    ep[f] *= rp;
    en[f] *= rn;
  }
}

__device__ void head_block(int hb, const u16* __restrict__ qkv, u16* __restrict__ of, float* pkv, unsigned* cnt, char* smem) {
  u16* phiT = reinterpret_cast<u16*>(smem);
  u16* vT = reinterpret_cast<u16*>(smem + 25344);
  float* kvs = reinterpret_cast<float*>(smem + 25344 + 16896);
  const int h = hb >> 3, c8 = hb & 7;
  const int tid = threadIdx.x, wid = tid >> 6, lane = tid & 63;
  const int row = c8 * 256 + tid;
  u32x4 kr[3], vr[3], qr[3];
  {
    const u32x4* kp = reinterpret_cast<const u32x4*>(qkv + ((size_t)(32 + h) * S_ + row) * 24);
    const u32x4* vp = reinterpret_cast<const u32x4*>(qkv + ((size_t)(64 + h) * S_ + row) * 24);
    const u32x4* qp = reinterpret_cast<const u32x4*>(qkv + ((size_t)h * S_ + row) * 24);
#pragma unroll
    for (int d = 0; d < 3; ++d) { kr[d] = kp[d]; vr[d] = vp[d]; qr[d] = qp[d]; }
  }
  for (int i = tid; i < 12 * 264; i += 256) phiT[36 * 264 + i] = 0;
  for (int i = tid; i < 14 * 264; i += 256) vT[18 * 264 + i] = (i < 264) ? (u16)0x3f80 : (u16)0;
  {
    float pk[18];
#pragma unroll
    for (int d = 0; d < 9; ++d) {
      pk[2 * d] = lo16(kr[d >> 2][d & 3]);
      pk[2 * d + 1] = hi16(kr[d >> 2][d & 3]);
    }
    float ep[18], en[18];
    softmax_pm(pk, ep, en);
#pragma unroll
    for (int f = 0; f < 18; ++f) {
      phiT[f * 264 + tid] = f2bf(ep[f]);
      phiT[(18 + f) * 264 + tid] = f2bf(en[f]);
      const unsigned w = vr[(f >> 1) >> 2][(f >> 1) & 3];
      vT[f * 264 + tid] = (u16)((f & 1) ? (w >> 16) : (w & 0xffff));
    }
  }
  __syncthreads();
  {
    f32x4 acc[3][2];
#pragma unroll
    for (int a = 0; a < 3; ++a)
#pragma unroll
      for (int b = 0; b < 2; ++b) acc[a][b] = (f32x4){0.f, 0.f, 0.f, 0.f};
#pragma unroll
    for (int ks = 0; ks < 2; ++ks) {
      const int s0 = wid * 64 + ks * 32 + 8 * (lane >> 4);
      bf16x8 bv[2];
#pragma unroll
      for (int b = 0; b < 2; ++b) bv[b] = *reinterpret_cast<const bf16x8*>(vT + (b * 16 + (lane & 15)) * 264 + s0);
#pragma unroll
      for (int a = 0; a < 3; ++a) {
        bf16x8 av = *reinterpret_cast<const bf16x8*>(phiT + (a * 16 + (lane & 15)) * 264 + s0);
#pragma unroll
        for (int b = 0; b < 2; ++b) acc[a][b] = __builtin_amdgcn_mfma_f32_16x16x32_bf16(av, bv[b], acc[a][b], 0, 0, 0);
      }
    }
#pragma unroll
    for (int a = 0; a < 3; ++a)
#pragma unroll
      for (int b = 0; b < 2; ++b)
#pragma unroll
        for (int j = 0; j < 4; ++j)
          kvs[(wid * 48 + a * 16 + (lane >> 4) * 4 + j) * 32 + b * 16 + (lane & 15)] = acc[a][b][j];
  }
  __syncthreads();
  float* mypart = pkv + (size_t)(h * 8 + c8) * 1536;
#pragma unroll
  for (int it = 0; it < 6; ++it) {
    const int i = it * 256 + tid;
    const float v = kvs[i] + kvs[1536 + i] + kvs[2 * 1536 + i] + kvs[3 * 1536 + i];
    __hip_atomic_store(mypart + i, v, __ATOMIC_RELAXED, __HIP_MEMORY_SCOPE_AGENT);
  }
  asm volatile("s_waitcnt vmcnt(0)" ::: "memory");
  __syncthreads();
  if (tid == 0) __hip_atomic_fetch_add(cnt + h * 32, 1u, __ATOMIC_RELAXED, __HIP_MEMORY_SCOPE_AGENT);
  float ep[18], en[18];
  {
    float pk[18];
#pragma unroll
    for (int d = 0; d < 9; ++d) {
      pk[2 * d] = lo16(qr[d >> 2][d & 3]);
      pk[2 * d + 1] = hi16(qr[d >> 2][d & 3]);
    }
    softmax_pm(pk, ep, en);
#pragma unroll
    for (int f = 0; f < 18; ++f) { ep[f] = rbf(ep[f]); en[f] = rbf(en[f]); }
  }
  if (tid == 0) {
    unsigned spins = 0;
    while (__hip_atomic_load(cnt + h * 32, __ATOMIC_RELAXED, __HIP_MEMORY_SCOPE_AGENT) < 8u) {
      __builtin_amdgcn_s_sleep(1);
      if (++spins > (1u << 24)) break;
    }
  }
  __syncthreads();
  float* kvf = reinterpret_cast<float*>(smem);
  {
    float* hp = pkv + (size_t)h * 8 * 1536;
    float pv[3][8];
#pragma unroll
    for (int it = 0; it < 3; ++it) {
      const int i = it * 256 + tid;
      const int f = i / 20, d = i - f * 20;
      const int src = (i < 720) ? (f * 32 + d) : 0;
#pragma unroll
      for (int c = 0; c < 8; ++c) pv[it][c] = __hip_atomic_load(hp + c * 1536 + src, __ATOMIC_RELAXED, __HIP_MEMORY_SCOPE_AGENT);
    }
#pragma unroll
    for (int it = 0; it < 3; ++it) {
      const int i = it * 256 + tid;
      float v = 0.f;
#pragma unroll
      for (int c = 0; c < 8; ++c) v += pv[it][c];
      if (i < 720) kvf[i] = rbf(v);
    }
  }
  __syncthreads();
  {
    float num[20];
#pragma unroll
    for (int d = 0; d < 20; ++d) num[d] = 0.f;
#pragma unroll
    for (int f = 0; f < 36; ++f) {
      const float ph = (f < 18) ? ep[f] : en[f - 18];
#pragma unroll
      for (int d4 = 0; d4 < 5; ++d4) {
        const f32x4 w = *reinterpret_cast<const f32x4*>(kvf + f * 20 + d4 * 4);
#pragma unroll
        for (int j = 0; j < 4; ++j) num[d4 * 4 + j] += ph * w[j];
      }
    }
    const float rd = 1.f / (rbf(num[18]) + 1e-6f);
#pragma unroll
    for (int d2 = 0; d2 < 9; ++d2) {
      const int k = h * 18 + 2 * d2;
      u16* dst = of + ((size_t)((row >> 4) * 18 + (k >> 5)) * 64 + ((k >> 3) & 3) * 16 + (row & 15)) * 8 + (k & 7);
      *reinterpret_cast<unsigned*>(dst) = pack2(rbf(num[2 * d2]) * rd, rbf(num[2 * d2 + 1]) * rd);
    }
  }
}

template <int BASE, int N0, int CNT, int I = 0>
__device__ __forceinline__ void g2_wreads(WF (&w)[5], unsigned a0, unsigned a1) {
  if constexpr (I < CNT) {
    tr_rd<BASE + (N0 + I) * 32>(w[I].lo, a0);
    tr_rd<BASE + (N0 + I) * 32>(w[I].hi, a1);
    g2_wreads<BASE, N0, CNT, I + 1>(w, a0, a1);
  }
}
template <int N0, int CNT>
__device__ __forceinline__ void g2_mma(WF (&w)[5], const bf16x8 (&ac)[2], f32x4 (&acc)[2][G2_NT]) {
#pragma unroll
  for (int i = 0; i < CNT; ++i) pin(w[i]);
#pragma unroll
  for (int i = 0; i < CNT; ++i)
#pragma unroll
    for (int r = 0; r < 2; ++r)
      acc[r][N0 + i] = __builtin_amdgcn_mfma_f32_16x16x32_bf16(cat(w[i]), ac[r], acc[r][N0 + i], 0, 0, 0);
}
constexpr int K2_HEADBLK = 256;

__global__ __launch_bounds__(256, 2) void k2_gemm2(const u16* __restrict__ hf, const u16* __restrict__ w2,
                                                   u16* __restrict__ part, const u16* __restrict__ qkv,
                                                   u16* __restrict__ of, float* pkv, unsigned* cnt) {
  __shared__ __attribute__((aligned(16))) char smem[G2_RING * G2_BUFB];
  const int tid = threadIdx.x, wid = tid >> 6, lane = tid & 63;
  int bid = blockIdx.x;
  if (bid < K2_HEADBLK) { head_block(bid, qkv, of, pkv, cnt, smem); return; }
  bid -= K2_HEADBLK;
  STAMP(2, 0);
  const int xcd = bid & 7, loc = bid >> 3;
  const int sp = xcd >> 1, mt = (xcd & 1) * 8 + (loc >> 2), nt4 = loc & 3;
  const int m0 = mt * 128, n0 = nt4 * 144, k0 = sp * 1152;
  const int g = lane >> 4, q = (lane & 15) >> 2, p = lane & 3;
  const bool odd = g & 1;
  const unsigned wa0 = lds_addr(smem) + (8 * g + 4 * (g & 1) + q) * G2_RB + p * 8;
  const unsigned wa1 = lds_addr(smem) + (8 * g + 4 * (1 - (g & 1)) + q) * G2_RB + p * 8;
  f32x4 acc[2][G2_NT];
#pragma unroll
  for (int r = 0; r < 2; ++r)
#pragma unroll
    for (int n = 0; n < G2_NT; ++n) acc[r][n] = (f32x4){0.f, 0.f, 0.f, 0.f};
  const u16* wp[5];
  int wdst[5];
#pragma unroll
  for (int i = 0; i < 5; ++i) {
    int ins = wid + 4 * i;
    if (ins >= 18) ins -= 18;
    int c = ins * 64 + lane;
    int row = c / 18, cc = c % 18;
    wp[i] = w2 + (size_t)(k0 + row) * D_ + n0 + cc * 8;
    wdst[i] = c * 16;
  }
  const u16* ap[2];
#pragma unroll
  for (int r = 0; r < 2; ++r) ap[r] = hf + ((size_t)((m0 >> 4) + wid * 2 + r) * 144 + (k0 >> 5)) * 512 + lane * 8;
  bf16x8 an[4][2][2];
  auto issue = [&](auto SL) {
    constexpr int slot = decltype(SL)::value;
    char* buf = smem + slot * G2_BUFB;
#pragma unroll
    for (int i = 0; i < 5; ++i) { glds16(wp[i], buf + wdst[i]); wp[i] += 64 * D_; }
#pragma unroll
    for (int r = 0; r < 2; ++r) {
      gld16_asm(an[slot][r][0], ap[r]);
      gld16_asm(an[slot][r][1], ap[r] + 512);
      ap[r] += 1024;
    }
  };
  issue(IC<0>{});
  issue(IC<1>{});
  issue(IC<2>{});
  wait_vm<18>();
  BAR();
  STAMP(2, 1);
  WF wA[5], wB[5];
  g2_wreads<0, 0, 5>(wA, wa0, wa1);
  auto stage = [&](auto U, auto LAST) {
    constexpr int u = decltype(U)::value;
    constexpr int rem = decltype(LAST)::value;
    constexpr int BASE = u * G2_BUFB;
    bf16x8 c0[2], c1[2];
#pragma unroll
    for (int r = 0; r < 2; ++r) {
      pin(an[u][r][0]); pin(an[u][r][1]);
      c0[r] = swap_halves_if(an[u][r][0], odd);
      c1[r] = swap_halves_if(an[u][r][1], odd);
    }
    g2_wreads<BASE, 5, 4>(wB, wa0, wa1);
    wait_lgkm<8>();
    g2_mma<0, 5>(wA, c0, acc);
    g2_wreads<BASE + 32 * G2_RB, 0, 5>(wA, wa0, wa1);
    wait_lgkm<10>();
    g2_mma<5, 4>(wB, c0, acc);
    g2_wreads<BASE + 32 * G2_RB, 5, 4>(wB, wa0, wa1);
    wait_lgkm<8>();
    g2_mma<0, 5>(wA, c1, acc);
    if constexpr (rem >= 1) {
      if constexpr (rem >= 2) wait_vm<9>(); else wait_vm<0>();
      BAR();
      if constexpr (rem >= 3) issue(IC<(u + 3) & 3>{});
      g2_wreads<((u + 1) & 3) * G2_BUFB, 0, 5>(wA, wa0, wa1);
      wait_lgkm<10>();
    } else {
      wait_lgkm<0>();
    }
    g2_mma<5, 4>(wB, c1, acc);
  };
#pragma unroll 1
  for (int it = 0; it < 3; ++it) {
    stage(IC<0>{}, IC<3>{}); stage(IC<1>{}, IC<3>{}); stage(IC<2>{}, IC<3>{}); stage(IC<3>{}, IC<3>{});
  }
  STAMP(2, 2);
  stage(IC<0>{}, IC<3>{}); stage(IC<1>{}, IC<3>{}); stage(IC<2>{}, IC<3>{});
  stage(IC<3>{}, IC<2>{}); stage(IC<0>{}, IC<1>{}); stage(IC<1>{}, IC<0>{});
  STAMP(2, 3);
#pragma unroll
  for (int r = 0; r < 2; ++r) {
    const int m = m0 + wid * 32 + r * 16 + (lane & 15);
    u16* pp = part + ((size_t)sp * S_ + m) * D_ + n0 + 4 * g;
#pragma unroll
    for (int n = 0; n < G2_NT; ++n) {
      u32x2 v = {pack2(acc[r][n][0], acc[r][n][1]), pack2(acc[r][n][2], acc[r][n][3])};
      ST_NT(reinterpret_cast<u32x2*>(pp + n * 16), v);
    }
  }
  asm volatile("s_waitcnt vmcnt(0)" ::: "memory");
  STAMP(2, 4);
}

__global__ __launch_bounds__(512, 2) void k3_final(const u16* __restrict__ of, const u16* __restrict__ wo,
                                                   const u16* __restrict__ part, const u16* __restrict__ b2,
                                                   const u16* __restrict__ x, u16* __restrict__ out) {
  __shared__ __attribute__((aligned(16))) char smem[576 * 128];
  const int tid = threadIdx.x, wid = tid >> 6, lane = tid & 63;
  const int bid = blockIdx.x;
  const int xcd = bid & 7, loc = bid >> 3;
  const int mt = xcd * 2 + loc / 9, nt = loc % 9;
  const int m0 = mt * 128, n0 = nt * 64;
  const int g = lane >> 4, q = (lane & 15) >> 2, p = lane & 3;
  const int wr = wid >> 1, wc = wid & 1;
  STAMP(3, 0);
#pragma unroll
  for (int i = 0; i < 9; ++i) {
    const int row = (wid * 9 + i) * 8 + (lane >> 3);
    const int f = ((row >> 1) & 1) | (((row >> 3) & 1) << 1);
    const int ch = (lane & 7) ^ (f << 1);
    glds16(wo + (size_t)row * D_ + n0 + ch * 8, smem + (wid * 9 + i) * 1024 + lane * 16);
  }
  bf16x8 an[2][18];
#pragma unroll
  for (int ks = 0; ks < 18; ++ks)
#pragma unroll
    for (int r = 0; r < 2; ++r)
      gld16_asm(an[r][ks], of + ((size_t)(((m0 >> 4) + wr * 2 + r) * 18 + ks) * 64 + lane) * 8);
  u32x2 py[2][2][4];
  u32x2 xx[2][2], bb[2];
#pragma unroll
  for (int r = 0; r < 2; ++r) {
    const int m = m0 + wr * 32 + r * 16 + (lane & 15);
#pragma unroll
    for (int n = 0; n < 2; ++n) {
      const int ncol = n0 + wc * 32 + n * 16 + 4 * g;
#pragma unroll
      for (int sI = 0; sI < SPLITK_; ++sI)
        asm volatile("global_load_dwordx2 %0, %1, off" : "=v"(py[r][n][sI]) : "v"(part + ((size_t)sI * S_ + m) * D_ + ncol) : "memory");
      asm volatile("global_load_dwordx2 %0, %1, off" : "=v"(xx[r][n]) : "v"(x + (size_t)m * D_ + ncol) : "memory");
    }
  }
#pragma unroll
  for (int n = 0; n < 2; ++n)
    asm volatile("global_load_dwordx2 %0, %1, off" : "=v"(bb[n]) : "v"(b2 + n0 + wc * 32 + n * 16 + 4 * g) : "memory");
  unsigned ad[2][2];
#pragma unroll
  for (int t = 0; t < 2; ++t) {
    const int row = 8 * g + 4 * t + q;
    const int f = ((row >> 1) & 1) | (((row >> 3) & 1) << 1);
#pragma unroll
    for (int n = 0; n < 2; ++n) ad[n][t] = lds_addr(smem) + row * 128 + (((wc * 2 + n) ^ f) * 32) + p * 8;
  }
  f32x4 acc[2][2];
#pragma unroll
  for (int r = 0; r < 2; ++r)
#pragma unroll
    for (int n = 0; n < 2; ++n) acc[r][n] = (f32x4){0.f, 0.f, 0.f, 0.f};
  STAMP(3, 1);
  wait_vm<22>();
  BAR();
  STAMP(3, 2);
#pragma unroll
  for (int ks = 0; ks < 18; ++ks) { pin(an[0][ks]); pin(an[1][ks]); }
  auto grp = [&](auto KB) {
    constexpr int kb = decltype(KB)::value;
    WF w[6][2];
#pragma unroll
    for (int k = 0; k < 6; ++k)
#pragma unroll
      for (int n = 0; n < 2; ++n) {
        if (k == 0) { tr_rd<0 * 4096>(w[0][n].lo, ad[n][0] + kb * 4096); tr_rd<0 * 4096>(w[0][n].hi, ad[n][1] + kb * 4096); }
        if (k == 1) { tr_rd<1 * 4096>(w[1][n].lo, ad[n][0] + kb * 4096); tr_rd<1 * 4096>(w[1][n].hi, ad[n][1] + kb * 4096); }
        if (k == 2) { tr_rd<2 * 4096>(w[2][n].lo, ad[n][0] + kb * 4096); tr_rd<2 * 4096>(w[2][n].hi, ad[n][1] + kb * 4096); }
        if (k == 3) { tr_rd<3 * 4096>(w[3][n].lo, ad[n][0] + kb * 4096); tr_rd<3 * 4096>(w[3][n].hi, ad[n][1] + kb * 4096); }
        if (k == 4) { tr_rd<4 * 4096>(w[4][n].lo, ad[n][0] + kb * 4096); tr_rd<4 * 4096>(w[4][n].hi, ad[n][1] + kb * 4096); }
        if (k == 5) { tr_rd<5 * 4096>(w[5][n].lo, ad[n][0] + kb * 4096); tr_rd<5 * 4096>(w[5][n].hi, ad[n][1] + kb * 4096); }
      }
    wait_lgkm<12>();
#pragma unroll
    for (int k = 0; k < 3; ++k)
#pragma unroll
      for (int n = 0; n < 2; ++n) pin(w[k][n]);
#pragma unroll
    for (int k = 0; k < 3; ++k)
#pragma unroll
      for (int n = 0; n < 2; ++n)
#pragma unroll
        for (int r = 0; r < 2; ++r)
          acc[r][n] = __builtin_amdgcn_mfma_f32_16x16x32_bf16(cat(w[k][n]), an[r][kb + k], acc[r][n], 0, 0, 0);
    wait_lgkm<0>();
#pragma unroll
    for (int k = 3; k < 6; ++k)
#pragma unroll
      for (int n = 0; n < 2; ++n) pin(w[k][n]);
#pragma unroll
    for (int k = 3; k < 6; ++k)
#pragma unroll
      for (int n = 0; n < 2; ++n)
#pragma unroll
        for (int r = 0; r < 2; ++r)
          acc[r][n] = __builtin_amdgcn_mfma_f32_16x16x32_bf16(cat(w[k][n]), an[r][kb + k], acc[r][n], 0, 0, 0);
  };
  grp(IC<0>{}); grp(IC<6>{}); grp(IC<12>{});
  wait_vm<0>();
  STAMP(3, 3);
#pragma unroll
  for (int n = 0; n < 2; ++n) asm volatile("" : "+v"(bb[n]));
#pragma unroll
  for (int r = 0; r < 2; ++r) {
    const int m = m0 + wr * 32 + r * 16 + (lane & 15);
#pragma unroll
    for (int n = 0; n < 2; ++n) {
      const int ncol = n0 + wc * 32 + n * 16 + 4 * g;
      asm volatile("" : "+v"(xx[r][n]));
#pragma unroll
      for (int sI = 0; sI < SPLITK_; ++sI) asm volatile("" : "+v"(py[r][n][sI]));
      f32x4 y = {0.f, 0.f, 0.f, 0.f};
#pragma unroll
      for (int sI = 0; sI < SPLITK_; ++sI) {
        y[0] += lo16(py[r][n][sI][0]); y[1] += hi16(py[r][n][sI][0]);
        y[2] += lo16(py[r][n][sI][1]); y[3] += hi16(py[r][n][sI][1]);
      }
      float bv[4] = {lo16(bb[n][0]), hi16(bb[n][0]), lo16(bb[n][1]), hi16(bb[n][1])};
      float xv[4] = {lo16(xx[r][n][0]), hi16(xx[r][n][0]), lo16(xx[r][n][1]), hi16(xx[r][n][1])};
      float o[4];
#pragma unroll
      for (int j = 0; j < 4; ++j) {
        float yy = y[j] + bv[j];
        float gg = yy / (1.f + __expf(-yy));
        o[j] = xv[j] + acc[r][n][j] * gg;
      }
      u32x2 v = {pack2(o[0], o[1]), pack2(o[2], o[3])};
      *reinterpret_cast<u32x2*>(out + (size_t)m * D_ + ncol) = v;
    }
  }
  STAMP(3, 4);
}

extern "C" void kernel_launch(void* const* d_in, const int* in_sizes, int n_in, void* d_out, int out_size, void* d_ws,
                              size_t ws_size, hipStream_t stream) {
  const u16* x = (const u16*)d_in[0];
  const u16* wqkv = (const u16*)d_in[1];
  const u16* wphi = (const u16*)d_in[2];
  const u16* wo = (const u16*)d_in[3];
  const u16* w1 = (const u16*)d_in[5];
  const u16* b1 = (const u16*)d_in[6];
  const u16* w2 = (const u16*)d_in[7];
  const u16* b2 = (const u16*)d_in[8];
  u16* out = (u16*)d_out;
  char* ws = (char*)d_ws;
  size_t off = 0;
  u16* xnf = (u16*)(ws + off); off += (size_t)S_ * D_ * 2;
  u16* wfold = (u16*)(ws + off); off += (size_t)D_ * NPH_ * 2;
  u16* qkv = (u16*)(ws + off); off += (size_t)96 * S_ * 24 * 2;
  u16* hf = (u16*)(ws + off); off += (size_t)S_ * F_ * 2;
  u16* part = (u16*)(ws + off); off += (size_t)SPLITK_ * S_ * D_ * 2;
  u16* of = (u16*)(ws + off); off += (size_t)S_ * D_ * 2;
  float* pkv = (float*)(ws + off); off += (size_t)256 * 1536 * 4;
  unsigned* cnt = (unsigned*)(ws + off); off += 32 * 32 * 4;

  k0_prep<<<512 + 192, 256, 0, stream>>>(x, wqkv, wphi, xnf, wfold, cnt);
  k1_gemm1<<<256, 512, 0, stream>>>(xnf, wfold, wqkv, w1, b1, qkv, hf);
  k2_gemm2<<<K2_HEADBLK + 256, 256, 0, stream>>>(hf, w2, part, qkv, of, pkv, cnt);
  k3_final<<<16 * 9, 512, 0, stream>>>(of, wo, part, b2, x, out);
}
```

```cpp
#include <hip/hip_runtime.h>
#include <hip/hip_bf16.h>
#include <stdint.h>

constexpr int S_ = 2048, D_ = 576, H_ = 32, DH_ = 18, F_ = 4608, NQKV_ = 1728;
constexpr int NPH_ = 1152;
constexpr int KPH_ = 40;
constexpr int KPHI_ = H_ * KPH_;
constexpr int SPLITK_ = 4;

typedef short bf16x8 __attribute__((ext_vector_type(8)));
typedef short s16x4 __attribute__((ext_vector_type(4)));
typedef float f32x4 __attribute__((ext_vector_type(4)));
typedef unsigned u32x4 __attribute__((ext_vector_type(4)));
typedef unsigned u32x2 __attribute__((ext_vector_type(2)));
typedef __attribute__((address_space(3))) s16x4* lds_s16x4_p;
typedef unsigned short u16;
template <int V> struct IC { static constexpr int value = V; };

#define STAMP(kid, i) do { } while (0)
#define ST_NT(p, v) (*(p) = (v))
#define WAIT_LGKM0() asm volatile("s_waitcnt lgkmcnt(0)" ::: "memory")
#define BAR() __builtin_amdgcn_s_barrier()

template <int N>
__device__ __forceinline__ void wait_vm() {
  static_assert(N >= 0 && N < 64, "vmcnt range");
  asm volatile("s_waitcnt vmcnt(%0)" ::"n"(N) : "memory");
}

__device__ __forceinline__ float bf2f(u16 v) { return __uint_as_float(((unsigned)v) << 16); }
__device__ __forceinline__ u16 f2bf(float f) {
  __hip_bfloat16 b = __float2bfloat16(f);
  return *reinterpret_cast<u16*>(&b);
}
__device__ __forceinline__ unsigned pack2(float lo, float hi) {
  return (unsigned)f2bf(lo) | ((unsigned)f2bf(hi) << 16);
}
__device__ __forceinline__ float rbf(float f) { return bf2f(f2bf(f)); }
__device__ __forceinline__ float lo16(unsigned u) { return __uint_as_float(u << 16); }
__device__ __forceinline__ float hi16(unsigned u) { return __uint_as_float(u & 0xffff0000u); }

__device__ __forceinline__ void glds16(const void* g, void* l) {
  __builtin_amdgcn_global_load_lds((const unsigned*)g, (unsigned*)l, 16, 0, 0);
}

__device__ __forceinline__ void l2_touch(const void* g, void* ldummy) {
  __builtin_amdgcn_global_load_lds((const unsigned*)g, (unsigned*)ldummy, 4, 0, 0);
}

__device__ __forceinline__ bf16x8 swap_halves_if(bf16x8 v, bool odd) {
  bf16x8 s = __builtin_shufflevector(v, v, 4, 5, 6, 7, 0, 1, 2, 3);
  return odd ? s : v;
}

__device__ __forceinline__ bf16x8 wfrag(const char* img, int o0, int o1, int off) {
  s16x4 a = __builtin_amdgcn_ds_read_tr16_b64_v4i16((lds_s16x4_p)(img + o0 + off));
  s16x4 b = __builtin_amdgcn_ds_read_tr16_b64_v4i16((lds_s16x4_p)(img + o1 + off));
  return __builtin_shufflevector(a, b, 0, 1, 2, 3, 4, 5, 6, 7);
}

struct WF { s16x4 lo, hi; };
template <int OFF>
__device__ __forceinline__ void tr_rd(s16x4& r, unsigned addr) {
  asm volatile("ds_read_b64_tr_b16 %0, %1 offset:%2" : "=v"(r) : "v"(addr), "n"(OFF) : "memory");
}
__device__ __forceinline__ void gld16_asm(bf16x8& r, const void* p) {
  asm volatile("global_load_dwordx4 %0, %1, off" : "=v"(r) : "v"(p) : "memory");
}
template <int N>
__device__ __forceinline__ void wait_lgkm() {
  static_assert(N >= 0 && N <= 15, "lgkmcnt range");
  asm volatile("s_waitcnt lgkmcnt(%0)" ::"n"(N) : "memory");
}
__device__ __forceinline__ void pin(WF& w) { asm volatile("" : "+v"(w.lo), "+v"(w.hi)); }
__device__ __forceinline__ void pin(bf16x8& w) { asm volatile("" : "+v"(w)); }
__device__ __forceinline__ bf16x8 cat(const WF& w) { return __builtin_shufflevector(w.lo, w.hi, 0, 1, 2, 3, 4, 5, 6, 7); }
__device__ __forceinline__ unsigned lds_addr(const void* p) {
  return (unsigned)(uintptr_t)(__attribute__((address_space(3))) const char*)p;
}

__global__ __launch_bounds__(256) void k0_prep(const u16* __restrict__ x, const u16* __restrict__ wqkv,
                                               const u16* __restrict__ wphi, u16* __restrict__ xnf,
                                               u16* __restrict__ wfold, unsigned* __restrict__ cnt) {
  __shared__ __attribute__((aligned(16))) float wph[18 * 20];
  const int tid = threadIdx.x, wid = tid >> 6, lane = tid & 63;
  const int bid = blockIdx.x;
  if (bid == 512 && tid < 32) cnt[tid * 32] = 0u;
  if (bid < 512) {
    const int row = bid * 4 + wid;
    const u16* xp = x + (size_t)row * D_;
    u32x4 a = *reinterpret_cast<const u32x4*>(xp + lane * 8);
    u32x4 b = {0u, 0u, 0u, 0u};
    if (lane < 8) b = *reinterpret_cast<const u32x4*>(xp + 512 + lane * 8);
    float v[16];
#pragma unroll
    for (int j = 0; j < 4; ++j) {
      v[2 * j] = lo16(a[j]); v[2 * j + 1] = hi16(a[j]);
      v[8 + 2 * j] = lo16(b[j]); v[8 + 2 * j + 1] = hi16(b[j]);
    }
    float s = 0.f;
#pragma unroll
    for (int j = 0; j < 16; ++j) s += v[j];
#pragma unroll
    for (int o = 32; o >= 1; o >>= 1) s += __shfl_xor(s, o);
    const float mean = s * (1.f / D_);
    float ss = 0.f;
#pragma unroll
    for (int j = 0; j < 8; ++j) { float d = v[j] - mean; ss += d * d; }
    if (lane < 8) {
#pragma unroll
      for (int j = 8; j < 16; ++j) { float d = v[j] - mean; ss += d * d; }
    }
#pragma unroll
    for (int o = 32; o >= 1; o >>= 1) ss += __shfl_xor(ss, o);
    const float rstd = rsqrtf(ss * (1.f / D_) + 1e-5f);
    u32x4 oa, ob;
#pragma unroll
    for (int j = 0; j < 4; ++j) {
      oa[j] = pack2((v[2 * j] - mean) * rstd, (v[2 * j + 1] - mean) * rstd);
      ob[j] = pack2((v[8 + 2 * j] - mean) * rstd, (v[8 + 2 * j + 1] - mean) * rstd);
    }
    {
      const int c = lane;
      u16* dst = xnf + ((size_t)((row >> 4) * 18 + (c >> 2)) * 64 + (c & 3) * 16 + (row & 15)) * 8;
      *reinterpret_cast<u32x4*>(dst) = oa;
    }
    if (lane < 8) {
      const int c = 64 + lane;
      u16* dst = xnf + ((size_t)((row >> 4) * 18 + (c >> 2)) * 64 + (c & 3) * 16 + (row & 15)) * 8;
      *reinterpret_cast<u32x4*>(dst) = ob;
    }
    return;
  }
  {
    const int fb = bid - 512;
    const int hh = fb / 3, k = (fb % 3) * 192 + tid;
    const int h = hh & 31;
    for (int i = tid; i < 18 * 20; i += 256) {
      const int d = i / 20, f = i % 20;
      wph[i] = (f < 18) ? bf2f(wphi[h * 324 + d * 18 + f]) : 0.f;
    }
    unsigned wv[9];
    if (tid < 192) {
      const u16* wp = wqkv + (size_t)k * NQKV_ + hh * 18;
#pragma unroll
      for (int d = 0; d < 9; ++d) wv[d] = *reinterpret_cast<const unsigned*>(wp + 2 * d);
    }
    __syncthreads();
    if (tid < 192) {
      float acc[20];
#pragma unroll
      for (int f = 0; f < 20; ++f) acc[f] = 0.f;
#pragma unroll
      for (int d = 0; d < 18; ++d) {
        const float w = (d & 1) ? hi16(wv[d >> 1]) : lo16(wv[d >> 1]);
#pragma unroll
        for (int f4 = 0; f4 < 5; ++f4) {
          f32x4 pw = *reinterpret_cast<const f32x4*>(wph + d * 20 + f4 * 4);
#pragma unroll
          for (int j = 0; j < 4; ++j) acc[f4 * 4 + j] += w * pw[j];
        }
      }
      u16* op = wfold + (size_t)k * NPH_ + hh * 18;
#pragma unroll
      for (int f = 0; f < 9; ++f) *reinterpret_cast<unsigned*>(op + 2 * f) = pack2(acc[2 * f], acc[2 * f + 1]);
    }
  }
}

constexpr int G1_BUFB = 192 * 128;
constexpr int G1_RING = 4;
constexpr int G1_XOFF = G1_RING * G1_BUFB;
constexpr int G1_BOFF = G1_XOFF + 576 * 32;
constexpr int G1_SMEM = G1_BOFF + 1024;
constexpr int G1_NST = 8;

template <int BASE, int I = 0>
__device__ __forceinline__ void g1_reads(WF (&w)[4], const unsigned (&ad)[4][2]) {
  if constexpr (I < 4) {
    if constexpr (BASE >= 49152) {
      tr_rd<BASE - 49152>(w[I].lo, ad[I][0] + 49152u);
      tr_rd<BASE - 49152>(w[I].hi, ad[I][1] + 49152u);
    } else {
      tr_rd<BASE>(w[I].lo, ad[I][0]);
      tr_rd<BASE>(w[I].hi, ad[I][1]);
    }
    g1_reads<BASE, I + 1>(w, ad);
  }
}
template <int KS>
__device__ __forceinline__ void g1_mma(WF (&w)[4], const bf16x8 (&xa)[2][18], f32x4 (&acc)[2][4]) {
#pragma unroll
  for (int i = 0; i < 4; ++i) pin(w[i]);
#pragma unroll
  for (int i = 0; i < 4; ++i)
#pragma unroll
    for (int r = 0; r < 2; ++r)
      acc[r][i] = __builtin_amdgcn_mfma_f32_16x16x32_bf16(cat(w[i]), xa[r][KS], acc[r][i], 0, 0, 0);
}

__device__ __forceinline__ float gelu_f(float v) {
  constexpr float c1 = -2.f * 0.7978845608028654f * 1.4426950408889634f;
  constexpr float c3 = c1 * 0.044715f;
  const float t = v * v;
  const float e = __builtin_amdgcn_exp2f(v * fmaf(t, c3, c1));
  return v * __builtin_amdgcn_rcpf(1.f + e);
}

typedef float f32x2 __attribute__((ext_vector_type(2)));
__device__ __forceinline__ unsigned cvt_pk(float lo, float hi) {
  unsigned r;
  asm("v_cvt_pk_bf16_f32 %0, %1, %2" : "=v"(r) : "v"(lo), "v"(hi));
  return r;
}
__device__ __forceinline__ unsigned gelu2_pk(f32x2 v) {
  constexpr float c1 = -2.f * 0.7978845608028654f * 1.4426950408889634f;
  constexpr float c3 = c1 * 0.044715f;
  const f32x2 t = v * v;
  const f32x2 pp = t * (f32x2){c3, c3} + (f32x2){c1, c1};
  const f32x2 u = v * pp;
  f32x2 e = {__builtin_amdgcn_exp2f(u[0]), __builtin_amdgcn_exp2f(u[1])};
  e = e + (f32x2){1.f, 1.f};
  const f32x2 r = {__builtin_amdgcn_rcpf(e[0]), __builtin_amdgcn_rcpf(e[1])};
  const f32x2 o = v * r;
  return cvt_pk(o[0], o[1]);
}

__device__ __forceinline__ void g1_store_tile(const f32x4& a, int tile, int mrow, int g, const u16* bias4,
                                              u16* __restrict__ qkv, u16* __restrict__ hf) {
  if (tile < NQKV_ / 16) {
    const int col = tile * 16 + 4 * g;
    const int hh = col / 18, f = col - hh * 18;
    u16* dst = qkv + ((size_t)hh * S_ + mrow) * 24 + f;
    const unsigned v0 = cvt_pk(a[0], a[1]), v1 = cvt_pk(a[2], a[3]);
    if (f != 16) {
      u32x2 v = {v0, v1};
      *reinterpret_cast<u32x2*>(dst) = v;
    } else {
      *reinterpret_cast<unsigned*>(dst) = v0;
      *reinterpret_cast<unsigned*>(qkv + ((size_t)(hh + 1) * S_ + mrow) * 24) = v1;
    }
  } else {
    u32x2 bb = *reinterpret_cast<const u32x2*>(bias4);
    float o[4];
    o[0] = gelu_f(a[0] + lo16(bb[0]));
    o[1] = gelu_f(a[1] + hi16(bb[0]));
    o[2] = gelu_f(a[2] + lo16(bb[1]));
    o[3] = gelu_f(a[3] + hi16(bb[1]));
    u32x2 v = {pack2(o[0], o[1]), pack2(o[2], o[3])};
    const int T = tile - NQKV_ / 16;
    u16* dst = hf + ((size_t)((mrow >> 4) * 144 + (T >> 1)) * 64 + ((T & 1) * 2 + (g >> 1)) * 16 + (mrow & 15)) * 8 + (g & 1) * 4;
    *reinterpret_cast<u32x2*>(dst) = v;
  }
}

__device__ __forceinline__ void g1_addr(unsigned (&ad)[4][2], char* smem, int lane) {
  const int g = lane >> 4, q = (lane & 15) >> 2, p = lane & 3;
#pragma unroll
  for (int t = 0; t < 2; ++t) {
    const int row = 8 * g + 4 * t + q;
    const int f = ((row >> 1) & 1) | (((row >> 3) & 1) << 1);
#pragma unroll
    for (int n = 0; n < 4; ++n) ad[n][t] = lds_addr(smem) + row * 128 + ((n ^ f) * 32) + p * 8;
  }
}

struct G1W { const u16* src; int stride; };

__device__ __forceinline__ void g1_wsetup(G1W& w, int col, int wid, int lane, const u16* wfold, const u16* wqkv, const u16* w1) {
  const int row = wid * 8 + (lane >> 3);
  const int f = ((row >> 1) & 1) | (((row >> 3) & 1) << 1);
  const int ch = (lane & 7) ^ (f << 1);
  if (col < NPH_) { w.src = wfold + (size_t)row * NPH_ + col + ch * 8; w.stride = 64 * NPH_; }
  else if (col < NQKV_) { w.src = wqkv + (size_t)row * NQKV_ + col + ch * 8; w.stride = 64 * NQKV_; }
  else { w.src = w1 + (size_t)row * F_ + (col - NQKV_) + ch * 8; w.stride = 64 * F_; }
}
__device__ __forceinline__ void g1_wissue(G1W& w, char* buf, int wid, int lane) {
#pragma unroll
  for (int i = 0; i < 3; ++i) {
    glds16(w.src, buf + i * 8192 + wid * 1024 + lane * 16);
    w.src += w.stride;
  }
}

template <int U>
__device__ __forceinline__ void g1_unit(G1W& wst, int unit0, const u16* wfold, const u16* wqkv,
                                        const u16* w1, char* smem, bf16x8 (&xa)[2][18], int m0,
                                        u16* __restrict__ qkv, u16* __restrict__ hf, WF (&wA)[4]) {
  int tz = threadIdx.x;
  asm volatile("" : "+v"(tz));
  const int wid = __builtin_amdgcn_readfirstlane(tz >> 6), lane = tz & 63;
  const int g = lane >> 4;
  unsigned ad[4][2];
  g1_addr(ad, smem, lane);
  f32x4 acc[2][4];
#pragma unroll
  for (int r = 0; r < 2; ++r)
#pragma unroll
    for (int n = 0; n < 4; ++n) acc[r][n] = (f32x4){0.f, 0.f, 0.f, 0.f};

  auto stage = [&](auto KC) {
    constexpr int kc = decltype(KC)::value;
    constexpr int s = U * 3 + kc;
    constexpr int CUR = (s % G1_RING) * G1_BUFB;
    if constexpr (U == 0) {
#pragma unroll
      for (int k = 0; k < 6; ++k) { pin(xa[0][kc * 6 + k]); pin(xa[1][kc * 6 + k]); }
    }
    WF wB[4];
    g1_reads<CUR + 1 * 4096>(wB, ad); wait_lgkm<8>(); g1_mma<kc * 6 + 0>(wA, xa, acc);
    g1_reads<CUR + 2 * 4096>(wA, ad); wait_lgkm<8>(); g1_mma<kc * 6 + 1>(wB, xa, acc);
    g1_reads<CUR + 3 * 4096>(wB, ad); wait_lgkm<8>(); g1_mma<kc * 6 + 2>(wA, xa, acc);
    g1_reads<CUR + 4 * 4096>(wA, ad); wait_lgkm<8>(); g1_mma<kc * 6 + 3>(wB, xa, acc);
    g1_reads<CUR + 5 * 4096>(wB, ad); wait_lgkm<8>(); g1_mma<kc * 6 + 4>(wA, xa, acc);
    if constexpr (s < 8) {
      constexpr int nl = (s == 0) ? 12 : ((s + 2 <= 8) ? 3 : 0);
      constexpr int nst = (U > 0 && kc <= 1) ? G1_NST : 0;
      wait_vm<nl + nst>();
      BAR();
      if constexpr (s + 3 <= 8) {
        constexpr int sn = s + 3;
        if constexpr (sn % 3 == 0) g1_wsetup(wst, (unit0 + sn / 3) * 64, wid, lane, wfold, wqkv, w1);
        g1_wissue(wst, smem + (sn % G1_RING) * G1_BUFB, wid, lane);
      }
      g1_reads<((s + 1) % G1_RING) * G1_BUFB>(wA, ad);
      wait_lgkm<8>();
    } else {
      wait_lgkm<0>();
    }
    g1_mma<kc * 6 + 5>(wB, xa, acc);
  };
  stage(IC<0>{}); stage(IC<1>{}); stage(IC<2>{});
  const u16* bias_lds = reinterpret_cast<const u16*>(smem + G1_BOFF);
  if ((unit0 + U) * 64 < NQKV_) {
#pragma unroll
    for (int n = 0; n < 4; ++n) {
      const int tile = (unit0 + U) * 4 + n;
#pragma unroll
      for (int r = 0; r < 2; ++r)
        g1_store_tile(acc[r][n], tile, m0 + wid * 32 + r * 16 + (lane & 15), g, bias_lds, qkv, hf);
    }
  } else {
    u32x2 bb[4];
#pragma unroll
    for (int n = 0; n < 4; ++n) bb[n] = *reinterpret_cast<const u32x2*>(bias_lds + (U * 4 + n) * 16 + 4 * g);
    const int T0 = (unit0 + U) * 4 - NQKV_ / 16;
#pragma unroll
    for (int r = 0; r < 2; ++r) {
      const int mrow = m0 + wid * 32 + r * 16 + (lane & 15);
      u16* dst = hf + ((size_t)((mrow >> 4) * 144 + (T0 >> 1)) * 64 + (g >> 1) * 16 + (mrow & 15)) * 8 + (g & 1) * 4;
#pragma unroll
      for (int n = 0; n < 4; ++n) {
        const f32x2 v0 = (f32x2){acc[r][n][0], acc[r][n][1]} + (f32x2){lo16(bb[n][0]), hi16(bb[n][0])};
        const f32x2 v1 = (f32x2){acc[r][n][2], acc[r][n][3]} + (f32x2){lo16(bb[n][1]), hi16(bb[n][1])};
        u32x2 v = {gelu2_pk(v0), gelu2_pk(v1)};
        ST_NT(reinterpret_cast<u32x2*>(dst + (n >> 1) * 512 + (n & 1) * 256), v);
      }
    }
  }
}

__global__ __launch_bounds__(512, 2) void k1_gemm1(const u16* __restrict__ xnf, const u16* __restrict__ wfold,
                                                   const u16* __restrict__ wqkv, const u16* __restrict__ w1,
                                                   const u16* __restrict__ b1, u16* __restrict__ qkv,
                                                   u16* __restrict__ hf) {
  __shared__ __attribute__((aligned(16))) char smem[G1_SMEM];
  const int tid = threadIdx.x, wid = tid >> 6, lane = tid & 63;
  const int bid = blockIdx.x;
  const int mt = bid >> 5, grp = bid & 31;
  const int m0 = mt * 256;
  const int unit0 = grp * 3;
  const bool extra = grp >= 20;
  const int xcol = 6144 + (extra ? (grp - 20) : 0) * 16;
  STAMP(1, 0);
  G1W wst;
  g1_wsetup(wst, unit0 * 64, wid, lane, wfold, wqkv, w1);
  g1_wissue(wst, smem + 0 * G1_BUFB, wid, lane);
  g1_wissue(wst, smem + 1 * G1_BUFB, wid, lane);
  g1_wissue(wst, smem + 2 * G1_BUFB, wid, lane);
#pragma unroll
  for (int i = 0; i < 3; ++i) {
    int ins = wid + 8 * i;
    if (ins >= 18) ins -= 18;
    const int c = ins * 64 + lane;
    const int pr = c >> 1, half = c & 1;
    const int k = (pr & ~0xC) | ((pr & 4) << 1) | ((pr & 8) >> 1);
    glds16(w1 + (size_t)k * F_ + (xcol - NQKV_) + half * 8, smem + G1_XOFF + c * 16);
  }
  {
    const int e = (wid & 1) * 64 + lane;
    int col = (e < 96) ? (unit0 * 64 + 2 * e - NQKV_) : (xcol - NQKV_ + 2 * (e - 96));
    if (col < 0 || e >= 104) col = 0;
    __builtin_amdgcn_global_load_lds((const unsigned*)(b1 + col), (unsigned*)(smem + G1_BOFF + e * 4), 4, 0, 0);
  }
  bf16x8 xa[2][18];
#pragma unroll
  for (int ks = 0; ks < 18; ++ks)
#pragma unroll
    for (int r = 0; r < 2; ++r)
      gld16_asm(xa[r][ks], xnf + ((size_t)(((m0 >> 4) + wid * 2 + r) * 18 + ks) * 64 + lane) * 8);
  wait_vm<24>();
  BAR();
  WF wA[4];
  {
    unsigned ad[4][2];
    g1_addr(ad, smem, lane);
    g1_reads<0>(wA, ad);
  }
  STAMP(1, 1);
  g1_unit<0>(wst, unit0, wfold, wqkv, w1, smem, xa, m0, qkv, hf, wA);
  STAMP(1, 2);
  g1_unit<1>(wst, unit0, wfold, wqkv, w1, smem, xa, m0, qkv, hf, wA);
  STAMP(1, 3);
  g1_unit<2>(wst, unit0, wfold, wqkv, w1, smem, xa, m0, qkv, hf, wA);
  STAMP(1, 4);
  if (extra) {
    int tz = threadIdx.x;
    asm volatile("" : "+v"(tz));
    const int wid2 = __builtin_amdgcn_readfirstlane(tz >> 6), lane2 = tz & 63;
    const int g = lane2 >> 4, q = (lane2 & 15) >> 2, p = lane2 & 3;
    unsigned ad[2];
#pragma unroll
    for (int t = 0; t < 2; ++t) {
      const int k = 8 * g + 4 * t + q;
      const int pr = (k & ~0xC) | ((k & 4) << 1) | ((k & 8) >> 1);
      ad[t] = lds_addr(smem) + G1_XOFF + pr * 32 + p * 8;
    }
    f32x4 acc[2] = {(f32x4){0.f, 0.f, 0.f, 0.f}, (f32x4){0.f, 0.f, 0.f, 0.f}};
#pragma unroll
    for (int kb = 0; kb < 18; kb += 6) {
      WF w[6];
      tr_rd<0 * 1024>(w[0].lo, ad[0] + kb * 1024); tr_rd<0 * 1024>(w[0].hi, ad[1] + kb * 1024);
      tr_rd<1 * 1024>(w[1].lo, ad[0] + kb * 1024); tr_rd<1 * 1024>(w[1].hi, ad[1] + kb * 1024);
      tr_rd<2 * 1024>(w[2].lo, ad[0] + kb * 1024); tr_rd<2 * 1024>(w[2].hi, ad[1] + kb * 1024);
      tr_rd<3 * 1024>(w[3].lo, ad[0] + kb * 1024); tr_rd<3 * 1024>(w[3].hi, ad[1] + kb * 1024);
      tr_rd<4 * 1024>(w[4].lo, ad[0] + kb * 1024); tr_rd<4 * 1024>(w[4].hi, ad[1] + kb * 1024);
      tr_rd<5 * 1024>(w[5].lo, ad[0] + kb * 1024); tr_rd<5 * 1024>(w[5].hi, ad[1] + kb * 1024);
      wait_lgkm<0>();
#pragma unroll
      for (int i = 0; i < 6; ++i) pin(w[i]);
#pragma unroll
      for (int i = 0; i < 6; ++i)
#pragma unroll
        for (int r = 0; r < 2; ++r)
          acc[r] = __builtin_amdgcn_mfma_f32_16x16x32_bf16(cat(w[i]), xa[r][kb + i], acc[r], 0, 0, 0);
    }
    const u16* bias_lds = reinterpret_cast<const u16*>(smem + G1_BOFF);
#pragma unroll
    for (int r = 0; r < 2; ++r)
      g1_store_tile(acc[r], xcol / 16, m0 + wid2 * 32 + r * 16 + (lane2 & 15), g, bias_lds + 192 + 4 * g, qkv, hf);
  }
}

constexpr int G2_NT = 9, G2_RB = G2_NT * 32;
constexpr int G2_BUFB = 64 * G2_RB;
constexpr int G2_RING = 4;
constexpr int G2_STAGES = 18;

__device__ __forceinline__ void softmax_pm(const float (&pk)[18], float (&ep)[18], float (&en)[18]) {
  float mx = pk[0], mn = pk[0];
#pragma unroll
  for (int f = 1; f < 18; ++f) {
    mx = fmaxf(mx, pk[f]);
    mn = fminf(mn, pk[f]);
  }
  float sp = 0.f, sn = 0.f;
#pragma unroll
  for (int f = 0; f < 18; ++f) {
    ep[f] = __expf(pk[f] - mx);
    en[f] = __expf(mn - pk[f]);
    sp += ep[f];
    sn += en[f];
  }
  const float rp = 1.f / sp, rn = 1.f / sn;
#pragma unroll
  for (int f = 0; f < 18; ++f) {
    ep[f] *= rp;
    en[f] *= rn;
  }
}

__device__ void head_block(int hb, const u16* __restrict__ qkv, u16* __restrict__ of, float* pkv, unsigned* cnt, char* smem) {
  u16* phiT = reinterpret_cast<u16*>(smem);
  u16* vT = reinterpret_cast<u16*>(smem + 25344);
  float* kvs = reinterpret_cast<float*>(smem + 25344 + 16896);
  const int h = hb >> 3, c8 = hb & 7;
  const int tid = threadIdx.x, wid = tid >> 6, lane = tid & 63;
  const int row = c8 * 256 + tid;
  u32x4 kr[3], vr[3], qr[3];
  {
    const u32x4* kp = reinterpret_cast<const u32x4*>(qkv + ((size_t)(32 + h) * S_ + row) * 24);
    const u32x4* vp = reinterpret_cast<const u32x4*>(qkv + ((size_t)(64 + h) * S_ + row) * 24);
    const u32x4* qp = reinterpret_cast<const u32x4*>(qkv + ((size_t)h * S_ + row) * 24);
#pragma unroll
    for (int d = 0; d < 3; ++d) { kr[d] = kp[d]; vr[d] = vp[d]; qr[d] = qp[d]; }
  }
  for (int i = tid; i < 12 * 264; i += 256) phiT[36 * 264 + i] = 0;
  for (int i = tid; i < 14 * 264; i += 256) vT[18 * 264 + i] = (i < 264) ? (u16)0x3f80 : (u16)0;
  {
    float pk[18];
#pragma unroll
    for (int d = 0; d < 9; ++d) {
      pk[2 * d] = lo16(kr[d >> 2][d & 3]);
      pk[2 * d + 1] = hi16(kr[d >> 2][d & 3]);
    }
    float ep[18], en[18];
    softmax_pm(pk, ep, en);
#pragma unroll
    for (int f = 0; f < 18; ++f) {
      phiT[f * 264 + tid] = f2bf(ep[f]);
      phiT[(18 + f) * 264 + tid] = f2bf(en[f]);
      const unsigned w = vr[(f >> 1) >> 2][(f >> 1) & 3];
      vT[f * 264 + tid] = (u16)((f & 1) ? (w >> 16) : (w & 0xffff));
    }
  }
  __syncthreads();
  {
    f32x4 acc[3][2];
#pragma unroll
    for (int a = 0; a < 3; ++a)
#pragma unroll
      for (int b = 0; b < 2; ++b) acc[a][b] = (f32x4){0.f, 0.f, 0.f, 0.f};
#pragma unroll
    for (int ks = 0; ks < 2; ++ks) {
      const int s0 = wid * 64 + ks * 32 + 8 * (lane >> 4);
      bf16x8 bv[2];
#pragma unroll
      for (int b = 0; b < 2; ++b) bv[b] = *reinterpret_cast<const bf16x8*>(vT + (b * 16 + (lane & 15)) * 264 + s0);
#pragma unroll
      for (int a = 0; a < 3; ++a) {
        bf16x8 av = *reinterpret_cast<const bf16x8*>(phiT + (a * 16 + (lane & 15)) * 264 + s0);
#pragma unroll
        for (int b = 0; b < 2; ++b) acc[a][b] = __builtin_amdgcn_mfma_f32_16x16x32_bf16(av, bv[b], acc[a][b], 0, 0, 0);
      }
    }
#pragma unroll
    for (int a = 0; a < 3; ++a)
#pragma unroll
      for (int b = 0; b < 2; ++b)
#pragma unroll
        for (int j = 0; j < 4; ++j)
          kvs[(wid * 48 + a * 16 + (lane >> 4) * 4 + j) * 32 + b * 16 + (lane & 15)] = acc[a][b][j];
  }
  __syncthreads();
  float* mypart = pkv + (size_t)(h * 8 + c8) * 1536;
#pragma unroll
  for (int it = 0; it < 6; ++it) {
    const int i = it * 256 + tid;
    const float v = kvs[i] + kvs[1536 + i] + kvs[2 * 1536 + i] + kvs[3 * 1536 + i];
    __hip_atomic_store(mypart + i, v, __ATOMIC_RELAXED, __HIP_MEMORY_SCOPE_AGENT);
  }
  asm volatile("s_waitcnt vmcnt(0)" ::: "memory");
  __syncthreads();
  if (tid == 0) __hip_atomic_fetch_add(cnt + h * 32, 1u, __ATOMIC_RELAXED, __HIP_MEMORY_SCOPE_AGENT);
  float ep[18], en[18];
  {
    float pk[18];
#pragma unroll
    for (int d = 0; d < 9; ++d) {
      pk[2 * d] = lo16(qr[d >> 2][d & 3]);
      pk[2 * d + 1] = hi16(qr[d >> 2][d & 3]);
    }
    softmax_pm(pk, ep, en);
#pragma unroll
    for (int f = 0; f < 18; ++f) { ep[f] = rbf(ep[f]); en[f] = rbf(en[f]); }
  }
  if (tid == 0) {
    unsigned spins = 0;
    while (__hip_atomic_load(cnt + h * 32, __ATOMIC_RELAXED, __HIP_MEMORY_SCOPE_AGENT) < 8u) {
      __builtin_amdgcn_s_sleep(1);
      if (++spins > (1u << 24)) break;
    }
  }
  __syncthreads();
  u16* kvT = reinterpret_cast<u16*>(smem);
  u16* phiR = reinterpret_cast<u16*>(smem + 4608);
  {
    float* hp = pkv + (size_t)h * 8 * 1536;
    float pv[8][8];
#pragma unroll
    for (int it = 0; it < 8; ++it) {
      const int i = it * 256 + tid;
      const int d = i >> 6, f = i & 63;
      const bool ok = (d < 19) && (f < 36);
      const int src = ok ? (f * 32 + d) : 0;
#pragma unroll
      for (int c = 0; c < 8; ++c)
        pv[it][c] = (it < 5) ? __hip_atomic_load(hp + c * 1536 + src, __ATOMIC_RELAXED, __HIP_MEMORY_SCOPE_AGENT) : 0.f;
    }
    {
      unsigned o[32];
#pragma unroll
      for (int f = 0; f < 9; ++f) { o[f] = pack2(ep[2 * f], ep[2 * f + 1]); o[9 + f] = pack2(en[2 * f], en[2 * f + 1]); }
#pragma unroll
      for (int f = 18; f < 32; ++f) o[f] = 0u;
#pragma unroll
      for (int c = 0; c < 8; ++c) {
        u32x4 v = {o[4 * c], o[4 * c + 1], o[4 * c + 2], o[4 * c + 3]};
        *reinterpret_cast<u32x4*>(phiR + tid * 72 + c * 8) = v;
      }
    }
#pragma unroll
    for (int it = 0; it < 8; ++it) {
      const int i = it * 256 + tid;
      const int d = i >> 6, f = i & 63;
      float v = 0.f;
#pragma unroll
      for (int c = 0; c < 8; ++c) v += pv[it][c];
      kvT[d * 72 + f] = ((d < 19) && (f < 36)) ? f2bf(v) : (u16)0;
    }
  }
  __syncthreads();
  {
    const int g = lane >> 4;
    bf16x8 ka[2][2];
#pragma unroll
    for (int t = 0; t < 2; ++t)
#pragma unroll
      for (int ks = 0; ks < 2; ++ks)
        ka[t][ks] = *reinterpret_cast<const bf16x8*>(kvT + (t * 16 + (lane & 15)) * 72 + ks * 32 + 8 * g);
#pragma unroll
    for (int rt = 0; rt < 4; ++rt) {
      const int rl = wid * 64 + rt * 16 + (lane & 15);
      f32x4 c0 = {0.f, 0.f, 0.f, 0.f}, c1 = {0.f, 0.f, 0.f, 0.f};
#pragma unroll
      for (int ks = 0; ks < 2; ++ks) {
        const bf16x8 pb = *reinterpret_cast<const bf16x8*>(phiR + rl * 72 + ks * 32 + 8 * g);
        c0 = __builtin_amdgcn_mfma_f32_16x16x32_bf16(ka[0][ks], pb, c0, 0, 0, 0);
        c1 = __builtin_amdgcn_mfma_f32_16x16x32_bf16(ka[1][ks], pb, c1, 0, 0, 0);
      }
      const float den = __shfl(c1[2], lane & 15);
      const float rd = 1.f / (rbf(den) + 1e-6f);
      const int grow = c8 * 256 + rl;
      u16* obase = of + (size_t)(grow >> 4) * 18 * 512 + (grow & 15) * 8;
#pragma unroll
      for (int j2 = 0; j2 < 2; ++j2) {
        const int k = h * 18 + 4 * g + 2 * j2;
        *reinterpret_cast<unsigned*>(obase + (k >> 5) * 512 + ((k >> 3) & 3) * 128 + (k & 7)) =
            pack2(rbf(c0[2 * j2]) * rd, rbf(c0[2 * j2 + 1]) * rd);
      }
      if (g == 0) {
        const int k = h * 18 + 16;
        *reinterpret_cast<unsigned*>(obase + (k >> 5) * 512 + ((k >> 3) & 3) * 128 + (k & 7)) =
            pack2(rbf(c1[0]) * rd, rbf(c1[1]) * rd);
      }
    }
  }
}

template <int BASE, int N0, int CNT, int I = 0>
__device__ __forceinline__ void g2_wreads(WF (&w)[5], unsigned a0, unsigned a1) {
  if constexpr (I < CNT) {
    tr_rd<BASE + (N0 + I) * 32>(w[I].lo, a0);
    tr_rd<BASE + (N0 + I) * 32>(w[I].hi, a1);
    g2_wreads<BASE, N0, CNT, I + 1>(w, a0, a1);
  }
}
template <int N0, int CNT>
__device__ __forceinline__ void g2_mma(WF (&w)[5], const bf16x8 (&ac)[2], f32x4 (&acc)[2][G2_NT]) {
#pragma unroll
  for (int i = 0; i < CNT; ++i) pin(w[i]);
#pragma unroll
  for (int i = 0; i < CNT; ++i)
#pragma unroll
    for (int r = 0; r < 2; ++r)
      acc[r][N0 + i] = __builtin_amdgcn_mfma_f32_16x16x32_bf16(cat(w[i]), ac[r], acc[r][N0 + i], 0, 0, 0);
}
constexpr int K2_HEADBLK = 256;

__global__ __launch_bounds__(256, 2) void k2_gemm2(const u16* __restrict__ hf, const u16* __restrict__ w2,
                                                   u16* __restrict__ part, const u16* __restrict__ qkv,
                                                   u16* __restrict__ of, float* pkv, unsigned* cnt) {
  __shared__ __attribute__((aligned(16))) char smem[G2_RING * G2_BUFB];
  const int tid = threadIdx.x, wid = tid >> 6, lane = tid & 63;
  int bid = blockIdx.x;
  if (bid < K2_HEADBLK) { head_block(bid, qkv, of, pkv, cnt, smem); return; }
  bid -= K2_HEADBLK;
  STAMP(2, 0);
  const int xcd = bid & 7, loc = bid >> 3;
  const int sp = xcd >> 1, mt = (xcd & 1) * 8 + (loc >> 2), nt4 = loc & 3;
  const int m0 = mt * 128, n0 = nt4 * 144, k0 = sp * 1152;
  const int g = lane >> 4, q = (lane & 15) >> 2, p = lane & 3;
  const bool odd = g & 1;
  const unsigned wa0 = lds_addr(smem) + (8 * g + 4 * (g & 1) + q) * G2_RB + p * 8;
  const unsigned wa1 = lds_addr(smem) + (8 * g + 4 * (1 - (g & 1)) + q) * G2_RB + p * 8;
  f32x4 acc[2][G2_NT];
#pragma unroll
  for (int r = 0; r < 2; ++r)
#pragma unroll
    for (int n = 0; n < G2_NT; ++n) acc[r][n] = (f32x4){0.f, 0.f, 0.f, 0.f};
  const u16* wp[5];
  int wdst[5];
#pragma unroll
  for (int i = 0; i < 5; ++i) {
    int ins = wid + 4 * i;
    if (ins >= 18) ins -= 18;
    int c = ins * 64 + lane;
    int row = c / 18, cc = c % 18;
    wp[i] = w2 + (size_t)(k0 + row) * D_ + n0 + cc * 8;
    wdst[i] = c * 16;
  }
  const u16* ap[2];
#pragma unroll
  for (int r = 0; r < 2; ++r) ap[r] = hf + ((size_t)((m0 >> 4) + wid * 2 + r) * 144 + (k0 >> 5)) * 512 + lane * 8;
  bf16x8 an[4][2][2];
  auto issue = [&](auto SL) {
    constexpr int slot = decltype(SL)::value;
    char* buf = smem + slot * G2_BUFB;
#pragma unroll
    for (int i = 0; i < 5; ++i) { glds16(wp[i], buf + wdst[i]); wp[i] += 64 * D_; }
#pragma unroll
    for (int r = 0; r < 2; ++r) {
      gld16_asm(an[slot][r][0], ap[r]);
      gld16_asm(an[slot][r][1], ap[r] + 512);
      ap[r] += 1024;
    }
  };
  issue(IC<0>{});
  issue(IC<1>{});
  issue(IC<2>{});
  wait_vm<18>();
  BAR();
  STAMP(2, 1);
  WF wA[5], wB[5];
  g2_wreads<0, 0, 5>(wA, wa0, wa1);
  auto stage = [&](auto U, auto LAST) {
    constexpr int u = decltype(U)::value;
    constexpr int rem = decltype(LAST)::value;
    constexpr int BASE = u * G2_BUFB;
    bf16x8 c0[2], c1[2];
#pragma unroll
    for (int r = 0; r < 2; ++r) {
      pin(an[u][r][0]); pin(an[u][r][1]);
      c0[r] = swap_halves_if(an[u][r][0], odd);
      c1[r] = swap_halves_if(an[u][r][1], odd);
    }
    g2_wreads<BASE, 5, 4>(wB, wa0, wa1);
    wait_lgkm<8>();
    g2_mma<0, 5>(wA, c0, acc);
    g2_wreads<BASE + 32 * G2_RB, 0, 5>(wA, wa0, wa1);
    wait_lgkm<10>();
    g2_mma<5, 4>(wB, c0, acc);
    g2_wreads<BASE + 32 * G2_RB, 5, 4>(wB, wa0, wa1);
    wait_lgkm<8>();
    g2_mma<0, 5>(wA, c1, acc);
    if constexpr (rem >= 1) {
      if constexpr (rem >= 2) wait_vm<9>(); else wait_vm<0>();
      BAR();
      if constexpr (rem >= 3) issue(IC<(u + 3) & 3>{});
      g2_wreads<((u + 1) & 3) * G2_BUFB, 0, 5>(wA, wa0, wa1);
      wait_lgkm<10>();
    } else {
      wait_lgkm<0>();
    }
    g2_mma<5, 4>(wB, c1, acc);
  };
#pragma unroll 1
  for (int it = 0; it < 3; ++it) {
    stage(IC<0>{}, IC<3>{}); stage(IC<1>{}, IC<3>{}); stage(IC<2>{}, IC<3>{}); stage(IC<3>{}, IC<3>{});
  }
  STAMP(2, 2);
  stage(IC<0>{}, IC<3>{}); stage(IC<1>{}, IC<3>{}); stage(IC<2>{}, IC<3>{});
  stage(IC<3>{}, IC<2>{}); stage(IC<0>{}, IC<1>{}); stage(IC<1>{}, IC<0>{});
  STAMP(2, 3);
#pragma unroll
  for (int r = 0; r < 2; ++r) {
    const int m = m0 + wid * 32 + r * 16 + (lane & 15);
    u16* pp = part + ((size_t)sp * S_ + m) * D_ + n0 + 4 * g;
#pragma unroll
    for (int n = 0; n < G2_NT; ++n) {
      u32x2 v = {pack2(acc[r][n][0], acc[r][n][1]), pack2(acc[r][n][2], acc[r][n][3])};
      ST_NT(reinterpret_cast<u32x2*>(pp + n * 16), v);
    }
  }
  asm volatile("s_waitcnt vmcnt(0)" ::: "memory");
  STAMP(2, 4);
}

__global__ __launch_bounds__(512, 2) void k3_final(const u16* __restrict__ of, const u16* __restrict__ wo,
                                                   const u16* __restrict__ part, const u16* __restrict__ b2,
                                                   const u16* __restrict__ x, u16* __restrict__ out) {
  __shared__ __attribute__((aligned(16))) char smem[576 * 128];
  const int tid = threadIdx.x, wid = tid >> 6, lane = tid & 63;
  const int bid = blockIdx.x;
  const int xcd = bid & 7, loc = bid >> 3;
  const int mt = xcd * 2 + loc / 9, nt = loc % 9;
  const int m0 = mt * 128, n0 = nt * 64;
  const int g = lane >> 4, q = (lane & 15) >> 2, p = lane & 3;
  const int wr = wid >> 1, wc = wid & 1;
  STAMP(3, 0);
#pragma unroll
  for (int i = 0; i < 9; ++i) {
    const int row = (wid * 9 + i) * 8 + (lane >> 3);
    const int f = ((row >> 1) & 1) | (((row >> 3) & 1) << 1);
    const int ch = (lane & 7) ^ (f << 1);
    glds16(wo + (size_t)row * D_ + n0 + ch * 8, smem + (wid * 9 + i) * 1024 + lane * 16);
  }
  bf16x8 an[2][18];
#pragma unroll
  for (int ks = 0; ks < 18; ++ks)
#pragma unroll
    for (int r = 0; r < 2; ++r)
      gld16_asm(an[r][ks], of + ((size_t)(((m0 >> 4) + wr * 2 + r) * 18 + ks) * 64 + lane) * 8);
  u32x2 py[2][2][4];
  u32x2 xx[2][2], bb[2];
#pragma unroll
  for (int r = 0; r < 2; ++r) {
    const int m = m0 + wr * 32 + r * 16 + (lane & 15);
#pragma unroll
    for (int n = 0; n < 2; ++n) {
      const int ncol = n0 + wc * 32 + n * 16 + 4 * g;
#pragma unroll
      for (int sI = 0; sI < SPLITK_; ++sI)
        asm volatile("global_load_dwordx2 %0, %1, off" : "=v"(py[r][n][sI]) : "v"(part + ((size_t)sI * S_ + m) * D_ + ncol) : "memory");
      asm volatile("global_load_dwordx2 %0, %1, off" : "=v"(xx[r][n]) : "v"(x + (size_t)m * D_ + ncol) : "memory");
    }
  }
#pragma unroll
  for (int n = 0; n < 2; ++n)
    asm volatile("global_load_dwordx2 %0, %1, off" : "=v"(bb[n]) : "v"(b2 + n0 + wc * 32 + n * 16 + 4 * g) : "memory");
  unsigned ad[2][2];
#pragma unroll
  for (int t = 0; t < 2; ++t) {
    const int row = 8 * g + 4 * t + q;
    const int f = ((row >> 1) & 1) | (((row >> 3) & 1) << 1);
#pragma unroll
    for (int n = 0; n < 2; ++n) ad[n][t] = lds_addr(smem) + row * 128 + (((wc * 2 + n) ^ f) * 32) + p * 8;
  }
  f32x4 acc[2][2];
#pragma unroll
  for (int r = 0; r < 2; ++r)
#pragma unroll
    for (int n = 0; n < 2; ++n) acc[r][n] = (f32x4){0.f, 0.f, 0.f, 0.f};
  STAMP(3, 1);
  wait_vm<22>();
  BAR();
  STAMP(3, 2);
#pragma unroll
  for (int ks = 0; ks < 18; ++ks) { pin(an[0][ks]); pin(an[1][ks]); }
  auto grp = [&](auto KB) {
    constexpr int kb = decltype(KB)::value;
    WF w[6][2];
#pragma unroll
    for (int k = 0; k < 6; ++k)
#pragma unroll
      for (int n = 0; n < 2; ++n) {
        if (k == 0) { tr_rd<0 * 4096>(w[0][n].lo, ad[n][0] + kb * 4096); tr_rd<0 * 4096>(w[0][n].hi, ad[n][1] + kb * 4096); }
        if (k == 1) { tr_rd<1 * 4096>(w[1][n].lo, ad[n][0] + kb * 4096); tr_rd<1 * 4096>(w[1][n].hi, ad[n][1] + kb * 4096); }
        if (k == 2) { tr_rd<2 * 4096>(w[2][n].lo, ad[n][0] + kb * 4096); tr_rd<2 * 4096>(w[2][n].hi, ad[n][1] + kb * 4096); }
        if (k == 3) { tr_rd<3 * 4096>(w[3][n].lo, ad[n][0] + kb * 4096); tr_rd<3 * 4096>(w[3][n].hi, ad[n][1] + kb * 4096); }
        if (k == 4) { tr_rd<4 * 4096>(w[4][n].lo, ad[n][0] + kb * 4096); tr_rd<4 * 4096>(w[4][n].hi, ad[n][1] + kb * 4096); }
        if (k == 5) { tr_rd<5 * 4096>(w[5][n].lo, ad[n][0] + kb * 4096); tr_rd<5 * 4096>(w[5][n].hi, ad[n][1] + kb * 4096); }
      }
    wait_lgkm<12>();
#pragma unroll
    for (int k = 0; k < 3; ++k)
#pragma unroll
      for (int n = 0; n < 2; ++n) pin(w[k][n]);
#pragma unroll
    for (int k = 0; k < 3; ++k)
#pragma unroll
      for (int n = 0; n < 2; ++n)
#pragma unroll
        for (int r = 0; r < 2; ++r)
          acc[r][n] = __builtin_amdgcn_mfma_f32_16x16x32_bf16(cat(w[k][n]), an[r][kb + k], acc[r][n], 0, 0, 0);
    wait_lgkm<0>();
#pragma unroll
    for (int k = 3; k < 6; ++k)
#pragma unroll
      for (int n = 0; n < 2; ++n) pin(w[k][n]);
#pragma unroll
    for (int k = 3; k < 6; ++k)
#pragma unroll
      for (int n = 0; n < 2; ++n)
#pragma unroll
        for (int r = 0; r < 2; ++r)
          acc[r][n] = __builtin_amdgcn_mfma_f32_16x16x32_bf16(cat(w[k][n]), an[r][kb + k], acc[r][n], 0, 0, 0);
  };
  grp(IC<0>{}); grp(IC<6>{}); grp(IC<12>{});
  wait_vm<0>();
  STAMP(3, 3);
#pragma unroll
  for (int n = 0; n < 2; ++n) asm volatile("" : "+v"(bb[n]));
#pragma unroll
  for (int r = 0; r < 2; ++r) {
    const int m = m0 + wr * 32 + r * 16 + (lane & 15);
#pragma unroll
    for (int n = 0; n < 2; ++n) {
      const int ncol = n0 + wc * 32 + n * 16 + 4 * g;
      asm volatile("" : "+v"(xx[r][n]));
#pragma unroll
      for (int sI = 0; sI < SPLITK_; ++sI) asm volatile("" : "+v"(py[r][n][sI]));
      f32x4 y = {0.f, 0.f, 0.f, 0.f};
#pragma unroll
      for (int sI = 0; sI < SPLITK_; ++sI) {
        y[0] += lo16(py[r][n][sI][0]); y[1] += hi16(py[r][n][sI][0]);
        y[2] += lo16(py[r][n][sI][1]); y[3] += hi16(py[r][n][sI][1]);
      }
      float bv[4] = {lo16(bb[n][0]), hi16(bb[n][0]), lo16(bb[n][1]), hi16(bb[n][1])};
      float xv[4] = {lo16(xx[r][n][0]), hi16(xx[r][n][0]), lo16(xx[r][n][1]), hi16(xx[r][n][1])};
      float o[4];
#pragma unroll
      for (int j = 0; j < 4; ++j) {
        float yy = y[j] + bv[j];
        float gg = yy / (1.f + __expf(-yy));
        o[j] = xv[j] + acc[r][n][j] * gg;
      }
      u32x2 v = {pack2(o[0], o[1]), pack2(o[2], o[3])};
      *reinterpret_cast<u32x2*>(out + (size_t)m * D_ + ncol) = v;
    }
  }
  STAMP(3, 4);
}

extern "C" void kernel_launch(void* const* d_in, const int* in_sizes, int n_in, void* d_out, int out_size, void* d_ws,
                              size_t ws_size, hipStream_t stream) {
  const u16* x = (const u16*)d_in[0];
  const u16* wqkv = (const u16*)d_in[1];
  const u16* wphi = (const u16*)d_in[2];
  const u16* wo = (const u16*)d_in[3];
  const u16* w1 = (const u16*)d_in[5];
  const u16* b1 = (const u16*)d_in[6];
  const u16* w2 = (const u16*)d_in[7];
  const u16* b2 = (const u16*)d_in[8];
  u16* out = (u16*)d_out;
  char* ws = (char*)d_ws;
  size_t off = 0;
  u16* xnf = (u16*)(ws + off); off += (size_t)S_ * D_ * 2;
  u16* wfold = (u16*)(ws + off); off += (size_t)D_ * NPH_ * 2;
  u16* qkv = (u16*)(ws + off); off += (size_t)96 * S_ * 24 * 2;
  u16* hf = (u16*)(ws + off); off += (size_t)S_ * F_ * 2;
  u16* part = (u16*)(ws + off); off += (size_t)SPLITK_ * S_ * D_ * 2;
  u16* of = (u16*)(ws + off); off += (size_t)S_ * D_ * 2;
  float* pkv = (float*)(ws + off); off += (size_t)256 * 1536 * 4;
  unsigned* cnt = (unsigned*)(ws + off); off += 32 * 32 * 4;

  k0_prep<<<512 + 192, 256, 0, stream>>>(x, wqkv, wphi, xnf, wfold, cnt);
  k1_gemm1<<<256, 512, 0, stream>>>(xnf, wfold, wqkv, w1, b1, qkv, hf);
  k2_gemm2<<<K2_HEADBLK + 256, 256, 0, stream>>>(hf, w2, part, qkv, of, pkv, cnt);
  k3_final<<<16 * 9, 512, 0, stream>>>(of, wo, part, b2, x, out);
}
```

```cpp
#include <hip/hip_runtime.h>
#include <hip/hip_bf16.h>
#include <stdint.h>

constexpr int S_ = 2048, D_ = 576, H_ = 32, DH_ = 18, F_ = 4608, NQKV_ = 1728;
constexpr int NPH_ = 1152;
constexpr int KPH_ = 40;
constexpr int KPHI_ = H_ * KPH_;
constexpr int SPLITK_ = 4;

typedef short bf16x8 __attribute__((ext_vector_type(8)));
typedef short s16x4 __attribute__((ext_vector_type(4)));
typedef float f32x4 __attribute__((ext_vector_type(4)));
typedef unsigned u32x4 __attribute__((ext_vector_type(4)));
typedef unsigned u32x2 __attribute__((ext_vector_type(2)));
typedef __attribute__((address_space(3))) s16x4* lds_s16x4_p;
typedef unsigned short u16;
template <int V> struct IC { static constexpr int value = V; };

#define STAMP(kid, i) do { } while (0)
#define ST_NT(p, v) (*(p) = (v))
#define WAIT_LGKM0() asm volatile("s_waitcnt lgkmcnt(0)" ::: "memory")
#define BAR() __builtin_amdgcn_s_barrier()

template <int N>
__device__ __forceinline__ void wait_vm() {
  static_assert(N >= 0 && N < 64, "vmcnt range");
  asm volatile("s_waitcnt vmcnt(%0)" ::"n"(N) : "memory");
}

__device__ __forceinline__ float bf2f(u16 v) { return __uint_as_float(((unsigned)v) << 16); }
__device__ __forceinline__ u16 f2bf(float f) {
  __hip_bfloat16 b = __float2bfloat16(f);
  return *reinterpret_cast<u16*>(&b);
}
__device__ __forceinline__ unsigned pack2(float lo, float hi) {
  return (unsigned)f2bf(lo) | ((unsigned)f2bf(hi) << 16);
}
__device__ __forceinline__ float rbf(float f) { return bf2f(f2bf(f)); }
__device__ __forceinline__ float lo16(unsigned u) { return __uint_as_float(u << 16); }
__device__ __forceinline__ float hi16(unsigned u) { return __uint_as_float(u & 0xffff0000u); }

__device__ __forceinline__ void glds16(const void* g, void* l) {
  __builtin_amdgcn_global_load_lds((const unsigned*)g, (unsigned*)l, 16, 0, 0);
}

__device__ __forceinline__ void l2_touch(const void* g, void* ldummy) {
  __builtin_amdgcn_global_load_lds((const unsigned*)g, (unsigned*)ldummy, 4, 0, 0);
}

__device__ __forceinline__ bf16x8 swap_halves_if(bf16x8 v, bool odd) {
  bf16x8 s = __builtin_shufflevector(v, v, 4, 5, 6, 7, 0, 1, 2, 3);
  return odd ? s : v;
}

__device__ __forceinline__ bf16x8 wfrag(const char* img, int o0, int o1, int off) {
  s16x4 a = __builtin_amdgcn_ds_read_tr16_b64_v4i16((lds_s16x4_p)(img + o0 + off));
  s16x4 b = __builtin_amdgcn_ds_read_tr16_b64_v4i16((lds_s16x4_p)(img + o1 + off));
  return __builtin_shufflevector(a, b, 0, 1, 2, 3, 4, 5, 6, 7);
}

struct WF { s16x4 lo, hi; };
template <int OFF>
__device__ __forceinline__ void tr_rd(s16x4& r, unsigned addr) {
  asm volatile("ds_read_b64_tr_b16 %0, %1 offset:%2" : "=v"(r) : "v"(addr), "n"(OFF) : "memory");
}
__device__ __forceinline__ void gld16_asm(bf16x8& r, const void* p) {
  asm volatile("global_load_dwordx4 %0, %1, off" : "=v"(r) : "v"(p) : "memory");
}
template <int N>
__device__ __forceinline__ void wait_lgkm() {
  static_assert(N >= 0 && N <= 15, "lgkmcnt range");
  asm volatile("s_waitcnt lgkmcnt(%0)" ::"n"(N) : "memory");
}
__device__ __forceinline__ void pin(WF& w) { asm volatile("" : "+v"(w.lo), "+v"(w.hi)); }
__device__ __forceinline__ void pin(bf16x8& w) { asm volatile("" : "+v"(w)); }
__device__ __forceinline__ bf16x8 cat(const WF& w) { return __builtin_shufflevector(w.lo, w.hi, 0, 1, 2, 3, 4, 5, 6, 7); }
__device__ __forceinline__ unsigned lds_addr(const void* p) {
  return (unsigned)(uintptr_t)(__attribute__((address_space(3))) const char*)p;
}

__global__ __launch_bounds__(256) void k0_prep(const u16* __restrict__ x, const u16* __restrict__ wqkv,
                                               const u16* __restrict__ wphi, u16* __restrict__ xnf,
                                               u16* __restrict__ wfold, unsigned* __restrict__ cnt) {
  __shared__ __attribute__((aligned(16))) float wph[18 * 20];
  const int tid = threadIdx.x, wid = tid >> 6, lane = tid & 63;
  const int bid = blockIdx.x;
  if (bid == 512 && tid < 32) cnt[tid * 32] = 0u;
  if (bid < 512) {
    const int row = bid * 4 + wid;
    const u16* xp = x + (size_t)row * D_;
    u32x4 a = *reinterpret_cast<const u32x4*>(xp + lane * 8);
    u32x4 b = {0u, 0u, 0u, 0u};
    if (lane < 8) b = *reinterpret_cast<const u32x4*>(xp + 512 + lane * 8);
    float v[16];
#pragma unroll
    for (int j = 0; j < 4; ++j) {
      v[2 * j] = lo16(a[j]); v[2 * j + 1] = hi16(a[j]);
      v[8 + 2 * j] = lo16(b[j]); v[8 + 2 * j + 1] = hi16(b[j]);
    }
    float s = 0.f;
#pragma unroll
    for (int j = 0; j < 16; ++j) s += v[j];
#pragma unroll
    for (int o = 32; o >= 1; o >>= 1) s += __shfl_xor(s, o);
    const float mean = s * (1.f / D_);
    float ss = 0.f;
#pragma unroll
    for (int j = 0; j < 8; ++j) { float d = v[j] - mean; ss += d * d; }
    if (lane < 8) {
#pragma unroll
      for (int j = 8; j < 16; ++j) { float d = v[j] - mean; ss += d * d; }
    }
#pragma unroll
    for (int o = 32; o >= 1; o >>= 1) ss += __shfl_xor(ss, o);
    const float rstd = rsqrtf(ss * (1.f / D_) + 1e-5f);
    u32x4 oa, ob;
#pragma unroll
    for (int j = 0; j < 4; ++j) {
      oa[j] = pack2((v[2 * j] - mean) * rstd, (v[2 * j + 1] - mean) * rstd);
      ob[j] = pack2((v[8 + 2 * j] - mean) * rstd, (v[8 + 2 * j + 1] - mean) * rstd);
    }
    {
      const int c = lane;
      u16* dst = xnf + ((size_t)((row >> 4) * 18 + (c >> 2)) * 64 + (c & 3) * 16 + (row & 15)) * 8;
      *reinterpret_cast<u32x4*>(dst) = oa;
    }
    if (lane < 8) {
      const int c = 64 + lane;
      u16* dst = xnf + ((size_t)((row >> 4) * 18 + (c >> 2)) * 64 + (c & 3) * 16 + (row & 15)) * 8;
      *reinterpret_cast<u32x4*>(dst) = ob;
    }
    return;
  }
  {
    const int fb = bid - 512;
    const int hh = fb / 3, k = (fb % 3) * 192 + tid;
    const int h = hh & 31;
    for (int i = tid; i < 18 * 20; i += 256) {
      const int d = i / 20, f = i % 20;
      wph[i] = (f < 18) ? bf2f(wphi[h * 324 + d * 18 + f]) : 0.f;
    }
    unsigned wv[9];
    if (tid < 192) {
      const u16* wp = wqkv + (size_t)k * NQKV_ + hh * 18;
#pragma unroll
      for (int d = 0; d < 9; ++d) wv[d] = *reinterpret_cast<const unsigned*>(wp + 2 * d);
    }
    __syncthreads();
    if (tid < 192) {
      float acc[20];
#pragma unroll
      for (int f = 0; f < 20; ++f) acc[f] = 0.f;
#pragma unroll
      for (int d = 0; d < 18; ++d) {
        const float w = (d & 1) ? hi16(wv[d >> 1]) : lo16(wv[d >> 1]);
#pragma unroll
        for (int f4 = 0; f4 < 5; ++f4) {
          f32x4 pw = *reinterpret_cast<const f32x4*>(wph + d * 20 + f4 * 4);
#pragma unroll
          for (int j = 0; j < 4; ++j) acc[f4 * 4 + j] += w * pw[j];
        }
      }
      u16* op = wfold + (size_t)k * NPH_ + hh * 18;
#pragma unroll
      for (int f = 0; f < 9; ++f) *reinterpret_cast<unsigned*>(op + 2 * f) = pack2(acc[2 * f], acc[2 * f + 1]);
    }
  }
}

constexpr int G1_BUFB = 192 * 128;
constexpr int G1_RING = 4;
constexpr int G1_XOFF = G1_RING * G1_BUFB;
constexpr int G1_BOFF = G1_XOFF + 576 * 32;
constexpr int G1_SMEM = G1_BOFF + 1024;
constexpr int G1_NST = 8;

template <int BASE, int I = 0>
__device__ __forceinline__ void g1_reads(WF (&w)[4], const unsigned (&ad)[4][2]) {
  if constexpr (I < 4) {
    if constexpr (BASE >= 49152) {
      tr_rd<BASE - 49152>(w[I].lo, ad[I][0] + 49152u);
      tr_rd<BASE - 49152>(w[I].hi, ad[I][1] + 49152u);
    } else {
      tr_rd<BASE>(w[I].lo, ad[I][0]);
      tr_rd<BASE>(w[I].hi, ad[I][1]);
    }
    g1_reads<BASE, I + 1>(w, ad);
  }
}
template <int KS>
__device__ __forceinline__ void g1_mma(WF (&w)[4], const bf16x8 (&xa)[2][18], f32x4 (&acc)[2][4]) {
#pragma unroll
  for (int i = 0; i < 4; ++i) pin(w[i]);
#pragma unroll
  for (int i = 0; i < 4; ++i)
#pragma unroll
    for (int r = 0; r < 2; ++r)
      acc[r][i] = __builtin_amdgcn_mfma_f32_16x16x32_bf16(cat(w[i]), xa[r][KS], acc[r][i], 0, 0, 0);
}

__device__ __forceinline__ float gelu_f(float v) {
  constexpr float c1 = -2.f * 0.7978845608028654f * 1.4426950408889634f;
  constexpr float c3 = c1 * 0.044715f;
  const float t = v * v;
  const float e = __builtin_amdgcn_exp2f(v * fmaf(t, c3, c1));
  return v * __builtin_amdgcn_rcpf(1.f + e);
}

typedef float f32x2 __attribute__((ext_vector_type(2)));
__device__ __forceinline__ unsigned cvt_pk(float lo, float hi) {
  unsigned r;
  asm("v_cvt_pk_bf16_f32 %0, %1, %2" : "=v"(r) : "v"(lo), "v"(hi));
  return r;
}
__device__ __forceinline__ unsigned gelu2_pk(f32x2 v) {
  constexpr float c1 = -2.f * 0.7978845608028654f * 1.4426950408889634f;
  constexpr float c3 = c1 * 0.044715f;
  const f32x2 t = v * v;
  const f32x2 pp = t * (f32x2){c3, c3} + (f32x2){c1, c1};
  const f32x2 u = v * pp;
  f32x2 e = {__builtin_amdgcn_exp2f(u[0]), __builtin_amdgcn_exp2f(u[1])};
  e = e + (f32x2){1.f, 1.f};
  const f32x2 r = {__builtin_amdgcn_rcpf(e[0]), __builtin_amdgcn_rcpf(e[1])};
  const f32x2 o = v * r;
  return cvt_pk(o[0], o[1]);
}

__device__ __forceinline__ void g1_store_tile(const f32x4& a, int tile, int mrow, int g, const u16* bias4,
                                              u16* __restrict__ qkv, u16* __restrict__ hf) {
  if (tile < NQKV_ / 16) {
    const int col = tile * 16 + 4 * g;
    const int hh = col / 18, f = col - hh * 18;
    u16* dst = qkv + ((size_t)hh * S_ + mrow) * 24 + f;
    const unsigned v0 = cvt_pk(a[0], a[1]), v1 = cvt_pk(a[2], a[3]);
    if (f != 16) {
      u32x2 v = {v0, v1};
      *reinterpret_cast<u32x2*>(dst) = v;
    } else {
      *reinterpret_cast<unsigned*>(dst) = v0;
      *reinterpret_cast<unsigned*>(qkv + ((size_t)(hh + 1) * S_ + mrow) * 24) = v1;
    }
  } else {
    u32x2 bb = *reinterpret_cast<const u32x2*>(bias4);
    float o[4];
    o[0] = gelu_f(a[0] + lo16(bb[0]));
    o[1] = gelu_f(a[1] + hi16(bb[0]));
    o[2] = gelu_f(a[2] + lo16(bb[1]));
    o[3] = gelu_f(a[3] + hi16(bb[1]));
    u32x2 v = {pack2(o[0], o[1]), pack2(o[2], o[3])};
    const int T = tile - NQKV_ / 16;
    u16* dst = hf + ((size_t)((mrow >> 4) * 144 + (T >> 1)) * 64 + ((T & 1) * 2 + (g >> 1)) * 16 + (mrow & 15)) * 8 + (g & 1) * 4;
    *reinterpret_cast<u32x2*>(dst) = v;
  }
}

__device__ __forceinline__ void g1_addr(unsigned (&ad)[4][2], char* smem, int lane) {
  const int g = lane >> 4, q = (lane & 15) >> 2, p = lane & 3;
#pragma unroll
  for (int t = 0; t < 2; ++t) {
    const int row = 8 * g + 4 * t + q;
    const int f = ((row >> 1) & 1) | (((row >> 3) & 1) << 1);
#pragma unroll
    for (int n = 0; n < 4; ++n) ad[n][t] = lds_addr(smem) + row * 128 + ((n ^ f) * 32) + p * 8;
  }
}

struct G1W { const u16* src; int stride; };

__device__ __forceinline__ void g1_wsetup(G1W& w, int col, int wid, int lane, const u16* wfold, const u16* wqkv, const u16* w1) {
  const int row = wid * 8 + (lane >> 3);
  const int f = ((row >> 1) & 1) | (((row >> 3) & 1) << 1);
  const int ch = (lane & 7) ^ (f << 1);
  if (col < NPH_) { w.src = wfold + (size_t)row * NPH_ + col + ch * 8; w.stride = 64 * NPH_; }
  else if (col < NQKV_) { w.src = wqkv + (size_t)row * NQKV_ + col + ch * 8; w.stride = 64 * NQKV_; }
  else { w.src = w1 + (size_t)row * F_ + (col - NQKV_) + ch * 8; w.stride = 64 * F_; }
}
__device__ __forceinline__ void g1_wissue(G1W& w, char* buf, int wid, int lane) {
#pragma unroll
  for (int i = 0; i < 3; ++i) {
    glds16(w.src, buf + i * 8192 + wid * 1024 + lane * 16);
    w.src += w.stride;
  }
}

template <int U>
__device__ __forceinline__ void g1_unit(G1W& wst, int unit0, const u16* wfold, const u16* wqkv,
                                        const u16* w1, char* smem, bf16x8 (&xa)[2][18], int m0,
                                        u16* __restrict__ qkv, u16* __restrict__ hf, WF (&wA)[4]) {
  int tz = threadIdx.x;
  asm volatile("" : "+v"(tz));
  const int wid = __builtin_amdgcn_readfirstlane(tz >> 6), lane = tz & 63;
  const int g = lane >> 4;
  unsigned ad[4][2];
  g1_addr(ad, smem, lane);
  f32x4 acc[2][4];
#pragma unroll
  for (int r = 0; r < 2; ++r)
#pragma unroll
    for (int n = 0; n < 4; ++n) acc[r][n] = (f32x4){0.f, 0.f, 0.f, 0.f};

  auto stage = [&](auto KC) {
    constexpr int kc = decltype(KC)::value;
    constexpr int s = U * 3 + kc;
    constexpr int CUR = (s % G1_RING) * G1_BUFB;
    if constexpr (U == 0) {
#pragma unroll
      for (int k = 0; k < 6; ++k) { pin(xa[0][kc * 6 + k]); pin(xa[1][kc * 6 + k]); }
    }
    WF wB[4];
    g1_reads<CUR + 1 * 4096>(wB, ad); wait_lgkm<8>(); g1_mma<kc * 6 + 0>(wA, xa, acc);
    g1_reads<CUR + 2 * 4096>(wA, ad); wait_lgkm<8>(); g1_mma<kc * 6 + 1>(wB, xa, acc);
    g1_reads<CUR + 3 * 4096>(wB, ad); wait_lgkm<8>(); g1_mma<kc * 6 + 2>(wA, xa, acc);
    g1_reads<CUR + 4 * 4096>(wA, ad); wait_lgkm<8>(); g1_mma<kc * 6 + 3>(wB, xa, acc);
    g1_reads<CUR + 5 * 4096>(wB, ad); wait_lgkm<8>(); g1_mma<kc * 6 + 4>(wA, xa, acc);
    if constexpr (s < 8) {
      constexpr int nl = (s == 0) ? 12 : ((s + 2 <= 8) ? 3 : 0);
      constexpr int nst = (U > 0 && kc <= 1) ? G1_NST : 0;
      wait_vm<nl + nst>();
      BAR();
      if constexpr (s + 3 <= 8) {
        constexpr int sn = s + 3;
        if constexpr (sn % 3 == 0) g1_wsetup(wst, (unit0 + sn / 3) * 64, wid, lane, wfold, wqkv, w1);
        g1_wissue(wst, smem + (sn % G1_RING) * G1_BUFB, wid, lane);
      }
      g1_reads<((s + 1) % G1_RING) * G1_BUFB>(wA, ad);
      wait_lgkm<8>();
    } else {
      wait_lgkm<0>();
    }
    g1_mma<kc * 6 + 5>(wB, xa, acc);
  };
  stage(IC<0>{}); stage(IC<1>{}); stage(IC<2>{});
  const u16* bias_lds = reinterpret_cast<const u16*>(smem + G1_BOFF);
  if ((unit0 + U) * 64 < NQKV_) {
#pragma unroll
    for (int n = 0; n < 4; ++n) {
      const int tile = (unit0 + U) * 4 + n;
#pragma unroll
      for (int r = 0; r < 2; ++r)
        g1_store_tile(acc[r][n], tile, m0 + wid * 32 + r * 16 + (lane & 15), g, bias_lds, qkv, hf);
    }
  } else {
    u32x2 bb[4];
#pragma unroll
    for (int n = 0; n < 4; ++n) bb[n] = *reinterpret_cast<const u32x2*>(bias_lds + (U * 4 + n) * 16 + 4 * g);
    const int T0 = (unit0 + U) * 4 - NQKV_ / 16;
#pragma unroll
    for (int r = 0; r < 2; ++r) {
      const int mrow = m0 + wid * 32 + r * 16 + (lane & 15);
      u16* dst = hf + ((size_t)((mrow >> 4) * 144 + (T0 >> 1)) * 64 + (g >> 1) * 16 + (mrow & 15)) * 8 + (g & 1) * 4;
#pragma unroll
      for (int n = 0; n < 4; ++n) {
        const f32x2 v0 = (f32x2){acc[r][n][0], acc[r][n][1]} + (f32x2){lo16(bb[n][0]), hi16(bb[n][0])};
        const f32x2 v1 = (f32x2){acc[r][n][2], acc[r][n][3]} + (f32x2){lo16(bb[n][1]), hi16(bb[n][1])};
        u32x2 v = {gelu2_pk(v0), gelu2_pk(v1)};
        ST_NT(reinterpret_cast<u32x2*>(dst + (n >> 1) * 512 + (n & 1) * 256), v);
      }
    }
  }
}

__global__ __launch_bounds__(512, 2) void k1_gemm1(const u16* __restrict__ xnf, const u16* __restrict__ wfold,
                                                   const u16* __restrict__ wqkv, const u16* __restrict__ w1,
                                                   const u16* __restrict__ b1, u16* __restrict__ qkv,
                                                   u16* __restrict__ hf) {
  __shared__ __attribute__((aligned(16))) char smem[G1_SMEM];
  const int tid = threadIdx.x, wid = tid >> 6, lane = tid & 63;
  const int bid = blockIdx.x;
  const int mt = bid >> 5, grp = bid & 31;
  const int m0 = mt * 256;
  const int unit0 = grp * 3;
  const bool extra = grp >= 20;
  const int xcol = 6144 + (extra ? (grp - 20) : 0) * 16;
  STAMP(1, 0);
  G1W wst;
  g1_wsetup(wst, unit0 * 64, wid, lane, wfold, wqkv, w1);
  g1_wissue(wst, smem + 0 * G1_BUFB, wid, lane);
  g1_wissue(wst, smem + 1 * G1_BUFB, wid, lane);
  g1_wissue(wst, smem + 2 * G1_BUFB, wid, lane);
#pragma unroll
  for (int i = 0; i < 3; ++i) {
    int ins = wid + 8 * i;
    if (ins >= 18) ins -= 18;
    const int c = ins * 64 + lane;
    const int pr = c >> 1, half = c & 1;
    const int k = (pr & ~0xC) | ((pr & 4) << 1) | ((pr & 8) >> 1);
    glds16(w1 + (size_t)k * F_ + (xcol - NQKV_) + half * 8, smem + G1_XOFF + c * 16);
  }
  {
    const int e = (wid & 1) * 64 + lane;
    int col = (e < 96) ? (unit0 * 64 + 2 * e - NQKV_) : (xcol - NQKV_ + 2 * (e - 96));
    if (col < 0 || e >= 104) col = 0;
    __builtin_amdgcn_global_load_lds((const unsigned*)(b1 + col), (unsigned*)(smem + G1_BOFF + e * 4), 4, 0, 0);
  }
  bf16x8 xa[2][18];
#pragma unroll
  for (int ks = 0; ks < 18; ++ks)
#pragma unroll
    for (int r = 0; r < 2; ++r)
      gld16_asm(xa[r][ks], xnf + ((size_t)(((m0 >> 4) + wid * 2 + r) * 18 + ks) * 64 + lane) * 8);
  wait_vm<24>();
  BAR();
  WF wA[4];
  {
    unsigned ad[4][2];
    g1_addr(ad, smem, lane);
    g1_reads<0>(wA, ad);
  }
  STAMP(1, 1);
  g1_unit<0>(wst, unit0, wfold, wqkv, w1, smem, xa, m0, qkv, hf, wA);
  STAMP(1, 2);
  g1_unit<1>(wst, unit0, wfold, wqkv, w1, smem, xa, m0, qkv, hf, wA);
  STAMP(1, 3);
  g1_unit<2>(wst, unit0, wfold, wqkv, w1, smem, xa, m0, qkv, hf, wA);
  STAMP(1, 4);
  if (extra) {
    int tz = threadIdx.x;
    asm volatile("" : "+v"(tz));
    const int wid2 = __builtin_amdgcn_readfirstlane(tz >> 6), lane2 = tz & 63;
    const int g = lane2 >> 4, q = (lane2 & 15) >> 2, p = lane2 & 3;
    unsigned ad[2];
#pragma unroll
    for (int t = 0; t < 2; ++t) {
      const int k = 8 * g + 4 * t + q;
      const int pr = (k & ~0xC) | ((k & 4) << 1) | ((k & 8) >> 1);
      ad[t] = lds_addr(smem) + G1_XOFF + pr * 32 + p * 8;
    }
    f32x4 acc[2] = {(f32x4){0.f, 0.f, 0.f, 0.f}, (f32x4){0.f, 0.f, 0.f, 0.f}};
#pragma unroll
    for (int kb = 0; kb < 18; kb += 6) {
      WF w[6];
      tr_rd<0 * 1024>(w[0].lo, ad[0] + kb * 1024); tr_rd<0 * 1024>(w[0].hi, ad[1] + kb * 1024);
      tr_rd<1 * 1024>(w[1].lo, ad[0] + kb * 1024); tr_rd<1 * 1024>(w[1].hi, ad[1] + kb * 1024);
      tr_rd<2 * 1024>(w[2].lo, ad[0] + kb * 1024); tr_rd<2 * 1024>(w[2].hi, ad[1] + kb * 1024);
      tr_rd<3 * 1024>(w[3].lo, ad[0] + kb * 1024); tr_rd<3 * 1024>(w[3].hi, ad[1] + kb * 1024);
      tr_rd<4 * 1024>(w[4].lo, ad[0] + kb * 1024); tr_rd<4 * 1024>(w[4].hi, ad[1] + kb * 1024);
      tr_rd<5 * 1024>(w[5].lo, ad[0] + kb * 1024); tr_rd<5 * 1024>(w[5].hi, ad[1] + kb * 1024);
      wait_lgkm<0>();
#pragma unroll
      for (int i = 0; i < 6; ++i) pin(w[i]);
#pragma unroll
      for (int i = 0; i < 6; ++i)
#pragma unroll
        for (int r = 0; r < 2; ++r)
          acc[r] = __builtin_amdgcn_mfma_f32_16x16x32_bf16(cat(w[i]), xa[r][kb + i], acc[r], 0, 0, 0);
    }
    const u16* bias_lds = reinterpret_cast<const u16*>(smem + G1_BOFF);
#pragma unroll
    for (int r = 0; r < 2; ++r)
      g1_store_tile(acc[r], xcol / 16, m0 + wid2 * 32 + r * 16 + (lane2 & 15), g, bias_lds + 192 + 4 * g, qkv, hf);
  }
}

constexpr int G2_NT = 9, G2_RB = G2_NT * 32;
constexpr int G2_BUFB = 64 * G2_RB;
constexpr int G2_RING = 4;
constexpr int G2_STAGES = 18;

__device__ __forceinline__ void softmax_pm(const float (&pk)[18], float (&ep)[18], float (&en)[18]) {
  float mx = pk[0], mn = pk[0];
#pragma unroll
  for (int f = 1; f < 18; ++f) {
    mx = fmaxf(mx, pk[f]);
    mn = fminf(mn, pk[f]);
  }
  float sp = 0.f, sn = 0.f;
#pragma unroll
  for (int f = 0; f < 18; ++f) {
    ep[f] = __expf(pk[f] - mx);
    en[f] = __expf(mn - pk[f]);
    sp += ep[f];
    sn += en[f];
  }
  const float rp = 1.f / sp, rn = 1.f / sn;
#pragma unroll
  for (int f = 0; f < 18; ++f) {
    ep[f] *= rp;
    en[f] *= rn;
  }
}

__device__ void head_block(int hb, const u16* __restrict__ qkv, u16* __restrict__ of, float* pkv, unsigned* cnt, char* smem) {
  u16* phiT = reinterpret_cast<u16*>(smem);
  u16* vT = reinterpret_cast<u16*>(smem + 25344);
  float* kvs = reinterpret_cast<float*>(smem + 25344 + 16896);
  const int h = hb >> 3, c8 = hb & 7;
  const int tid = threadIdx.x, wid = tid >> 6, lane = tid & 63;
  const int row = c8 * 256 + tid;
  u32x4 kr[3], vr[3], qr[3];
  {
    const u32x4* kp = reinterpret_cast<const u32x4*>(qkv + ((size_t)(32 + h) * S_ + row) * 24);
    const u32x4* vp = reinterpret_cast<const u32x4*>(qkv + ((size_t)(64 + h) * S_ + row) * 24);
    const u32x4* qp = reinterpret_cast<const u32x4*>(qkv + ((size_t)h * S_ + row) * 24);
#pragma unroll
    for (int d = 0; d < 3; ++d) { kr[d] = kp[d]; vr[d] = vp[d]; qr[d] = qp[d]; }
  }
  for (int i = tid; i < 12 * 264; i += 256) phiT[36 * 264 + i] = 0;
  for (int i = tid; i < 14 * 264; i += 256) vT[18 * 264 + i] = (i < 264) ? (u16)0x3f80 : (u16)0;
  {
    float pk[18];
#pragma unroll
    for (int d = 0; d < 9; ++d) {
      pk[2 * d] = lo16(kr[d >> 2][d & 3]);
      pk[2 * d + 1] = hi16(kr[d >> 2][d & 3]);
    }
    float ep[18], en[18];
    softmax_pm(pk, ep, en);
#pragma unroll
    for (int f = 0; f < 18; ++f) {
      phiT[f * 264 + tid] = f2bf(ep[f]);
      phiT[(18 + f) * 264 + tid] = f2bf(en[f]);
      const unsigned w = vr[(f >> 1) >> 2][(f >> 1) & 3];
      vT[f * 264 + tid] = (u16)((f & 1) ? (w >> 16) : (w & 0xffff));
    }
  }
  __syncthreads();
  {
    f32x4 acc[3][2];
#pragma unroll
    for (int a = 0; a < 3; ++a)
#pragma unroll
      for (int b = 0; b < 2; ++b) acc[a][b] = (f32x4){0.f, 0.f, 0.f, 0.f};
#pragma unroll
    for (int ks = 0; ks < 2; ++ks) {
      const int s0 = wid * 64 + ks * 32 + 8 * (lane >> 4);
      bf16x8 bv[2];
#pragma unroll
      for (int b = 0; b < 2; ++b) bv[b] = *reinterpret_cast<const bf16x8*>(vT + (b * 16 + (lane & 15)) * 264 + s0);
#pragma unroll
      for (int a = 0; a < 3; ++a) {
        bf16x8 av = *reinterpret_cast<const bf16x8*>(phiT + (a * 16 + (lane & 15)) * 264 + s0);
#pragma unroll
        for (int b = 0; b < 2; ++b) acc[a][b] = __builtin_amdgcn_mfma_f32_16x16x32_bf16(av, bv[b], acc[a][b], 0, 0, 0);
      }
    }
#pragma unroll
    for (int a = 0; a < 3; ++a)
#pragma unroll
      for (int b = 0; b < 2; ++b)
#pragma unroll
        for (int j = 0; j < 4; ++j)
          kvs[(wid * 48 + a * 16 + (lane >> 4) * 4 + j) * 32 + b * 16 + (lane & 15)] = acc[a][b][j];
  }
  __syncthreads();
  float* mypart = pkv + (size_t)(h * 8 + c8) * 1536;
#pragma unroll
  for (int it = 0; it < 6; ++it) {
    const int i = it * 256 + tid;
    const float v = kvs[i] + kvs[1536 + i] + kvs[2 * 1536 + i] + kvs[3 * 1536 + i];
    __hip_atomic_store(mypart + i, v, __ATOMIC_RELAXED, __HIP_MEMORY_SCOPE_AGENT);
  }
  asm volatile("s_waitcnt vmcnt(0)" ::: "memory");
  __syncthreads();
  if (tid == 0) __hip_atomic_fetch_add(cnt + h * 32, 1u, __ATOMIC_RELAXED, __HIP_MEMORY_SCOPE_AGENT);
  float ep[18], en[18];
  {
    float pk[18];
#pragma unroll
    for (int d = 0; d < 9; ++d) {
      pk[2 * d] = lo16(qr[d >> 2][d & 3]);
      pk[2 * d + 1] = hi16(qr[d >> 2][d & 3]);
    }
    softmax_pm(pk, ep, en);
#pragma unroll
    for (int f = 0; f < 18; ++f) { ep[f] = rbf(ep[f]); en[f] = rbf(en[f]); }
  }
  if (tid == 0) {
    unsigned spins = 0;
    while (__hip_atomic_load(cnt + h * 32, __ATOMIC_RELAXED, __HIP_MEMORY_SCOPE_AGENT) < 8u) {
      __builtin_amdgcn_s_sleep(1);
      if (++spins > (1u << 24)) break;
    }
  }
  __syncthreads();
  u16* kvT = reinterpret_cast<u16*>(smem);
  u16* phiR = reinterpret_cast<u16*>(smem + 4608);
  {
    float* hp = pkv + (size_t)h * 8 * 1536;
    float pv[5][8];
#pragma unroll
    for (int it = 0; it < 5; ++it) {
      const int i = it * 256 + tid;
#pragma unroll
      for (int c = 0; c < 8; ++c) pv[it][c] = __hip_atomic_load(hp + c * 1536 + i, __ATOMIC_RELAXED, __HIP_MEMORY_SCOPE_AGENT);
    }
    {
      unsigned o[32];
#pragma unroll
      for (int f = 0; f < 9; ++f) { o[f] = pack2(ep[2 * f], ep[2 * f + 1]); o[9 + f] = pack2(en[2 * f], en[2 * f + 1]); }
#pragma unroll
      for (int f = 18; f < 32; ++f) o[f] = 0u;
#pragma unroll
      for (int c = 0; c < 8; ++c) {
        u32x4 v = {o[4 * c], o[4 * c + 1], o[4 * c + 2], o[4 * c + 3]};
        *reinterpret_cast<u32x4*>(phiR + tid * 72 + c * 8) = v;
      }
    }
#pragma unroll
    for (int it = 0; it < 8; ++it) {
      const int i = it * 256 + tid;
      const int f = i >> 5, d = i & 31;
      float v = 0.f;
      if (it < 5) {
#pragma unroll
        for (int c = 0; c < 8; ++c) v += pv[it < 5 ? it : 0][c];
      }
      kvT[d * 72 + f] = ((d < 19) && (f < 36)) ? f2bf(v) : (u16)0;
    }
  }
  __syncthreads();
  {
    const int g = lane >> 4;
    bf16x8 ka[2][2];
#pragma unroll
    for (int t = 0; t < 2; ++t)
#pragma unroll
      for (int ks = 0; ks < 2; ++ks)
        ka[t][ks] = *reinterpret_cast<const bf16x8*>(kvT + (t * 16 + (lane & 15)) * 72 + ks * 32 + 8 * g);
#pragma unroll
    for (int rt = 0; rt < 4; ++rt) {
      const int rl = wid * 64 + rt * 16 + (lane & 15);
      f32x4 c0 = {0.f, 0.f, 0.f, 0.f}, c1 = {0.f, 0.f, 0.f, 0.f};
#pragma unroll
      for (int ks = 0; ks < 2; ++ks) {
        const bf16x8 pb = *reinterpret_cast<const bf16x8*>(phiR + rl * 72 + ks * 32 + 8 * g);
        c0 = __builtin_amdgcn_mfma_f32_16x16x32_bf16(ka[0][ks], pb, c0, 0, 0, 0);
        c1 = __builtin_amdgcn_mfma_f32_16x16x32_bf16(ka[1][ks], pb, c1, 0, 0, 0);
      }
      const float den = __shfl(c1[2], lane & 15);
      const float rd = 1.f / (rbf(den) + 1e-6f);
      const int grow = c8 * 256 + rl;
      u16* obase = of + (size_t)(grow >> 4) * 18 * 512 + (grow & 15) * 8;
#pragma unroll
      for (int j2 = 0; j2 < 2; ++j2) {
        const int k = h * 18 + 4 * g + 2 * j2;
        *reinterpret_cast<unsigned*>(obase + (k >> 5) * 512 + ((k >> 3) & 3) * 128 + (k & 7)) =
            pack2(rbf(c0[2 * j2]) * rd, rbf(c0[2 * j2 + 1]) * rd);
      }
      if (g == 0) {
        const int k = h * 18 + 16;
        *reinterpret_cast<unsigned*>(obase + (k >> 5) * 512 + ((k >> 3) & 3) * 128 + (k & 7)) =
            pack2(rbf(c1[0]) * rd, rbf(c1[1]) * rd);
      }
    }
  }
}

template <int BASE, int N0, int CNT, int I = 0>
__device__ __forceinline__ void g2_wreads(WF (&w)[5], unsigned a0, unsigned a1) {
  if constexpr (I < CNT) {
    tr_rd<BASE + (N0 + I) * 32>(w[I].lo, a0);
    tr_rd<BASE + (N0 + I) * 32>(w[I].hi, a1);
    g2_wreads<BASE, N0, CNT, I + 1>(w, a0, a1);
  }
}
template <int N0, int CNT>
__device__ __forceinline__ void g2_mma(WF (&w)[5], const bf16x8 (&ac)[2], f32x4 (&acc)[2][G2_NT]) {
#pragma unroll
  for (int i = 0; i < CNT; ++i) pin(w[i]);
#pragma unroll
  for (int i = 0; i < CNT; ++i)
#pragma unroll
    for (int r = 0; r < 2; ++r)
      acc[r][N0 + i] = __builtin_amdgcn_mfma_f32_16x16x32_bf16(cat(w[i]), ac[r], acc[r][N0 + i], 0, 0, 0);
}
constexpr int K2_HEADBLK = 256;

__global__ __launch_bounds__(256, 2) void k2_gemm2(const u16* __restrict__ hf, const u16* __restrict__ w2,
                                                   u16* __restrict__ part, const u16* __restrict__ qkv,
                                                   u16* __restrict__ of, float* pkv, unsigned* cnt) {
  __shared__ __attribute__((aligned(16))) char smem[G2_RING * G2_BUFB];
  const int tid = threadIdx.x, wid = tid >> 6, lane = tid & 63;
  int bid = blockIdx.x;
  if (bid < K2_HEADBLK) { head_block(bid, qkv, of, pkv, cnt, smem); return; }
  bid -= K2_HEADBLK;
  STAMP(2, 0);
  const int xcd = bid & 7, loc = bid >> 3;
  const int sp = xcd >> 1, mt = (xcd & 1) * 8 + (loc >> 2), nt4 = loc & 3;
  const int m0 = mt * 128, n0 = nt4 * 144, k0 = sp * 1152;
  const int g = lane >> 4, q = (lane & 15) >> 2, p = lane & 3;
  const bool odd = g & 1;
  const unsigned wa0 = lds_addr(smem) + (8 * g + 4 * (g & 1) + q) * G2_RB + p * 8;
  const unsigned wa1 = lds_addr(smem) + (8 * g + 4 * (1 - (g & 1)) + q) * G2_RB + p * 8;
  f32x4 acc[2][G2_NT];
#pragma unroll
  for (int r = 0; r < 2; ++r)
#pragma unroll
    for (int n = 0; n < G2_NT; ++n) acc[r][n] = (f32x4){0.f, 0.f, 0.f, 0.f};
  const u16* wp[5];
  int wdst[5];
#pragma unroll
  for (int i = 0; i < 5; ++i) {
    int ins = wid + 4 * i;
    if (ins >= 18) ins -= 18;
    int c = ins * 64 + lane;
    int row = c / 18, cc = c % 18;
    wp[i] = w2 + (size_t)(k0 + row) * D_ + n0 + cc * 8;
    wdst[i] = c * 16;
  }
  const u16* ap[2];
#pragma unroll
  for (int r = 0; r < 2; ++r) ap[r] = hf + ((size_t)((m0 >> 4) + wid * 2 + r) * 144 + (k0 >> 5)) * 512 + lane * 8;
  bf16x8 an[4][2][2];
  auto issue = [&](auto SL) {
    constexpr int slot = decltype(SL)::value;
    char* buf = smem + slot * G2_BUFB;
#pragma unroll
    for (int i = 0; i < 5; ++i) { glds16(wp[i], buf + wdst[i]); wp[i] += 64 * D_; }
#pragma unroll
    for (int r = 0; r < 2; ++r) {
      gld16_asm(an[slot][r][0], ap[r]);
      gld16_asm(an[slot][r][1], ap[r] + 512);
      ap[r] += 1024;
    }
  };
  issue(IC<0>{});
  issue(IC<1>{});
  issue(IC<2>{});
  wait_vm<18>();
  BAR();
  STAMP(2, 1);
  WF wA[5], wB[5];
  g2_wreads<0, 0, 5>(wA, wa0, wa1);
  auto stage = [&](auto U, auto LAST) {
    constexpr int u = decltype(U)::value;
    constexpr int rem = decltype(LAST)::value;
    constexpr int BASE = u * G2_BUFB;
    bf16x8 c0[2], c1[2];
#pragma unroll
    for (int r = 0; r < 2; ++r) {
      pin(an[u][r][0]); pin(an[u][r][1]);
      c0[r] = swap_halves_if(an[u][r][0], odd);
      c1[r] = swap_halves_if(an[u][r][1], odd);
    }
    g2_wreads<BASE, 5, 4>(wB, wa0, wa1);
    wait_lgkm<8>();
    g2_mma<0, 5>(wA, c0, acc);
    g2_wreads<BASE + 32 * G2_RB, 0, 5>(wA, wa0, wa1);
    wait_lgkm<10>();
    g2_mma<5, 4>(wB, c0, acc);
    g2_wreads<BASE + 32 * G2_RB, 5, 4>(wB, wa0, wa1);
    wait_lgkm<8>();
    g2_mma<0, 5>(wA, c1, acc);
    if constexpr (rem >= 1) {
      if constexpr (rem >= 2) wait_vm<9>(); else wait_vm<0>();
      BAR();
      if constexpr (rem >= 3) issue(IC<(u + 3) & 3>{});
      g2_wreads<((u + 1) & 3) * G2_BUFB, 0, 5>(wA, wa0, wa1);
      wait_lgkm<10>();
    } else {
      wait_lgkm<0>();
    }
    g2_mma<5, 4>(wB, c1, acc);
  };
#pragma unroll 1
  for (int it = 0; it < 3; ++it) {
    stage(IC<0>{}, IC<3>{}); stage(IC<1>{}, IC<3>{}); stage(IC<2>{}, IC<3>{}); stage(IC<3>{}, IC<3>{});
  }
  STAMP(2, 2);
  stage(IC<0>{}, IC<3>{}); stage(IC<1>{}, IC<3>{}); stage(IC<2>{}, IC<3>{});
  stage(IC<3>{}, IC<2>{}); stage(IC<0>{}, IC<1>{}); stage(IC<1>{}, IC<0>{});
  STAMP(2, 3);
#pragma unroll
  for (int r = 0; r < 2; ++r) {
    const int m = m0 + wid * 32 + r * 16 + (lane & 15);
    u16* pp = part + ((size_t)sp * S_ + m) * D_ + n0 + 4 * g;
#pragma unroll
    for (int n = 0; n < G2_NT; ++n) {
      u32x2 v = {pack2(acc[r][n][0], acc[r][n][1]), pack2(acc[r][n][2], acc[r][n][3])};
      ST_NT(reinterpret_cast<u32x2*>(pp + n * 16), v);
    }
  }
  asm volatile("s_waitcnt vmcnt(0)" ::: "memory");
  STAMP(2, 4);
}

__global__ __launch_bounds__(512, 2) void k3_final(const u16* __restrict__ of, const u16* __restrict__ wo,
                                                   const u16* __restrict__ part, const u16* __restrict__ b2,
                                                   const u16* __restrict__ x, u16* __restrict__ out) {
  __shared__ __attribute__((aligned(16))) char smem[576 * 128];
  const int tid = threadIdx.x, wid = tid >> 6, lane = tid & 63;
  const int bid = blockIdx.x;
  const int xcd = bid & 7, loc = bid >> 3;
  const int mt = xcd * 2 + loc / 9, nt = loc % 9;
  const int m0 = mt * 128, n0 = nt * 64;
  const int g = lane >> 4, q = (lane & 15) >> 2, p = lane & 3;
  const int wr = wid >> 1, wc = wid & 1;
  STAMP(3, 0);
#pragma unroll
  for (int i = 0; i < 9; ++i) {
    const int row = (wid * 9 + i) * 8 + (lane >> 3);
    const int f = ((row >> 1) & 1) | (((row >> 3) & 1) << 1);
    const int ch = (lane & 7) ^ (f << 1);
    glds16(wo + (size_t)row * D_ + n0 + ch * 8, smem + (wid * 9 + i) * 1024 + lane * 16);
  }
  bf16x8 an[2][18];
#pragma unroll
  for (int ks = 0; ks < 18; ++ks)
#pragma unroll
    for (int r = 0; r < 2; ++r)
      gld16_asm(an[r][ks], of + ((size_t)(((m0 >> 4) + wr * 2 + r) * 18 + ks) * 64 + lane) * 8);
  u32x2 py[2][2][4];
  u32x2 xx[2][2], bb[2];
#pragma unroll
  for (int r = 0; r < 2; ++r) {
    const int m = m0 + wr * 32 + r * 16 + (lane & 15);
#pragma unroll
    for (int n = 0; n < 2; ++n) {
      const int ncol = n0 + wc * 32 + n * 16 + 4 * g;
#pragma unroll
      for (int sI = 0; sI < SPLITK_; ++sI)
        asm volatile("global_load_dwordx2 %0, %1, off" : "=v"(py[r][n][sI]) : "v"(part + ((size_t)sI * S_ + m) * D_ + ncol) : "memory");
      asm volatile("global_load_dwordx2 %0, %1, off" : "=v"(xx[r][n]) : "v"(x + (size_t)m * D_ + ncol) : "memory");
    }
  }
#pragma unroll
  for (int n = 0; n < 2; ++n)
    asm volatile("global_load_dwordx2 %0, %1, off" : "=v"(bb[n]) : "v"(b2 + n0 + wc * 32 + n * 16 + 4 * g) : "memory");
  unsigned ad[2][2];
#pragma unroll
  for (int t = 0; t < 2; ++t) {
    const int row = 8 * g + 4 * t + q;
    const int f = ((row >> 1) & 1) | (((row >> 3) & 1) << 1);
#pragma unroll
    for (int n = 0; n < 2; ++n) ad[n][t] = lds_addr(smem) + row * 128 + (((wc * 2 + n) ^ f) * 32) + p * 8;
  }
  f32x4 acc[2][2];
#pragma unroll
  for (int r = 0; r < 2; ++r)
#pragma unroll
    for (int n = 0; n < 2; ++n) acc[r][n] = (f32x4){0.f, 0.f, 0.f, 0.f};
  STAMP(3, 1);
  wait_vm<22>();
  BAR();
  STAMP(3, 2);
#pragma unroll
  for (int ks = 0; ks < 18; ++ks) { pin(an[0][ks]); pin(an[1][ks]); }
  auto grp = [&](auto KB) {
    constexpr int kb = decltype(KB)::value;
    WF w[6][2];
#pragma unroll
    for (int k = 0; k < 6; ++k)
#pragma unroll
      for (int n = 0; n < 2; ++n) {
        if (k == 0) { tr_rd<0 * 4096>(w[0][n].lo, ad[n][0] + kb * 4096); tr_rd<0 * 4096>(w[0][n].hi, ad[n][1] + kb * 4096); }
        if (k == 1) { tr_rd<1 * 4096>(w[1][n].lo, ad[n][0] + kb * 4096); tr_rd<1 * 4096>(w[1][n].hi, ad[n][1] + kb * 4096); }
        if (k == 2) { tr_rd<2 * 4096>(w[2][n].lo, ad[n][0] + kb * 4096); tr_rd<2 * 4096>(w[2][n].hi, ad[n][1] + kb * 4096); }
        if (k == 3) { tr_rd<3 * 4096>(w[3][n].lo, ad[n][0] + kb * 4096); tr_rd<3 * 4096>(w[3][n].hi, ad[n][1] + kb * 4096); }
        if (k == 4) { tr_rd<4 * 4096>(w[4][n].lo, ad[n][0] + kb * 4096); tr_rd<4 * 4096>(w[4][n].hi, ad[n][1] + kb * 4096); }
        if (k == 5) { tr_rd<5 * 4096>(w[5][n].lo, ad[n][0] + kb * 4096); tr_rd<5 * 4096>(w[5][n].hi, ad[n][1] + kb * 4096); }
      }
    wait_lgkm<12>();
#pragma unroll
    for (int k = 0; k < 3; ++k)
#pragma unroll
      for (int n = 0; n < 2; ++n) pin(w[k][n]);
#pragma unroll
    for (int k = 0; k < 3; ++k)
#pragma unroll
      for (int n = 0; n < 2; ++n)
#pragma unroll
        for (int r = 0; r < 2; ++r)
          acc[r][n] = __builtin_amdgcn_mfma_f32_16x16x32_bf16(cat(w[k][n]), an[r][kb + k], acc[r][n], 0, 0, 0);
    wait_lgkm<0>();
#pragma unroll
    for (int k = 3; k < 6; ++k)
#pragma unroll
      for (int n = 0; n < 2; ++n) pin(w[k][n]);
#pragma unroll
    for (int k = 3; k < 6; ++k)
#pragma unroll
      for (int n = 0; n < 2; ++n)
#pragma unroll
        for (int r = 0; r < 2; ++r)
          acc[r][n] = __builtin_amdgcn_mfma_f32_16x16x32_bf16(cat(w[k][n]), an[r][kb + k], acc[r][n], 0, 0, 0);
  };
  grp(IC<0>{}); grp(IC<6>{}); grp(IC<12>{});
  wait_vm<0>();
  STAMP(3, 3);
#pragma unroll
  for (int n = 0; n < 2; ++n) asm volatile("" : "+v"(bb[n]));
#pragma unroll
  for (int r = 0; r < 2; ++r) {
    const int m = m0 + wr * 32 + r * 16 + (lane & 15);
#pragma unroll
    for (int n = 0; n < 2; ++n) {
      const int ncol = n0 + wc * 32 + n * 16 + 4 * g;
      asm volatile("" : "+v"(xx[r][n]));
#pragma unroll
      for (int sI = 0; sI < SPLITK_; ++sI) asm volatile("" : "+v"(py[r][n][sI]));
      f32x4 y = {0.f, 0.f, 0.f, 0.f};
#pragma unroll
      for (int sI = 0; sI < SPLITK_; ++sI) {
        y[0] += lo16(py[r][n][sI][0]); y[1] += hi16(py[r][n][sI][0]);
        y[2] += lo16(py[r][n][sI][1]); y[3] += hi16(py[r][n][sI][1]);
      }
      float bv[4] = {lo16(bb[n][0]), hi16(bb[n][0]), lo16(bb[n][1]), hi16(bb[n][1])};
      float xv[4] = {lo16(xx[r][n][0]), hi16(xx[r][n][0]), lo16(xx[r][n][1]), hi16(xx[r][n][1])};
      float o[4];
#pragma unroll
      for (int j = 0; j < 4; ++j) {
        float yy = y[j] + bv[j];
        float gg = yy / (1.f + __expf(-yy));
        o[j] = xv[j] + acc[r][n][j] * gg;
      }
      u32x2 v = {pack2(o[0], o[1]), pack2(o[2], o[3])};
      *reinterpret_cast<u32x2*>(out + (size_t)m * D_ + ncol) = v;
    }
  }
  STAMP(3, 4);
}

extern "C" void kernel_launch(void* const* d_in, const int* in_sizes, int n_in, void* d_out, int out_size, void* d_ws,
                              size_t ws_size, hipStream_t stream) {
  const u16* x = (const u16*)d_in[0];
  const u16* wqkv = (const u16*)d_in[1];
  const u16* wphi = (const u16*)d_in[2];
  const u16* wo = (const u16*)d_in[3];
  const u16* w1 = (const u16*)d_in[5];
  const u16* b1 = (const u16*)d_in[6];
  const u16* w2 = (const u16*)d_in[7];
  const u16* b2 = (const u16*)d_in[8];
  u16* out = (u16*)d_out;
  char* ws = (char*)d_ws;
  size_t off = 0;
  u16* xnf = (u16*)(ws + off); off += (size_t)S_ * D_ * 2;
  u16* wfold = (u16*)(ws + off); off += (size_t)D_ * NPH_ * 2;
  u16* qkv = (u16*)(ws + off); off += (size_t)96 * S_ * 24 * 2;
  u16* hf = (u16*)(ws + off); off += (size_t)S_ * F_ * 2;
  u16* part = (u16*)(ws + off); off += (size_t)SPLITK_ * S_ * D_ * 2;
  u16* of = (u16*)(ws + off); off += (size_t)S_ * D_ * 2;
  float* pkv = (float*)(ws + off); off += (size_t)256 * 1536 * 4;
  unsigned* cnt = (unsigned*)(ws + off); off += 32 * 32 * 4;

  k0_prep<<<512 + 192, 256, 0, stream>>>(x, wqkv, wphi, xnf, wfold, cnt);
  k1_gemm1<<<256, 512, 0, stream>>>(xnf, wfold, wqkv, w1, b1, qkv, hf);
  k2_gemm2<<<K2_HEADBLK + 256, 256, 0, stream>>>(hf, w2, part, qkv, of, pkv, cnt);
  k3_final<<<16 * 9, 512, 0, stream>>>(of, wo, part, b2, x, out);
}
```

```cpp
#include <hip/hip_runtime.h>
#include <hip/hip_bf16.h>
#include <stdint.h>

constexpr int S_ = 2048, D_ = 576, H_ = 32, DH_ = 18, F_ = 4608, NQKV_ = 1728;
constexpr int NPH_ = 1152;
constexpr int KPH_ = 40;
constexpr int KPHI_ = H_ * KPH_;
constexpr int SPLITK_ = 4;

typedef short bf16x8 __attribute__((ext_vector_type(8)));
typedef short s16x4 __attribute__((ext_vector_type(4)));
typedef float f32x4 __attribute__((ext_vector_type(4)));
typedef unsigned u32x4 __attribute__((ext_vector_type(4)));
typedef unsigned u32x2 __attribute__((ext_vector_type(2)));
typedef __attribute__((address_space(3))) s16x4* lds_s16x4_p;
typedef unsigned short u16;
template <int V> struct IC { static constexpr int value = V; };

#define STAMP(kid, i) do { } while (0)
#define ST_NT(p, v) (*(p) = (v))
#define WAIT_LGKM0() asm volatile("s_waitcnt lgkmcnt(0)" ::: "memory")
#define BAR() __builtin_amdgcn_s_barrier()

template <int N>
__device__ __forceinline__ void wait_vm() {
  static_assert(N >= 0 && N < 64, "vmcnt range");
  asm volatile("s_waitcnt vmcnt(%0)" ::"n"(N) : "memory");
}

__device__ __forceinline__ float bf2f(u16 v) { return __uint_as_float(((unsigned)v) << 16); }
__device__ __forceinline__ u16 f2bf(float f) {
  __hip_bfloat16 b = __float2bfloat16(f);
  return *reinterpret_cast<u16*>(&b);
}
__device__ __forceinline__ unsigned pack2(float lo, float hi) {
  return (unsigned)f2bf(lo) | ((unsigned)f2bf(hi) << 16);
}
__device__ __forceinline__ float rbf(float f) { return bf2f(f2bf(f)); }
__device__ __forceinline__ float lo16(unsigned u) { return __uint_as_float(u << 16); }
__device__ __forceinline__ float hi16(unsigned u) { return __uint_as_float(u & 0xffff0000u); }

__device__ __forceinline__ void glds16(const void* g, void* l) {
  __builtin_amdgcn_global_load_lds((const unsigned*)g, (unsigned*)l, 16, 0, 0);
}

__device__ __forceinline__ void l2_touch(const void* g, void* ldummy) {
  __builtin_amdgcn_global_load_lds((const unsigned*)g, (unsigned*)ldummy, 4, 0, 0);
}

__device__ __forceinline__ bf16x8 swap_halves_if(bf16x8 v, bool odd) {
  bf16x8 s = __builtin_shufflevector(v, v, 4, 5, 6, 7, 0, 1, 2, 3);
  return odd ? s : v;
}

__device__ __forceinline__ bf16x8 wfrag(const char* img, int o0, int o1, int off) {
  s16x4 a = __builtin_amdgcn_ds_read_tr16_b64_v4i16((lds_s16x4_p)(img + o0 + off));
  s16x4 b = __builtin_amdgcn_ds_read_tr16_b64_v4i16((lds_s16x4_p)(img + o1 + off));
  return __builtin_shufflevector(a, b, 0, 1, 2, 3, 4, 5, 6, 7);
}

struct WF { s16x4 lo, hi; };
template <int OFF>
__device__ __forceinline__ void tr_rd(s16x4& r, unsigned addr) {
  asm volatile("ds_read_b64_tr_b16 %0, %1 offset:%2" : "=v"(r) : "v"(addr), "n"(OFF) : "memory");
}
__device__ __forceinline__ void gld16_asm(bf16x8& r, const void* p) {
  asm volatile("global_load_dwordx4 %0, %1, off" : "=v"(r) : "v"(p) : "memory");
}
template <int N>
__device__ __forceinline__ void wait_lgkm() {
  static_assert(N >= 0 && N <= 15, "lgkmcnt range");
  asm volatile("s_waitcnt lgkmcnt(%0)" ::"n"(N) : "memory");
}
__device__ __forceinline__ void pin(WF& w) { asm volatile("" : "+v"(w.lo), "+v"(w.hi)); }
__device__ __forceinline__ void pin(bf16x8& w) { asm volatile("" : "+v"(w)); }
__device__ __forceinline__ bf16x8 cat(const WF& w) { return __builtin_shufflevector(w.lo, w.hi, 0, 1, 2, 3, 4, 5, 6, 7); }
__device__ __forceinline__ unsigned lds_addr(const void* p) {
  return (unsigned)(uintptr_t)(__attribute__((address_space(3))) const char*)p;
}

__global__ __launch_bounds__(256) void k0_prep(const u16* __restrict__ x, const u16* __restrict__ wqkv,
                                               const u16* __restrict__ wphi, u16* __restrict__ xnf,
                                               u16* __restrict__ wfold, unsigned* __restrict__ cnt) {
  __shared__ __attribute__((aligned(16))) float wph[18 * 20];
  const int tid = threadIdx.x, wid = tid >> 6, lane = tid & 63;
  const int bid = blockIdx.x;
  if (bid == 512 && tid < 32) cnt[tid * 32] = 0u;
  if (bid < 512) {
    const int row = bid * 4 + wid;
    const u16* xp = x + (size_t)row * D_;
    u32x4 a = *reinterpret_cast<const u32x4*>(xp + lane * 8);
    u32x4 b = {0u, 0u, 0u, 0u};
    if (lane < 8) b = *reinterpret_cast<const u32x4*>(xp + 512 + lane * 8);
    float v[16];
#pragma unroll
    for (int j = 0; j < 4; ++j) {
      v[2 * j] = lo16(a[j]); v[2 * j + 1] = hi16(a[j]);
      v[8 + 2 * j] = lo16(b[j]); v[8 + 2 * j + 1] = hi16(b[j]);
    }
    float s = 0.f;
#pragma unroll
    for (int j = 0; j < 16; ++j) s += v[j];
#pragma unroll
    for (int o = 32; o >= 1; o >>= 1) s += __shfl_xor(s, o);
    const float mean = s * (1.f / D_);
    float ss = 0.f;
#pragma unroll
    for (int j = 0; j < 8; ++j) { float d = v[j] - mean; ss += d * d; }
    if (lane < 8) {
#pragma unroll
      for (int j = 8; j < 16; ++j) { float d = v[j] - mean; ss += d * d; }
    }
#pragma unroll
    for (int o = 32; o >= 1; o >>= 1) ss += __shfl_xor(ss, o);
    const float rstd = rsqrtf(ss * (1.f / D_) + 1e-5f);
    u32x4 oa, ob;
#pragma unroll
    for (int j = 0; j < 4; ++j) {
      oa[j] = pack2((v[2 * j] - mean) * rstd, (v[2 * j + 1] - mean) * rstd);
      ob[j] = pack2((v[8 + 2 * j] - mean) * rstd, (v[8 + 2 * j + 1] - mean) * rstd);
    }
    {
      const int c = lane;
      u16* dst = xnf + ((size_t)((row >> 4) * 18 + (c >> 2)) * 64 + (c & 3) * 16 + (row & 15)) * 8;
      *reinterpret_cast<u32x4*>(dst) = oa;
    }
    if (lane < 8) {
      const int c = 64 + lane;
      u16* dst = xnf + ((size_t)((row >> 4) * 18 + (c >> 2)) * 64 + (c & 3) * 16 + (row & 15)) * 8;
      *reinterpret_cast<u32x4*>(dst) = ob;
    }
    return;
  }
  {
    const int fb = bid - 512;
    const int hh = fb / 3, k = (fb % 3) * 192 + tid;
    const int h = hh & 31;
    for (int i = tid; i < 18 * 20; i += 256) {
      const int d = i / 20, f = i % 20;
      wph[i] = (f < 18) ? bf2f(wphi[h * 324 + d * 18 + f]) : 0.f;
    }
    unsigned wv[9];
    if (tid < 192) {
      const u16* wp = wqkv + (size_t)k * NQKV_ + hh * 18;
#pragma unroll
      for (int d = 0; d < 9; ++d) wv[d] = *reinterpret_cast<const unsigned*>(wp + 2 * d);
    }
    __syncthreads();
    if (tid < 192) {
      float acc[20];
#pragma unroll
      for (int f = 0; f < 20; ++f) acc[f] = 0.f;
#pragma unroll
      for (int d = 0; d < 18; ++d) {
        const float w = (d & 1) ? hi16(wv[d >> 1]) : lo16(wv[d >> 1]);
#pragma unroll
        for (int f4 = 0; f4 < 5; ++f4) {
          f32x4 pw = *reinterpret_cast<const f32x4*>(wph + d * 20 + f4 * 4);
#pragma unroll
          for (int j = 0; j < 4; ++j) acc[f4 * 4 + j] += w * pw[j];
        }
      }
      u16* op = wfold + (size_t)k * NPH_ + hh * 18;
#pragma unroll
      for (int f = 0; f < 9; ++f) *reinterpret_cast<unsigned*>(op + 2 * f) = pack2(acc[2 * f], acc[2 * f + 1]);
    }
  }
}

constexpr int G1_BUFB = 192 * 128;
constexpr int G1_RING = 4;
constexpr int G1_XOFF = G1_RING * G1_BUFB;
constexpr int G1_BOFF = G1_XOFF + 576 * 32;
constexpr int G1_SMEM = G1_BOFF + 1024;
constexpr int G1_NST = 8;

template <int BASE, int I = 0>
__device__ __forceinline__ void g1_reads(WF (&w)[4], const unsigned (&ad)[4][2]) {
  if constexpr (I < 4) {
    if constexpr (BASE >= 49152) {
      tr_rd<BASE - 49152>(w[I].lo, ad[I][0] + 49152u);
      tr_rd<BASE - 49152>(w[I].hi, ad[I][1] + 49152u);
    } else {
      tr_rd<BASE>(w[I].lo, ad[I][0]);
      tr_rd<BASE>(w[I].hi, ad[I][1]);
    }
    g1_reads<BASE, I + 1>(w, ad);
  }
}
template <int KS>
__device__ __forceinline__ void g1_mma(WF (&w)[4], const bf16x8 (&xa)[2][18], f32x4 (&acc)[2][4]) {
#pragma unroll
  for (int i = 0; i < 4; ++i) pin(w[i]);
#pragma unroll
  for (int i = 0; i < 4; ++i)
#pragma unroll
    for (int r = 0; r < 2; ++r)
      acc[r][i] = __builtin_amdgcn_mfma_f32_16x16x32_bf16(cat(w[i]), xa[r][KS], acc[r][i], 0, 0, 0);
}

__device__ __forceinline__ float gelu_f(float v) {
  constexpr float c1 = -2.f * 0.7978845608028654f * 1.4426950408889634f;
  constexpr float c3 = c1 * 0.044715f;
  const float t = v * v;
  const float e = __builtin_amdgcn_exp2f(v * fmaf(t, c3, c1));
  return v * __builtin_amdgcn_rcpf(1.f + e);
}

typedef float f32x2 __attribute__((ext_vector_type(2)));
__device__ __forceinline__ unsigned cvt_pk(float lo, float hi) {
  unsigned r;
  asm("v_cvt_pk_bf16_f32 %0, %1, %2" : "=v"(r) : "v"(lo), "v"(hi));
  return r;
}
__device__ __forceinline__ unsigned gelu2_pk(f32x2 v) {
  constexpr float c1 = -2.f * 0.7978845608028654f * 1.4426950408889634f;
  constexpr float c3 = c1 * 0.044715f;
  const f32x2 t = v * v;
  const f32x2 pp = t * (f32x2){c3, c3} + (f32x2){c1, c1};
  const f32x2 u = v * pp;
  f32x2 e = {__builtin_amdgcn_exp2f(u[0]), __builtin_amdgcn_exp2f(u[1])};
  e = e + (f32x2){1.f, 1.f};
  const f32x2 r = {__builtin_amdgcn_rcpf(e[0]), __builtin_amdgcn_rcpf(e[1])};
  const f32x2 o = v * r;
  return cvt_pk(o[0], o[1]);
}

__device__ __forceinline__ void g1_store_tile(const f32x4& a, int tile, int mrow, int g, const u16* bias4,
                                              u16* __restrict__ qkv, u16* __restrict__ hf) {
  if (tile < NQKV_ / 16) {
    const int col = tile * 16 + 4 * g;
    const int hh = col / 18, f = col - hh * 18;
    u16* dst = qkv + ((size_t)hh * S_ + mrow) * 24 + f;
    const unsigned v0 = cvt_pk(a[0], a[1]), v1 = cvt_pk(a[2], a[3]);
    if (f != 16) {
      u32x2 v = {v0, v1};
      *reinterpret_cast<u32x2*>(dst) = v;
    } else {
      *reinterpret_cast<unsigned*>(dst) = v0;
      *reinterpret_cast<unsigned*>(qkv + ((size_t)(hh + 1) * S_ + mrow) * 24) = v1;
    }
  } else {
    u32x2 bb = *reinterpret_cast<const u32x2*>(bias4);
    float o[4];
    o[0] = gelu_f(a[0] + lo16(bb[0]));
    o[1] = gelu_f(a[1] + hi16(bb[0]));
    o[2] = gelu_f(a[2] + lo16(bb[1]));
    o[3] = gelu_f(a[3] + hi16(bb[1]));
    u32x2 v = {pack2(o[0], o[1]), pack2(o[2], o[3])};
    const int T = tile - NQKV_ / 16;
    u16* dst = hf + ((size_t)((mrow >> 4) * 144 + (T >> 1)) * 64 + ((T & 1) * 2 + (g >> 1)) * 16 + (mrow & 15)) * 8 + (g & 1) * 4;
    *reinterpret_cast<u32x2*>(dst) = v;
  }
}

__device__ __forceinline__ void g1_addr(unsigned (&ad)[4][2], char* smem, int lane) {
  const int g = lane >> 4, q = (lane & 15) >> 2, p = lane & 3;
#pragma unroll
  for (int t = 0; t < 2; ++t) {
    const int row = 8 * g + 4 * t + q;
    const int f = ((row >> 1) & 1) | (((row >> 3) & 1) << 1);
#pragma unroll
    for (int n = 0; n < 4; ++n) ad[n][t] = lds_addr(smem) + row * 128 + ((n ^ f) * 32) + p * 8;
  }
}

struct G1W { const u16* src; int stride; };

__device__ __forceinline__ void g1_wsetup(G1W& w, int col, int wid, int lane, const u16* wfold, const u16* wqkv, const u16* w1) {
  const int row = wid * 8 + (lane >> 3);
  const int f = ((row >> 1) & 1) | (((row >> 3) & 1) << 1);
  const int ch = (lane & 7) ^ (f << 1);
  if (col < NPH_) { w.src = wfold + (size_t)row * NPH_ + col + ch * 8; w.stride = 64 * NPH_; }
  else if (col < NQKV_) { w.src = wqkv + (size_t)row * NQKV_ + col + ch * 8; w.stride = 64 * NQKV_; }
  else { w.src = w1 + (size_t)row * F_ + (col - NQKV_) + ch * 8; w.stride = 64 * F_; }
}
__device__ __forceinline__ void g1_wissue(G1W& w, char* buf, int wid, int lane) {
#pragma unroll
  for (int i = 0; i < 3; ++i) {
    glds16(w.src, buf + i * 8192 + wid * 1024 + lane * 16);
    w.src += w.stride;
  }
}

template <int U>
__device__ __forceinline__ void g1_unit(G1W& wst, int unit0, const u16* wfold, const u16* wqkv,
                                        const u16* w1, char* smem, bf16x8 (&xa)[2][18], int m0,
                                        u16* __restrict__ qkv, u16* __restrict__ hf, WF (&wA)[4]) {
  int tz = threadIdx.x;
  asm volatile("" : "+v"(tz));
  const int wid = __builtin_amdgcn_readfirstlane(tz >> 6), lane = tz & 63;
  const int g = lane >> 4;
  unsigned ad[4][2];
  g1_addr(ad, smem, lane);
  f32x4 acc[2][4];
#pragma unroll
  for (int r = 0; r < 2; ++r)
#pragma unroll
    for (int n = 0; n < 4; ++n) acc[r][n] = (f32x4){0.f, 0.f, 0.f, 0.f};

  auto stage = [&](auto KC) {
    constexpr int kc = decltype(KC)::value;
    constexpr int s = U * 3 + kc;
    constexpr int CUR = (s % G1_RING) * G1_BUFB;
    if constexpr (U == 0) {
#pragma unroll
      for (int k = 0; k < 6; ++k) { pin(xa[0][kc * 6 + k]); pin(xa[1][kc * 6 + k]); }
    }
    WF wB[4];
    g1_reads<CUR + 1 * 4096>(wB, ad); wait_lgkm<8>(); g1_mma<kc * 6 + 0>(wA, xa, acc);
    g1_reads<CUR + 2 * 4096>(wA, ad); wait_lgkm<8>(); g1_mma<kc * 6 + 1>(wB, xa, acc);
    g1_reads<CUR + 3 * 4096>(wB, ad); wait_lgkm<8>(); g1_mma<kc * 6 + 2>(wA, xa, acc);
    g1_reads<CUR + 4 * 4096>(wA, ad); wait_lgkm<8>(); g1_mma<kc * 6 + 3>(wB, xa, acc);
    g1_reads<CUR + 5 * 4096>(wB, ad); wait_lgkm<8>(); g1_mma<kc * 6 + 4>(wA, xa, acc);
    if constexpr (s < 8) {
      constexpr int nl = (s == 0) ? 12 : ((s + 2 <= 8) ? 3 : 0);
      constexpr int nst = (U > 0 && kc <= 1) ? G1_NST : 0;
      wait_vm<nl + nst>();
      BAR();
      if constexpr (s + 3 <= 8) {
        constexpr int sn = s + 3;
        if constexpr (sn % 3 == 0) g1_wsetup(wst, (unit0 + sn / 3) * 64, wid, lane, wfold, wqkv, w1);
        g1_wissue(wst, smem + (sn % G1_RING) * G1_BUFB, wid, lane);
      }
      g1_reads<((s + 1) % G1_RING) * G1_BUFB>(wA, ad);
      wait_lgkm<8>();
    } else {
      wait_lgkm<0>();
    }
    g1_mma<kc * 6 + 5>(wB, xa, acc);
  };
  stage(IC<0>{}); stage(IC<1>{}); stage(IC<2>{});
  const u16* bias_lds = reinterpret_cast<const u16*>(smem + G1_BOFF);
  if ((unit0 + U) * 64 < NQKV_) {
#pragma unroll
    for (int n = 0; n < 4; ++n) {
      const int tile = (unit0 + U) * 4 + n;
#pragma unroll
      for (int r = 0; r < 2; ++r)
        g1_store_tile(acc[r][n], tile, m0 + wid * 32 + r * 16 + (lane & 15), g, bias_lds, qkv, hf);
    }
  } else {
    u32x2 bb[4];
#pragma unroll
    for (int n = 0; n < 4; ++n) bb[n] = *reinterpret_cast<const u32x2*>(bias_lds + (U * 4 + n) * 16 + 4 * g);
    const int T0 = (unit0 + U) * 4 - NQKV_ / 16;
#pragma unroll
    for (int r = 0; r < 2; ++r) {
      const int mrow = m0 + wid * 32 + r * 16 + (lane & 15);
      u16* dst = hf + ((size_t)((mrow >> 4) * 144 + (T0 >> 1)) * 64 + (g >> 1) * 16 + (mrow & 15)) * 8 + (g & 1) * 4;
#pragma unroll
      for (int n = 0; n < 4; ++n) {
        const f32x2 v0 = (f32x2){acc[r][n][0], acc[r][n][1]} + (f32x2){lo16(bb[n][0]), hi16(bb[n][0])};
        const f32x2 v1 = (f32x2){acc[r][n][2], acc[r][n][3]} + (f32x2){lo16(bb[n][1]), hi16(bb[n][1])};
        u32x2 v = {gelu2_pk(v0), gelu2_pk(v1)};
        ST_NT(reinterpret_cast<u32x2*>(dst + (n >> 1) * 512 + (n & 1) * 256), v);
      }
    }
  }
}

__global__ __launch_bounds__(512, 2) void k1_gemm1(const u16* __restrict__ xnf, const u16* __restrict__ wfold,
                                                   const u16* __restrict__ wqkv, const u16* __restrict__ w1,
                                                   const u16* __restrict__ b1, u16* __restrict__ qkv,
                                                   u16* __restrict__ hf) {
  __shared__ __attribute__((aligned(16))) char smem[G1_SMEM];
  const int tid = threadIdx.x, wid = tid >> 6, lane = tid & 63;
  const int bid = blockIdx.x;
  const int mt = bid >> 5, grp = bid & 31;
  const int m0 = mt * 256;
  const int unit0 = grp * 3;
  const bool extra = grp >= 20;
  const int xcol = 6144 + (extra ? (grp - 20) : 0) * 16;
  STAMP(1, 0);
  G1W wst;
  g1_wsetup(wst, unit0 * 64, wid, lane, wfold, wqkv, w1);
  g1_wissue(wst, smem + 0 * G1_BUFB, wid, lane);
  g1_wissue(wst, smem + 1 * G1_BUFB, wid, lane);
  g1_wissue(wst, smem + 2 * G1_BUFB, wid, lane);
#pragma unroll
  for (int i = 0; i < 3; ++i) {
    int ins = wid + 8 * i;
    if (ins >= 18) ins -= 18;
    const int c = ins * 64 + lane;
    const int pr = c >> 1, half = c & 1;
    const int k = (pr & ~0xC) | ((pr & 4) << 1) | ((pr & 8) >> 1);
    glds16(w1 + (size_t)k * F_ + (xcol - NQKV_) + half * 8, smem + G1_XOFF + c * 16);
  }
  {
    const int e = (wid & 1) * 64 + lane;
    int col = (e < 96) ? (unit0 * 64 + 2 * e - NQKV_) : (xcol - NQKV_ + 2 * (e - 96));
    if (col < 0 || e >= 104) col = 0;
    __builtin_amdgcn_global_load_lds((const unsigned*)(b1 + col), (unsigned*)(smem + G1_BOFF + e * 4), 4, 0, 0);
  }
  bf16x8 xa[2][18];
#pragma unroll
  for (int ks = 0; ks < 18; ++ks)
#pragma unroll
    for (int r = 0; r < 2; ++r)
      gld16_asm(xa[r][ks], xnf + ((size_t)(((m0 >> 4) + wid * 2 + r) * 18 + ks) * 64 + lane) * 8);
  wait_vm<24>();
  BAR();
  WF wA[4];
  {
    unsigned ad[4][2];
    g1_addr(ad, smem, lane);
    g1_reads<0>(wA, ad);
  }
  STAMP(1, 1);
  g1_unit<0>(wst, unit0, wfold, wqkv, w1, smem, xa, m0, qkv, hf, wA);
  STAMP(1, 2);
  g1_unit<1>(wst, unit0, wfold, wqkv, w1, smem, xa, m0, qkv, hf, wA);
  STAMP(1, 3);
  g1_unit<2>(wst, unit0, wfold, wqkv, w1, smem, xa, m0, qkv, hf, wA);
  STAMP(1, 4);
  if (extra) {
    int tz = threadIdx.x;
    asm volatile("" : "+v"(tz));
    const int wid2 = __builtin_amdgcn_readfirstlane(tz >> 6), lane2 = tz & 63;
    const int g = lane2 >> 4, q = (lane2 & 15) >> 2, p = lane2 & 3;
    unsigned ad[2];
#pragma unroll
    for (int t = 0; t < 2; ++t) {
      const int k = 8 * g + 4 * t + q;
      const int pr = (k & ~0xC) | ((k & 4) << 1) | ((k & 8) >> 1);
      ad[t] = lds_addr(smem) + G1_XOFF + pr * 32 + p * 8;
    }
    f32x4 acc[2] = {(f32x4){0.f, 0.f, 0.f, 0.f}, (f32x4){0.f, 0.f, 0.f, 0.f}};
#pragma unroll
    for (int kb = 0; kb < 18; kb += 6) {
      WF w[6];
      tr_rd<0 * 1024>(w[0].lo, ad[0] + kb * 1024); tr_rd<0 * 1024>(w[0].hi, ad[1] + kb * 1024);
      tr_rd<1 * 1024>(w[1].lo, ad[0] + kb * 1024); tr_rd<1 * 1024>(w[1].hi, ad[1] + kb * 1024);
      tr_rd<2 * 1024>(w[2].lo, ad[0] + kb * 1024); tr_rd<2 * 1024>(w[2].hi, ad[1] + kb * 1024);
      tr_rd<3 * 1024>(w[3].lo, ad[0] + kb * 1024); tr_rd<3 * 1024>(w[3].hi, ad[1] + kb * 1024);
      tr_rd<4 * 1024>(w[4].lo, ad[0] + kb * 1024); tr_rd<4 * 1024>(w[4].hi, ad[1] + kb * 1024);
      tr_rd<5 * 1024>(w[5].lo, ad[0] + kb * 1024); tr_rd<5 * 1024>(w[5].hi, ad[1] + kb * 1024);
      wait_lgkm<0>();
#pragma unroll
      for (int i = 0; i < 6; ++i) pin(w[i]);
#pragma unroll
      for (int i = 0; i < 6; ++i)
#pragma unroll
        for (int r = 0; r < 2; ++r)
          acc[r] = __builtin_amdgcn_mfma_f32_16x16x32_bf16(cat(w[i]), xa[r][kb + i], acc[r], 0, 0, 0);
    }
    const u16* bias_lds = reinterpret_cast<const u16*>(smem + G1_BOFF);
#pragma unroll
    for (int r = 0; r < 2; ++r)
      g1_store_tile(acc[r], xcol / 16, m0 + wid2 * 32 + r * 16 + (lane2 & 15), g, bias_lds + 192 + 4 * g, qkv, hf);
  }
}

constexpr int G2_NT = 9, G2_RB = G2_NT * 32;
constexpr int G2_BUFB = 64 * G2_RB;
constexpr int G2_RING = 4;
constexpr int G2_STAGES = 18;

__device__ __forceinline__ void softmax_pm(const float (&pk)[18], float (&ep)[18], float (&en)[18]) {
  float mx = pk[0], mn = pk[0];
#pragma unroll
  for (int f = 1; f < 18; ++f) {
    mx = fmaxf(mx, pk[f]);
    mn = fminf(mn, pk[f]);
  }
  float sp = 0.f, sn = 0.f;
#pragma unroll
  for (int f = 0; f < 18; ++f) {
    ep[f] = __expf(pk[f] - mx);
    en[f] = __expf(mn - pk[f]);
    sp += ep[f];
    sn += en[f];
  }
  const float rp = 1.f / sp, rn = 1.f / sn;
#pragma unroll
  for (int f = 0; f < 18; ++f) {
    ep[f] *= rp;
    en[f] *= rn;
  }
}

__device__ void head_block(int hb, const u16* __restrict__ qkv, u16* __restrict__ of, float* pkv, unsigned* cnt, char* smem) {
  u16* phiT = reinterpret_cast<u16*>(smem);
  u16* vT = reinterpret_cast<u16*>(smem + 25344);
  float* kvs = reinterpret_cast<float*>(smem + 25344 + 16896);
  const int h = hb >> 3, c8 = hb & 7;
  const int tid = threadIdx.x, wid = tid >> 6, lane = tid & 63;
  const int row = c8 * 256 + tid;
  u32x4 kr[3], vr[3], qr[3];
  {
    const u32x4* kp = reinterpret_cast<const u32x4*>(qkv + ((size_t)(32 + h) * S_ + row) * 24);
    const u32x4* vp = reinterpret_cast<const u32x4*>(qkv + ((size_t)(64 + h) * S_ + row) * 24);
    const u32x4* qp = reinterpret_cast<const u32x4*>(qkv + ((size_t)h * S_ + row) * 24);
#pragma unroll
    for (int d = 0; d < 3; ++d) { kr[d] = kp[d]; vr[d] = vp[d]; qr[d] = qp[d]; }
  }
  for (int i = tid; i < 12 * 264; i += 256) phiT[36 * 264 + i] = 0;
  for (int i = tid; i < 14 * 264; i += 256) vT[18 * 264 + i] = (i < 264) ? (u16)0x3f80 : (u16)0;
  {
    float pk[18];
#pragma unroll
    for (int d = 0; d < 9; ++d) {
      pk[2 * d] = lo16(kr[d >> 2][d & 3]);
      pk[2 * d + 1] = hi16(kr[d >> 2][d & 3]);
    }
    float ep[18], en[18];
    softmax_pm(pk, ep, en);
#pragma unroll
    for (int f = 0; f < 18; ++f) {
      phiT[f * 264 + tid] = f2bf(ep[f]);
      phiT[(18 + f) * 264 + tid] = f2bf(en[f]);
      const unsigned w = vr[(f >> 1) >> 2][(f >> 1) & 3];
      vT[f * 264 + tid] = (u16)((f & 1) ? (w >> 16) : (w & 0xffff));
    }
  }
  __syncthreads();
  {
    f32x4 acc[3][2];
#pragma unroll
    for (int a = 0; a < 3; ++a)
#pragma unroll
      for (int b = 0; b < 2; ++b) acc[a][b] = (f32x4){0.f, 0.f, 0.f, 0.f};
#pragma unroll
    for (int ks = 0; ks < 2; ++ks) {
      const int s0 = wid * 64 + ks * 32 + 8 * (lane >> 4);
      bf16x8 bv[2];
#pragma unroll
      for (int b = 0; b < 2; ++b) bv[b] = *reinterpret_cast<const bf16x8*>(vT + (b * 16 + (lane & 15)) * 264 + s0);
#pragma unroll
      for (int a = 0; a < 3; ++a) {
        bf16x8 av = *reinterpret_cast<const bf16x8*>(phiT + (a * 16 + (lane & 15)) * 264 + s0);
#pragma unroll
        for (int b = 0; b < 2; ++b) acc[a][b] = __builtin_amdgcn_mfma_f32_16x16x32_bf16(av, bv[b], acc[a][b], 0, 0, 0);
      }
    }
#pragma unroll
    for (int a = 0; a < 3; ++a)
#pragma unroll
      for (int b = 0; b < 2; ++b)
#pragma unroll
        for (int j = 0; j < 4; ++j)
          kvs[(wid * 48 + a * 16 + (lane >> 4) * 4 + j) * 32 + b * 16 + (lane & 15)] = acc[a][b][j];
  }
  __syncthreads();
  float* mypart = pkv + (size_t)(h * 8 + c8) * 1536;
#pragma unroll
  for (int it = 0; it < 6; ++it) {
    const int i = it * 256 + tid;
    const float v = kvs[i] + kvs[1536 + i] + kvs[2 * 1536 + i] + kvs[3 * 1536 + i];
    __hip_atomic_store(mypart + i, v, __ATOMIC_RELAXED, __HIP_MEMORY_SCOPE_AGENT);
  }
  asm volatile("s_waitcnt vmcnt(0)" ::: "memory");
  __syncthreads();
  if (tid == 0) __hip_atomic_fetch_add(cnt + h * 32, 1u, __ATOMIC_RELAXED, __HIP_MEMORY_SCOPE_AGENT);
  float ep[18], en[18];
  {
    float pk[18];
#pragma unroll
    for (int d = 0; d < 9; ++d) {
      pk[2 * d] = lo16(qr[d >> 2][d & 3]);
      pk[2 * d + 1] = hi16(qr[d >> 2][d & 3]);
    }
    softmax_pm(pk, ep, en);
#pragma unroll
    for (int f = 0; f < 18; ++f) { ep[f] = rbf(ep[f]); en[f] = rbf(en[f]); }
  }
  if (tid == 0) {
    unsigned spins = 0;
    while (__hip_atomic_load(cnt + h * 32, __ATOMIC_RELAXED, __HIP_MEMORY_SCOPE_AGENT) < 8u) {
      __builtin_amdgcn_s_sleep(1);
      if (++spins > (1u << 24)) break;
    }
  }
  __syncthreads();
  u16* kvT = reinterpret_cast<u16*>(smem);
  u16* phiR = reinterpret_cast<u16*>(smem + 4608);
  {
    float* hp = pkv + (size_t)h * 8 * 1536;
    float pv[5][8];
#pragma unroll
    for (int it = 0; it < 5; ++it) {
      const int i = it * 256 + tid;
#pragma unroll
      for (int c = 0; c < 8; ++c) pv[it][c] = __hip_atomic_load(hp + c * 1536 + i, __ATOMIC_RELAXED, __HIP_MEMORY_SCOPE_AGENT);
    }
    {
      unsigned o[32];
#pragma unroll
      for (int f = 0; f < 9; ++f) { o[f] = pack2(ep[2 * f], ep[2 * f + 1]); o[9 + f] = pack2(en[2 * f], en[2 * f + 1]); }
#pragma unroll
      for (int f = 18; f < 32; ++f) o[f] = 0u;
#pragma unroll
      for (int c = 0; c < 8; ++c) {
        u32x4 v = {o[4 * c], o[4 * c + 1], o[4 * c + 2], o[4 * c + 3]};
        *reinterpret_cast<u32x4*>(phiR + tid * 72 + c * 8) = v;
      }
    }
#pragma unroll
    for (int it = 0; it < 8; ++it) {
      const int i = it * 256 + tid;
      const int f = i >> 5, d = i & 31;
      float v = 0.f;
      if (it < 5) {
#pragma unroll
        for (int c = 0; c < 8; ++c) v += pv[it < 5 ? it : 0][c];
      }
      kvT[d * 72 + f] = ((d < 19) && (f < 36)) ? f2bf(v) : (u16)0;
    }
  }
  __syncthreads();
  {
    const int g = lane >> 4;
    bf16x8 ka[2][2];
#pragma unroll
    for (int t = 0; t < 2; ++t)
#pragma unroll
      for (int ks = 0; ks < 2; ++ks)
        ka[t][ks] = *reinterpret_cast<const bf16x8*>(kvT + (t * 16 + (lane & 15)) * 72 + ks * 32 + 8 * g);
#pragma unroll
    for (int rt = 0; rt < 4; ++rt) {
      const int rl = wid * 64 + rt * 16 + (lane & 15);
      f32x4 c0 = {0.f, 0.f, 0.f, 0.f}, c1 = {0.f, 0.f, 0.f, 0.f};
#pragma unroll
      for (int ks = 0; ks < 2; ++ks) {
        const bf16x8 pb = *reinterpret_cast<const bf16x8*>(phiR + rl * 72 + ks * 32 + 8 * g);
        c0 = __builtin_amdgcn_mfma_f32_16x16x32_bf16(ka[0][ks], pb, c0, 0, 0, 0);
        c1 = __builtin_amdgcn_mfma_f32_16x16x32_bf16(ka[1][ks], pb, c1, 0, 0, 0);
      }
      const float den = __shfl(c1[2], lane & 15);
      const float rd = 1.f / (rbf(den) + 1e-6f);
      const int grow = c8 * 256 + rl;
      u16* obase = of + (size_t)(grow >> 4) * 18 * 512 + (grow & 15) * 8;
#pragma unroll
      for (int j2 = 0; j2 < 2; ++j2) {
        const int k = h * 18 + 4 * g + 2 * j2;
        *reinterpret_cast<unsigned*>(obase + (k >> 5) * 512 + ((k >> 3) & 3) * 128 + (k & 7)) =
            pack2(rbf(c0[2 * j2]) * rd, rbf(c0[2 * j2 + 1]) * rd);
      }
      if (g == 0) {
        const int k = h * 18 + 16;
        *reinterpret_cast<unsigned*>(obase + (k >> 5) * 512 + ((k >> 3) & 3) * 128 + (k & 7)) =
            pack2(rbf(c1[0]) * rd, rbf(c1[1]) * rd);
      }
    }
  }
}

template <int BASE, int CNT, int I = 0>
__device__ __forceinline__ void g2_wreads(WF (&w)[5], unsigned a0, unsigned a1) {
  if constexpr (I < CNT) {
    tr_rd<BASE + I * 32>(w[I].lo, a0);
    tr_rd<BASE + I * 32>(w[I].hi, a1);
    g2_wreads<BASE, CNT, I + 1>(w, a0, a1);
  }
}
__device__ __forceinline__ void g2_mma(WF (&w)[5], const bf16x8 (&ac)[4], f32x4 (&acc)[4][5]) {
#pragma unroll
  for (int i = 0; i < 5; ++i) pin(w[i]);
#pragma unroll
  for (int i = 0; i < 5; ++i)
#pragma unroll
    for (int r = 0; r < 4; ++r)
      acc[r][i] = __builtin_amdgcn_mfma_f32_16x16x32_bf16(cat(w[i]), ac[r], acc[r][i], 0, 0, 0);
}

constexpr int K2_HEADBLK = 256;

__global__ __launch_bounds__(256, 2) void k2_gemm2(const u16* __restrict__ hf, const u16* __restrict__ w2,
                                                   u16* __restrict__ part, const u16* __restrict__ qkv,
                                                   u16* __restrict__ of, float* pkv, unsigned* cnt) {
  __shared__ __attribute__((aligned(16))) char smem[G2_RING * G2_BUFB];
  const int tid = threadIdx.x, wid = tid >> 6, lane = tid & 63;
  int bid = blockIdx.x;
  if (bid < K2_HEADBLK) { head_block(bid, qkv, of, pkv, cnt, smem); return; }
  bid -= K2_HEADBLK;
  const int wm = wid >> 1, wn = wid & 1;
  const int xcd = bid & 7, loc = bid >> 3;
  const int sp = xcd >> 1, mt = (xcd & 1) * 8 + (loc >> 2), nt4 = loc & 3;
  const int m0 = mt * 128, n0 = nt4 * 144, k0 = sp * 1152;
  const int g = lane >> 4, q = (lane & 15) >> 2, p = lane & 3;
  const bool odd = g & 1;
  const unsigned wa0 = lds_addr(smem) + (8 * g + 4 * (g & 1) + q) * G2_RB + p * 8 + wn * 128;
  const unsigned wa1 = lds_addr(smem) + (8 * g + 4 * (1 - (g & 1)) + q) * G2_RB + p * 8 + wn * 128;
  f32x4 acc[4][5];
#pragma unroll
  for (int r = 0; r < 4; ++r)
#pragma unroll
    for (int n = 0; n < 5; ++n) acc[r][n] = (f32x4){0.f, 0.f, 0.f, 0.f};
  const u16* wp[5];
  int wdst[5];
#pragma unroll
  for (int i = 0; i < 5; ++i) {
    int ins = wid + 4 * i;
    if (ins >= 18) ins -= 18;
    int c = ins * 64 + lane;
    int row = c / 18, cc = c % 18;
    wp[i] = w2 + (size_t)(k0 + row) * D_ + n0 + cc * 8;
    wdst[i] = c * 16;
  }
  const u16* ap = hf + ((size_t)((m0 >> 4) + wm * 4) * 144 + (k0 >> 5)) * 512 + lane * 8;
  bf16x8 an[3][4][2];
  auto issueW = [&](auto SL) {
    constexpr int slot = decltype(SL)::value;
    char* buf = smem + slot * G2_BUFB;
#pragma unroll
    for (int i = 0; i < 5; ++i) { glds16(wp[i], buf + wdst[i]); wp[i] += 64 * D_; }
  };
  auto issueA = [&](auto SL) {
    constexpr int slot = decltype(SL)::value;
#pragma unroll
    for (int r = 0; r < 4; ++r) {
      gld16_asm(an[slot][r][0], ap + (size_t)r * 144 * 512);
      gld16_asm(an[slot][r][1], ap + (size_t)r * 144 * 512 + 512);
    }
    ap += 1024;
  };
  issueW(IC<0>{}); issueA(IC<0>{}); issueW(IC<1>{}); issueW(IC<2>{}); issueA(IC<1>{});
  wait_vm<18>();
  BAR();
  WF wA[5], wB[5];
  g2_wreads<0, 5>(wA, wa0, wa1);
  auto stage = [&](auto U, auto AS, auto LAST, auto WN) {
    constexpr int u = decltype(U)::value;
    constexpr int as = decltype(AS)::value;
    constexpr int rem = decltype(LAST)::value;
    constexpr int BASE = u * G2_BUFB;
    bf16x8 c0[4], c1[4];
#pragma unroll
    for (int r = 0; r < 4; ++r) {
      pin(an[as][r][0]); pin(an[as][r][1]);
      c0[r] = swap_halves_if(an[as][r][0], odd);
      c1[r] = swap_halves_if(an[as][r][1], odd);
    }
    g2_wreads<BASE + 32 * G2_RB, 5>(wB, wa0, wa1);
    wait_lgkm<10>();
    g2_mma(wA, c0, acc);
    if constexpr (rem >= 1) {
      wait_vm<decltype(WN)::value>();
      BAR();
      if constexpr (rem >= 2) issueA(IC<(as + 2) % 3>{});
      if constexpr (rem >= 3) issueW(IC<(u + 3) & 3>{});
      g2_wreads<((u + 1) & 3) * G2_BUFB, 5>(wA, wa0, wa1);
      wait_lgkm<10>();
    } else {
      wait_lgkm<0>();
    }
    g2_mma(wB, c1, acc);
  };
  stage(IC<0>{}, IC<0>{}, IC<3>{}, IC<0>{});
  stage(IC<1>{}, IC<1>{}, IC<3>{}, IC<5>{});
  stage(IC<2>{}, IC<2>{}, IC<3>{}, IC<5>{});
  stage(IC<3>{}, IC<0>{}, IC<3>{}, IC<5>{});
  stage(IC<0>{}, IC<1>{}, IC<3>{}, IC<5>{});
  stage(IC<1>{}, IC<2>{}, IC<3>{}, IC<5>{});
  stage(IC<2>{}, IC<0>{}, IC<3>{}, IC<5>{});
  stage(IC<3>{}, IC<1>{}, IC<3>{}, IC<5>{});
  stage(IC<0>{}, IC<2>{}, IC<3>{}, IC<5>{});
  stage(IC<1>{}, IC<0>{}, IC<3>{}, IC<5>{});
  stage(IC<2>{}, IC<1>{}, IC<3>{}, IC<5>{});
  stage(IC<3>{}, IC<2>{}, IC<3>{}, IC<5>{});
  stage(IC<0>{}, IC<0>{}, IC<3>{}, IC<5>{});
  stage(IC<1>{}, IC<1>{}, IC<3>{}, IC<5>{});
  stage(IC<2>{}, IC<2>{}, IC<3>{}, IC<5>{});
  stage(IC<3>{}, IC<0>{}, IC<2>{}, IC<5>{});
  stage(IC<0>{}, IC<1>{}, IC<1>{}, IC<0>{});
  stage(IC<1>{}, IC<2>{}, IC<0>{}, IC<0>{});
#pragma unroll
  for (int r = 0; r < 4; ++r) {
    const int m = m0 + wm * 64 + r * 16 + (lane & 15);
    u16* pp = part + ((size_t)sp * S_ + m) * D_ + n0 + wn * 64 + 4 * g;
#pragma unroll
    for (int n = 0; n < 5; ++n) {
      if (n == 0 && wn == 1) continue;
      u32x2 v = {cvt_pk(acc[r][n][0], acc[r][n][1]), cvt_pk(acc[r][n][2], acc[r][n][3])};
      *reinterpret_cast<u32x2*>(pp + n * 16) = v;
    }
  }
}

__global__ __launch_bounds__(512, 2) void k3_final(const u16* __restrict__ of, const u16* __restrict__ wo,
                                                   const u16* __restrict__ part, const u16* __restrict__ b2,
                                                   const u16* __restrict__ x, u16* __restrict__ out) {
  __shared__ __attribute__((aligned(16))) char smem[576 * 128];
  const int tid = threadIdx.x, wid = tid >> 6, lane = tid & 63;
  const int bid = blockIdx.x;
  const int xcd = bid & 7, loc = bid >> 3;
  const int mt = xcd * 2 + loc / 9, nt = loc % 9;
  const int m0 = mt * 128, n0 = nt * 64;
  const int g = lane >> 4, q = (lane & 15) >> 2, p = lane & 3;
  const int wr = wid >> 1, wc = wid & 1;
  STAMP(3, 0);
#pragma unroll
  for (int i = 0; i < 9; ++i) {
    const int row = (wid * 9 + i) * 8 + (lane >> 3);
    const int f = ((row >> 1) & 1) | (((row >> 3) & 1) << 1);
    const int ch = (lane & 7) ^ (f << 1);
    glds16(wo + (size_t)row * D_ + n0 + ch * 8, smem + (wid * 9 + i) * 1024 + lane * 16);
  }
  bf16x8 an[2][18];
#pragma unroll
  for (int ks = 0; ks < 18; ++ks)
#pragma unroll
    for (int r = 0; r < 2; ++r)
      gld16_asm(an[r][ks], of + ((size_t)(((m0 >> 4) + wr * 2 + r) * 18 + ks) * 64 + lane) * 8);
  u32x2 py[2][2][4];
  u32x2 xx[2][2], bb[2];
#pragma unroll
  for (int r = 0; r < 2; ++r) {
    const int m = m0 + wr * 32 + r * 16 + (lane & 15);
#pragma unroll
    for (int n = 0; n < 2; ++n) {
      const int ncol = n0 + wc * 32 + n * 16 + 4 * g;
#pragma unroll
      for (int sI = 0; sI < SPLITK_; ++sI)
        asm volatile("global_load_dwordx2 %0, %1, off" : "=v"(py[r][n][sI]) : "v"(part + ((size_t)sI * S_ + m) * D_ + ncol) : "memory");
      asm volatile("global_load_dwordx2 %0, %1, off" : "=v"(xx[r][n]) : "v"(x + (size_t)m * D_ + ncol) : "memory");
    }
  }
#pragma unroll
  for (int n = 0; n < 2; ++n)
    asm volatile("global_load_dwordx2 %0, %1, off" : "=v"(bb[n]) : "v"(b2 + n0 + wc * 32 + n * 16 + 4 * g) : "memory");
  unsigned ad[2][2];
#pragma unroll
  for (int t = 0; t < 2; ++t) {
    const int row = 8 * g + 4 * t + q;
    const int f = ((row >> 1) & 1) | (((row >> 3) & 1) << 1);
#pragma unroll
    for (int n = 0; n < 2; ++n) ad[n][t] = lds_addr(smem) + row * 128 + (((wc * 2 + n) ^ f) * 32) + p * 8;
  }
  f32x4 acc[2][2];
#pragma unroll
  for (int r = 0; r < 2; ++r)
#pragma unroll
    for (int n = 0; n < 2; ++n) acc[r][n] = (f32x4){0.f, 0.f, 0.f, 0.f};
  STAMP(3, 1);
  wait_vm<22>();
  BAR();
  STAMP(3, 2);
#pragma unroll
  for (int ks = 0; ks < 18; ++ks) { pin(an[0][ks]); pin(an[1][ks]); }
  auto grp = [&](auto KB) {
    constexpr int kb = decltype(KB)::value;
    WF w[6][2];
#pragma unroll
    for (int k = 0; k < 6; ++k)
#pragma unroll
      for (int n = 0; n < 2; ++n) {
        if (k == 0) { tr_rd<0 * 4096>(w[0][n].lo, ad[n][0] + kb * 4096); tr_rd<0 * 4096>(w[0][n].hi, ad[n][1] + kb * 4096); }
        if (k == 1) { tr_rd<1 * 4096>(w[1][n].lo, ad[n][0] + kb * 4096); tr_rd<1 * 4096>(w[1][n].hi, ad[n][1] + kb * 4096); }
        if (k == 2) { tr_rd<2 * 4096>(w[2][n].lo, ad[n][0] + kb * 4096); tr_rd<2 * 4096>(w[2][n].hi, ad[n][1] + kb * 4096); }
        if (k == 3) { tr_rd<3 * 4096>(w[3][n].lo, ad[n][0] + kb * 4096); tr_rd<3 * 4096>(w[3][n].hi, ad[n][1] + kb * 4096); }
        if (k == 4) { tr_rd<4 * 4096>(w[4][n].lo, ad[n][0] + kb * 4096); tr_rd<4 * 4096>(w[4][n].hi, ad[n][1] + kb * 4096); }
        if (k == 5) { tr_rd<5 * 4096>(w[5][n].lo, ad[n][0] + kb * 4096); tr_rd<5 * 4096>(w[5][n].hi, ad[n][1] + kb * 4096); }
      }
    wait_lgkm<12>();
#pragma unroll
    for (int k = 0; k < 3; ++k)
#pragma unroll
      for (int n = 0; n < 2; ++n) pin(w[k][n]);
#pragma unroll
    for (int k = 0; k < 3; ++k)
#pragma unroll
      for (int n = 0; n < 2; ++n)
#pragma unroll
        for (int r = 0; r < 2; ++r)
          acc[r][n] = __builtin_amdgcn_mfma_f32_16x16x32_bf16(cat(w[k][n]), an[r][kb + k], acc[r][n], 0, 0, 0);
    wait_lgkm<0>();
#pragma unroll
    for (int k = 3; k < 6; ++k)
#pragma unroll
      for (int n = 0; n < 2; ++n) pin(w[k][n]);
#pragma unroll
    for (int k = 3; k < 6; ++k)
#pragma unroll
      for (int n = 0; n < 2; ++n)
#pragma unroll
        for (int r = 0; r < 2; ++r)
          acc[r][n] = __builtin_amdgcn_mfma_f32_16x16x32_bf16(cat(w[k][n]), an[r][kb + k], acc[r][n], 0, 0, 0);
  };
  grp(IC<0>{}); grp(IC<6>{}); grp(IC<12>{});
  wait_vm<0>();
  STAMP(3, 3);
#pragma unroll
  for (int n = 0; n < 2; ++n) asm volatile("" : "+v"(bb[n]));
#pragma unroll
  for (int r = 0; r < 2; ++r) {
    const int m = m0 + wr * 32 + r * 16 + (lane & 15);
#pragma unroll
    for (int n = 0; n < 2; ++n) {
      const int ncol = n0 + wc * 32 + n * 16 + 4 * g;
      asm volatile("" : "+v"(xx[r][n]));
#pragma unroll
      for (int sI = 0; sI < SPLITK_; ++sI) asm volatile("" : "+v"(py[r][n][sI]));
      f32x4 y = {0.f, 0.f, 0.f, 0.f};
#pragma unroll
      for (int sI = 0; sI < SPLITK_; ++sI) {
        y[0] += lo16(py[r][n][sI][0]); y[1] += hi16(py[r][n][sI][0]);
        y[2] += lo16(py[r][n][sI][1]); y[3] += hi16(py[r][n][sI][1]);
      }
      float bv[4] = {lo16(bb[n][0]), hi16(bb[n][0]), lo16(bb[n][1]), hi16(bb[n][1])};
      float xv[4] = {lo16(xx[r][n][0]), hi16(xx[r][n][0]), lo16(xx[r][n][1]), hi16(xx[r][n][1])};
      float o[4];
#pragma unroll
      for (int j = 0; j < 4; ++j) {
        float yy = y[j] + bv[j];
        float gg = yy / (1.f + __expf(-yy));
        o[j] = xv[j] + acc[r][n][j] * gg;
      }
      u32x2 v = {pack2(o[0], o[1]), pack2(o[2], o[3])};
      *reinterpret_cast<u32x2*>(out + (size_t)m * D_ + ncol) = v;
    }
  }
  STAMP(3, 4);
}

extern "C" void kernel_launch(void* const* d_in, const int* in_sizes, int n_in, void* d_out, int out_size, void* d_ws,
                              size_t ws_size, hipStream_t stream) {
  const u16* x = (const u16*)d_in[0];
  const u16* wqkv = (const u16*)d_in[1];
  const u16* wphi = (const u16*)d_in[2];
  const u16* wo = (const u16*)d_in[3];
  const u16* w1 = (const u16*)d_in[5];
  const u16* b1 = (const u16*)d_in[6];
  const u16* w2 = (const u16*)d_in[7];
  const u16* b2 = (const u16*)d_in[8];
  u16* out = (u16*)d_out;
  char* ws = (char*)d_ws;
  size_t off = 0;
  u16* xnf = (u16*)(ws + off); off += (size_t)S_ * D_ * 2;
  u16* wfold = (u16*)(ws + off); off += (size_t)D_ * NPH_ * 2;
  u16* qkv = (u16*)(ws + off); off += (size_t)96 * S_ * 24 * 2;
  u16* hf = (u16*)(ws + off); off += (size_t)S_ * F_ * 2;
  u16* part = (u16*)(ws + off); off += (size_t)SPLITK_ * S_ * D_ * 2;
  u16* of = (u16*)(ws + off); off += (size_t)S_ * D_ * 2;
  float* pkv = (float*)(ws + off); off += (size_t)256 * 1536 * 4;
  unsigned* cnt = (unsigned*)(ws + off); off += 32 * 32 * 4;

  k0_prep<<<512 + 192, 256, 0, stream>>>(x, wqkv, wphi, xnf, wfold, cnt);
  k1_gemm1<<<256, 512, 0, stream>>>(xnf, wfold, wqkv, w1, b1, qkv, hf);
  k2_gemm2<<<K2_HEADBLK + 256, 256, 0, stream>>>(hf, w2, part, qkv, of, pkv, cnt);
  k3_final<<<16 * 9, 512, 0, stream>>>(of, wo, part, b2, x, out);
}
```

```cpp
#include <hip/hip_runtime.h>
#include <hip/hip_bf16.h>
#include <stdint.h>

constexpr int S_ = 2048, D_ = 576, H_ = 32, DH_ = 18, F_ = 4608, NQKV_ = 1728;
constexpr int NPH_ = 1152;
constexpr int KPH_ = 40;
constexpr int KPHI_ = H_ * KPH_;
constexpr int SPLITK_ = 4;

typedef short bf16x8 __attribute__((ext_vector_type(8)));
typedef short s16x4 __attribute__((ext_vector_type(4)));
typedef float f32x4 __attribute__((ext_vector_type(4)));
typedef unsigned u32x4 __attribute__((ext_vector_type(4)));
typedef unsigned u32x2 __attribute__((ext_vector_type(2)));
typedef __attribute__((address_space(3))) s16x4* lds_s16x4_p;
typedef unsigned short u16;
template <int V> struct IC { static constexpr int value = V; };

#define STAMP(kid, i) do { } while (0)
#define ST_NT(p, v) (*(p) = (v))
#define WAIT_LGKM0() asm volatile("s_waitcnt lgkmcnt(0)" ::: "memory")
#define BAR() __builtin_amdgcn_s_barrier()

template <int N>
__device__ __forceinline__ void wait_vm() {
  static_assert(N >= 0 && N < 64, "vmcnt range");
  asm volatile("s_waitcnt vmcnt(%0)" ::"n"(N) : "memory");
}

__device__ __forceinline__ float bf2f(u16 v) { return __uint_as_float(((unsigned)v) << 16); }
__device__ __forceinline__ u16 f2bf(float f) {
  __hip_bfloat16 b = __float2bfloat16(f);
  return *reinterpret_cast<u16*>(&b);
}
__device__ __forceinline__ unsigned pack2(float lo, float hi) {
  return (unsigned)f2bf(lo) | ((unsigned)f2bf(hi) << 16);
}
__device__ __forceinline__ float rbf(float f) { return bf2f(f2bf(f)); }
__device__ __forceinline__ float lo16(unsigned u) { return __uint_as_float(u << 16); }
__device__ __forceinline__ float hi16(unsigned u) { return __uint_as_float(u & 0xffff0000u); }

__device__ __forceinline__ void glds16(const void* g, void* l) {
  __builtin_amdgcn_global_load_lds((const unsigned*)g, (unsigned*)l, 16, 0, 0);
}

__device__ __forceinline__ void l2_touch(const void* g, void* ldummy) {
  __builtin_amdgcn_global_load_lds((const unsigned*)g, (unsigned*)ldummy, 4, 0, 0);
}

__device__ __forceinline__ bf16x8 swap_halves_if(bf16x8 v, bool odd) {
  bf16x8 s = __builtin_shufflevector(v, v, 4, 5, 6, 7, 0, 1, 2, 3);
  return odd ? s : v;
}

__device__ __forceinline__ bf16x8 wfrag(const char* img, int o0, int o1, int off) {
  s16x4 a = __builtin_amdgcn_ds_read_tr16_b64_v4i16((lds_s16x4_p)(img + o0 + off));
  s16x4 b = __builtin_amdgcn_ds_read_tr16_b64_v4i16((lds_s16x4_p)(img + o1 + off));
  return __builtin_shufflevector(a, b, 0, 1, 2, 3, 4, 5, 6, 7);
}

struct WF { s16x4 lo, hi; };
template <int OFF>
__device__ __forceinline__ void tr_rd(s16x4& r, unsigned addr) {
  asm volatile("ds_read_b64_tr_b16 %0, %1 offset:%2" : "=v"(r) : "v"(addr), "n"(OFF) : "memory");
}
__device__ __forceinline__ void gld16_asm(bf16x8& r, const void* p) {
  asm volatile("global_load_dwordx4 %0, %1, off" : "=v"(r) : "v"(p) : "memory");
}
template <int N>
__device__ __forceinline__ void wait_lgkm() {
  static_assert(N >= 0 && N <= 15, "lgkmcnt range");
  asm volatile("s_waitcnt lgkmcnt(%0)" ::"n"(N) : "memory");
}
__device__ __forceinline__ void pin(WF& w) { asm volatile("" : "+v"(w.lo), "+v"(w.hi)); }
__device__ __forceinline__ void pin(bf16x8& w) { asm volatile("" : "+v"(w)); }
__device__ __forceinline__ bf16x8 cat(const WF& w) { return __builtin_shufflevector(w.lo, w.hi, 0, 1, 2, 3, 4, 5, 6, 7); }
__device__ __forceinline__ unsigned lds_addr(const void* p) {
  return (unsigned)(uintptr_t)(__attribute__((address_space(3))) const char*)p;
}

__global__ __launch_bounds__(256) void k0_prep(const u16* __restrict__ x, const u16* __restrict__ wqkv,
                                               const u16* __restrict__ wphi, u16* __restrict__ xnf,
                                               u16* __restrict__ wfold, unsigned* __restrict__ cnt) {
  __shared__ __attribute__((aligned(16))) float wph[18 * 20];
  const int tid = threadIdx.x, wid = tid >> 6, lane = tid & 63;
  const int bid = blockIdx.x;
  if (bid == 512 && tid < 32) cnt[tid * 32] = 0u;
  if (bid < 512) {
    const int row = bid * 4 + wid;
    const u16* xp = x + (size_t)row * D_;
    u32x4 a = *reinterpret_cast<const u32x4*>(xp + lane * 8);
    u32x4 b = {0u, 0u, 0u, 0u};
    if (lane < 8) b = *reinterpret_cast<const u32x4*>(xp + 512 + lane * 8);
    float v[16];
#pragma unroll
    for (int j = 0; j < 4; ++j) {
      v[2 * j] = lo16(a[j]); v[2 * j + 1] = hi16(a[j]);
      v[8 + 2 * j] = lo16(b[j]); v[8 + 2 * j + 1] = hi16(b[j]);
    }
    float s = 0.f;
#pragma unroll
    for (int j = 0; j < 16; ++j) s += v[j];
#pragma unroll
    for (int o = 32; o >= 1; o >>= 1) s += __shfl_xor(s, o);
    const float mean = s * (1.f / D_);
    float ss = 0.f;
#pragma unroll
    for (int j = 0; j < 8; ++j) { float d = v[j] - mean; ss += d * d; }
    if (lane < 8) {
#pragma unroll
      for (int j = 8; j < 16; ++j) { float d = v[j] - mean; ss += d * d; }
    }
#pragma unroll
    for (int o = 32; o >= 1; o >>= 1) ss += __shfl_xor(ss, o);
    const float rstd = rsqrtf(ss * (1.f / D_) + 1e-5f);
    u32x4 oa, ob;
#pragma unroll
    for (int j = 0; j < 4; ++j) {
      oa[j] = pack2((v[2 * j] - mean) * rstd, (v[2 * j + 1] - mean) * rstd);
      ob[j] = pack2((v[8 + 2 * j] - mean) * rstd, (v[8 + 2 * j + 1] - mean) * rstd);
    }
    {
      const int c = lane;
      u16* dst = xnf + ((size_t)((row >> 4) * 18 + (c >> 2)) * 64 + (c & 3) * 16 + (row & 15)) * 8;
      *reinterpret_cast<u32x4*>(dst) = oa;
    }
    if (lane < 8) {
      const int c = 64 + lane;
      u16* dst = xnf + ((size_t)((row >> 4) * 18 + (c >> 2)) * 64 + (c & 3) * 16 + (row & 15)) * 8;
      *reinterpret_cast<u32x4*>(dst) = ob;
    }
    return;
  }
  {
    const int fb = bid - 512;
    const int hh = fb / 3, k = (fb % 3) * 192 + tid;
    const int h = hh & 31;
    for (int i = tid; i < 18 * 20; i += 256) {
      const int d = i / 20, f = i % 20;
      wph[i] = (f < 18) ? bf2f(wphi[h * 324 + d * 18 + f]) : 0.f;
    }
    unsigned wv[9];
    if (tid < 192) {
      const u16* wp = wqkv + (size_t)k * NQKV_ + hh * 18;
#pragma unroll
      for (int d = 0; d < 9; ++d) wv[d] = *reinterpret_cast<const unsigned*>(wp + 2 * d);
    }
    __syncthreads();
    if (tid < 192) {
      float acc[20];
#pragma unroll
      for (int f = 0; f < 20; ++f) acc[f] = 0.f;
#pragma unroll
      for (int d = 0; d < 18; ++d) {
        const float w = (d & 1) ? hi16(wv[d >> 1]) : lo16(wv[d >> 1]);
#pragma unroll
        for (int f4 = 0; f4 < 5; ++f4) {
          f32x4 pw = *reinterpret_cast<const f32x4*>(wph + d * 20 + f4 * 4);
#pragma unroll
          for (int j = 0; j < 4; ++j) acc[f4 * 4 + j] += w * pw[j];
        }
      }
      u16* op = wfold + (size_t)k * NPH_ + hh * 18;
#pragma unroll
      for (int f = 0; f < 9; ++f) *reinterpret_cast<unsigned*>(op + 2 * f) = pack2(acc[2 * f], acc[2 * f + 1]);
    }
  }
}

constexpr int G1_BUFB = 192 * 128;
constexpr int G1_RING = 4;
constexpr int G1_XOFF = G1_RING * G1_BUFB;
constexpr int G1_BOFF = G1_XOFF + 576 * 32;
constexpr int G1_SMEM = G1_BOFF + 1024;
constexpr int G1_NST = 8;

template <int BASE, int I = 0>
__device__ __forceinline__ void g1_reads(WF (&w)[4], const unsigned (&ad)[4][2]) {
  if constexpr (I < 4) {
    if constexpr (BASE >= 49152) {
      tr_rd<BASE - 49152>(w[I].lo, ad[I][0] + 49152u);
      tr_rd<BASE - 49152>(w[I].hi, ad[I][1] + 49152u);
    } else {
      tr_rd<BASE>(w[I].lo, ad[I][0]);
      tr_rd<BASE>(w[I].hi, ad[I][1]);
    }
    g1_reads<BASE, I + 1>(w, ad);
  }
}
template <int KS>
__device__ __forceinline__ void g1_mma(WF (&w)[4], const bf16x8 (&xa)[2][18], f32x4 (&acc)[2][4]) {
#pragma unroll
  for (int i = 0; i < 4; ++i) pin(w[i]);
#pragma unroll
  for (int i = 0; i < 4; ++i)
#pragma unroll
    for (int r = 0; r < 2; ++r)
      acc[r][i] = __builtin_amdgcn_mfma_f32_16x16x32_bf16(cat(w[i]), xa[r][KS], acc[r][i], 0, 0, 0);
  __builtin_amdgcn_sched_barrier(0);
}

__device__ __forceinline__ float gelu_f(float v) {
  constexpr float c1 = -2.f * 0.7978845608028654f * 1.4426950408889634f;
  constexpr float c3 = c1 * 0.044715f;
  const float t = v * v;
  const float e = __builtin_amdgcn_exp2f(v * fmaf(t, c3, c1));
  return v * __builtin_amdgcn_rcpf(1.f + e);
}

typedef float f32x2 __attribute__((ext_vector_type(2)));
__device__ __forceinline__ unsigned cvt_pk(float lo, float hi) {
  unsigned r;
  asm("v_cvt_pk_bf16_f32 %0, %1, %2" : "=v"(r) : "v"(lo), "v"(hi));
  return r;
}
__device__ __forceinline__ unsigned gelu2_pk(f32x2 v) {
  constexpr float c1 = -2.f * 0.7978845608028654f * 1.4426950408889634f;
  constexpr float c3 = c1 * 0.044715f;
  const f32x2 t = v * v;
  const f32x2 pp = t * (f32x2){c3, c3} + (f32x2){c1, c1};
  const f32x2 u = v * pp;
  f32x2 e = {__builtin_amdgcn_exp2f(u[0]), __builtin_amdgcn_exp2f(u[1])};
  e = e + (f32x2){1.f, 1.f};
  const f32x2 r = {__builtin_amdgcn_rcpf(e[0]), __builtin_amdgcn_rcpf(e[1])};
  const f32x2 o = v * r;
  return cvt_pk(o[0], o[1]);
}

__device__ __forceinline__ void g1_store_tile(const f32x4& a, int tile, int mrow, int g, const u16* bias4,
                                              u16* __restrict__ qkv, u16* __restrict__ hf) {
  if (tile < NQKV_ / 16) {
    const int col = tile * 16 + 4 * g;
    const int hh = col / 18, f = col - hh * 18;
    u16* dst = qkv + ((size_t)hh * S_ + mrow) * 24 + f;
    const unsigned v0 = cvt_pk(a[0], a[1]), v1 = cvt_pk(a[2], a[3]);
    if (f != 16) {
      u32x2 v = {v0, v1};
      *reinterpret_cast<u32x2*>(dst) = v;
    } else {
      *reinterpret_cast<unsigned*>(dst) = v0;
      *reinterpret_cast<unsigned*>(qkv + ((size_t)(hh + 1) * S_ + mrow) * 24) = v1;
    }
  } else {
    u32x2 bb = *reinterpret_cast<const u32x2*>(bias4);
    float o[4];
    o[0] = gelu_f(a[0] + lo16(bb[0]));
    o[1] = gelu_f(a[1] + hi16(bb[0]));
    o[2] = gelu_f(a[2] + lo16(bb[1]));
    o[3] = gelu_f(a[3] + hi16(bb[1]));
    u32x2 v = {pack2(o[0], o[1]), pack2(o[2], o[3])};
    const int T = tile - NQKV_ / 16;
    u16* dst = hf + ((size_t)((mrow >> 4) * 144 + (T >> 1)) * 64 + ((T & 1) * 2 + (g >> 1)) * 16 + (mrow & 15)) * 8 + (g & 1) * 4;
    *reinterpret_cast<u32x2*>(dst) = v;
  }
}

__device__ __forceinline__ void g1_addr(unsigned (&ad)[4][2], char* smem, int lane) {
  const int g = lane >> 4, q = (lane & 15) >> 2, p = lane & 3;
#pragma unroll
  for (int t = 0; t < 2; ++t) {
    const int row = 8 * g + 4 * t + q;
    const int f = ((row >> 1) & 1) | (((row >> 3) & 1) << 1);
#pragma unroll
    for (int n = 0; n < 4; ++n) ad[n][t] = lds_addr(smem) + row * 128 + ((n ^ f) * 32) + p * 8;
  }
}

struct G1W { const u16* src; int stride; };

__device__ __forceinline__ void g1_wsetup(G1W& w, int col, int wid, int lane, const u16* wfold, const u16* wqkv, const u16* w1) {
  const int row = wid * 8 + (lane >> 3);
  const int f = ((row >> 1) & 1) | (((row >> 3) & 1) << 1);
  const int ch = (lane & 7) ^ (f << 1);
  if (col < NPH_) { w.src = wfold + (size_t)row * NPH_ + col + ch * 8; w.stride = 64 * NPH_; }
  else if (col < NQKV_) { w.src = wqkv + (size_t)row * NQKV_ + col + ch * 8; w.stride = 64 * NQKV_; }
  else { w.src = w1 + (size_t)row * F_ + (col - NQKV_) + ch * 8; w.stride = 64 * F_; }
}
__device__ __forceinline__ void g1_wissue(G1W& w, char* buf, int wid, int lane) {
#pragma unroll
  for (int i = 0; i < 3; ++i) {
    glds16(w.src, buf + i * 8192 + wid * 1024 + lane * 16);
    w.src += w.stride;
  }
}

template <int U>
__device__ __forceinline__ void g1_unit(G1W& wst, int unit0, const u16* wfold, const u16* wqkv,
                                        const u16* w1, char* smem, bf16x8 (&xa)[2][18], int m0,
                                        u16* __restrict__ qkv, u16* __restrict__ hf, WF (&wA)[4]) {
  int tz = threadIdx.x;
  asm volatile("" : "+v"(tz));
  const int wid = __builtin_amdgcn_readfirstlane(tz >> 6), lane = tz & 63;
  const int g = lane >> 4;
  unsigned ad[4][2];
  g1_addr(ad, smem, lane);
  f32x4 acc[2][4];
#pragma unroll
  for (int r = 0; r < 2; ++r)
#pragma unroll
    for (int n = 0; n < 4; ++n) acc[r][n] = (f32x4){0.f, 0.f, 0.f, 0.f};

  auto stage = [&](auto KC) {
    constexpr int kc = decltype(KC)::value;
    constexpr int s = U * 3 + kc;
    constexpr int CUR = (s % G1_RING) * G1_BUFB;
    if constexpr (U == 0) {
#pragma unroll
      for (int k = 0; k < 6; ++k) { pin(xa[0][kc * 6 + k]); pin(xa[1][kc * 6 + k]); }
    }
    WF wB[4];
    g1_reads<CUR + 1 * 4096>(wB, ad); wait_lgkm<8>(); g1_mma<kc * 6 + 0>(wA, xa, acc);
    g1_reads<CUR + 2 * 4096>(wA, ad); wait_lgkm<8>(); g1_mma<kc * 6 + 1>(wB, xa, acc);
    g1_reads<CUR + 3 * 4096>(wB, ad); wait_lgkm<8>(); g1_mma<kc * 6 + 2>(wA, xa, acc);
    g1_reads<CUR + 4 * 4096>(wA, ad); wait_lgkm<8>(); g1_mma<kc * 6 + 3>(wB, xa, acc);
    g1_reads<CUR + 5 * 4096>(wB, ad); wait_lgkm<8>(); g1_mma<kc * 6 + 4>(wA, xa, acc);
    if constexpr (s < 8) {
      constexpr int nl = (s == 0) ? 12 : ((s + 2 <= 8) ? 3 : 0);
      constexpr int nst = (U > 0 && kc <= 1) ? G1_NST : 0;
      wait_vm<nl + nst>();
      BAR();
      if constexpr (s + 3 <= 8) {
        constexpr int sn = s + 3;
        if constexpr (sn % 3 == 0) g1_wsetup(wst, (unit0 + sn / 3) * 64, wid, lane, wfold, wqkv, w1);
        g1_wissue(wst, smem + (sn % G1_RING) * G1_BUFB, wid, lane);
      }
      g1_reads<((s + 1) % G1_RING) * G1_BUFB>(wA, ad);
      wait_lgkm<8>();
    } else {
      wait_lgkm<0>();
    }
    g1_mma<kc * 6 + 5>(wB, xa, acc);
  };
  stage(IC<0>{}); stage(IC<1>{}); stage(IC<2>{});
  const u16* bias_lds = reinterpret_cast<const u16*>(smem + G1_BOFF);
  if ((unit0 + U) * 64 < NQKV_) {
#pragma unroll
    for (int n = 0; n < 4; ++n) {
      const int tile = (unit0 + U) * 4 + n;
#pragma unroll
      for (int r = 0; r < 2; ++r)
        g1_store_tile(acc[r][n], tile, m0 + wid * 32 + r * 16 + (lane & 15), g, bias_lds, qkv, hf);
    }
  } else {
    u32x2 bb[4];
#pragma unroll
    for (int n = 0; n < 4; ++n) bb[n] = *reinterpret_cast<const u32x2*>(bias_lds + (U * 4 + n) * 16 + 4 * g);
    const int T0 = (unit0 + U) * 4 - NQKV_ / 16;
#pragma unroll
    for (int r = 0; r < 2; ++r) {
      const int mrow = m0 + wid * 32 + r * 16 + (lane & 15);
      u16* dst = hf + ((size_t)((mrow >> 4) * 144 + (T0 >> 1)) * 64 + (g >> 1) * 16 + (mrow & 15)) * 8 + (g & 1) * 4;
#pragma unroll
      for (int n = 0; n < 4; ++n) {
        const f32x2 v0 = (f32x2){acc[r][n][0], acc[r][n][1]} + (f32x2){lo16(bb[n][0]), hi16(bb[n][0])};
        const f32x2 v1 = (f32x2){acc[r][n][2], acc[r][n][3]} + (f32x2){lo16(bb[n][1]), hi16(bb[n][1])};
        u32x2 v = {gelu2_pk(v0), gelu2_pk(v1)};
        ST_NT(reinterpret_cast<u32x2*>(dst + (n >> 1) * 512 + (n & 1) * 256), v);
      }
    }
  }
}

__global__ __launch_bounds__(512, 2) void k1_gemm1(const u16* __restrict__ xnf, const u16* __restrict__ wfold,
                                                   const u16* __restrict__ wqkv, const u16* __restrict__ w1,
                                                   const u16* __restrict__ b1, u16* __restrict__ qkv,
                                                   u16* __restrict__ hf) {
  __shared__ __attribute__((aligned(16))) char smem[G1_SMEM];
  const int tid = threadIdx.x, wid = tid >> 6, lane = tid & 63;
  const int bid = blockIdx.x;
  const int mt = bid >> 5, grp = bid & 31;
  const int m0 = mt * 256;
  const int unit0 = grp * 3;
  const bool extra = grp >= 20;
  const int xcol = 6144 + (extra ? (grp - 20) : 0) * 16;
  STAMP(1, 0);
  G1W wst;
  g1_wsetup(wst, unit0 * 64, wid, lane, wfold, wqkv, w1);
  g1_wissue(wst, smem + 0 * G1_BUFB, wid, lane);
  g1_wissue(wst, smem + 1 * G1_BUFB, wid, lane);
  g1_wissue(wst, smem + 2 * G1_BUFB, wid, lane);
#pragma unroll
  for (int i = 0; i < 3; ++i) {
    int ins = wid + 8 * i;
    if (ins >= 18) ins -= 18;
    const int c = ins * 64 + lane;
    const int pr = c >> 1, half = c & 1;
    const int k = (pr & ~0xC) | ((pr & 4) << 1) | ((pr & 8) >> 1);
    glds16(w1 + (size_t)k * F_ + (xcol - NQKV_) + half * 8, smem + G1_XOFF + c * 16);
  }
  {
    const int e = (wid & 1) * 64 + lane;
    int col = (e < 96) ? (unit0 * 64 + 2 * e - NQKV_) : (xcol - NQKV_ + 2 * (e - 96));
    if (col < 0 || e >= 104) col = 0;
    __builtin_amdgcn_global_load_lds((const unsigned*)(b1 + col), (unsigned*)(smem + G1_BOFF + e * 4), 4, 0, 0);
  }
  bf16x8 xa[2][18];
#pragma unroll
  for (int ks = 0; ks < 18; ++ks)
#pragma unroll
    for (int r = 0; r < 2; ++r)
      gld16_asm(xa[r][ks], xnf + ((size_t)(((m0 >> 4) + wid * 2 + r) * 18 + ks) * 64 + lane) * 8);
  wait_vm<24>();
  BAR();
  WF wA[4];
  {
    unsigned ad[4][2];
    g1_addr(ad, smem, lane);
    g1_reads<0>(wA, ad);
  }
  STAMP(1, 1);
  g1_unit<0>(wst, unit0, wfold, wqkv, w1, smem, xa, m0, qkv, hf, wA);
  STAMP(1, 2);
  g1_unit<1>(wst, unit0, wfold, wqkv, w1, smem, xa, m0, qkv, hf, wA);
  STAMP(1, 3);
  g1_unit<2>(wst, unit0, wfold, wqkv, w1, smem, xa, m0, qkv, hf, wA);
  STAMP(1, 4);
  if (extra) {
    int tz = threadIdx.x;
    asm volatile("" : "+v"(tz));
    const int wid2 = __builtin_amdgcn_readfirstlane(tz >> 6), lane2 = tz & 63;
    const int g = lane2 >> 4, q = (lane2 & 15) >> 2, p = lane2 & 3;
    unsigned ad[2];
#pragma unroll
    for (int t = 0; t < 2; ++t) {
      const int k = 8 * g + 4 * t + q;
      const int pr = (k & ~0xC) | ((k & 4) << 1) | ((k & 8) >> 1);
      ad[t] = lds_addr(smem) + G1_XOFF + pr * 32 + p * 8;
    }
    f32x4 acc[2] = {(f32x4){0.f, 0.f, 0.f, 0.f}, (f32x4){0.f, 0.f, 0.f, 0.f}};
#pragma unroll
    for (int kb = 0; kb < 18; kb += 6) {
      WF w[6];
      tr_rd<0 * 1024>(w[0].lo, ad[0] + kb * 1024); tr_rd<0 * 1024>(w[0].hi, ad[1] + kb * 1024);
      tr_rd<1 * 1024>(w[1].lo, ad[0] + kb * 1024); tr_rd<1 * 1024>(w[1].hi, ad[1] + kb * 1024);
      tr_rd<2 * 1024>(w[2].lo, ad[0] + kb * 1024); tr_rd<2 * 1024>(w[2].hi, ad[1] + kb * 1024);
      tr_rd<3 * 1024>(w[3].lo, ad[0] + kb * 1024); tr_rd<3 * 1024>(w[3].hi, ad[1] + kb * 1024);
      tr_rd<4 * 1024>(w[4].lo, ad[0] + kb * 1024); tr_rd<4 * 1024>(w[4].hi, ad[1] + kb * 1024);
      tr_rd<5 * 1024>(w[5].lo, ad[0] + kb * 1024); tr_rd<5 * 1024>(w[5].hi, ad[1] + kb * 1024);
      wait_lgkm<0>();
#pragma unroll
      for (int i = 0; i < 6; ++i) pin(w[i]);
#pragma unroll
      for (int i = 0; i < 6; ++i)
#pragma unroll
        for (int r = 0; r < 2; ++r)
          acc[r] = __builtin_amdgcn_mfma_f32_16x16x32_bf16(cat(w[i]), xa[r][kb + i], acc[r], 0, 0, 0);
    }
    const u16* bias_lds = reinterpret_cast<const u16*>(smem + G1_BOFF);
#pragma unroll
    for (int r = 0; r < 2; ++r)
      g1_store_tile(acc[r], xcol / 16, m0 + wid2 * 32 + r * 16 + (lane2 & 15), g, bias_lds + 192 + 4 * g, qkv, hf);
  }
}

constexpr int G2_NT = 9, G2_RB = G2_NT * 32;
constexpr int G2_BUFB = 64 * G2_RB;
constexpr int G2_RING = 4;
constexpr int G2_STAGES = 18;

__device__ __forceinline__ void softmax_pm(const float (&pk)[18], float (&ep)[18], float (&en)[18]) {
  float mx = pk[0], mn = pk[0];
#pragma unroll
  for (int f = 1; f < 18; ++f) {
    mx = fmaxf(mx, pk[f]);
    mn = fminf(mn, pk[f]);
  }
  float sp = 0.f, sn = 0.f;
#pragma unroll
  for (int f = 0; f < 18; ++f) {
    ep[f] = __expf(pk[f] - mx);
    en[f] = __expf(mn - pk[f]);
    sp += ep[f];
    sn += en[f];
  }
  const float rp = 1.f / sp, rn = 1.f / sn;
#pragma unroll
  for (int f = 0; f < 18; ++f) {
    ep[f] *= rp;
    en[f] *= rn;
  }
}

__device__ void head_block(int hb, const u16* __restrict__ qkv, u16* __restrict__ of, float* pkv, unsigned* cnt, char* smem) {
  u16* phiT = reinterpret_cast<u16*>(smem);
  u16* vT = reinterpret_cast<u16*>(smem + 25344);
  float* kvs = reinterpret_cast<float*>(smem + 25344 + 16896);
  const int h = hb >> 3, c8 = hb & 7;
  const int tid = threadIdx.x, wid = tid >> 6, lane = tid & 63;
  const int row = c8 * 256 + tid;
  u32x4 kr[3], vr[3], qr[3];
  {
    const u32x4* kp = reinterpret_cast<const u32x4*>(qkv + ((size_t)(32 + h) * S_ + row) * 24);
    const u32x4* vp = reinterpret_cast<const u32x4*>(qkv + ((size_t)(64 + h) * S_ + row) * 24);
    const u32x4* qp = reinterpret_cast<const u32x4*>(qkv + ((size_t)h * S_ + row) * 24);
#pragma unroll
    for (int d = 0; d < 3; ++d) { kr[d] = kp[d]; vr[d] = vp[d]; qr[d] = qp[d]; }
  }
  for (int i = tid; i < 12 * 264; i += 256) phiT[36 * 264 + i] = 0;
  for (int i = tid; i < 14 * 264; i += 256) vT[18 * 264 + i] = (i < 264) ? (u16)0x3f80 : (u16)0;
  {
    float pk[18];
#pragma unroll
    for (int d = 0; d < 9; ++d) {
      pk[2 * d] = lo16(kr[d >> 2][d & 3]);
      pk[2 * d + 1] = hi16(kr[d >> 2][d & 3]);
    }
    float ep[18], en[18];
    softmax_pm(pk, ep, en);
#pragma unroll
    for (int f = 0; f < 18; ++f) {
      phiT[f * 264 + tid] = f2bf(ep[f]);
      phiT[(18 + f) * 264 + tid] = f2bf(en[f]);
      const unsigned w = vr[(f >> 1) >> 2][(f >> 1) & 3];
      vT[f * 264 + tid] = (u16)((f & 1) ? (w >> 16) : (w & 0xffff));
    }
  }
  __syncthreads();
  {
    f32x4 acc[3][2];
#pragma unroll
    for (int a = 0; a < 3; ++a)
#pragma unroll
      for (int b = 0; b < 2; ++b) acc[a][b] = (f32x4){0.f, 0.f, 0.f, 0.f};
#pragma unroll
    for (int ks = 0; ks < 2; ++ks) {
      const int s0 = wid * 64 + ks * 32 + 8 * (lane >> 4);
      bf16x8 bv[2];
#pragma unroll
      for (int b = 0; b < 2; ++b) bv[b] = *reinterpret_cast<const bf16x8*>(vT + (b * 16 + (lane & 15)) * 264 + s0);
#pragma unroll
      for (int a = 0; a < 3; ++a) {
        bf16x8 av = *reinterpret_cast<const bf16x8*>(phiT + (a * 16 + (lane & 15)) * 264 + s0);
#pragma unroll
        for (int b = 0; b < 2; ++b) acc[a][b] = __builtin_amdgcn_mfma_f32_16x16x32_bf16(av, bv[b], acc[a][b], 0, 0, 0);
      }
    }
#pragma unroll
    for (int a = 0; a < 3; ++a)
#pragma unroll
      for (int b = 0; b < 2; ++b)
#pragma unroll
        for (int j = 0; j < 4; ++j)
          kvs[(wid * 48 + a * 16 + (lane >> 4) * 4 + j) * 32 + b * 16 + (lane & 15)] = acc[a][b][j];
  }
  __syncthreads();
  float* mypart = pkv + (size_t)(h * 8 + c8) * 1536;
#pragma unroll
  for (int it = 0; it < 6; ++it) {
    const int i = it * 256 + tid;
    const float v = kvs[i] + kvs[1536 + i] + kvs[2 * 1536 + i] + kvs[3 * 1536 + i];
    __hip_atomic_store(mypart + i, v, __ATOMIC_RELAXED, __HIP_MEMORY_SCOPE_AGENT);
  }
  asm volatile("s_waitcnt vmcnt(0)" ::: "memory");
  __syncthreads();
  if (tid == 0) __hip_atomic_fetch_add(cnt + h * 32, 1u, __ATOMIC_RELAXED, __HIP_MEMORY_SCOPE_AGENT);
  float ep[18], en[18];
  {
    float pk[18];
#pragma unroll
    for (int d = 0; d < 9; ++d) {
      pk[2 * d] = lo16(qr[d >> 2][d & 3]);
      pk[2 * d + 1] = hi16(qr[d >> 2][d & 3]);
    }
    softmax_pm(pk, ep, en);
#pragma unroll
    for (int f = 0; f < 18; ++f) { ep[f] = rbf(ep[f]); en[f] = rbf(en[f]); }
  }
  if (tid == 0) {
    unsigned spins = 0;
    while (__hip_atomic_load(cnt + h * 32, __ATOMIC_RELAXED, __HIP_MEMORY_SCOPE_AGENT) < 8u) {
      __builtin_amdgcn_s_sleep(1);
      if (++spins > (1u << 24)) break;
    }
  }
  __syncthreads();
  u16* kvT = reinterpret_cast<u16*>(smem);
  u16* phiR = reinterpret_cast<u16*>(smem + 4608);
  {
    float* hp = pkv + (size_t)h * 8 * 1536;
    float pv[5][8];
#pragma unroll
    for (int it = 0; it < 5; ++it) {
      const int i = it * 256 + tid;
#pragma unroll
      for (int c = 0; c < 8; ++c) pv[it][c] = __hip_atomic_load(hp + c * 1536 + i, __ATOMIC_RELAXED, __HIP_MEMORY_SCOPE_AGENT);
    }
    {
      unsigned o[32];
#pragma unroll
      for (int f = 0; f < 9; ++f) { o[f] = pack2(ep[2 * f], ep[2 * f + 1]); o[9 + f] = pack2(en[2 * f], en[2 * f + 1]); }
#pragma unroll
      for (int f = 18; f < 32; ++f) o[f] = 0u;
#pragma unroll
      for (int c = 0; c < 8; ++c) {
        u32x4 v = {o[4 * c], o[4 * c + 1], o[4 * c + 2], o[4 * c + 3]};
        *reinterpret_cast<u32x4*>(phiR + tid * 72 + c * 8) = v;
      }
    }
#pragma unroll
    for (int it = 0; it < 8; ++it) {
      const int i = it * 256 + tid;
      const int f = i >> 5, d = i & 31;
      float v = 0.f;
      if (it < 5) {
#pragma unroll
        for (int c = 0; c < 8; ++c) v += pv[it < 5 ? it : 0][c];
      }
      kvT[d * 72 + f] = ((d < 19) && (f < 36)) ? f2bf(v) : (u16)0;
    }
  }
  __syncthreads();
  {
    const int g = lane >> 4;
    bf16x8 ka[2][2];
#pragma unroll
    for (int t = 0; t < 2; ++t)
#pragma unroll
      for (int ks = 0; ks < 2; ++ks)
        ka[t][ks] = *reinterpret_cast<const bf16x8*>(kvT + (t * 16 + (lane & 15)) * 72 + ks * 32 + 8 * g);
#pragma unroll
    for (int rt = 0; rt < 4; ++rt) {
      const int rl = wid * 64 + rt * 16 + (lane & 15);
      f32x4 c0 = {0.f, 0.f, 0.f, 0.f}, c1 = {0.f, 0.f, 0.f, 0.f};
#pragma unroll
      for (int ks = 0; ks < 2; ++ks) {
        const bf16x8 pb = *reinterpret_cast<const bf16x8*>(phiR + rl * 72 + ks * 32 + 8 * g);
        c0 = __builtin_amdgcn_mfma_f32_16x16x32_bf16(ka[0][ks], pb, c0, 0, 0, 0);
        c1 = __builtin_amdgcn_mfma_f32_16x16x32_bf16(ka[1][ks], pb, c1, 0, 0, 0);
      }
      const float den = __shfl(c1[2], lane & 15);
      const float rd = 1.f / (rbf(den) + 1e-6f);
      const int grow = c8 * 256 + rl;
      u16* obase = of + (size_t)(grow >> 4) * 18 * 512 + (grow & 15) * 8;
#pragma unroll
      for (int j2 = 0; j2 < 2; ++j2) {
        const int k = h * 18 + 4 * g + 2 * j2;
        *reinterpret_cast<unsigned*>(obase + (k >> 5) * 512 + ((k >> 3) & 3) * 128 + (k & 7)) =
            pack2(rbf(c0[2 * j2]) * rd, rbf(c0[2 * j2 + 1]) * rd);
      }
      if (g == 0) {
        const int k = h * 18 + 16;
        *reinterpret_cast<unsigned*>(obase + (k >> 5) * 512 + ((k >> 3) & 3) * 128 + (k & 7)) =
            pack2(rbf(c1[0]) * rd, rbf(c1[1]) * rd);
      }
    }
  }
}

template <int BASE, int N0, int CNT, int I = 0>
__device__ __forceinline__ void g2_wreads(WF (&w)[5], unsigned a0, unsigned a1) {
  if constexpr (I < CNT) {
    tr_rd<BASE + (N0 + I) * 32>(w[I].lo, a0);
    tr_rd<BASE + (N0 + I) * 32>(w[I].hi, a1);
    g2_wreads<BASE, N0, CNT, I + 1>(w, a0, a1);
  }
}
template <int N0, int CNT>
__device__ __forceinline__ void g2_mma(WF (&w)[5], const bf16x8 (&ac)[2], f32x4 (&acc)[2][G2_NT]) {
#pragma unroll
  for (int i = 0; i < CNT; ++i) pin(w[i]);
#pragma unroll
  for (int i = 0; i < CNT; ++i)
#pragma unroll
    for (int r = 0; r < 2; ++r)
      acc[r][N0 + i] = __builtin_amdgcn_mfma_f32_16x16x32_bf16(cat(w[i]), ac[r], acc[r][N0 + i], 0, 0, 0);
  __builtin_amdgcn_sched_barrier(0);
}
constexpr int K2_HEADBLK = 256;

__global__ __launch_bounds__(256, 2) void k2_gemm2(const u16* __restrict__ hf, const u16* __restrict__ w2,
                                                   u16* __restrict__ part, const u16* __restrict__ qkv,
                                                   u16* __restrict__ of, float* pkv, unsigned* cnt) {
  __shared__ __attribute__((aligned(16))) char smem[G2_RING * G2_BUFB];
  const int tid = threadIdx.x, wid = tid >> 6, lane = tid & 63;
  int bid = blockIdx.x;
  if (bid < K2_HEADBLK) { head_block(bid, qkv, of, pkv, cnt, smem); return; }
  bid -= K2_HEADBLK;
  STAMP(2, 0);
  const int xcd = bid & 7, loc = bid >> 3;
  const int sp = xcd >> 1, mt = (xcd & 1) * 8 + (loc >> 2), nt4 = loc & 3;
  const int m0 = mt * 128, n0 = nt4 * 144, k0 = sp * 1152;
  const int g = lane >> 4, q = (lane & 15) >> 2, p = lane & 3;
  const bool odd = g & 1;
  const unsigned wa0 = lds_addr(smem) + (8 * g + 4 * (g & 1) + q) * G2_RB + p * 8;
  const unsigned wa1 = lds_addr(smem) + (8 * g + 4 * (1 - (g & 1)) + q) * G2_RB + p * 8;
  f32x4 acc[2][G2_NT];
#pragma unroll
  for (int r = 0; r < 2; ++r)
#pragma unroll
    for (int n = 0; n < G2_NT; ++n) acc[r][n] = (f32x4){0.f, 0.f, 0.f, 0.f};
  const u16* wp[5];
  int wdst[5];
#pragma unroll
  for (int i = 0; i < 5; ++i) {
    int ins = wid + 4 * i;
    if (ins >= 18) ins -= 18;
    int c = ins * 64 + lane;
    int row = c / 18, cc = c % 18;
    wp[i] = w2 + (size_t)(k0 + row) * D_ + n0 + cc * 8;
    wdst[i] = c * 16;
  }
  const u16* ap[2];
#pragma unroll
  for (int r = 0; r < 2; ++r) ap[r] = hf + ((size_t)((m0 >> 4) + wid * 2 + r) * 144 + (k0 >> 5)) * 512 + lane * 8;
  bf16x8 an[4][2][2];
  auto issue = [&](auto SL) {
    constexpr int slot = decltype(SL)::value;
    char* buf = smem + slot * G2_BUFB;
#pragma unroll
    for (int i = 0; i < 5; ++i) { glds16(wp[i], buf + wdst[i]); wp[i] += 64 * D_; }
#pragma unroll
    for (int r = 0; r < 2; ++r) {
      gld16_asm(an[slot][r][0], ap[r]);
      gld16_asm(an[slot][r][1], ap[r] + 512);
      ap[r] += 1024;
    }
  };
  issue(IC<0>{});
  issue(IC<1>{});
  issue(IC<2>{});
  wait_vm<18>();
  BAR();
  STAMP(2, 1);
  WF wA[5], wB[5];
  g2_wreads<0, 0, 5>(wA, wa0, wa1);
  auto stage = [&](auto U, auto LAST) {
    constexpr int u = decltype(U)::value;
    constexpr int rem = decltype(LAST)::value;
    constexpr int BASE = u * G2_BUFB;
    bf16x8 c0[2], c1[2];
#pragma unroll
    for (int r = 0; r < 2; ++r) {
      pin(an[u][r][0]); pin(an[u][r][1]);
      c0[r] = swap_halves_if(an[u][r][0], odd);
      c1[r] = swap_halves_if(an[u][r][1], odd);
    }
    g2_wreads<BASE, 5, 4>(wB, wa0, wa1);
    wait_lgkm<8>();
    g2_mma<0, 5>(wA, c0, acc);
    g2_wreads<BASE + 32 * G2_RB, 0, 5>(wA, wa0, wa1);
    wait_lgkm<10>();
    g2_mma<5, 4>(wB, c0, acc);
    g2_wreads<BASE + 32 * G2_RB, 5, 4>(wB, wa0, wa1);
    wait_lgkm<8>();
    g2_mma<0, 5>(wA, c1, acc);
    if constexpr (rem >= 1) {
      if constexpr (rem >= 2) wait_vm<9>(); else wait_vm<0>();
      BAR();
      if constexpr (rem >= 3) issue(IC<(u + 3) & 3>{});
      g2_wreads<((u + 1) & 3) * G2_BUFB, 0, 5>(wA, wa0, wa1);
      wait_lgkm<10>();
    } else {
      wait_lgkm<0>();
    }
    g2_mma<5, 4>(wB, c1, acc);
  };
#pragma unroll 1
  for (int it = 0; it < 3; ++it) {
    stage(IC<0>{}, IC<3>{}); stage(IC<1>{}, IC<3>{}); stage(IC<2>{}, IC<3>{}); stage(IC<3>{}, IC<3>{});
  }
  STAMP(2, 2);
  stage(IC<0>{}, IC<3>{}); stage(IC<1>{}, IC<3>{}); stage(IC<2>{}, IC<3>{});
  stage(IC<3>{}, IC<2>{}); stage(IC<0>{}, IC<1>{}); stage(IC<1>{}, IC<0>{});
  STAMP(2, 3);
#pragma unroll
  for (int r = 0; r < 2; ++r) {
    const int m = m0 + wid * 32 + r * 16 + (lane & 15);
    u16* pp = part + ((size_t)sp * S_ + m) * D_ + n0 + 4 * g;
#pragma unroll
    for (int n = 0; n < G2_NT; ++n) {
      u32x2 v = {pack2(acc[r][n][0], acc[r][n][1]), pack2(acc[r][n][2], acc[r][n][3])};
      ST_NT(reinterpret_cast<u32x2*>(pp + n * 16), v);
    }
  }
  asm volatile("s_waitcnt vmcnt(0)" ::: "memory");
  STAMP(2, 4);
}

__global__ __launch_bounds__(512, 2) void k3_final(const u16* __restrict__ of, const u16* __restrict__ wo,
                                                   const u16* __restrict__ part, const u16* __restrict__ b2,
                                                   const u16* __restrict__ x, u16* __restrict__ out) {
  __shared__ __attribute__((aligned(16))) char smem[576 * 128];
  const int tid = threadIdx.x, wid = tid >> 6, lane = tid & 63;
  const int bid = blockIdx.x;
  const int xcd = bid & 7, loc = bid >> 3;
  const int mt = xcd * 2 + loc / 9, nt = loc % 9;
  const int m0 = mt * 128, n0 = nt * 64;
  const int g = lane >> 4, q = (lane & 15) >> 2, p = lane & 3;
  const int wr = wid >> 1, wc = wid & 1;
  STAMP(3, 0);
#pragma unroll
  for (int i = 0; i < 9; ++i) {
    const int row = (wid * 9 + i) * 8 + (lane >> 3);
    const int f = ((row >> 1) & 1) | (((row >> 3) & 1) << 1);
    const int ch = (lane & 7) ^ (f << 1);
    glds16(wo + (size_t)row * D_ + n0 + ch * 8, smem + (wid * 9 + i) * 1024 + lane * 16);
  }
  bf16x8 an[2][18];
#pragma unroll
  for (int ks = 0; ks < 18; ++ks)
#pragma unroll
    for (int r = 0; r < 2; ++r)
      gld16_asm(an[r][ks], of + ((size_t)(((m0 >> 4) + wr * 2 + r) * 18 + ks) * 64 + lane) * 8);
  u32x2 py[2][2][4];
  u32x2 xx[2][2], bb[2];
#pragma unroll
  for (int r = 0; r < 2; ++r) {
    const int m = m0 + wr * 32 + r * 16 + (lane & 15);
#pragma unroll
    for (int n = 0; n < 2; ++n) {
      const int ncol = n0 + wc * 32 + n * 16 + 4 * g;
#pragma unroll
      for (int sI = 0; sI < SPLITK_; ++sI)
        asm volatile("global_load_dwordx2 %0, %1, off" : "=v"(py[r][n][sI]) : "v"(part + ((size_t)sI * S_ + m) * D_ + ncol) : "memory");
      asm volatile("global_load_dwordx2 %0, %1, off" : "=v"(xx[r][n]) : "v"(x + (size_t)m * D_ + ncol) : "memory");
    }
  }
#pragma unroll
  for (int n = 0; n < 2; ++n)
    asm volatile("global_load_dwordx2 %0, %1, off" : "=v"(bb[n]) : "v"(b2 + n0 + wc * 32 + n * 16 + 4 * g) : "memory");
  unsigned ad[2][2];
#pragma unroll
  for (int t = 0; t < 2; ++t) {
    const int row = 8 * g + 4 * t + q;
    const int f = ((row >> 1) & 1) | (((row >> 3) & 1) << 1);
#pragma unroll
    for (int n = 0; n < 2; ++n) ad[n][t] = lds_addr(smem) + row * 128 + (((wc * 2 + n) ^ f) * 32) + p * 8;
  }
  f32x4 acc[2][2];
#pragma unroll
  for (int r = 0; r < 2; ++r)
#pragma unroll
    for (int n = 0; n < 2; ++n) acc[r][n] = (f32x4){0.f, 0.f, 0.f, 0.f};
  STAMP(3, 1);
  wait_vm<22>();
  BAR();
  STAMP(3, 2);
#pragma unroll
  for (int ks = 0; ks < 18; ++ks) { pin(an[0][ks]); pin(an[1][ks]); }
  auto grp = [&](auto KB) {
    constexpr int kb = decltype(KB)::value;
    WF w[6][2];
#pragma unroll
    for (int k = 0; k < 6; ++k)
#pragma unroll
      for (int n = 0; n < 2; ++n) {
        if (k == 0) { tr_rd<0 * 4096>(w[0][n].lo, ad[n][0] + kb * 4096); tr_rd<0 * 4096>(w[0][n].hi, ad[n][1] + kb * 4096); }
        if (k == 1) { tr_rd<1 * 4096>(w[1][n].lo, ad[n][0] + kb * 4096); tr_rd<1 * 4096>(w[1][n].hi, ad[n][1] + kb * 4096); }
        if (k == 2) { tr_rd<2 * 4096>(w[2][n].lo, ad[n][0] + kb * 4096); tr_rd<2 * 4096>(w[2][n].hi, ad[n][1] + kb * 4096); }
        if (k == 3) { tr_rd<3 * 4096>(w[3][n].lo, ad[n][0] + kb * 4096); tr_rd<3 * 4096>(w[3][n].hi, ad[n][1] + kb * 4096); }
        if (k == 4) { tr_rd<4 * 4096>(w[4][n].lo, ad[n][0] + kb * 4096); tr_rd<4 * 4096>(w[4][n].hi, ad[n][1] + kb * 4096); }
        if (k == 5) { tr_rd<5 * 4096>(w[5][n].lo, ad[n][0] + kb * 4096); tr_rd<5 * 4096>(w[5][n].hi, ad[n][1] + kb * 4096); }
      }
    wait_lgkm<12>();
#pragma unroll
    for (int k = 0; k < 3; ++k)
#pragma unroll
      for (int n = 0; n < 2; ++n) pin(w[k][n]);
#pragma unroll
    for (int k = 0; k < 3; ++k)
#pragma unroll
      for (int n = 0; n < 2; ++n)
#pragma unroll
        for (int r = 0; r < 2; ++r)
          acc[r][n] = __builtin_amdgcn_mfma_f32_16x16x32_bf16(cat(w[k][n]), an[r][kb + k], acc[r][n], 0, 0, 0);
    wait_lgkm<0>();
#pragma unroll
    for (int k = 3; k < 6; ++k)
#pragma unroll
      for (int n = 0; n < 2; ++n) pin(w[k][n]);
#pragma unroll
    for (int k = 3; k < 6; ++k)
#pragma unroll
      for (int n = 0; n < 2; ++n)
#pragma unroll
        for (int r = 0; r < 2; ++r)
          acc[r][n] = __builtin_amdgcn_mfma_f32_16x16x32_bf16(cat(w[k][n]), an[r][kb + k], acc[r][n], 0, 0, 0);
  };
  grp(IC<0>{}); grp(IC<6>{}); grp(IC<12>{});
  wait_vm<0>();
  STAMP(3, 3);
#pragma unroll
  for (int n = 0; n < 2; ++n) asm volatile("" : "+v"(bb[n]));
#pragma unroll
  for (int r = 0; r < 2; ++r) {
    const int m = m0 + wr * 32 + r * 16 + (lane & 15);
#pragma unroll
    for (int n = 0; n < 2; ++n) {
      const int ncol = n0 + wc * 32 + n * 16 + 4 * g;
      asm volatile("" : "+v"(xx[r][n]));
#pragma unroll
      for (int sI = 0; sI < SPLITK_; ++sI) asm volatile("" : "+v"(py[r][n][sI]));
      f32x4 y = {0.f, 0.f, 0.f, 0.f};
#pragma unroll
      for (int sI = 0; sI < SPLITK_; ++sI) {
        y[0] += lo16(py[r][n][sI][0]); y[1] += hi16(py[r][n][sI][0]);
        y[2] += lo16(py[r][n][sI][1]); y[3] += hi16(py[r][n][sI][1]);
      }
      float bv[4] = {lo16(bb[n][0]), hi16(bb[n][0]), lo16(bb[n][1]), hi16(bb[n][1])};
      float xv[4] = {lo16(xx[r][n][0]), hi16(xx[r][n][0]), lo16(xx[r][n][1]), hi16(xx[r][n][1])};
      float o[4];
#pragma unroll
      for (int j = 0; j < 4; ++j) {
        float yy = y[j] + bv[j];
        float gg = yy / (1.f + __expf(-yy));
        o[j] = xv[j] + acc[r][n][j] * gg;
      }
      u32x2 v = {pack2(o[0], o[1]), pack2(o[2], o[3])};
      *reinterpret_cast<u32x2*>(out + (size_t)m * D_ + ncol) = v;
    }
  }
  STAMP(3, 4);
}

extern "C" void kernel_launch(void* const* d_in, const int* in_sizes, int n_in, void* d_out, int out_size, void* d_ws,
                              size_t ws_size, hipStream_t stream) {
  const u16* x = (const u16*)d_in[0];
  const u16* wqkv = (const u16*)d_in[1];
  const u16* wphi = (const u16*)d_in[2];
  const u16* wo = (const u16*)d_in[3];
  const u16* w1 = (const u16*)d_in[5];
  const u16* b1 = (const u16*)d_in[6];
  const u16* w2 = (const u16*)d_in[7];
  const u16* b2 = (const u16*)d_in[8];
  u16* out = (u16*)d_out;
  char* ws = (char*)d_ws;
  size_t off = 0;
  u16* xnf = (u16*)(ws + off); off += (size_t)S_ * D_ * 2;
  u16* wfold = (u16*)(ws + off); off += (size_t)D_ * NPH_ * 2;
  u16* qkv = (u16*)(ws + off); off += (size_t)96 * S_ * 24 * 2;
  u16* hf = (u16*)(ws + off); off += (size_t)S_ * F_ * 2;
  u16* part = (u16*)(ws + off); off += (size_t)SPLITK_ * S_ * D_ * 2;
  u16* of = (u16*)(ws + off); off += (size_t)S_ * D_ * 2;
  float* pkv = (float*)(ws + off); off += (size_t)256 * 1536 * 4;
  unsigned* cnt = (unsigned*)(ws + off); off += 32 * 32 * 4;

  k0_prep<<<512 + 192, 256, 0, stream>>>(x, wqkv, wphi, xnf, wfold, cnt);
  k1_gemm1<<<256, 512, 0, stream>>>(xnf, wfold, wqkv, w1, b1, qkv, hf);
  k2_gemm2<<<K2_HEADBLK + 256, 256, 0, stream>>>(hf, w2, part, qkv, of, pkv, cnt);
  k3_final<<<16 * 9, 512, 0, stream>>>(of, wo, part, b2, x, out);
}
```

```cpp
#include <hip/hip_runtime.h>
#include <hip/hip_bf16.h>
#include <stdint.h>

constexpr int S_ = 2048, D_ = 576, H_ = 32, DH_ = 18, F_ = 4608, NQKV_ = 1728;
constexpr int NPH_ = 1152;
constexpr int KPH_ = 40;
constexpr int KPHI_ = H_ * KPH_;
constexpr int SPLITK_ = 4;

typedef short bf16x8 __attribute__((ext_vector_type(8)));
typedef short s16x4 __attribute__((ext_vector_type(4)));
typedef float f32x4 __attribute__((ext_vector_type(4)));
typedef unsigned u32x4 __attribute__((ext_vector_type(4)));
typedef unsigned u32x2 __attribute__((ext_vector_type(2)));
typedef __attribute__((address_space(3))) s16x4* lds_s16x4_p;
typedef unsigned short u16;
template <int V> struct IC { static constexpr int value = V; };

#define STAMP(kid, i) do { } while (0)
#define ST_NT(p, v) (*(p) = (v))
#define WAIT_LGKM0() asm volatile("s_waitcnt lgkmcnt(0)" ::: "memory")
#define BAR() __builtin_amdgcn_s_barrier()

template <int N>
__device__ __forceinline__ void wait_vm() {
  static_assert(N >= 0 && N < 64, "vmcnt range");
  asm volatile("s_waitcnt vmcnt(%0)" ::"n"(N) : "memory");
}

__device__ __forceinline__ float bf2f(u16 v) { return __uint_as_float(((unsigned)v) << 16); }
__device__ __forceinline__ u16 f2bf(float f) {
  __hip_bfloat16 b = __float2bfloat16(f);
  return *reinterpret_cast<u16*>(&b);
}
__device__ __forceinline__ unsigned pack2(float lo, float hi) {
  return (unsigned)f2bf(lo) | ((unsigned)f2bf(hi) << 16);
}
__device__ __forceinline__ float rbf(float f) { return bf2f(f2bf(f)); }
__device__ __forceinline__ float lo16(unsigned u) { return __uint_as_float(u << 16); }
__device__ __forceinline__ float hi16(unsigned u) { return __uint_as_float(u & 0xffff0000u); }

__device__ __forceinline__ void glds16(const void* g, void* l) {
  __builtin_amdgcn_global_load_lds((const unsigned*)g, (unsigned*)l, 16, 0, 0);
}

__device__ __forceinline__ void l2_touch(const void* g, void* ldummy) {
  __builtin_amdgcn_global_load_lds((const unsigned*)g, (unsigned*)ldummy, 4, 0, 0);
}

__device__ __forceinline__ bf16x8 swap_halves_if(bf16x8 v, bool odd) {
  bf16x8 s = __builtin_shufflevector(v, v, 4, 5, 6, 7, 0, 1, 2, 3);
  return odd ? s : v;
}

__device__ __forceinline__ bf16x8 wfrag(const char* img, int o0, int o1, int off) {
  s16x4 a = __builtin_amdgcn_ds_read_tr16_b64_v4i16((lds_s16x4_p)(img + o0 + off));
  s16x4 b = __builtin_amdgcn_ds_read_tr16_b64_v4i16((lds_s16x4_p)(img + o1 + off));
  return __builtin_shufflevector(a, b, 0, 1, 2, 3, 4, 5, 6, 7);
}

struct WF { s16x4 lo, hi; };
template <int OFF>
__device__ __forceinline__ void tr_rd(s16x4& r, unsigned addr) {
  asm volatile("ds_read_b64_tr_b16 %0, %1 offset:%2" : "=v"(r) : "v"(addr), "n"(OFF) : "memory");
}
__device__ __forceinline__ void gld16_asm(bf16x8& r, const void* p) {
  asm volatile("global_load_dwordx4 %0, %1, off" : "=v"(r) : "v"(p) : "memory");
}
template <int N>
__device__ __forceinline__ void wait_lgkm() {
  static_assert(N >= 0 && N <= 15, "lgkmcnt range");
  asm volatile("s_waitcnt lgkmcnt(%0)" ::"n"(N) : "memory");
}
__device__ __forceinline__ void pin(WF& w) { asm volatile("" : "+v"(w.lo), "+v"(w.hi)); }
__device__ __forceinline__ void pin(bf16x8& w) { asm volatile("" : "+v"(w)); }
__device__ __forceinline__ bf16x8 cat(const WF& w) { return __builtin_shufflevector(w.lo, w.hi, 0, 1, 2, 3, 4, 5, 6, 7); }
__device__ __forceinline__ unsigned lds_addr(const void* p) {
  return (unsigned)(uintptr_t)(__attribute__((address_space(3))) const char*)p;
}

__global__ __launch_bounds__(256) void k0_prep(const u16* __restrict__ x, const u16* __restrict__ wqkv,
                                               const u16* __restrict__ wphi, u16* __restrict__ xnf,
                                               u16* __restrict__ wfold, unsigned* __restrict__ cnt) {
  __shared__ __attribute__((aligned(16))) float wph[18 * 20];
  const int tid = threadIdx.x, wid = tid >> 6, lane = tid & 63;
  const int bid = blockIdx.x;
  if (bid == 512 && tid < 32) cnt[tid * 32] = 0u;
  if (bid < 512) {
    const int row = bid * 4 + wid;
    const u16* xp = x + (size_t)row * D_;
    u32x4 a = *reinterpret_cast<const u32x4*>(xp + lane * 8);
    u32x4 b = {0u, 0u, 0u, 0u};
    if (lane < 8) b = *reinterpret_cast<const u32x4*>(xp + 512 + lane * 8);
    float v[16];
#pragma unroll
    for (int j = 0; j < 4; ++j) {
      v[2 * j] = lo16(a[j]); v[2 * j + 1] = hi16(a[j]);
      v[8 + 2 * j] = lo16(b[j]); v[8 + 2 * j + 1] = hi16(b[j]);
    }
    float s = 0.f;
#pragma unroll
    for (int j = 0; j < 16; ++j) s += v[j];
#pragma unroll
    for (int o = 32; o >= 1; o >>= 1) s += __shfl_xor(s, o);
    const float mean = s * (1.f / D_);
    float ss = 0.f;
#pragma unroll
    for (int j = 0; j < 8; ++j) { float d = v[j] - mean; ss += d * d; }
    if (lane < 8) {
#pragma unroll
      for (int j = 8; j < 16; ++j) { float d = v[j] - mean; ss += d * d; }
    }
#pragma unroll
    for (int o = 32; o >= 1; o >>= 1) ss += __shfl_xor(ss, o);
    const float rstd = rsqrtf(ss * (1.f / D_) + 1e-5f);
    u32x4 oa, ob;
#pragma unroll
    for (int j = 0; j < 4; ++j) {
      oa[j] = pack2((v[2 * j] - mean) * rstd, (v[2 * j + 1] - mean) * rstd);
      ob[j] = pack2((v[8 + 2 * j] - mean) * rstd, (v[8 + 2 * j + 1] - mean) * rstd);
    }
    {
      const int c = lane;
      u16* dst = xnf + ((size_t)((row >> 4) * 18 + (c >> 2)) * 64 + (c & 3) * 16 + (row & 15)) * 8;
      *reinterpret_cast<u32x4*>(dst) = oa;
    }
    if (lane < 8) {
      const int c = 64 + lane;
      u16* dst = xnf + ((size_t)((row >> 4) * 18 + (c >> 2)) * 64 + (c & 3) * 16 + (row & 15)) * 8;
      *reinterpret_cast<u32x4*>(dst) = ob;
    }
    return;
  }
  {
    const int fb = bid - 512;
    const int hh = fb / 3, k = (fb % 3) * 192 + tid;
    const int h = hh & 31;
    for (int i = tid; i < 18 * 20; i += 256) {
      const int d = i / 20, f = i % 20;
      wph[i] = (f < 18) ? bf2f(wphi[h * 324 + d * 18 + f]) : 0.f;
    }
    unsigned wv[9];
    if (tid < 192) {
      const u16* wp = wqkv + (size_t)k * NQKV_ + hh * 18;
#pragma unroll
      for (int d = 0; d < 9; ++d) wv[d] = *reinterpret_cast<const unsigned*>(wp + 2 * d);
    }
    __syncthreads();
    if (tid < 192) {
      float acc[20];
#pragma unroll
      for (int f = 0; f < 20; ++f) acc[f] = 0.f;
#pragma unroll
      for (int d = 0; d < 18; ++d) {
        const float w = (d & 1) ? hi16(wv[d >> 1]) : lo16(wv[d >> 1]);
#pragma unroll
        for (int f4 = 0; f4 < 5; ++f4) {
          f32x4 pw = *reinterpret_cast<const f32x4*>(wph + d * 20 + f4 * 4);
#pragma unroll
          for (int j = 0; j < 4; ++j) acc[f4 * 4 + j] += w * pw[j];
        }
      }
      u16* op = wfold + (size_t)k * NPH_ + hh * 18;
#pragma unroll
      for (int f = 0; f < 9; ++f) *reinterpret_cast<unsigned*>(op + 2 * f) = pack2(acc[2 * f], acc[2 * f + 1]);
    }
  }
}

constexpr int G1_BUFB = 192 * 128;
constexpr int G1_RING = 4;
constexpr int G1_XOFF = G1_RING * G1_BUFB;
constexpr int G1_BOFF = G1_XOFF + 576 * 32;
constexpr int G1_SMEM = G1_BOFF + 1024;
constexpr int G1_NST = 8;

template <int BASE, int I = 0>
__device__ __forceinline__ void g1_reads(WF (&w)[4], const unsigned (&ad)[4][2]) {
  if constexpr (I < 4) {
    if constexpr (BASE >= 49152) {
      tr_rd<BASE - 49152>(w[I].lo, ad[I][0] + 49152u);
      tr_rd<BASE - 49152>(w[I].hi, ad[I][1] + 49152u);
    } else {
      tr_rd<BASE>(w[I].lo, ad[I][0]);
      tr_rd<BASE>(w[I].hi, ad[I][1]);
    }
    g1_reads<BASE, I + 1>(w, ad);
  }
}
template <int KS>
__device__ __forceinline__ void g1_mma(WF (&w)[4], const bf16x8 (&xa)[2][18], f32x4 (&acc)[2][4]) {
#pragma unroll
  for (int i = 0; i < 4; ++i) pin(w[i]);
#pragma unroll
  for (int i = 0; i < 4; ++i)
#pragma unroll
    for (int r = 0; r < 2; ++r)
      acc[r][i] = __builtin_amdgcn_mfma_f32_16x16x32_bf16(cat(w[i]), xa[r][KS], acc[r][i], 0, 0, 0);
}

__device__ __forceinline__ float gelu_f(float v) {
  constexpr float c1 = -2.f * 0.7978845608028654f * 1.4426950408889634f;
  constexpr float c3 = c1 * 0.044715f;
  const float t = v * v;
  const float e = __builtin_amdgcn_exp2f(v * fmaf(t, c3, c1));
  return v * __builtin_amdgcn_rcpf(1.f + e);
}

typedef float f32x2 __attribute__((ext_vector_type(2)));
__device__ __forceinline__ unsigned cvt_pk(float lo, float hi) {
  unsigned r;
  asm("v_cvt_pk_bf16_f32 %0, %1, %2" : "=v"(r) : "v"(lo), "v"(hi));
  return r;
}
__device__ __forceinline__ unsigned gelu2_pk(f32x2 v) {
  constexpr float c1 = -2.f * 0.7978845608028654f * 1.4426950408889634f;
  constexpr float c3 = c1 * 0.044715f;
  const f32x2 t = v * v;
  const f32x2 pp = t * (f32x2){c3, c3} + (f32x2){c1, c1};
  const f32x2 u = v * pp;
  f32x2 e = {__builtin_amdgcn_exp2f(u[0]), __builtin_amdgcn_exp2f(u[1])};
  e = e + (f32x2){1.f, 1.f};
  const f32x2 r = {__builtin_amdgcn_rcpf(e[0]), __builtin_amdgcn_rcpf(e[1])};
  const f32x2 o = v * r;
  return cvt_pk(o[0], o[1]);
}

__device__ __forceinline__ void g1_store_tile(const f32x4& a, int tile, int mrow, int g, const u16* bias4,
                                              u16* __restrict__ qkv, u16* __restrict__ hf) {
  if (tile < NQKV_ / 16) {
    const int col = tile * 16 + 4 * g;
    const int hh = col / 18, f = col - hh * 18;
    u16* dst = qkv + ((size_t)hh * S_ + mrow) * 24 + f;
    const unsigned v0 = cvt_pk(a[0], a[1]), v1 = cvt_pk(a[2], a[3]);
    if (f != 16) {
      u32x2 v = {v0, v1};
      *reinterpret_cast<u32x2*>(dst) = v;
    } else {
      *reinterpret_cast<unsigned*>(dst) = v0;
      *reinterpret_cast<unsigned*>(qkv + ((size_t)(hh + 1) * S_ + mrow) * 24) = v1;
    }
  } else {
    u32x2 bb = *reinterpret_cast<const u32x2*>(bias4);
    float o[4];
    o[0] = gelu_f(a[0] + lo16(bb[0]));
    o[1] = gelu_f(a[1] + hi16(bb[0]));
    o[2] = gelu_f(a[2] + lo16(bb[1]));
    o[3] = gelu_f(a[3] + hi16(bb[1]));
    u32x2 v = {pack2(o[0], o[1]), pack2(o[2], o[3])};
    const int T = tile - NQKV_ / 16;
    u16* dst = hf + ((size_t)((mrow >> 4) * 144 + (T >> 1)) * 64 + ((T & 1) * 2 + (g >> 1)) * 16 + (mrow & 15)) * 8 + (g & 1) * 4;
    *reinterpret_cast<u32x2*>(dst) = v;
  }
}

__device__ __forceinline__ void g1_addr(unsigned (&ad)[4][2], char* smem, int lane) {
  const int g = lane >> 4, q = (lane & 15) >> 2, p = lane & 3;
#pragma unroll
  for (int t = 0; t < 2; ++t) {
    const int row = 8 * g + 4 * t + q;
    const int f = ((row >> 1) & 1) | (((row >> 3) & 1) << 1);
#pragma unroll
    for (int n = 0; n < 4; ++n) ad[n][t] = lds_addr(smem) + row * 128 + ((n ^ f) * 32) + p * 8;
  }
}

struct G1W { const u16* src; int stride; };

__device__ __forceinline__ void g1_wsetup(G1W& w, int col, int wid, int lane, const u16* wfold, const u16* wqkv, const u16* w1) {
  const int row = wid * 8 + (lane >> 3);
  const int f = ((row >> 1) & 1) | (((row >> 3) & 1) << 1);
  const int ch = (lane & 7) ^ (f << 1);
  if (col < NPH_) { w.src = wfold + (size_t)row * NPH_ + col + ch * 8; w.stride = 64 * NPH_; }
  else if (col < NQKV_) { w.src = wqkv + (size_t)row * NQKV_ + col + ch * 8; w.stride = 64 * NQKV_; }
  else { w.src = w1 + (size_t)row * F_ + (col - NQKV_) + ch * 8; w.stride = 64 * F_; }
}
__device__ __forceinline__ void g1_wissue(G1W& w, char* buf, int wid, int lane) {
#pragma unroll
  for (int i = 0; i < 3; ++i) {
    glds16(w.src, buf + i * 8192 + wid * 1024 + lane * 16);
    w.src += w.stride;
  }
}

template <int U>
__device__ __forceinline__ void g1_unit(G1W& wst, int unit0, const u16* wfold, const u16* wqkv,
                                        const u16* w1, char* smem, bf16x8 (&xa)[2][18], int m0,
                                        u16* __restrict__ qkv, u16* __restrict__ hf, WF (&wA)[4]) {
  int tz = threadIdx.x;
  asm volatile("" : "+v"(tz));
  const int wid = __builtin_amdgcn_readfirstlane(tz >> 6), lane = tz & 63;
  const int g = lane >> 4;
  unsigned ad[4][2];
  g1_addr(ad, smem, lane);
  f32x4 acc[2][4];
#pragma unroll
  for (int r = 0; r < 2; ++r)
#pragma unroll
    for (int n = 0; n < 4; ++n) acc[r][n] = (f32x4){0.f, 0.f, 0.f, 0.f};

  auto stage = [&](auto KC) {
    constexpr int kc = decltype(KC)::value;
    constexpr int s = U * 3 + kc;
    constexpr int CUR = (s % G1_RING) * G1_BUFB;
    if constexpr (U == 0) {
#pragma unroll
      for (int k = 0; k < 6; ++k) { pin(xa[0][kc * 6 + k]); pin(xa[1][kc * 6 + k]); }
    }
    WF wB[4];
    g1_reads<CUR + 1 * 4096>(wB, ad); wait_lgkm<8>(); g1_mma<kc * 6 + 0>(wA, xa, acc);
    g1_reads<CUR + 2 * 4096>(wA, ad); wait_lgkm<8>(); g1_mma<kc * 6 + 1>(wB, xa, acc);
    g1_reads<CUR + 3 * 4096>(wB, ad); wait_lgkm<8>(); g1_mma<kc * 6 + 2>(wA, xa, acc);
    g1_reads<CUR + 4 * 4096>(wA, ad); wait_lgkm<8>(); g1_mma<kc * 6 + 3>(wB, xa, acc);
    g1_reads<CUR + 5 * 4096>(wB, ad); wait_lgkm<8>(); g1_mma<kc * 6 + 4>(wA, xa, acc);
    if constexpr (s < 8) {
      constexpr int nl = (s == 0) ? 19 : ((s == 1) ? 7 : ((s + 2 <= 8) ? 3 : 0));
      constexpr int nst = (U > 0 && kc <= 1) ? G1_NST : 0;
      wait_vm<nl + nst>();
      BAR();
      if constexpr (s + 3 <= 8) {
        constexpr int sn = s + 3;
        if constexpr (sn % 3 == 0) g1_wsetup(wst, (unit0 + sn / 3) * 64, wid, lane, wfold, wqkv, w1);
        g1_wissue(wst, smem + (sn % G1_RING) * G1_BUFB, wid, lane);
      }
      g1_reads<((s + 1) % G1_RING) * G1_BUFB>(wA, ad);
      wait_lgkm<8>();
    } else {
      wait_lgkm<0>();
    }
    g1_mma<kc * 6 + 5>(wB, xa, acc);
  };
  stage(IC<0>{}); stage(IC<1>{}); stage(IC<2>{});
  const u16* bias_lds = reinterpret_cast<const u16*>(smem + G1_BOFF);
  if ((unit0 + U) * 64 < NQKV_) {
#pragma unroll
    for (int n = 0; n < 4; ++n) {
      const int tile = (unit0 + U) * 4 + n;
#pragma unroll
      for (int r = 0; r < 2; ++r)
        g1_store_tile(acc[r][n], tile, m0 + wid * 32 + r * 16 + (lane & 15), g, bias_lds, qkv, hf);
    }
  } else {
    u32x2 bb[4];
#pragma unroll
    for (int n = 0; n < 4; ++n) bb[n] = *reinterpret_cast<const u32x2*>(bias_lds + (U * 4 + n) * 16 + 4 * g);
    const int T0 = (unit0 + U) * 4 - NQKV_ / 16;
#pragma unroll
    for (int r = 0; r < 2; ++r) {
      const int mrow = m0 + wid * 32 + r * 16 + (lane & 15);
      u16* dst = hf + ((size_t)((mrow >> 4) * 144 + (T0 >> 1)) * 64 + (g >> 1) * 16 + (mrow & 15)) * 8 + (g & 1) * 4;
#pragma unroll
      for (int n = 0; n < 4; ++n) {
        const f32x2 v0 = (f32x2){acc[r][n][0], acc[r][n][1]} + (f32x2){lo16(bb[n][0]), hi16(bb[n][0])};
        const f32x2 v1 = (f32x2){acc[r][n][2], acc[r][n][3]} + (f32x2){lo16(bb[n][1]), hi16(bb[n][1])};
        u32x2 v = {gelu2_pk(v0), gelu2_pk(v1)};
        ST_NT(reinterpret_cast<u32x2*>(dst + (n >> 1) * 512 + (n & 1) * 256), v);
      }
    }
  }
}

__global__ __launch_bounds__(512, 2) void k1_gemm1(const u16* __restrict__ xnf, const u16* __restrict__ wfold,
                                                   const u16* __restrict__ wqkv, const u16* __restrict__ w1,
                                                   const u16* __restrict__ b1, u16* __restrict__ qkv,
                                                   u16* __restrict__ hf) {
  __shared__ __attribute__((aligned(16))) char smem[G1_SMEM];
  const int tid = threadIdx.x, wid = tid >> 6, lane = tid & 63;
  const int bid = blockIdx.x;
  const int mt = bid >> 5, grp = bid & 31;
  const int m0 = mt * 256;
  const int unit0 = grp * 3;
  const bool extra = grp >= 20;
  const int xcol = 6144 + (extra ? (grp - 20) : 0) * 16;
  STAMP(1, 0);
  G1W wst;
  g1_wsetup(wst, unit0 * 64, wid, lane, wfold, wqkv, w1);
  bf16x8 xa[2][18];
  const u16* xbase = xnf + ((size_t)((m0 >> 4) + wid * 2) * 18 * 64 + lane) * 8;
#pragma unroll
  for (int st = 0; st < 3; ++st) {
    g1_wissue(wst, smem + st * G1_BUFB, wid, lane);
#pragma unroll
    for (int ks = st * 6; ks < st * 6 + 6; ++ks)
#pragma unroll
      for (int r = 0; r < 2; ++r) gld16_asm(xa[r][ks], xbase + (size_t)(r * 18 + ks) * 512);
  }
#pragma unroll
  for (int i = 0; i < 3; ++i) {
    int ins = wid + 8 * i;
    if (ins >= 18) ins -= 18;
    const int c = ins * 64 + lane;
    const int pr = c >> 1, half = c & 1;
    const int k = (pr & ~0xC) | ((pr & 4) << 1) | ((pr & 8) >> 1);
    glds16(w1 + (size_t)k * F_ + (xcol - NQKV_) + half * 8, smem + G1_XOFF + c * 16);
  }
  {
    const int e = (wid & 1) * 64 + lane;
    int col = (e < 96) ? (unit0 * 64 + 2 * e - NQKV_) : (xcol - NQKV_ + 2 * (e - 96));
    if (col < 0 || e >= 104) col = 0;
    __builtin_amdgcn_global_load_lds((const unsigned*)(b1 + col), (unsigned*)(smem + G1_BOFF + e * 4), 4, 0, 0);
  }
  wait_vm<34>();
  BAR();
  WF wA[4];
  {
    unsigned ad[4][2];
    g1_addr(ad, smem, lane);
    g1_reads<0>(wA, ad);
  }
  STAMP(1, 1);
  g1_unit<0>(wst, unit0, wfold, wqkv, w1, smem, xa, m0, qkv, hf, wA);
  STAMP(1, 2);
  g1_unit<1>(wst, unit0, wfold, wqkv, w1, smem, xa, m0, qkv, hf, wA);
  STAMP(1, 3);
  g1_unit<2>(wst, unit0, wfold, wqkv, w1, smem, xa, m0, qkv, hf, wA);
  STAMP(1, 4);
  if (extra) {
    int tz = threadIdx.x;
    asm volatile("" : "+v"(tz));
    const int wid2 = __builtin_amdgcn_readfirstlane(tz >> 6), lane2 = tz & 63;
    const int g = lane2 >> 4, q = (lane2 & 15) >> 2, p = lane2 & 3;
    unsigned ad[2];
#pragma unroll
    for (int t = 0; t < 2; ++t) {
      const int k = 8 * g + 4 * t + q;
      const int pr = (k & ~0xC) | ((k & 4) << 1) | ((k & 8) >> 1);
      ad[t] = lds_addr(smem) + G1_XOFF + pr * 32 + p * 8;
    }
    f32x4 acc[2] = {(f32x4){0.f, 0.f, 0.f, 0.f}, (f32x4){0.f, 0.f, 0.f, 0.f}};
#pragma unroll
    for (int kb = 0; kb < 18; kb += 6) {
      WF w[6];
      tr_rd<0 * 1024>(w[0].lo, ad[0] + kb * 1024); tr_rd<0 * 1024>(w[0].hi, ad[1] + kb * 1024);
      tr_rd<1 * 1024>(w[1].lo, ad[0] + kb * 1024); tr_rd<1 * 1024>(w[1].hi, ad[1] + kb * 1024);
      tr_rd<2 * 1024>(w[2].lo, ad[0] + kb * 1024); tr_rd<2 * 1024>(w[2].hi, ad[1] + kb * 1024);
      tr_rd<3 * 1024>(w[3].lo, ad[0] + kb * 1024); tr_rd<3 * 1024>(w[3].hi, ad[1] + kb * 1024);
      tr_rd<4 * 1024>(w[4].lo, ad[0] + kb * 1024); tr_rd<4 * 1024>(w[4].hi, ad[1] + kb * 1024);
      tr_rd<5 * 1024>(w[5].lo, ad[0] + kb * 1024); tr_rd<5 * 1024>(w[5].hi, ad[1] + kb * 1024);
      wait_lgkm<0>();
#pragma unroll
      for (int i = 0; i < 6; ++i) pin(w[i]);
#pragma unroll
      for (int i = 0; i < 6; ++i)
#pragma unroll
        for (int r = 0; r < 2; ++r)
          acc[r] = __builtin_amdgcn_mfma_f32_16x16x32_bf16(cat(w[i]), xa[r][kb + i], acc[r], 0, 0, 0);
    }
    const u16* bias_lds = reinterpret_cast<const u16*>(smem + G1_BOFF);
#pragma unroll
    for (int r = 0; r < 2; ++r)
      g1_store_tile(acc[r], xcol / 16, m0 + wid2 * 32 + r * 16 + (lane2 & 15), g, bias_lds + 192 + 4 * g, qkv, hf);
  }
}

constexpr int G2_NT = 9, G2_RB = G2_NT * 32;
constexpr int G2_BUFB = 64 * G2_RB;
constexpr int G2_RING = 4;
constexpr int G2_STAGES = 18;

__device__ __forceinline__ void softmax_pm(const float (&pk)[18], float (&ep)[18], float (&en)[18]) {
  float mx = pk[0], mn = pk[0];
#pragma unroll
  for (int f = 1; f < 18; ++f) {
    mx = fmaxf(mx, pk[f]);
    mn = fminf(mn, pk[f]);
  }
  float sp = 0.f, sn = 0.f;
#pragma unroll
  for (int f = 0; f < 18; ++f) {
    ep[f] = __expf(pk[f] - mx);
    en[f] = __expf(mn - pk[f]);
    sp += ep[f];
    sn += en[f];
  }
  const float rp = 1.f / sp, rn = 1.f / sn;
#pragma unroll
  for (int f = 0; f < 18; ++f) {
    ep[f] *= rp;
    en[f] *= rn;
  }
}

__device__ void head_block(int hb, const u16* __restrict__ qkv, u16* __restrict__ of, float* pkv, unsigned* cnt, char* smem) {
  u16* phiT = reinterpret_cast<u16*>(smem);
  u16* vT = reinterpret_cast<u16*>(smem + 25344);
  float* kvs = reinterpret_cast<float*>(smem + 25344 + 16896);
  const int h = hb >> 3, c8 = hb & 7;
  const int tid = threadIdx.x, wid = tid >> 6, lane = tid & 63;
  const int row = c8 * 256 + tid;
  u32x4 kr[3], vr[3], qr[3];
  {
    const u32x4* kp = reinterpret_cast<const u32x4*>(qkv + ((size_t)(32 + h) * S_ + row) * 24);
    const u32x4* vp = reinterpret_cast<const u32x4*>(qkv + ((size_t)(64 + h) * S_ + row) * 24);
    const u32x4* qp = reinterpret_cast<const u32x4*>(qkv + ((size_t)h * S_ + row) * 24);
#pragma unroll
    for (int d = 0; d < 3; ++d) { kr[d] = kp[d]; vr[d] = vp[d]; qr[d] = qp[d]; }
  }
  for (int i = tid; i < 12 * 264; i += 256) phiT[36 * 264 + i] = 0;
  for (int i = tid; i < 14 * 264; i += 256) vT[18 * 264 + i] = (i < 264) ? (u16)0x3f80 : (u16)0;
  {
    float pk[18];
#pragma unroll
    for (int d = 0; d < 9; ++d) {
      pk[2 * d] = lo16(kr[d >> 2][d & 3]);
      pk[2 * d + 1] = hi16(kr[d >> 2][d & 3]);
    }
    float ep[18], en[18];
    softmax_pm(pk, ep, en);
#pragma unroll
    for (int f = 0; f < 18; ++f) {
      phiT[f * 264 + tid] = f2bf(ep[f]);
      phiT[(18 + f) * 264 + tid] = f2bf(en[f]);
      const unsigned w = vr[(f >> 1) >> 2][(f >> 1) & 3];
      vT[f * 264 + tid] = (u16)((f & 1) ? (w >> 16) : (w & 0xffff));
    }
  }
  __syncthreads();
  {
    f32x4 acc[3][2];
#pragma unroll
    for (int a = 0; a < 3; ++a)
#pragma unroll
      for (int b = 0; b < 2; ++b) acc[a][b] = (f32x4){0.f, 0.f, 0.f, 0.f};
#pragma unroll
    for (int ks = 0; ks < 2; ++ks) {
      const int s0 = wid * 64 + ks * 32 + 8 * (lane >> 4);
      bf16x8 bv[2];
#pragma unroll
      for (int b = 0; b < 2; ++b) bv[b] = *reinterpret_cast<const bf16x8*>(vT + (b * 16 + (lane & 15)) * 264 + s0);
#pragma unroll
      for (int a = 0; a < 3; ++a) {
        bf16x8 av = *reinterpret_cast<const bf16x8*>(phiT + (a * 16 + (lane & 15)) * 264 + s0);
#pragma unroll
        for (int b = 0; b < 2; ++b) acc[a][b] = __builtin_amdgcn_mfma_f32_16x16x32_bf16(av, bv[b], acc[a][b], 0, 0, 0);
      }
    }
#pragma unroll
    for (int a = 0; a < 3; ++a)
#pragma unroll
      for (int b = 0; b < 2; ++b)
#pragma unroll
        for (int j = 0; j < 4; ++j)
          kvs[(wid * 48 + a * 16 + (lane >> 4) * 4 + j) * 32 + b * 16 + (lane & 15)] = acc[a][b][j];
  }
  __syncthreads();
  float* mypart = pkv + (size_t)(h * 8 + c8) * 1536;
#pragma unroll
  for (int it = 0; it < 6; ++it) {
    const int i = it * 256 + tid;
    const float v = kvs[i] + kvs[1536 + i] + kvs[2 * 1536 + i] + kvs[3 * 1536 + i];
    __hip_atomic_store(mypart + i, v, __ATOMIC_RELAXED, __HIP_MEMORY_SCOPE_AGENT);
  }
  asm volatile("s_waitcnt vmcnt(0)" ::: "memory");
  __syncthreads();
  if (tid == 0) __hip_atomic_fetch_add(cnt + h * 32, 1u, __ATOMIC_RELAXED, __HIP_MEMORY_SCOPE_AGENT);
  float ep[18], en[18];
  {
    float pk[18];
#pragma unroll
    for (int d = 0; d < 9; ++d) {
      pk[2 * d] = lo16(qr[d >> 2][d & 3]);
      pk[2 * d + 1] = hi16(qr[d >> 2][d & 3]);
    }
    softmax_pm(pk, ep, en);
#pragma unroll
    for (int f = 0; f < 18; ++f) { ep[f] = rbf(ep[f]); en[f] = rbf(en[f]); }
  }
  if (tid == 0) {
    unsigned spins = 0;
    while (__hip_atomic_load(cnt + h * 32, __ATOMIC_RELAXED, __HIP_MEMORY_SCOPE_AGENT) < 8u) {
      __builtin_amdgcn_s_sleep(1);
      if (++spins > (1u << 24)) break;
    }
  }
  __syncthreads();
  u16* kvT = reinterpret_cast<u16*>(smem);
  u16* phiR = reinterpret_cast<u16*>(smem + 4608);
  {
    float* hp = pkv + (size_t)h * 8 * 1536;
    float pv[5][8];
#pragma unroll
    for (int it = 0; it < 5; ++it) {
      const int i = it * 256 + tid;
#pragma unroll
      for (int c = 0; c < 8; ++c) pv[it][c] = __hip_atomic_load(hp + c * 1536 + i, __ATOMIC_RELAXED, __HIP_MEMORY_SCOPE_AGENT);
    }
    {
      unsigned o[32];
#pragma unroll
      for (int f = 0; f < 9; ++f) { o[f] = pack2(ep[2 * f], ep[2 * f + 1]); o[9 + f] = pack2(en[2 * f], en[2 * f + 1]); }
#pragma unroll
      for (int f = 18; f < 32; ++f) o[f] = 0u;
#pragma unroll
      for (int c = 0; c < 8; ++c) {
        u32x4 v = {o[4 * c], o[4 * c + 1], o[4 * c + 2], o[4 * c + 3]};
        *reinterpret_cast<u32x4*>(phiR + tid * 72 + c * 8) = v;
      }
    }
#pragma unroll
    for (int it = 0; it < 8; ++it) {
      const int i = it * 256 + tid;
      const int f = i >> 5, d = i & 31;
      float v = 0.f;
      if (it < 5) {
#pragma unroll
        for (int c = 0; c < 8; ++c) v += pv[it < 5 ? it : 0][c];
      }
      kvT[d * 72 + f] = ((d < 19) && (f < 36)) ? f2bf(v) : (u16)0;
    }
  }
  __syncthreads();
  {
    const int g = lane >> 4;
    bf16x8 ka[2][2];
#pragma unroll
    for (int t = 0; t < 2; ++t)
#pragma unroll
      for (int ks = 0; ks < 2; ++ks)
        ka[t][ks] = *reinterpret_cast<const bf16x8*>(kvT + (t * 16 + (lane & 15)) * 72 + ks * 32 + 8 * g);
#pragma unroll
    for (int rt = 0; rt < 4; ++rt) {
      const int rl = wid * 64 + rt * 16 + (lane & 15);
      f32x4 c0 = {0.f, 0.f, 0.f, 0.f}, c1 = {0.f, 0.f, 0.f, 0.f};
#pragma unroll
      for (int ks = 0; ks < 2; ++ks) {
        const bf16x8 pb = *reinterpret_cast<const bf16x8*>(phiR + rl * 72 + ks * 32 + 8 * g);
        c0 = __builtin_amdgcn_mfma_f32_16x16x32_bf16(ka[0][ks], pb, c0, 0, 0, 0);
        c1 = __builtin_amdgcn_mfma_f32_16x16x32_bf16(ka[1][ks], pb, c1, 0, 0, 0);
      }
      const float den = __shfl(c1[2], lane & 15);
      const float rd = 1.f / (rbf(den) + 1e-6f);
      const int grow = c8 * 256 + rl;
      u16* obase = of + (size_t)(grow >> 4) * 18 * 512 + (grow & 15) * 8;
#pragma unroll
      for (int j2 = 0; j2 < 2; ++j2) {
        const int k = h * 18 + 4 * g + 2 * j2;
        *reinterpret_cast<unsigned*>(obase + (k >> 5) * 512 + ((k >> 3) & 3) * 128 + (k & 7)) =
            pack2(rbf(c0[2 * j2]) * rd, rbf(c0[2 * j2 + 1]) * rd);
      }
      if (g == 0) {
        const int k = h * 18 + 16;
        *reinterpret_cast<unsigned*>(obase + (k >> 5) * 512 + ((k >> 3) & 3) * 128 + (k & 7)) =
            pack2(rbf(c1[0]) * rd, rbf(c1[1]) * rd);
      }
    }
  }
}

template <int BASE, int N0, int CNT, int I = 0>
__device__ __forceinline__ void g2_wreads(WF (&w)[5], unsigned a0, unsigned a1) {
  if constexpr (I < CNT) {
    tr_rd<BASE + (N0 + I) * 32>(w[I].lo, a0);
    tr_rd<BASE + (N0 + I) * 32>(w[I].hi, a1);
    g2_wreads<BASE, N0, CNT, I + 1>(w, a0, a1);
  }
}
template <int N0, int CNT>
__device__ __forceinline__ void g2_mma(WF (&w)[5], const bf16x8 (&ac)[2], f32x4 (&acc)[2][G2_NT]) {
#pragma unroll
  for (int i = 0; i < CNT; ++i) pin(w[i]);
#pragma unroll
  for (int i = 0; i < CNT; ++i)
#pragma unroll
    for (int r = 0; r < 2; ++r)
      acc[r][N0 + i] = __builtin_amdgcn_mfma_f32_16x16x32_bf16(cat(w[i]), ac[r], acc[r][N0 + i], 0, 0, 0);
}
constexpr int K2_HEADBLK = 256;

__global__ __launch_bounds__(256, 2) void k2_gemm2(const u16* __restrict__ hf, const u16* __restrict__ w2,
                                                   u16* __restrict__ part, const u16* __restrict__ qkv,
                                                   u16* __restrict__ of, float* pkv, unsigned* cnt) {
  __shared__ __attribute__((aligned(16))) char smem[G2_RING * G2_BUFB];
  const int tid = threadIdx.x, wid = tid >> 6, lane = tid & 63;
  int bid = blockIdx.x;
  if (bid < K2_HEADBLK) { head_block(bid, qkv, of, pkv, cnt, smem); return; }
  bid -= K2_HEADBLK;
  STAMP(2, 0);
  const int xcd = bid & 7, loc = bid >> 3;
  const int sp = xcd >> 1, mt = (xcd & 1) * 8 + (loc >> 2), nt4 = loc & 3;
  const int m0 = mt * 128, n0 = nt4 * 144, k0 = sp * 1152;
  const int g = lane >> 4, q = (lane & 15) >> 2, p = lane & 3;
  const bool odd = g & 1;
  const unsigned wa0 = lds_addr(smem) + (8 * g + 4 * (g & 1) + q) * G2_RB + p * 8;
  const unsigned wa1 = lds_addr(smem) + (8 * g + 4 * (1 - (g & 1)) + q) * G2_RB + p * 8;
  f32x4 acc[2][G2_NT];
#pragma unroll
  for (int r = 0; r < 2; ++r)
#pragma unroll
    for (int n = 0; n < G2_NT; ++n) acc[r][n] = (f32x4){0.f, 0.f, 0.f, 0.f};
  const u16* wp[5];
  int wdst[5];
#pragma unroll
  for (int i = 0; i < 5; ++i) {
    int ins = wid + 4 * i;
    if (ins >= 18) ins -= 18;
    int c = ins * 64 + lane;
    int row = c / 18, cc = c % 18;
    wp[i] = w2 + (size_t)(k0 + row) * D_ + n0 + cc * 8;
    wdst[i] = c * 16;
  }
  const u16* ap[2];
#pragma unroll
  for (int r = 0; r < 2; ++r) ap[r] = hf + ((size_t)((m0 >> 4) + wid * 2 + r) * 144 + (k0 >> 5)) * 512 + lane * 8;
  bf16x8 an[4][2][2];
  auto issue = [&](auto SL) {
    constexpr int slot = decltype(SL)::value;
    char* buf = smem + slot * G2_BUFB;
#pragma unroll
    for (int i = 0; i < 5; ++i) { glds16(wp[i], buf + wdst[i]); wp[i] += 64 * D_; }
#pragma unroll
    for (int r = 0; r < 2; ++r) {
      gld16_asm(an[slot][r][0], ap[r]);
      gld16_asm(an[slot][r][1], ap[r] + 512);
      ap[r] += 1024;
    }
  };
  issue(IC<0>{});
  issue(IC<1>{});
  issue(IC<2>{});
  wait_vm<18>();
  BAR();
  STAMP(2, 1);
  WF wA[5], wB[5];
  g2_wreads<0, 0, 5>(wA, wa0, wa1);
  auto stage = [&](auto U, auto LAST) {
    constexpr int u = decltype(U)::value;
    constexpr int rem = decltype(LAST)::value;
    constexpr int BASE = u * G2_BUFB;
    bf16x8 c0[2], c1[2];
#pragma unroll
    for (int r = 0; r < 2; ++r) {
      pin(an[u][r][0]); pin(an[u][r][1]);
      c0[r] = swap_halves_if(an[u][r][0], odd);
      c1[r] = swap_halves_if(an[u][r][1], odd);
    }
    g2_wreads<BASE, 5, 4>(wB, wa0, wa1);
    wait_lgkm<8>();
    g2_mma<0, 5>(wA, c0, acc);
    g2_wreads<BASE + 32 * G2_RB, 0, 5>(wA, wa0, wa1);
    wait_lgkm<10>();
    g2_mma<5, 4>(wB, c0, acc);
    g2_wreads<BASE + 32 * G2_RB, 5, 4>(wB, wa0, wa1);
    wait_lgkm<8>();
    g2_mma<0, 5>(wA, c1, acc);
    if constexpr (rem >= 1) {
      if constexpr (rem >= 2) wait_vm<9>(); else wait_vm<0>();
      BAR();
      if constexpr (rem >= 3) issue(IC<(u + 3) & 3>{});
      g2_wreads<((u + 1) & 3) * G2_BUFB, 0, 5>(wA, wa0, wa1);
      wait_lgkm<10>();
    } else {
      wait_lgkm<0>();
    }
    g2_mma<5, 4>(wB, c1, acc);
  };
#pragma unroll 1
  for (int it = 0; it < 3; ++it) {
    stage(IC<0>{}, IC<3>{}); stage(IC<1>{}, IC<3>{}); stage(IC<2>{}, IC<3>{}); stage(IC<3>{}, IC<3>{});
  }
  STAMP(2, 2);
  stage(IC<0>{}, IC<3>{}); stage(IC<1>{}, IC<3>{}); stage(IC<2>{}, IC<3>{});
  stage(IC<3>{}, IC<2>{}); stage(IC<0>{}, IC<1>{}); stage(IC<1>{}, IC<0>{});
  STAMP(2, 3);
#pragma unroll
  for (int r = 0; r < 2; ++r) {
    const int m = m0 + wid * 32 + r * 16 + (lane & 15);
    u16* pp = part + ((size_t)sp * S_ + m) * D_ + n0 + 4 * g;
#pragma unroll
    for (int n = 0; n < G2_NT; ++n) {
      u32x2 v = {pack2(acc[r][n][0], acc[r][n][1]), pack2(acc[r][n][2], acc[r][n][3])};
      ST_NT(reinterpret_cast<u32x2*>(pp + n * 16), v);
    }
  }
  asm volatile("s_waitcnt vmcnt(0)" ::: "memory");
  STAMP(2, 4);
}

__global__ __launch_bounds__(512, 2) void k3_final(const u16* __restrict__ of, const u16* __restrict__ wo,
                                                   const u16* __restrict__ part, const u16* __restrict__ b2,
                                                   const u16* __restrict__ x, u16* __restrict__ out) {
  __shared__ __attribute__((aligned(16))) char smem[576 * 128];
  const int tid = threadIdx.x, wid = tid >> 6, lane = tid & 63;
  const int bid = blockIdx.x;
  const int xcd = bid & 7, loc = bid >> 3;
  const int mt = xcd * 2 + loc / 9, nt = loc % 9;
  const int m0 = mt * 128, n0 = nt * 64;
  const int g = lane >> 4, q = (lane & 15) >> 2, p = lane & 3;
  const int wr = wid >> 1, wc = wid & 1;
  STAMP(3, 0);
#pragma unroll
  for (int i = 0; i < 9; ++i) {
    const int row = (wid * 9 + i) * 8 + (lane >> 3);
    const int f = ((row >> 1) & 1) | (((row >> 3) & 1) << 1);
    const int ch = (lane & 7) ^ (f << 1);
    glds16(wo + (size_t)row * D_ + n0 + ch * 8, smem + (wid * 9 + i) * 1024 + lane * 16);
  }
  bf16x8 an[2][18];
#pragma unroll
  for (int ks = 0; ks < 18; ++ks)
#pragma unroll
    for (int r = 0; r < 2; ++r)
      gld16_asm(an[r][ks], of + ((size_t)(((m0 >> 4) + wr * 2 + r) * 18 + ks) * 64 + lane) * 8);
  u32x2 py[2][2][4];
  u32x2 xx[2][2], bb[2];
#pragma unroll
  for (int r = 0; r < 2; ++r) {
    const int m = m0 + wr * 32 + r * 16 + (lane & 15);
#pragma unroll
    for (int n = 0; n < 2; ++n) {
      const int ncol = n0 + wc * 32 + n * 16 + 4 * g;
#pragma unroll
      for (int sI = 0; sI < SPLITK_; ++sI)
        asm volatile("global_load_dwordx2 %0, %1, off" : "=v"(py[r][n][sI]) : "v"(part + ((size_t)sI * S_ + m) * D_ + ncol) : "memory");
      asm volatile("global_load_dwordx2 %0, %1, off" : "=v"(xx[r][n]) : "v"(x + (size_t)m * D_ + ncol) : "memory");
    }
  }
#pragma unroll
  for (int n = 0; n < 2; ++n)
    asm volatile("global_load_dwordx2 %0, %1, off" : "=v"(bb[n]) : "v"(b2 + n0 + wc * 32 + n * 16 + 4 * g) : "memory");
  unsigned ad[2][2];
#pragma unroll
  for (int t = 0; t < 2; ++t) {
    const int row = 8 * g + 4 * t + q;
    const int f = ((row >> 1) & 1) | (((row >> 3) & 1) << 1);
#pragma unroll
    for (int n = 0; n < 2; ++n) ad[n][t] = lds_addr(smem) + row * 128 + (((wc * 2 + n) ^ f) * 32) + p * 8;
  }
  f32x4 acc[2][2];
#pragma unroll
  for (int r = 0; r < 2; ++r)
#pragma unroll
    for (int n = 0; n < 2; ++n) acc[r][n] = (f32x4){0.f, 0.f, 0.f, 0.f};
  STAMP(3, 1);
  wait_vm<22>();
  BAR();
  STAMP(3, 2);
#pragma unroll
  for (int ks = 0; ks < 18; ++ks) { pin(an[0][ks]); pin(an[1][ks]); }
  auto grp = [&](auto KB) {
    constexpr int kb = decltype(KB)::value;
    WF w[6][2];
#pragma unroll
    for (int k = 0; k < 6; ++k)
#pragma unroll
      for (int n = 0; n < 2; ++n) {
        if (k == 0) { tr_rd<0 * 4096>(w[0][n].lo, ad[n][0] + kb * 4096); tr_rd<0 * 4096>(w[0][n].hi, ad[n][1] + kb * 4096); }
        if (k == 1) { tr_rd<1 * 4096>(w[1][n].lo, ad[n][0] + kb * 4096); tr_rd<1 * 4096>(w[1][n].hi, ad[n][1] + kb * 4096); }
        if (k == 2) { tr_rd<2 * 4096>(w[2][n].lo, ad[n][0] + kb * 4096); tr_rd<2 * 4096>(w[2][n].hi, ad[n][1] + kb * 4096); }
        if (k == 3) { tr_rd<3 * 4096>(w[3][n].lo, ad[n][0] + kb * 4096); tr_rd<3 * 4096>(w[3][n].hi, ad[n][1] + kb * 4096); }
        if (k == 4) { tr_rd<4 * 4096>(w[4][n].lo, ad[n][0] + kb * 4096); tr_rd<4 * 4096>(w[4][n].hi, ad[n][1] + kb * 4096); }
        if (k == 5) { tr_rd<5 * 4096>(w[5][n].lo, ad[n][0] + kb * 4096); tr_rd<5 * 4096>(w[5][n].hi, ad[n][1] + kb * 4096); }
      }
    wait_lgkm<12>();
#pragma unroll
    for (int k = 0; k < 3; ++k)
#pragma unroll
      for (int n = 0; n < 2; ++n) pin(w[k][n]);
#pragma unroll
    for (int k = 0; k < 3; ++k)
#pragma unroll
      for (int n = 0; n < 2; ++n)
#pragma unroll
        for (int r = 0; r < 2; ++r)
          acc[r][n] = __builtin_amdgcn_mfma_f32_16x16x32_bf16(cat(w[k][n]), an[r][kb + k], acc[r][n], 0, 0, 0);
    wait_lgkm<0>();
#pragma unroll
    for (int k = 3; k < 6; ++k)
#pragma unroll
      for (int n = 0; n < 2; ++n) pin(w[k][n]);
#pragma unroll
    for (int k = 3; k < 6; ++k)
#pragma unroll
      for (int n = 0; n < 2; ++n)
#pragma unroll
        for (int r = 0; r < 2; ++r)
          acc[r][n] = __builtin_amdgcn_mfma_f32_16x16x32_bf16(cat(w[k][n]), an[r][kb + k], acc[r][n], 0, 0, 0);
  };
  grp(IC<0>{}); grp(IC<6>{}); grp(IC<12>{});
  wait_vm<0>();
  STAMP(3, 3);
#pragma unroll
  for (int n = 0; n < 2; ++n) asm volatile("" : "+v"(bb[n]));
#pragma unroll
  for (int r = 0; r < 2; ++r) {
    const int m = m0 + wr * 32 + r * 16 + (lane & 15);
#pragma unroll
    for (int n = 0; n < 2; ++n) {
      const int ncol = n0 + wc * 32 + n * 16 + 4 * g;
      asm volatile("" : "+v"(xx[r][n]));
#pragma unroll
      for (int sI = 0; sI < SPLITK_; ++sI) asm volatile("" : "+v"(py[r][n][sI]));
      f32x4 y = {0.f, 0.f, 0.f, 0.f};
#pragma unroll
      for (int sI = 0; sI < SPLITK_; ++sI) {
        y[0] += lo16(py[r][n][sI][0]); y[1] += hi16(py[r][n][sI][0]);
        y[2] += lo16(py[r][n][sI][1]); y[3] += hi16(py[r][n][sI][1]);
      }
      float bv[4] = {lo16(bb[n][0]), hi16(bb[n][0]), lo16(bb[n][1]), hi16(bb[n][1])};
      float xv[4] = {lo16(xx[r][n][0]), hi16(xx[r][n][0]), lo16(xx[r][n][1]), hi16(xx[r][n][1])};
      float o[4];
#pragma unroll
      for (int j = 0; j < 4; ++j) {
        float yy = y[j] + bv[j];
        float gg = yy / (1.f + __expf(-yy));
        o[j] = xv[j] + acc[r][n][j] * gg;
      }
      u32x2 v = {pack2(o[0], o[1]), pack2(o[2], o[3])};
      *reinterpret_cast<u32x2*>(out + (size_t)m * D_ + ncol) = v;
    }
  }
  STAMP(3, 4);
}

extern "C" void kernel_launch(void* const* d_in, const int* in_sizes, int n_in, void* d_out, int out_size, void* d_ws,
                              size_t ws_size, hipStream_t stream) {
  const u16* x = (const u16*)d_in[0];
  const u16* wqkv = (const u16*)d_in[1];
  const u16* wphi = (const u16*)d_in[2];
  const u16* wo = (const u16*)d_in[3];
  const u16* w1 = (const u16*)d_in[5];
  const u16* b1 = (const u16*)d_in[6];
  const u16* w2 = (const u16*)d_in[7];
  const u16* b2 = (const u16*)d_in[8];
  u16* out = (u16*)d_out;
  char* ws = (char*)d_ws;
  size_t off = 0;
  u16* xnf = (u16*)(ws + off); off += (size_t)S_ * D_ * 2;
  u16* wfold = (u16*)(ws + off); off += (size_t)D_ * NPH_ * 2;
  u16* qkv = (u16*)(ws + off); off += (size_t)96 * S_ * 24 * 2;
  u16* hf = (u16*)(ws + off); off += (size_t)S_ * F_ * 2;
  u16* part = (u16*)(ws + off); off += (size_t)SPLITK_ * S_ * D_ * 2;
  u16* of = (u16*)(ws + off); off += (size_t)S_ * D_ * 2;
  float* pkv = (float*)(ws + off); off += (size_t)256 * 1536 * 4;
  unsigned* cnt = (unsigned*)(ws + off); off += 32 * 32 * 4;

  k0_prep<<<512 + 192, 256, 0, stream>>>(x, wqkv, wphi, xnf, wfold, cnt);
  k1_gemm1<<<256, 512, 0, stream>>>(xnf, wfold, wqkv, w1, b1, qkv, hf);
  k2_gemm2<<<K2_HEADBLK + 256, 256, 0, stream>>>(hf, w2, part, qkv, of, pkv, cnt);
  k3_final<<<16 * 9, 512, 0, stream>>>(of, wo, part, b2, x, out);
}
```

```cpp
#include <hip/hip_runtime.h>
#include <hip/hip_bf16.h>
#include <stdint.h>

constexpr int S_ = 2048, D_ = 576, H_ = 32, DH_ = 18, F_ = 4608, NQKV_ = 1728;
constexpr int NPH_ = 1152;
constexpr int KPH_ = 40;
constexpr int KPHI_ = H_ * KPH_;
constexpr int SPLITK_ = 4;

typedef short bf16x8 __attribute__((ext_vector_type(8)));
typedef short s16x4 __attribute__((ext_vector_type(4)));
typedef float f32x4 __attribute__((ext_vector_type(4)));
typedef unsigned u32x4 __attribute__((ext_vector_type(4)));
typedef unsigned u32x2 __attribute__((ext_vector_type(2)));
typedef __attribute__((address_space(3))) s16x4* lds_s16x4_p;
typedef unsigned short u16;
template <int V> struct IC { static constexpr int value = V; };

#define STAMP(kid, i) do { } while (0)
#define ST_NT(p, v) (*(p) = (v))
#define WAIT_LGKM0() asm volatile("s_waitcnt lgkmcnt(0)" ::: "memory")
#define BAR() __builtin_amdgcn_s_barrier()

template <int N>
__device__ __forceinline__ void wait_vm() {
  static_assert(N >= 0 && N < 64, "vmcnt range");
  asm volatile("s_waitcnt vmcnt(%0)" ::"n"(N) : "memory");
}

__device__ __forceinline__ float bf2f(u16 v) { return __uint_as_float(((unsigned)v) << 16); }
__device__ __forceinline__ u16 f2bf(float f) {
  __hip_bfloat16 b = __float2bfloat16(f);
  return *reinterpret_cast<u16*>(&b);
}
__device__ __forceinline__ unsigned pack2(float lo, float hi) {
  return (unsigned)f2bf(lo) | ((unsigned)f2bf(hi) << 16);
}
__device__ __forceinline__ float rbf(float f) { return bf2f(f2bf(f)); }
__device__ __forceinline__ float lo16(unsigned u) { return __uint_as_float(u << 16); }
__device__ __forceinline__ float hi16(unsigned u) { return __uint_as_float(u & 0xffff0000u); }

__device__ __forceinline__ void glds16(const void* g, void* l) {
  __builtin_amdgcn_global_load_lds((const unsigned*)g, (unsigned*)l, 16, 0, 0);
}

__device__ __forceinline__ void l2_touch(const void* g, void* ldummy) {
  __builtin_amdgcn_global_load_lds((const unsigned*)g, (unsigned*)ldummy, 4, 0, 0);
}

__device__ __forceinline__ bf16x8 swap_halves_if(bf16x8 v, bool odd) {
  bf16x8 s = __builtin_shufflevector(v, v, 4, 5, 6, 7, 0, 1, 2, 3);
  return odd ? s : v;
}

__device__ __forceinline__ bf16x8 wfrag(const char* img, int o0, int o1, int off) {
  s16x4 a = __builtin_amdgcn_ds_read_tr16_b64_v4i16((lds_s16x4_p)(img + o0 + off));
  s16x4 b = __builtin_amdgcn_ds_read_tr16_b64_v4i16((lds_s16x4_p)(img + o1 + off));
  return __builtin_shufflevector(a, b, 0, 1, 2, 3, 4, 5, 6, 7);
}

struct WF { s16x4 lo, hi; };
template <int OFF>
__device__ __forceinline__ void tr_rd(s16x4& r, unsigned addr) {
  asm volatile("ds_read_b64_tr_b16 %0, %1 offset:%2" : "=v"(r) : "v"(addr), "n"(OFF) : "memory");
}
__device__ __forceinline__ void gld16_asm(bf16x8& r, const void* p) {
  asm volatile("global_load_dwordx4 %0, %1, off" : "=v"(r) : "v"(p) : "memory");
}
template <int N>
__device__ __forceinline__ void wait_lgkm() {
  static_assert(N >= 0 && N <= 15, "lgkmcnt range");
  asm volatile("s_waitcnt lgkmcnt(%0)" ::"n"(N) : "memory");
}
__device__ __forceinline__ void pin(WF& w) { asm volatile("" : "+v"(w.lo), "+v"(w.hi)); }
__device__ __forceinline__ void pin(bf16x8& w) { asm volatile("" : "+v"(w)); }
__device__ __forceinline__ bf16x8 cat(const WF& w) { return __builtin_shufflevector(w.lo, w.hi, 0, 1, 2, 3, 4, 5, 6, 7); }
__device__ __forceinline__ unsigned lds_addr(const void* p) {
  return (unsigned)(uintptr_t)(__attribute__((address_space(3))) const char*)p;
}

__global__ __launch_bounds__(256) void k0_prep(const u16* __restrict__ x, const u16* __restrict__ wqkv,
                                               const u16* __restrict__ wphi, u16* __restrict__ xnf,
                                               u16* __restrict__ wfold, unsigned* __restrict__ cnt,
                                               const u16* __restrict__ w1, const u16* __restrict__ w2,
                                               const u16* __restrict__ wo) {
  __shared__ __attribute__((aligned(16))) float wph[18 * 20];
  const int tid = threadIdx.x, wid = tid >> 6, lane = tid & 63;
  const int bid = blockIdx.x;
  if (bid >= 704) {
    const int i = (bid - 704) * 256 + tid;
    const u16* p = (i < 41472) ? (w1 + (size_t)i * 64) : ((i < 82944) ? (w2 + (size_t)(i - 41472) * 64) : (wo + (size_t)(i - 82944) * 64));
    __builtin_amdgcn_global_load_lds((const unsigned*)p, (unsigned*)(reinterpret_cast<char*>(wph) + lane * 4), 4, 0, 0);
    asm volatile("s_waitcnt vmcnt(0)" ::: "memory");
    return;
  }
  if (bid == 512 && tid < 32) cnt[tid * 32] = 0u;
  if (bid < 512) {
    const int row = bid * 4 + wid;
    const u16* xp = x + (size_t)row * D_;
    u32x4 a = *reinterpret_cast<const u32x4*>(xp + lane * 8);
    u32x4 b = {0u, 0u, 0u, 0u};
    if (lane < 8) b = *reinterpret_cast<const u32x4*>(xp + 512 + lane * 8);
    float v[16];
#pragma unroll
    for (int j = 0; j < 4; ++j) {
      v[2 * j] = lo16(a[j]); v[2 * j + 1] = hi16(a[j]);
      v[8 + 2 * j] = lo16(b[j]); v[8 + 2 * j + 1] = hi16(b[j]);
    }
    float s = 0.f;
#pragma unroll
    for (int j = 0; j < 16; ++j) s += v[j];
#pragma unroll
    for (int o = 32; o >= 1; o >>= 1) s += __shfl_xor(s, o);
    const float mean = s * (1.f / D_);
    float ss = 0.f;
#pragma unroll
    for (int j = 0; j < 8; ++j) { float d = v[j] - mean; ss += d * d; }
    if (lane < 8) {
#pragma unroll
      for (int j = 8; j < 16; ++j) { float d = v[j] - mean; ss += d * d; }
    }
#pragma unroll
    for (int o = 32; o >= 1; o >>= 1) ss += __shfl_xor(ss, o);
    const float rstd = rsqrtf(ss * (1.f / D_) + 1e-5f);
    u32x4 oa, ob;
#pragma unroll
    for (int j = 0; j < 4; ++j) {
      oa[j] = pack2((v[2 * j] - mean) * rstd, (v[2 * j + 1] - mean) * rstd);
      ob[j] = pack2((v[8 + 2 * j] - mean) * rstd, (v[8 + 2 * j + 1] - mean) * rstd);
    }
    {
      const int c = lane;
      u16* dst = xnf + ((size_t)((row >> 4) * 18 + (c >> 2)) * 64 + (c & 3) * 16 + (row & 15)) * 8;
      *reinterpret_cast<u32x4*>(dst) = oa;
    }
    if (lane < 8) {
      const int c = 64 + lane;
      u16* dst = xnf + ((size_t)((row >> 4) * 18 + (c >> 2)) * 64 + (c & 3) * 16 + (row & 15)) * 8;
      *reinterpret_cast<u32x4*>(dst) = ob;
    }
    return;
  }
  {
    const int fb = bid - 512;
    const int hh = fb / 3, k = (fb % 3) * 192 + tid;
    const int h = hh & 31;
    for (int i = tid; i < 18 * 20; i += 256) {
      const int d = i / 20, f = i % 20;
      wph[i] = (f < 18) ? bf2f(wphi[h * 324 + d * 18 + f]) : 0.f;
    }
    unsigned wv[9];
    if (tid < 192) {
      const u16* wp = wqkv + (size_t)k * NQKV_ + hh * 18;
#pragma unroll
      for (int d = 0; d < 9; ++d) wv[d] = *reinterpret_cast<const unsigned*>(wp + 2 * d);
    }
    __syncthreads();
    if (tid < 192) {
      float acc[20];
#pragma unroll
      for (int f = 0; f < 20; ++f) acc[f] = 0.f;
#pragma unroll
      for (int d = 0; d < 18; ++d) {
        const float w = (d & 1) ? hi16(wv[d >> 1]) : lo16(wv[d >> 1]);
#pragma unroll
        for (int f4 = 0; f4 < 5; ++f4) {
          f32x4 pw = *reinterpret_cast<const f32x4*>(wph + d * 20 + f4 * 4);
#pragma unroll
          for (int j = 0; j < 4; ++j) acc[f4 * 4 + j] += w * pw[j];
        }
      }
      u16* op = wfold + (size_t)k * NPH_ + hh * 18;
#pragma unroll
      for (int f = 0; f < 9; ++f) *reinterpret_cast<unsigned*>(op + 2 * f) = pack2(acc[2 * f], acc[2 * f + 1]);
    }
  }
}

constexpr int G1_BUFB = 192 * 128;
constexpr int G1_RING = 4;
constexpr int G1_XOFF = G1_RING * G1_BUFB;
constexpr int G1_BOFF = G1_XOFF + 576 * 32;
constexpr int G1_SMEM = G1_BOFF + 1024;
constexpr int G1_NST = 8;

template <int BASE, int I = 0>
__device__ __forceinline__ void g1_reads(WF (&w)[4], const unsigned (&ad)[4][2]) {
  if constexpr (I < 4) {
    if constexpr (BASE >= 49152) {
      tr_rd<BASE - 49152>(w[I].lo, ad[I][0] + 49152u);
      tr_rd<BASE - 49152>(w[I].hi, ad[I][1] + 49152u);
    } else {
      tr_rd<BASE>(w[I].lo, ad[I][0]);
      tr_rd<BASE>(w[I].hi, ad[I][1]);
    }
    g1_reads<BASE, I + 1>(w, ad);
  }
}
template <int KS>
__device__ __forceinline__ void g1_mma(WF (&w)[4], const bf16x8 (&xa)[2][18], f32x4 (&acc)[2][4]) {
#pragma unroll
  for (int i = 0; i < 4; ++i) pin(w[i]);
#pragma unroll
  for (int i = 0; i < 4; ++i)
#pragma unroll
    for (int r = 0; r < 2; ++r)
      acc[r][i] = __builtin_amdgcn_mfma_f32_16x16x32_bf16(cat(w[i]), xa[r][KS], acc[r][i], 0, 0, 0);
}

__device__ __forceinline__ float gelu_f(float v) {
  constexpr float c1 = -2.f * 0.7978845608028654f * 1.4426950408889634f;
  constexpr float c3 = c1 * 0.044715f;
  const float t = v * v;
  const float e = __builtin_amdgcn_exp2f(v * fmaf(t, c3, c1));
  return v * __builtin_amdgcn_rcpf(1.f + e);
}

typedef float f32x2 __attribute__((ext_vector_type(2)));
__device__ __forceinline__ unsigned cvt_pk(float lo, float hi) {
  unsigned r;
  asm("v_cvt_pk_bf16_f32 %0, %1, %2" : "=v"(r) : "v"(lo), "v"(hi));
  return r;
}
__device__ __forceinline__ unsigned gelu2_pk(f32x2 v) {
  constexpr float c1 = -2.f * 0.7978845608028654f * 1.4426950408889634f;
  constexpr float c3 = c1 * 0.044715f;
  const f32x2 t = v * v;
  const f32x2 pp = t * (f32x2){c3, c3} + (f32x2){c1, c1};
  const f32x2 u = v * pp;
  f32x2 e = {__builtin_amdgcn_exp2f(u[0]), __builtin_amdgcn_exp2f(u[1])};
  e = e + (f32x2){1.f, 1.f};
  const f32x2 r = {__builtin_amdgcn_rcpf(e[0]), __builtin_amdgcn_rcpf(e[1])};
  const f32x2 o = v * r;
  return cvt_pk(o[0], o[1]);
}

__device__ __forceinline__ void g1_store_tile(const f32x4& a, int tile, int mrow, int g, const u16* bias4,
                                              u16* __restrict__ qkv, u16* __restrict__ hf) {
  if (tile < NQKV_ / 16) {
    const int col = tile * 16 + 4 * g;
    const int hh = col / 18, f = col - hh * 18;
    u16* dst = qkv + ((size_t)hh * S_ + mrow) * 24 + f;
    const unsigned v0 = cvt_pk(a[0], a[1]), v1 = cvt_pk(a[2], a[3]);
    if (f != 16) {
      u32x2 v = {v0, v1};
      *reinterpret_cast<u32x2*>(dst) = v;
    } else {
      *reinterpret_cast<unsigned*>(dst) = v0;
      *reinterpret_cast<unsigned*>(qkv + ((size_t)(hh + 1) * S_ + mrow) * 24) = v1;
    }
  } else {
    u32x2 bb = *reinterpret_cast<const u32x2*>(bias4);
    float o[4];
    o[0] = gelu_f(a[0] + lo16(bb[0]));
    o[1] = gelu_f(a[1] + hi16(bb[0]));
    o[2] = gelu_f(a[2] + lo16(bb[1]));
    o[3] = gelu_f(a[3] + hi16(bb[1]));
    u32x2 v = {pack2(o[0], o[1]), pack2(o[2], o[3])};
    const int T = tile - NQKV_ / 16;
    u16* dst = hf + ((size_t)((mrow >> 4) * 144 + (T >> 1)) * 64 + ((T & 1) * 2 + (g >> 1)) * 16 + (mrow & 15)) * 8 + (g & 1) * 4;
    *reinterpret_cast<u32x2*>(dst) = v;
  }
}

__device__ __forceinline__ void g1_addr(unsigned (&ad)[4][2], char* smem, int lane) {
  const int g = lane >> 4, q = (lane & 15) >> 2, p = lane & 3;
#pragma unroll
  for (int t = 0; t < 2; ++t) {
    const int row = 8 * g + 4 * t + q;
    const int f = ((row >> 1) & 1) | (((row >> 3) & 1) << 1);
#pragma unroll
    for (int n = 0; n < 4; ++n) ad[n][t] = lds_addr(smem) + row * 128 + ((n ^ f) * 32) + p * 8;
  }
}

struct G1W { const u16* src; int stride; };

__device__ __forceinline__ void g1_wsetup(G1W& w, int col, int wid, int lane, const u16* wfold, const u16* wqkv, const u16* w1) {
  const int row = wid * 8 + (lane >> 3);
  const int f = ((row >> 1) & 1) | (((row >> 3) & 1) << 1);
  const int ch = (lane & 7) ^ (f << 1);
  if (col < NPH_) { w.src = wfold + (size_t)row * NPH_ + col + ch * 8; w.stride = 64 * NPH_; }
  else if (col < NQKV_) { w.src = wqkv + (size_t)row * NQKV_ + col + ch * 8; w.stride = 64 * NQKV_; }
  else { w.src = w1 + (size_t)row * F_ + (col - NQKV_) + ch * 8; w.stride = 64 * F_; }
}
__device__ __forceinline__ void g1_wissue(G1W& w, char* buf, int wid, int lane) {
#pragma unroll
  for (int i = 0; i < 3; ++i) {
    glds16(w.src, buf + i * 8192 + wid * 1024 + lane * 16);
    w.src += w.stride;
  }
}

template <int U>
__device__ __forceinline__ void g1_unit(G1W& wst, int unit0, const u16* wfold, const u16* wqkv,
                                        const u16* w1, char* smem, bf16x8 (&xa)[2][18], int m0,
                                        u16* __restrict__ qkv, u16* __restrict__ hf, WF (&wA)[4]) {
  int tz = threadIdx.x;
  asm volatile("" : "+v"(tz));
  const int wid = __builtin_amdgcn_readfirstlane(tz >> 6), lane = tz & 63;
  const int g = lane >> 4;
  unsigned ad[4][2];
  g1_addr(ad, smem, lane);
  f32x4 acc[2][4];
#pragma unroll
  for (int r = 0; r < 2; ++r)
#pragma unroll
    for (int n = 0; n < 4; ++n) acc[r][n] = (f32x4){0.f, 0.f, 0.f, 0.f};

  auto stage = [&](auto KC) {
    constexpr int kc = decltype(KC)::value;
    constexpr int s = U * 3 + kc;
    constexpr int CUR = (s % G1_RING) * G1_BUFB;
    if constexpr (U == 0) {
#pragma unroll
      for (int k = 0; k < 6; ++k) { pin(xa[0][kc * 6 + k]); pin(xa[1][kc * 6 + k]); }
    }
    WF wB[4];
    g1_reads<CUR + 1 * 4096>(wB, ad); wait_lgkm<8>(); g1_mma<kc * 6 + 0>(wA, xa, acc);
    g1_reads<CUR + 2 * 4096>(wA, ad); wait_lgkm<8>(); g1_mma<kc * 6 + 1>(wB, xa, acc);
    g1_reads<CUR + 3 * 4096>(wB, ad); wait_lgkm<8>(); g1_mma<kc * 6 + 2>(wA, xa, acc);
    g1_reads<CUR + 4 * 4096>(wA, ad); wait_lgkm<8>(); g1_mma<kc * 6 + 3>(wB, xa, acc);
    g1_reads<CUR + 5 * 4096>(wB, ad); wait_lgkm<8>(); g1_mma<kc * 6 + 4>(wA, xa, acc);
    if constexpr (s < 8) {
      constexpr int nl = (s == 0) ? 19 : ((s == 1) ? 7 : ((s + 2 <= 8) ? 3 : 0));
      constexpr int nst = (U > 0 && kc <= 1) ? G1_NST : 0;
      wait_vm<nl + nst>();
      BAR();
      if constexpr (s + 3 <= 8) {
        constexpr int sn = s + 3;
        if constexpr (sn % 3 == 0) g1_wsetup(wst, (unit0 + sn / 3) * 64, wid, lane, wfold, wqkv, w1);
        g1_wissue(wst, smem + (sn % G1_RING) * G1_BUFB, wid, lane);
      }
      g1_reads<((s + 1) % G1_RING) * G1_BUFB>(wA, ad);
      wait_lgkm<8>();
    } else {
      wait_lgkm<0>();
    }
    g1_mma<kc * 6 + 5>(wB, xa, acc);
  };
  stage(IC<0>{}); stage(IC<1>{}); stage(IC<2>{});
  const u16* bias_lds = reinterpret_cast<const u16*>(smem + G1_BOFF);
  if ((unit0 + U) * 64 < NQKV_) {
#pragma unroll
    for (int n = 0; n < 4; ++n) {
      const int tile = (unit0 + U) * 4 + n;
#pragma unroll
      for (int r = 0; r < 2; ++r)
        g1_store_tile(acc[r][n], tile, m0 + wid * 32 + r * 16 + (lane & 15), g, bias_lds, qkv, hf);
    }
  } else {
    u32x2 bb[4];
#pragma unroll
    for (int n = 0; n < 4; ++n) bb[n] = *reinterpret_cast<const u32x2*>(bias_lds + (U * 4 + n) * 16 + 4 * g);
    const int T0 = (unit0 + U) * 4 - NQKV_ / 16;
#pragma unroll
    for (int r = 0; r < 2; ++r) {
      const int mrow = m0 + wid * 32 + r * 16 + (lane & 15);
      u16* dst = hf + ((size_t)((mrow >> 4) * 144 + (T0 >> 1)) * 64 + (g >> 1) * 16 + (mrow & 15)) * 8 + (g & 1) * 4;
#pragma unroll
      for (int n = 0; n < 4; ++n) {
        const f32x2 v0 = (f32x2){acc[r][n][0], acc[r][n][1]} + (f32x2){lo16(bb[n][0]), hi16(bb[n][0])};
        const f32x2 v1 = (f32x2){acc[r][n][2], acc[r][n][3]} + (f32x2){lo16(bb[n][1]), hi16(bb[n][1])};
        u32x2 v = {gelu2_pk(v0), gelu2_pk(v1)};
        ST_NT(reinterpret_cast<u32x2*>(dst + (n >> 1) * 512 + (n & 1) * 256), v);
      }
    }
  }
}

__global__ __launch_bounds__(512, 2) void k1_gemm1(const u16* __restrict__ xnf, const u16* __restrict__ wfold,
                                                   const u16* __restrict__ wqkv, const u16* __restrict__ w1,
                                                   const u16* __restrict__ b1, u16* __restrict__ qkv,
                                                   u16* __restrict__ hf) {
  __shared__ __attribute__((aligned(16))) char smem[G1_SMEM];
  const int tid = threadIdx.x, wid = tid >> 6, lane = tid & 63;
  const int bid = blockIdx.x;
  const int mt = bid >> 5, grp = bid & 31;
  const int m0 = mt * 256;
  const int unit0 = grp * 3;
  const bool extra = grp >= 20;
  const int xcol = 6144 + (extra ? (grp - 20) : 0) * 16;
  STAMP(1, 0);
  G1W wst;
  g1_wsetup(wst, unit0 * 64, wid, lane, wfold, wqkv, w1);
  bf16x8 xa[2][18];
  const u16* xbase = xnf + ((size_t)((m0 >> 4) + wid * 2) * 18 * 64 + lane) * 8;
#pragma unroll
  for (int st = 0; st < 3; ++st) {
    g1_wissue(wst, smem + st * G1_BUFB, wid, lane);
#pragma unroll
    for (int ks = st * 6; ks < st * 6 + 6; ++ks)
#pragma unroll
      for (int r = 0; r < 2; ++r) gld16_asm(xa[r][ks], xbase + (size_t)(r * 18 + ks) * 512);
  }
#pragma unroll
  for (int i = 0; i < 3; ++i) {
    int ins = wid + 8 * i;
    if (ins >= 18) ins -= 18;
    const int c = ins * 64 + lane;
    const int pr = c >> 1, half = c & 1;
    const int k = (pr & ~0xC) | ((pr & 4) << 1) | ((pr & 8) >> 1);
    glds16(w1 + (size_t)k * F_ + (xcol - NQKV_) + half * 8, smem + G1_XOFF + c * 16);
  }
  {
    const int e = (wid & 1) * 64 + lane;
    int col = (e < 96) ? (unit0 * 64 + 2 * e - NQKV_) : (xcol - NQKV_ + 2 * (e - 96));
    if (col < 0 || e >= 104) col = 0;
    __builtin_amdgcn_global_load_lds((const unsigned*)(b1 + col), (unsigned*)(smem + G1_BOFF + e * 4), 4, 0, 0);
  }
  wait_vm<34>();
  BAR();
  WF wA[4];
  {
    unsigned ad[4][2];
    g1_addr(ad, smem, lane);
    g1_reads<0>(wA, ad);
  }
  STAMP(1, 1);
  g1_unit<0>(wst, unit0, wfold, wqkv, w1, smem, xa, m0, qkv, hf, wA);
  STAMP(1, 2);
  g1_unit<1>(wst, unit0, wfold, wqkv, w1, smem, xa, m0, qkv, hf, wA);
  STAMP(1, 3);
  g1_unit<2>(wst, unit0, wfold, wqkv, w1, smem, xa, m0, qkv, hf, wA);
  STAMP(1, 4);
  if (extra) {
    int tz = threadIdx.x;
    asm volatile("" : "+v"(tz));
    const int wid2 = __builtin_amdgcn_readfirstlane(tz >> 6), lane2 = tz & 63;
    const int g = lane2 >> 4, q = (lane2 & 15) >> 2, p = lane2 & 3;
    unsigned ad[2];
#pragma unroll
    for (int t = 0; t < 2; ++t) {
      const int k = 8 * g + 4 * t + q;
      const int pr = (k & ~0xC) | ((k & 4) << 1) | ((k & 8) >> 1);
      ad[t] = lds_addr(smem) + G1_XOFF + pr * 32 + p * 8;
    }
    f32x4 acc[2] = {(f32x4){0.f, 0.f, 0.f, 0.f}, (f32x4){0.f, 0.f, 0.f, 0.f}};
#pragma unroll
    for (int kb = 0; kb < 18; kb += 6) {
      WF w[6];
      tr_rd<0 * 1024>(w[0].lo, ad[0] + kb * 1024); tr_rd<0 * 1024>(w[0].hi, ad[1] + kb * 1024);
      tr_rd<1 * 1024>(w[1].lo, ad[0] + kb * 1024); tr_rd<1 * 1024>(w[1].hi, ad[1] + kb * 1024);
      tr_rd<2 * 1024>(w[2].lo, ad[0] + kb * 1024); tr_rd<2 * 1024>(w[2].hi, ad[1] + kb * 1024);
      tr_rd<3 * 1024>(w[3].lo, ad[0] + kb * 1024); tr_rd<3 * 1024>(w[3].hi, ad[1] + kb * 1024);
      tr_rd<4 * 1024>(w[4].lo, ad[0] + kb * 1024); tr_rd<4 * 1024>(w[4].hi, ad[1] + kb * 1024);
      tr_rd<5 * 1024>(w[5].lo, ad[0] + kb * 1024); tr_rd<5 * 1024>(w[5].hi, ad[1] + kb * 1024);
      wait_lgkm<0>();
#pragma unroll
      for (int i = 0; i < 6; ++i) pin(w[i]);
#pragma unroll
      for (int i = 0; i < 6; ++i)
#pragma unroll
        for (int r = 0; r < 2; ++r)
          acc[r] = __builtin_amdgcn_mfma_f32_16x16x32_bf16(cat(w[i]), xa[r][kb + i], acc[r], 0, 0, 0);
    }
    const u16* bias_lds = reinterpret_cast<const u16*>(smem + G1_BOFF);
#pragma unroll
    for (int r = 0; r < 2; ++r)
      g1_store_tile(acc[r], xcol / 16, m0 + wid2 * 32 + r * 16 + (lane2 & 15), g, bias_lds + 192 + 4 * g, qkv, hf);
  }
}

constexpr int G2_NT = 9, G2_RB = G2_NT * 32;
constexpr int G2_BUFB = 64 * G2_RB;
constexpr int G2_RING = 4;
constexpr int G2_STAGES = 18;

__device__ __forceinline__ void softmax_pm(const float (&pk)[18], float (&ep)[18], float (&en)[18]) {
  float mx = pk[0], mn = pk[0];
#pragma unroll
  for (int f = 1; f < 18; ++f) {
    mx = fmaxf(mx, pk[f]);
    mn = fminf(mn, pk[f]);
  }
  float sp = 0.f, sn = 0.f;
#pragma unroll
  for (int f = 0; f < 18; ++f) {
    ep[f] = __expf(pk[f] - mx);
    en[f] = __expf(mn - pk[f]);
    sp += ep[f];
    sn += en[f];
  }
  const float rp = 1.f / sp, rn = 1.f / sn;
#pragma unroll
  for (int f = 0; f < 18; ++f) {
    ep[f] *= rp;
    en[f] *= rn;
  }
}

__device__ void head_block(int hb, const u16* __restrict__ qkv, u16* __restrict__ of, float* pkv, unsigned* cnt, char* smem) {
  u16* phiT = reinterpret_cast<u16*>(smem);
  u16* vT = reinterpret_cast<u16*>(smem + 25344);
  float* kvs = reinterpret_cast<float*>(smem + 25344 + 16896);
  const int h = hb >> 3, c8 = hb & 7;
  const int tid = threadIdx.x, wid = tid >> 6, lane = tid & 63;
  const int row = c8 * 256 + tid;
  u32x4 kr[3], vr[3], qr[3];
  {
    const u32x4* kp = reinterpret_cast<const u32x4*>(qkv + ((size_t)(32 + h) * S_ + row) * 24);
    const u32x4* vp = reinterpret_cast<const u32x4*>(qkv + ((size_t)(64 + h) * S_ + row) * 24);
    const u32x4* qp = reinterpret_cast<const u32x4*>(qkv + ((size_t)h * S_ + row) * 24);
#pragma unroll
    for (int d = 0; d < 3; ++d) { kr[d] = kp[d]; vr[d] = vp[d]; qr[d] = qp[d]; }
  }
  for (int i = tid; i < 12 * 264; i += 256) phiT[36 * 264 + i] = 0;
  for (int i = tid; i < 14 * 264; i += 256) vT[18 * 264 + i] = (i < 264) ? (u16)0x3f80 : (u16)0;
  {
    float pk[18];
#pragma unroll
    for (int d = 0; d < 9; ++d) {
      pk[2 * d] = lo16(kr[d >> 2][d & 3]);
      pk[2 * d + 1] = hi16(kr[d >> 2][d & 3]);
    }
    float ep[18], en[18];
    softmax_pm(pk, ep, en);
#pragma unroll
    for (int f = 0; f < 18; ++f) {
      phiT[f * 264 + tid] = f2bf(ep[f]);
      phiT[(18 + f) * 264 + tid] = f2bf(en[f]);
      const unsigned w = vr[(f >> 1) >> 2][(f >> 1) & 3];
      vT[f * 264 + tid] = (u16)((f & 1) ? (w >> 16) : (w & 0xffff));
    }
  }
  __syncthreads();
  {
    f32x4 acc[3][2];
#pragma unroll
    for (int a = 0; a < 3; ++a)
#pragma unroll
      for (int b = 0; b < 2; ++b) acc[a][b] = (f32x4){0.f, 0.f, 0.f, 0.f};
#pragma unroll
    for (int ks = 0; ks < 2; ++ks) {
      const int s0 = wid * 64 + ks * 32 + 8 * (lane >> 4);
      bf16x8 bv[2];
#pragma unroll
      for (int b = 0; b < 2; ++b) bv[b] = *reinterpret_cast<const bf16x8*>(vT + (b * 16 + (lane & 15)) * 264 + s0);
#pragma unroll
      for (int a = 0; a < 3; ++a) {
        bf16x8 av = *reinterpret_cast<const bf16x8*>(phiT + (a * 16 + (lane & 15)) * 264 + s0);
#pragma unroll
        for (int b = 0; b < 2; ++b) acc[a][b] = __builtin_amdgcn_mfma_f32_16x16x32_bf16(av, bv[b], acc[a][b], 0, 0, 0);
      }
    }
#pragma unroll
    for (int a = 0; a < 3; ++a)
#pragma unroll
      for (int b = 0; b < 2; ++b)
#pragma unroll
        for (int j = 0; j < 4; ++j)
          kvs[(wid * 48 + a * 16 + (lane >> 4) * 4 + j) * 32 + b * 16 + (lane & 15)] = acc[a][b][j];
  }
  __syncthreads();
  float* mypart = pkv + (size_t)(h * 8 + c8) * 1536;
#pragma unroll
  for (int it = 0; it < 6; ++it) {
    const int i = it * 256 + tid;
    const float v = kvs[i] + kvs[1536 + i] + kvs[2 * 1536 + i] + kvs[3 * 1536 + i];
    __hip_atomic_store(mypart + i, v, __ATOMIC_RELAXED, __HIP_MEMORY_SCOPE_AGENT);
  }
  asm volatile("s_waitcnt vmcnt(0)" ::: "memory");
  __syncthreads();
  if (tid == 0) __hip_atomic_fetch_add(cnt + h * 32, 1u, __ATOMIC_RELAXED, __HIP_MEMORY_SCOPE_AGENT);
  float ep[18], en[18];
  {
    float pk[18];
#pragma unroll
    for (int d = 0; d < 9; ++d) {
      pk[2 * d] = lo16(qr[d >> 2][d & 3]);
      pk[2 * d + 1] = hi16(qr[d >> 2][d & 3]);
    }
    softmax_pm(pk, ep, en);
#pragma unroll
    for (int f = 0; f < 18; ++f) { ep[f] = rbf(ep[f]); en[f] = rbf(en[f]); }
  }
  if (tid == 0) {
    unsigned spins = 0;
    while (__hip_atomic_load(cnt + h * 32, __ATOMIC_RELAXED, __HIP_MEMORY_SCOPE_AGENT) < 8u) {
      __builtin_amdgcn_s_sleep(1);
      if (++spins > (1u << 24)) break;
    }
  }
  __syncthreads();
  u16* kvT = reinterpret_cast<u16*>(smem);
  u16* phiR = reinterpret_cast<u16*>(smem + 4608);
  {
    float* hp = pkv + (size_t)h * 8 * 1536;
    float pv[5][8];
#pragma unroll
    for (int it = 0; it < 5; ++it) {
      const int i = it * 256 + tid;
#pragma unroll
      for (int c = 0; c < 8; ++c) pv[it][c] = __hip_atomic_load(hp + c * 1536 + i, __ATOMIC_RELAXED, __HIP_MEMORY_SCOPE_AGENT);
    }
    {
      unsigned o[32];
#pragma unroll
      for (int f = 0; f < 9; ++f) { o[f] = pack2(ep[2 * f], ep[2 * f + 1]); o[9 + f] = pack2(en[2 * f], en[2 * f + 1]); }
#pragma unroll
      for (int f = 18; f < 32; ++f) o[f] = 0u;
#pragma unroll
      for (int c = 0; c < 8; ++c) {
        u32x4 v = {o[4 * c], o[4 * c + 1], o[4 * c + 2], o[4 * c + 3]};
        *reinterpret_cast<u32x4*>(phiR + tid * 72 + c * 8) = v;
      }
    }
#pragma unroll
    for (int it = 0; it < 8; ++it) {
      const int i = it * 256 + tid;
      const int f = i >> 5, d = i & 31;
      float v = 0.f;
      if (it < 5) {
#pragma unroll
        for (int c = 0; c < 8; ++c) v += pv[it < 5 ? it : 0][c];
      }
      kvT[d * 72 + f] = ((d < 19) && (f < 36)) ? f2bf(v) : (u16)0;
    }
  }
  __syncthreads();
  {
    const int g = lane >> 4;
    bf16x8 ka[2][2];
#pragma unroll
    for (int t = 0; t < 2; ++t)
#pragma unroll
      for (int ks = 0; ks < 2; ++ks)
        ka[t][ks] = *reinterpret_cast<const bf16x8*>(kvT + (t * 16 + (lane & 15)) * 72 + ks * 32 + 8 * g);
#pragma unroll
    for (int rt = 0; rt < 4; ++rt) {
      const int rl = wid * 64 + rt * 16 + (lane & 15);
      f32x4 c0 = {0.f, 0.f, 0.f, 0.f}, c1 = {0.f, 0.f, 0.f, 0.f};
#pragma unroll
      for (int ks = 0; ks < 2; ++ks) {
        const bf16x8 pb = *reinterpret_cast<const bf16x8*>(phiR + rl * 72 + ks * 32 + 8 * g);
        c0 = __builtin_amdgcn_mfma_f32_16x16x32_bf16(ka[0][ks], pb, c0, 0, 0, 0);
        c1 = __builtin_amdgcn_mfma_f32_16x16x32_bf16(ka[1][ks], pb, c1, 0, 0, 0);
      }
      const float den = __shfl(c1[2], lane & 15);
      const float rd = 1.f / (rbf(den) + 1e-6f);
      const int grow = c8 * 256 + rl;
      u16* obase = of + (size_t)(grow >> 4) * 18 * 512 + (grow & 15) * 8;
#pragma unroll
      for (int j2 = 0; j2 < 2; ++j2) {
        const int k = h * 18 + 4 * g + 2 * j2;
        *reinterpret_cast<unsigned*>(obase + (k >> 5) * 512 + ((k >> 3) & 3) * 128 + (k & 7)) =
            pack2(rbf(c0[2 * j2]) * rd, rbf(c0[2 * j2 + 1]) * rd);
      }
      if (g == 0) {
        const int k = h * 18 + 16;
        *reinterpret_cast<unsigned*>(obase + (k >> 5) * 512 + ((k >> 3) & 3) * 128 + (k & 7)) =
            pack2(rbf(c1[0]) * rd, rbf(c1[1]) * rd);
      }
    }
  }
}

template <int BASE, int N0, int CNT, int I = 0>
__device__ __forceinline__ void g2_wreads(WF (&w)[5], unsigned a0, unsigned a1) {
  if constexpr (I < CNT) {
    tr_rd<BASE + (N0 + I) * 32>(w[I].lo, a0);
    tr_rd<BASE + (N0 + I) * 32>(w[I].hi, a1);
    g2_wreads<BASE, N0, CNT, I + 1>(w, a0, a1);
  }
}
template <int N0, int CNT>
__device__ __forceinline__ void g2_mma(WF (&w)[5], const bf16x8 (&ac)[2], f32x4 (&acc)[2][G2_NT]) {
#pragma unroll
  for (int i = 0; i < CNT; ++i) pin(w[i]);
#pragma unroll
  for (int i = 0; i < CNT; ++i)
#pragma unroll
    for (int r = 0; r < 2; ++r)
      acc[r][N0 + i] = __builtin_amdgcn_mfma_f32_16x16x32_bf16(cat(w[i]), ac[r], acc[r][N0 + i], 0, 0, 0);
}
constexpr int K2_HEADBLK = 256;

__global__ __launch_bounds__(256, 2) void k2_gemm2(const u16* __restrict__ hf, const u16* __restrict__ w2,
                                                   u16* __restrict__ part, const u16* __restrict__ qkv,
                                                   u16* __restrict__ of, float* pkv, unsigned* cnt) {
  __shared__ __attribute__((aligned(16))) char smem[G2_RING * G2_BUFB];
  const int tid = threadIdx.x, wid = tid >> 6, lane = tid & 63;
  int bid = blockIdx.x;
  if (bid < K2_HEADBLK) { head_block(bid, qkv, of, pkv, cnt, smem); return; }
  bid -= K2_HEADBLK;
  __builtin_amdgcn_s_setprio(3);
  STAMP(2, 0);
  const int xcd = bid & 7, loc = bid >> 3;
  const int sp = xcd >> 1, mt = (xcd & 1) * 8 + (loc >> 2), nt4 = loc & 3;
  const int m0 = mt * 128, n0 = nt4 * 144, k0 = sp * 1152;
  const int g = lane >> 4, q = (lane & 15) >> 2, p = lane & 3;
  const bool odd = g & 1;
  const unsigned wa0 = lds_addr(smem) + (8 * g + 4 * (g & 1) + q) * G2_RB + p * 8;
  const unsigned wa1 = lds_addr(smem) + (8 * g + 4 * (1 - (g & 1)) + q) * G2_RB + p * 8;
  f32x4 acc[2][G2_NT];
#pragma unroll
  for (int r = 0; r < 2; ++r)
#pragma unroll
    for (int n = 0; n < G2_NT; ++n) acc[r][n] = (f32x4){0.f, 0.f, 0.f, 0.f};
  const u16* wp[5];
  int wdst[5];
#pragma unroll
  for (int i = 0; i < 5; ++i) {
    int ins = wid + 4 * i;
    if (ins >= 18) ins -= 18;
    int c = ins * 64 + lane;
    int row = c / 18, cc = c % 18;
    wp[i] = w2 + (size_t)(k0 + row) * D_ + n0 + cc * 8;
    wdst[i] = c * 16;
  }
  const u16* ap[2];
#pragma unroll
  for (int r = 0; r < 2; ++r) ap[r] = hf + ((size_t)((m0 >> 4) + wid * 2 + r) * 144 + (k0 >> 5)) * 512 + lane * 8;
  bf16x8 an[4][2][2];
  auto issue = [&](auto SL) {
    constexpr int slot = decltype(SL)::value;
    char* buf = smem + slot * G2_BUFB;
#pragma unroll
    for (int i = 0; i < 5; ++i) { glds16(wp[i], buf + wdst[i]); wp[i] += 64 * D_; }
#pragma unroll
    for (int r = 0; r < 2; ++r) {
      gld16_asm(an[slot][r][0], ap[r]);
      gld16_asm(an[slot][r][1], ap[r] + 512);
      ap[r] += 1024;
    }
  };
  issue(IC<0>{});
  issue(IC<1>{});
  issue(IC<2>{});
  wait_vm<18>();
  BAR();
  STAMP(2, 1);
  WF wA[5], wB[5];
  g2_wreads<0, 0, 5>(wA, wa0, wa1);
  auto stage = [&](auto U, auto LAST) {
    constexpr int u = decltype(U)::value;
    constexpr int rem = decltype(LAST)::value;
    constexpr int BASE = u * G2_BUFB;
    bf16x8 c0[2], c1[2];
#pragma unroll
    for (int r = 0; r < 2; ++r) {
      pin(an[u][r][0]); pin(an[u][r][1]);
      c0[r] = swap_halves_if(an[u][r][0], odd);
      c1[r] = swap_halves_if(an[u][r][1], odd);
    }
    g2_wreads<BASE, 5, 4>(wB, wa0, wa1);
    wait_lgkm<8>();
    g2_mma<0, 5>(wA, c0, acc);
    g2_wreads<BASE + 32 * G2_RB, 0, 5>(wA, wa0, wa1);
    wait_lgkm<10>();
    g2_mma<5, 4>(wB, c0, acc);
    g2_wreads<BASE + 32 * G2_RB, 5, 4>(wB, wa0, wa1);
    wait_lgkm<8>();
    g2_mma<0, 5>(wA, c1, acc);
    if constexpr (rem >= 1) {
      if constexpr (rem >= 2) wait_vm<9>(); else wait_vm<0>();
      BAR();
      if constexpr (rem >= 3) issue(IC<(u + 3) & 3>{});
      g2_wreads<((u + 1) & 3) * G2_BUFB, 0, 5>(wA, wa0, wa1);
      wait_lgkm<10>();
    } else {
      wait_lgkm<0>();
    }
    g2_mma<5, 4>(wB, c1, acc);
  };
#pragma unroll 1
  for (int it = 0; it < 3; ++it) {
    stage(IC<0>{}, IC<3>{}); stage(IC<1>{}, IC<3>{}); stage(IC<2>{}, IC<3>{}); stage(IC<3>{}, IC<3>{});
  }
  STAMP(2, 2);
  stage(IC<0>{}, IC<3>{}); stage(IC<1>{}, IC<3>{}); stage(IC<2>{}, IC<3>{});
  stage(IC<3>{}, IC<2>{}); stage(IC<0>{}, IC<1>{}); stage(IC<1>{}, IC<0>{});
  STAMP(2, 3);
#pragma unroll
  for (int r = 0; r < 2; ++r) {
    const int m = m0 + wid * 32 + r * 16 + (lane & 15);
    u16* pp = part + ((size_t)sp * S_ + m) * D_ + n0 + 4 * g;
#pragma unroll
    for (int n = 0; n < G2_NT; ++n) {
      u32x2 v = {pack2(acc[r][n][0], acc[r][n][1]), pack2(acc[r][n][2], acc[r][n][3])};
      ST_NT(reinterpret_cast<u32x2*>(pp + n * 16), v);
    }
  }
  asm volatile("s_waitcnt vmcnt(0)" ::: "memory");
  STAMP(2, 4);
}

__global__ __launch_bounds__(512, 2) void k3_final(const u16* __restrict__ of, const u16* __restrict__ wo,
                                                   const u16* __restrict__ part, const u16* __restrict__ b2,
                                                   const u16* __restrict__ x, u16* __restrict__ out) {
  __shared__ __attribute__((aligned(16))) char smem[576 * 128];
  const int tid = threadIdx.x, wid = tid >> 6, lane = tid & 63;
  const int bid = blockIdx.x;
  const int xcd = bid & 7, loc = bid >> 3;
  const int mt = xcd * 2 + loc / 9, nt = loc % 9;
  const int m0 = mt * 128, n0 = nt * 64;
  const int g = lane >> 4, q = (lane & 15) >> 2, p = lane & 3;
  const int wr = wid >> 1, wc = wid & 1;
  STAMP(3, 0);
#pragma unroll
  for (int i = 0; i < 9; ++i) {
    const int row = (wid * 9 + i) * 8 + (lane >> 3);
    const int f = ((row >> 1) & 1) | (((row >> 3) & 1) << 1);
    const int ch = (lane & 7) ^ (f << 1);
    glds16(wo + (size_t)row * D_ + n0 + ch * 8, smem + (wid * 9 + i) * 1024 + lane * 16);
  }
  bf16x8 an[2][18];
#pragma unroll
  for (int ks = 0; ks < 18; ++ks)
#pragma unroll
    for (int r = 0; r < 2; ++r)
      gld16_asm(an[r][ks], of + ((size_t)(((m0 >> 4) + wr * 2 + r) * 18 + ks) * 64 + lane) * 8);
  u32x2 py[2][2][4];
  u32x2 xx[2][2], bb[2];
#pragma unroll
  for (int r = 0; r < 2; ++r) {
    const int m = m0 + wr * 32 + r * 16 + (lane & 15);
#pragma unroll
    for (int n = 0; n < 2; ++n) {
      const int ncol = n0 + wc * 32 + n * 16 + 4 * g;
#pragma unroll
      for (int sI = 0; sI < SPLITK_; ++sI)
        asm volatile("global_load_dwordx2 %0, %1, off" : "=v"(py[r][n][sI]) : "v"(part + ((size_t)sI * S_ + m) * D_ + ncol) : "memory");
      asm volatile("global_load_dwordx2 %0, %1, off" : "=v"(xx[r][n]) : "v"(x + (size_t)m * D_ + ncol) : "memory");
    }
  }
#pragma unroll
  for (int n = 0; n < 2; ++n)
    asm volatile("global_load_dwordx2 %0, %1, off" : "=v"(bb[n]) : "v"(b2 + n0 + wc * 32 + n * 16 + 4 * g) : "memory");
  unsigned ad[2][2];
#pragma unroll
  for (int t = 0; t < 2; ++t) {
    const int row = 8 * g + 4 * t + q;
    const int f = ((row >> 1) & 1) | (((row >> 3) & 1) << 1);
#pragma unroll
    for (int n = 0; n < 2; ++n) ad[n][t] = lds_addr(smem) + row * 128 + (((wc * 2 + n) ^ f) * 32) + p * 8;
  }
  f32x4 acc[2][2];
#pragma unroll
  for (int r = 0; r < 2; ++r)
#pragma unroll
    for (int n = 0; n < 2; ++n) acc[r][n] = (f32x4){0.f, 0.f, 0.f, 0.f};
  STAMP(3, 1);
  wait_vm<22>();
  BAR();
  STAMP(3, 2);
#pragma unroll
  for (int ks = 0; ks < 18; ++ks) { pin(an[0][ks]); pin(an[1][ks]); }
  auto grp = [&](auto KB) {
    constexpr int kb = decltype(KB)::value;
    WF w[6][2];
#pragma unroll
    for (int k = 0; k < 6; ++k)
#pragma unroll
      for (int n = 0; n < 2; ++n) {
        if (k == 0) { tr_rd<0 * 4096>(w[0][n].lo, ad[n][0] + kb * 4096); tr_rd<0 * 4096>(w[0][n].hi, ad[n][1] + kb * 4096); }
        if (k == 1) { tr_rd<1 * 4096>(w[1][n].lo, ad[n][0] + kb * 4096); tr_rd<1 * 4096>(w[1][n].hi, ad[n][1] + kb * 4096); }
        if (k == 2) { tr_rd<2 * 4096>(w[2][n].lo, ad[n][0] + kb * 4096); tr_rd<2 * 4096>(w[2][n].hi, ad[n][1] + kb * 4096); }
        if (k == 3) { tr_rd<3 * 4096>(w[3][n].lo, ad[n][0] + kb * 4096); tr_rd<3 * 4096>(w[3][n].hi, ad[n][1] + kb * 4096); }
        if (k == 4) { tr_rd<4 * 4096>(w[4][n].lo, ad[n][0] + kb * 4096); tr_rd<4 * 4096>(w[4][n].hi, ad[n][1] + kb * 4096); }
        if (k == 5) { tr_rd<5 * 4096>(w[5][n].lo, ad[n][0] + kb * 4096); tr_rd<5 * 4096>(w[5][n].hi, ad[n][1] + kb * 4096); }
      }
    wait_lgkm<12>();
#pragma unroll
    for (int k = 0; k < 3; ++k)
#pragma unroll
      for (int n = 0; n < 2; ++n) pin(w[k][n]);
#pragma unroll
    for (int k = 0; k < 3; ++k)
#pragma unroll
      for (int n = 0; n < 2; ++n)
#pragma unroll
        for (int r = 0; r < 2; ++r)
          acc[r][n] = __builtin_amdgcn_mfma_f32_16x16x32_bf16(cat(w[k][n]), an[r][kb + k], acc[r][n], 0, 0, 0);
    wait_lgkm<0>();
#pragma unroll
    for (int k = 3; k < 6; ++k)
#pragma unroll
      for (int n = 0; n < 2; ++n) pin(w[k][n]);
#pragma unroll
    for (int k = 3; k < 6; ++k)
#pragma unroll
      for (int n = 0; n < 2; ++n)
#pragma unroll
        for (int r = 0; r < 2; ++r)
          acc[r][n] = __builtin_amdgcn_mfma_f32_16x16x32_bf16(cat(w[k][n]), an[r][kb + k], acc[r][n], 0, 0, 0);
  };
  grp(IC<0>{}); grp(IC<6>{}); grp(IC<12>{});
  wait_vm<0>();
  STAMP(3, 3);
#pragma unroll
  for (int n = 0; n < 2; ++n) asm volatile("" : "+v"(bb[n]));
#pragma unroll
  for (int r = 0; r < 2; ++r) {
    const int m = m0 + wr * 32 + r * 16 + (lane & 15);
#pragma unroll
    for (int n = 0; n < 2; ++n) {
      const int ncol = n0 + wc * 32 + n * 16 + 4 * g;
      asm volatile("" : "+v"(xx[r][n]));
#pragma unroll
      for (int sI = 0; sI < SPLITK_; ++sI) asm volatile("" : "+v"(py[r][n][sI]));
      f32x4 y = {0.f, 0.f, 0.f, 0.f};
#pragma unroll
      for (int sI = 0; sI < SPLITK_; ++sI) {
        y[0] += lo16(py[r][n][sI][0]); y[1] += hi16(py[r][n][sI][0]);
        y[2] += lo16(py[r][n][sI][1]); y[3] += hi16(py[r][n][sI][1]);
      }
      float bv[4] = {lo16(bb[n][0]), hi16(bb[n][0]), lo16(bb[n][1]), hi16(bb[n][1])};
      float xv[4] = {lo16(xx[r][n][0]), hi16(xx[r][n][0]), lo16(xx[r][n][1]), hi16(xx[r][n][1])};
      float o[4];
#pragma unroll
      for (int j = 0; j < 4; ++j) {
        float yy = y[j] + bv[j];
        float gg = yy / (1.f + __expf(-yy));
        o[j] = xv[j] + acc[r][n][j] * gg;
      }
      u32x2 v = {pack2(o[0], o[1]), pack2(o[2], o[3])};
      *reinterpret_cast<u32x2*>(out + (size_t)m * D_ + ncol) = v;
    }
  }
  STAMP(3, 4);
}

extern "C" void kernel_launch(void* const* d_in, const int* in_sizes, int n_in, void* d_out, int out_size, void* d_ws,
                              size_t ws_size, hipStream_t stream) {
  const u16* x = (const u16*)d_in[0];
  const u16* wqkv = (const u16*)d_in[1];
  const u16* wphi = (const u16*)d_in[2];
  const u16* wo = (const u16*)d_in[3];
  const u16* w1 = (const u16*)d_in[5];
  const u16* b1 = (const u16*)d_in[6];
  const u16* w2 = (const u16*)d_in[7];
  const u16* b2 = (const u16*)d_in[8];
  u16* out = (u16*)d_out;
  char* ws = (char*)d_ws;
  size_t off = 0;
  u16* xnf = (u16*)(ws + off); off += (size_t)S_ * D_ * 2;
  u16* wfold = (u16*)(ws + off); off += (size_t)D_ * NPH_ * 2;
  u16* qkv = (u16*)(ws + off); off += (size_t)96 * S_ * 24 * 2;
  u16* hf = (u16*)(ws + off); off += (size_t)S_ * F_ * 2;
  u16* part = (u16*)(ws + off); off += (size_t)SPLITK_ * S_ * D_ * 2;
  u16* of = (u16*)(ws + off); off += (size_t)S_ * D_ * 2;
  float* pkv = (float*)(ws + off); off += (size_t)256 * 1536 * 4;
  unsigned* cnt = (unsigned*)(ws + off); off += 32 * 32 * 4;

  k0_prep<<<704 + 344, 256, 0, stream>>>(x, wqkv, wphi, xnf, wfold, cnt, w1, w2, wo);
  k1_gemm1<<<256, 512, 0, stream>>>(xnf, wfold, wqkv, w1, b1, qkv, hf);
  k2_gemm2<<<K2_HEADBLK + 256, 256, 0, stream>>>(hf, w2, part, qkv, of, pkv, cnt);
  k3_final<<<16 * 9, 512, 0, stream>>>(of, wo, part, b2, x, out);
}
```

```cpp
#include <hip/hip_runtime.h>
#include <hip/hip_bf16.h>
#include <stdint.h>

constexpr int S_ = 2048, D_ = 576, H_ = 32, DH_ = 18, F_ = 4608, NQKV_ = 1728;
constexpr int NPH_ = 1152;
constexpr int KPH_ = 40;
constexpr int KPHI_ = H_ * KPH_;
constexpr int SPLITK_ = 4;

typedef short bf16x8 __attribute__((ext_vector_type(8)));
typedef short s16x4 __attribute__((ext_vector_type(4)));
typedef float f32x4 __attribute__((ext_vector_type(4)));
typedef unsigned u32x4 __attribute__((ext_vector_type(4)));
typedef unsigned u32x2 __attribute__((ext_vector_type(2)));
typedef __attribute__((address_space(3))) s16x4* lds_s16x4_p;
typedef unsigned short u16;
template <int V> struct IC { static constexpr int value = V; };

#define ST_NT(p, v) (*(p) = (v))
#define WAIT_LGKM0() asm volatile("s_waitcnt lgkmcnt(0)" ::: "memory")
#define BAR() __builtin_amdgcn_s_barrier()

template <int N>
__device__ __forceinline__ void wait_vm() {
  static_assert(N >= 0 && N < 64, "vmcnt range");
  asm volatile("s_waitcnt vmcnt(%0)" ::"n"(N) : "memory");
}

__device__ __forceinline__ float bf2f(u16 v) { return __uint_as_float(((unsigned)v) << 16); }
__device__ __forceinline__ u16 f2bf(float f) {
  __hip_bfloat16 b = __float2bfloat16(f);
  return *reinterpret_cast<u16*>(&b);
}
__device__ __forceinline__ unsigned pack2(float lo, float hi) {
  return (unsigned)f2bf(lo) | ((unsigned)f2bf(hi) << 16);
}
__device__ __forceinline__ float rbf(float f) { return bf2f(f2bf(f)); }
__device__ __forceinline__ float lo16(unsigned u) { return __uint_as_float(u << 16); }
__device__ __forceinline__ float hi16(unsigned u) { return __uint_as_float(u & 0xffff0000u); }

__device__ __forceinline__ void glds16(const void* g, void* l) {
  __builtin_amdgcn_global_load_lds((const unsigned*)g, (unsigned*)l, 16, 0, 0);
}

__device__ __forceinline__ void l2_touch(const void* g, void* ldummy) {
  __builtin_amdgcn_global_load_lds((const unsigned*)g, (unsigned*)ldummy, 4, 0, 0);
}

__device__ __forceinline__ bf16x8 swap_halves_if(bf16x8 v, bool odd) {
  bf16x8 s = __builtin_shufflevector(v, v, 4, 5, 6, 7, 0, 1, 2, 3);
  return odd ? s : v;
}

__device__ __forceinline__ bf16x8 wfrag(const char* img, int o0, int o1, int off) {
  s16x4 a = __builtin_amdgcn_ds_read_tr16_b64_v4i16((lds_s16x4_p)(img + o0 + off));
  s16x4 b = __builtin_amdgcn_ds_read_tr16_b64_v4i16((lds_s16x4_p)(img + o1 + off));
  return __builtin_shufflevector(a, b, 0, 1, 2, 3, 4, 5, 6, 7);
}

struct WF { s16x4 lo, hi; };
template <int OFF>
__device__ __forceinline__ void tr_rd(s16x4& r, unsigned addr) {
  asm volatile("ds_read_b64_tr_b16 %0, %1 offset:%2" : "=v"(r) : "v"(addr), "n"(OFF) : "memory");
}
__device__ __forceinline__ void gld16_asm(bf16x8& r, const void* p) {
  asm volatile("global_load_dwordx4 %0, %1, off" : "=v"(r) : "v"(p) : "memory");
}
template <int N>
__device__ __forceinline__ void wait_lgkm() {
  static_assert(N >= 0 && N <= 15, "lgkmcnt range");
  asm volatile("s_waitcnt lgkmcnt(%0)" ::"n"(N) : "memory");
}
__device__ __forceinline__ void pin(WF& w) { asm volatile("" : "+v"(w.lo), "+v"(w.hi)); }
__device__ __forceinline__ void pin(bf16x8& w) { asm volatile("" : "+v"(w)); }
__device__ __forceinline__ bf16x8 cat(const WF& w) { return __builtin_shufflevector(w.lo, w.hi, 0, 1, 2, 3, 4, 5, 6, 7); }
__device__ __forceinline__ unsigned lds_addr(const void* p) {
  return (unsigned)(uintptr_t)(__attribute__((address_space(3))) const char*)p;
}


__global__ __launch_bounds__(256) void k0_prep(const u16* __restrict__ x, const u16* __restrict__ wqkv,
                                               const u16* __restrict__ wphi, u16* __restrict__ xnf,
                                               u16* __restrict__ wfold, unsigned* __restrict__ cnt,
                                               const u16* __restrict__ w1, const u16* __restrict__ w2,
                                               const u16* __restrict__ wo) {
  __shared__ __attribute__((aligned(16))) float wph[18 * 20];
  const int tid = threadIdx.x, wid = tid >> 6, lane = tid & 63;
  const int bid = blockIdx.x;
  if (bid >= 704) {
    const int i = (bid - 704) * 256 + tid;
    const u16* p = (i < 41472) ? (w1 + (size_t)i * 64) : ((i < 82944) ? (w2 + (size_t)(i - 41472) * 64) : (wo + (size_t)(i - 82944) * 64));
    __builtin_amdgcn_global_load_lds((const unsigned*)p, (unsigned*)(reinterpret_cast<char*>(wph) + lane * 4), 4, 0, 0);
    asm volatile("s_waitcnt vmcnt(0)" ::: "memory");
    return;
  }
  if (bid == 512 && tid < 32) cnt[tid * 32] = 0u;
  if (bid < 512) {
    const int row = bid * 4 + wid;
    const u16* xp = x + (size_t)row * D_;
    u32x4 a = *reinterpret_cast<const u32x4*>(xp + lane * 8);
    u32x4 b = {0u, 0u, 0u, 0u};
    if (lane < 8) b = *reinterpret_cast<const u32x4*>(xp + 512 + lane * 8);
    float v[16];
#pragma unroll
    for (int j = 0; j < 4; ++j) {
      v[2 * j] = lo16(a[j]); v[2 * j + 1] = hi16(a[j]);
      v[8 + 2 * j] = lo16(b[j]); v[8 + 2 * j + 1] = hi16(b[j]);
    }
    float s = 0.f;
#pragma unroll
    for (int j = 0; j < 16; ++j) s += v[j];
#pragma unroll
    for (int o = 32; o >= 1; o >>= 1) s += __shfl_xor(s, o);
    const float mean = s * (1.f / D_);
    float ss = 0.f;
#pragma unroll
    for (int j = 0; j < 8; ++j) { float d = v[j] - mean; ss += d * d; }
    if (lane < 8) {
#pragma unroll
      for (int j = 8; j < 16; ++j) { float d = v[j] - mean; ss += d * d; }
    }
#pragma unroll
    for (int o = 32; o >= 1; o >>= 1) ss += __shfl_xor(ss, o);
    const float rstd = rsqrtf(ss * (1.f / D_) + 1e-5f);
    u32x4 oa, ob;
#pragma unroll
    for (int j = 0; j < 4; ++j) {
      oa[j] = pack2((v[2 * j] - mean) * rstd, (v[2 * j + 1] - mean) * rstd);
      ob[j] = pack2((v[8 + 2 * j] - mean) * rstd, (v[8 + 2 * j + 1] - mean) * rstd);
    }
    {
      const int c = lane;
      u16* dst = xnf + ((size_t)((row >> 4) * 18 + (c >> 2)) * 64 + (c & 3) * 16 + (row & 15)) * 8;
      *reinterpret_cast<u32x4*>(dst) = oa;
    }
    if (lane < 8) {
      const int c = 64 + lane;
      u16* dst = xnf + ((size_t)((row >> 4) * 18 + (c >> 2)) * 64 + (c & 3) * 16 + (row & 15)) * 8;
      *reinterpret_cast<u32x4*>(dst) = ob;
    }
    return;
  }
  {
    const int fb = bid - 512;
    const int hh = fb / 3, k = (fb % 3) * 192 + tid;
    const int h = hh & 31;
    for (int i = tid; i < 18 * 20; i += 256) {
      const int d = i / 20, f = i % 20;
      wph[i] = (f < 18) ? bf2f(wphi[h * 324 + d * 18 + f]) : 0.f;
    }
    unsigned wv[9];
    if (tid < 192) {
      const u16* wp = wqkv + (size_t)k * NQKV_ + hh * 18;
#pragma unroll
      for (int d = 0; d < 9; ++d) wv[d] = *reinterpret_cast<const unsigned*>(wp + 2 * d);
    }
    __syncthreads();
    if (tid < 192) {
      float acc[20];
#pragma unroll
      for (int f = 0; f < 20; ++f) acc[f] = 0.f;
#pragma unroll
      for (int d = 0; d < 18; ++d) {
        const float w = (d & 1) ? hi16(wv[d >> 1]) : lo16(wv[d >> 1]);
#pragma unroll
        for (int f4 = 0; f4 < 5; ++f4) {
          f32x4 pw = *reinterpret_cast<const f32x4*>(wph + d * 20 + f4 * 4);
#pragma unroll
          for (int j = 0; j < 4; ++j) acc[f4 * 4 + j] += w * pw[j];
        }
      }
      u16* op = wfold + (size_t)k * NPH_ + hh * 18;
#pragma unroll
      for (int f = 0; f < 9; ++f) *reinterpret_cast<unsigned*>(op + 2 * f) = pack2(acc[2 * f], acc[2 * f + 1]);
    }
  }
}

constexpr int G1_BUFB = 192 * 128;
constexpr int G1_RING = 4;
constexpr int G1_XOFF = G1_RING * G1_BUFB;
constexpr int G1_BOFF = G1_XOFF + 576 * 32;
constexpr int G1_SMEM = G1_BOFF + 1024;
constexpr int G1_NST = 8;

template <int BASE, int I = 0>
__device__ __forceinline__ void g1_reads(WF (&w)[4], const unsigned (&ad)[4][2]) {
  if constexpr (I < 4) {
    if constexpr (BASE >= 49152) {
      tr_rd<BASE - 49152>(w[I].lo, ad[I][0] + 49152u);
      tr_rd<BASE - 49152>(w[I].hi, ad[I][1] + 49152u);
    } else {
      tr_rd<BASE>(w[I].lo, ad[I][0]);
      tr_rd<BASE>(w[I].hi, ad[I][1]);
    }
    g1_reads<BASE, I + 1>(w, ad);
  }
}
template <int KS>
__device__ __forceinline__ void g1_mma(WF (&w)[4], const bf16x8 (&xa)[2][18], f32x4 (&acc)[2][4]) {
#pragma unroll
  for (int i = 0; i < 4; ++i) pin(w[i]);
#pragma unroll
  for (int i = 0; i < 4; ++i)
#pragma unroll
    for (int r = 0; r < 2; ++r)
      acc[r][i] = __builtin_amdgcn_mfma_f32_16x16x32_bf16(cat(w[i]), xa[r][KS], acc[r][i], 0, 0, 0);
}

__device__ __forceinline__ float gelu_f(float v) {
  constexpr float c1 = -2.f * 0.7978845608028654f * 1.4426950408889634f;
  constexpr float c3 = c1 * 0.044715f;
  const float t = v * v;
  const float e = __builtin_amdgcn_exp2f(v * fmaf(t, c3, c1));
  return v * __builtin_amdgcn_rcpf(1.f + e);
}

typedef float f32x2 __attribute__((ext_vector_type(2)));
__device__ __forceinline__ unsigned cvt_pk(float lo, float hi) {
  unsigned r;
  asm("v_cvt_pk_bf16_f32 %0, %1, %2" : "=v"(r) : "v"(lo), "v"(hi));
  return r;
}
__device__ __forceinline__ unsigned gelu2_pk(f32x2 v) {
  constexpr float c1 = -2.f * 0.7978845608028654f * 1.4426950408889634f;
  constexpr float c3 = c1 * 0.044715f;
  const f32x2 t = v * v;
  const f32x2 pp = t * (f32x2){c3, c3} + (f32x2){c1, c1};
  const f32x2 u = v * pp;
  f32x2 e = {__builtin_amdgcn_exp2f(u[0]), __builtin_amdgcn_exp2f(u[1])};
  e = e + (f32x2){1.f, 1.f};
  const f32x2 r = {__builtin_amdgcn_rcpf(e[0]), __builtin_amdgcn_rcpf(e[1])};
  const f32x2 o = v * r;
  return cvt_pk(o[0], o[1]);
}

__device__ __forceinline__ void g1_store_tile(const f32x4& a, int tile, int mrow, int g, const u16* bias4,
                                              u16* __restrict__ qkv, u16* __restrict__ hf) {
  if (tile < NQKV_ / 16) {
    const int col = tile * 16 + 4 * g;
    const int hh = col / 18, f = col - hh * 18;
    u16* dst = qkv + ((size_t)hh * S_ + mrow) * 24 + f;
    const unsigned v0 = cvt_pk(a[0], a[1]), v1 = cvt_pk(a[2], a[3]);
    if (f != 16) {
      u32x2 v = {v0, v1};
      *reinterpret_cast<u32x2*>(dst) = v;
    } else {
      *reinterpret_cast<unsigned*>(dst) = v0;
      *reinterpret_cast<unsigned*>(qkv + ((size_t)(hh + 1) * S_ + mrow) * 24) = v1;
    }
  } else {
    u32x2 bb = *reinterpret_cast<const u32x2*>(bias4);
    float o[4];
    o[0] = gelu_f(a[0] + lo16(bb[0]));
    o[1] = gelu_f(a[1] + hi16(bb[0]));
    o[2] = gelu_f(a[2] + lo16(bb[1]));
    o[3] = gelu_f(a[3] + hi16(bb[1]));
    u32x2 v = {pack2(o[0], o[1]), pack2(o[2], o[3])};
    const int T = tile - NQKV_ / 16;
    u16* dst = hf + ((size_t)((mrow >> 4) * 144 + (T >> 1)) * 64 + ((T & 1) * 2 + (g >> 1)) * 16 + (mrow & 15)) * 8 + (g & 1) * 4;
    *reinterpret_cast<u32x2*>(dst) = v;
  }
}

__device__ __forceinline__ void g1_addr(unsigned (&ad)[4][2], char* smem, int lane) {
  const int g = lane >> 4, q = (lane & 15) >> 2, p = lane & 3;
#pragma unroll
  for (int t = 0; t < 2; ++t) {
    const int row = 8 * g + 4 * t + q;
    const int f = ((row >> 1) & 1) | (((row >> 3) & 1) << 1);
#pragma unroll
    for (int n = 0; n < 4; ++n) ad[n][t] = lds_addr(smem) + row * 128 + ((n ^ f) * 32) + p * 8;
  }
}

struct G1W { const u16* src; int stride; };

__device__ __forceinline__ void g1_wsetup(G1W& w, int col, int wid, int lane, const u16* wfold, const u16* wqkv, const u16* w1) {
  const int row = wid * 8 + (lane >> 3);
  const int f = ((row >> 1) & 1) | (((row >> 3) & 1) << 1);
  const int ch = (lane & 7) ^ (f << 1);
  if (col < NPH_) { w.src = wfold + (size_t)row * NPH_ + col + ch * 8; w.stride = 64 * NPH_; }
  else if (col < NQKV_) { w.src = wqkv + (size_t)row * NQKV_ + col + ch * 8; w.stride = 64 * NQKV_; }
  else { w.src = w1 + (size_t)row * F_ + (col - NQKV_) + ch * 8; w.stride = 64 * F_; }
}
__device__ __forceinline__ void g1_wissue(G1W& w, char* buf, int wid, int lane) {
#pragma unroll
  for (int i = 0; i < 3; ++i) {
    glds16(w.src, buf + i * 8192 + wid * 1024 + lane * 16);
    w.src += w.stride;
  }
}

template <int U>
__device__ __forceinline__ void g1_unit(G1W& wst, int unit0, const u16* wfold, const u16* wqkv,
                                        const u16* w1, char* smem, bf16x8 (&xa)[2][18], int m0,
                                        u16* __restrict__ qkv, u16* __restrict__ hf, WF (&wA)[4]) {
  int tz = threadIdx.x;
  asm volatile("" : "+v"(tz));
  const int wid = __builtin_amdgcn_readfirstlane(tz >> 6), lane = tz & 63;
  const int g = lane >> 4;
  unsigned ad[4][2];
  g1_addr(ad, smem, lane);
  f32x4 acc[2][4];
#pragma unroll
  for (int r = 0; r < 2; ++r)
#pragma unroll
    for (int n = 0; n < 4; ++n) acc[r][n] = (f32x4){0.f, 0.f, 0.f, 0.f};

  auto stage = [&](auto KC) {
    constexpr int kc = decltype(KC)::value;
    constexpr int s = U * 3 + kc;
    constexpr int CUR = (s % G1_RING) * G1_BUFB;
    if constexpr (U == 0) {
#pragma unroll
      for (int k = 0; k < 6; ++k) { pin(xa[0][kc * 6 + k]); pin(xa[1][kc * 6 + k]); }
    }
    WF wB[4];
    g1_reads<CUR + 1 * 4096>(wB, ad); wait_lgkm<8>(); g1_mma<kc * 6 + 0>(wA, xa, acc);
    g1_reads<CUR + 2 * 4096>(wA, ad); wait_lgkm<8>(); g1_mma<kc * 6 + 1>(wB, xa, acc);
    g1_reads<CUR + 3 * 4096>(wB, ad); wait_lgkm<8>(); g1_mma<kc * 6 + 2>(wA, xa, acc);
    g1_reads<CUR + 4 * 4096>(wA, ad); wait_lgkm<8>(); g1_mma<kc * 6 + 3>(wB, xa, acc);
    g1_reads<CUR + 5 * 4096>(wB, ad); wait_lgkm<8>(); g1_mma<kc * 6 + 4>(wA, xa, acc);
    if constexpr (s < 8) {
      constexpr int nl = (s == 0) ? 19 : ((s == 1) ? 7 : ((s + 2 <= 8) ? 3 : 0));
      constexpr int nst = (U > 0 && kc <= 1) ? G1_NST : 0;
      wait_vm<nl + nst>();
      BAR();
      if constexpr (s + 3 <= 8) {
        constexpr int sn = s + 3;
        if constexpr (sn % 3 == 0) g1_wsetup(wst, (unit0 + sn / 3) * 64, wid, lane, wfold, wqkv, w1);
        g1_wissue(wst, smem + (sn % G1_RING) * G1_BUFB, wid, lane);
      }
      g1_reads<((s + 1) % G1_RING) * G1_BUFB>(wA, ad);
      wait_lgkm<8>();
    } else {
      wait_lgkm<0>();
    }
    g1_mma<kc * 6 + 5>(wB, xa, acc);
  };
  stage(IC<0>{}); stage(IC<1>{}); stage(IC<2>{});
  const u16* bias_lds = reinterpret_cast<const u16*>(smem + G1_BOFF);
  if ((unit0 + U) * 64 < NQKV_) {
#pragma unroll
    for (int n = 0; n < 4; ++n) {
      const int tile = (unit0 + U) * 4 + n;
#pragma unroll
      for (int r = 0; r < 2; ++r)
        g1_store_tile(acc[r][n], tile, m0 + wid * 32 + r * 16 + (lane & 15), g, bias_lds, qkv, hf);
    }
  } else {
    u32x2 bb[4];
#pragma unroll
    for (int n = 0; n < 4; ++n) bb[n] = *reinterpret_cast<const u32x2*>(bias_lds + (U * 4 + n) * 16 + 4 * g);
    const int T0 = (unit0 + U) * 4 - NQKV_ / 16;
#pragma unroll
    for (int r = 0; r < 2; ++r) {
      const int mrow = m0 + wid * 32 + r * 16 + (lane & 15);
      u16* dst = hf + ((size_t)((mrow >> 4) * 144 + (T0 >> 1)) * 64 + (g >> 1) * 16 + (mrow & 15)) * 8 + (g & 1) * 4;
#pragma unroll
      for (int n = 0; n < 4; ++n) {
        const f32x2 v0 = (f32x2){acc[r][n][0], acc[r][n][1]} + (f32x2){lo16(bb[n][0]), hi16(bb[n][0])};
        const f32x2 v1 = (f32x2){acc[r][n][2], acc[r][n][3]} + (f32x2){lo16(bb[n][1]), hi16(bb[n][1])};
        u32x2 v = {gelu2_pk(v0), gelu2_pk(v1)};
        ST_NT(reinterpret_cast<u32x2*>(dst + (n >> 1) * 512 + (n & 1) * 256), v);
      }
    }
  }
}

__global__ __launch_bounds__(512, 2) void k1_gemm1(const u16* __restrict__ xnf, const u16* __restrict__ wfold,
                                                   const u16* __restrict__ wqkv, const u16* __restrict__ w1,
                                                   const u16* __restrict__ b1, u16* __restrict__ qkv,
                                                   u16* __restrict__ hf) {
  __shared__ __attribute__((aligned(16))) char smem[G1_SMEM];
  const int tid = threadIdx.x, wid = tid >> 6, lane = tid & 63;
  const int bid = blockIdx.x;
  const int mt = bid >> 5, grp = bid & 31;
  const int m0 = mt * 256;
  const int unit0 = grp * 3;
  const bool extra = grp >= 20;
  const int xcol = 6144 + (extra ? (grp - 20) : 0) * 16;
  G1W wst;
  g1_wsetup(wst, unit0 * 64, wid, lane, wfold, wqkv, w1);
  bf16x8 xa[2][18];
  const u16* xbase = xnf + ((size_t)((m0 >> 4) + wid * 2) * 18 * 64 + lane) * 8;
#pragma unroll
  for (int st = 0; st < 3; ++st) {
    g1_wissue(wst, smem + st * G1_BUFB, wid, lane);
#pragma unroll
    for (int ks = st * 6; ks < st * 6 + 6; ++ks)
#pragma unroll
      for (int r = 0; r < 2; ++r) gld16_asm(xa[r][ks], xbase + (size_t)(r * 18 + ks) * 512);
  }
#pragma unroll
  for (int i = 0; i < 3; ++i) {
    int ins = wid + 8 * i;
    if (ins >= 18) ins -= 18;
    const int c = ins * 64 + lane;
    const int pr = c >> 1, half = c & 1;
    const int k = (pr & ~0xC) | ((pr & 4) << 1) | ((pr & 8) >> 1);
    glds16(w1 + (size_t)k * F_ + (xcol - NQKV_) + half * 8, smem + G1_XOFF + c * 16);
  }
  {
    const int e = (wid & 1) * 64 + lane;
    int col = (e < 96) ? (unit0 * 64 + 2 * e - NQKV_) : (xcol - NQKV_ + 2 * (e - 96));
    if (col < 0 || e >= 104) col = 0;
    __builtin_amdgcn_global_load_lds((const unsigned*)(b1 + col), (unsigned*)(smem + G1_BOFF + e * 4), 4, 0, 0);
  }
  wait_vm<34>();
  BAR();
  WF wA[4];
  {
    unsigned ad[4][2];
    g1_addr(ad, smem, lane);
    g1_reads<0>(wA, ad);
  }
  g1_unit<0>(wst, unit0, wfold, wqkv, w1, smem, xa, m0, qkv, hf, wA);
  g1_unit<1>(wst, unit0, wfold, wqkv, w1, smem, xa, m0, qkv, hf, wA);
  g1_unit<2>(wst, unit0, wfold, wqkv, w1, smem, xa, m0, qkv, hf, wA);
  if (extra) {
    int tz = threadIdx.x;
    asm volatile("" : "+v"(tz));
    const int wid2 = __builtin_amdgcn_readfirstlane(tz >> 6), lane2 = tz & 63;
    const int g = lane2 >> 4, q = (lane2 & 15) >> 2, p = lane2 & 3;
    unsigned ad[2];
#pragma unroll
    for (int t = 0; t < 2; ++t) {
      const int k = 8 * g + 4 * t + q;
      const int pr = (k & ~0xC) | ((k & 4) << 1) | ((k & 8) >> 1);
      ad[t] = lds_addr(smem) + G1_XOFF + pr * 32 + p * 8;
    }
    f32x4 acc[2] = {(f32x4){0.f, 0.f, 0.f, 0.f}, (f32x4){0.f, 0.f, 0.f, 0.f}};
#pragma unroll
    for (int kb = 0; kb < 18; kb += 6) {
      WF w[6];
      tr_rd<0 * 1024>(w[0].lo, ad[0] + kb * 1024); tr_rd<0 * 1024>(w[0].hi, ad[1] + kb * 1024);
      tr_rd<1 * 1024>(w[1].lo, ad[0] + kb * 1024); tr_rd<1 * 1024>(w[1].hi, ad[1] + kb * 1024);
      tr_rd<2 * 1024>(w[2].lo, ad[0] + kb * 1024); tr_rd<2 * 1024>(w[2].hi, ad[1] + kb * 1024);
      tr_rd<3 * 1024>(w[3].lo, ad[0] + kb * 1024); tr_rd<3 * 1024>(w[3].hi, ad[1] + kb * 1024);
      tr_rd<4 * 1024>(w[4].lo, ad[0] + kb * 1024); tr_rd<4 * 1024>(w[4].hi, ad[1] + kb * 1024);
      tr_rd<5 * 1024>(w[5].lo, ad[0] + kb * 1024); tr_rd<5 * 1024>(w[5].hi, ad[1] + kb * 1024);
      wait_lgkm<0>();
#pragma unroll
      for (int i = 0; i < 6; ++i) pin(w[i]);
#pragma unroll
      for (int i = 0; i < 6; ++i)
#pragma unroll
        for (int r = 0; r < 2; ++r)
          acc[r] = __builtin_amdgcn_mfma_f32_16x16x32_bf16(cat(w[i]), xa[r][kb + i], acc[r], 0, 0, 0);
    }
    const u16* bias_lds = reinterpret_cast<const u16*>(smem + G1_BOFF);
#pragma unroll
    for (int r = 0; r < 2; ++r)
      g1_store_tile(acc[r], xcol / 16, m0 + wid2 * 32 + r * 16 + (lane2 & 15), g, bias_lds + 192 + 4 * g, qkv, hf);
  }
}

constexpr int G2_NT = 9, G2_RB = G2_NT * 32;
constexpr int G2_BUFB = 64 * G2_RB;
constexpr int G2_RING = 4;
constexpr int G2_STAGES = 18;

__device__ __forceinline__ void softmax_pm(const float (&pk)[18], float (&ep)[18], float (&en)[18]) {
  float mx = pk[0], mn = pk[0];
#pragma unroll
  for (int f = 1; f < 18; ++f) {
    mx = fmaxf(mx, pk[f]);
    mn = fminf(mn, pk[f]);
  }
  float sp = 0.f, sn = 0.f;
#pragma unroll
  for (int f = 0; f < 18; ++f) {
    ep[f] = __expf(pk[f] - mx);
    en[f] = __expf(mn - pk[f]);
    sp += ep[f];
    sn += en[f];
  }
  const float rp = 1.f / sp, rn = 1.f / sn;
#pragma unroll
  for (int f = 0; f < 18; ++f) {
    ep[f] *= rp;
    en[f] *= rn;
  }
}

__device__ void head_block(int hb, const u16* __restrict__ qkv, u16* __restrict__ of, float* pkv, unsigned* cnt, char* smem) {
  u16* phiT = reinterpret_cast<u16*>(smem);
  u16* vT = reinterpret_cast<u16*>(smem + 25344);
  float* kvs = reinterpret_cast<float*>(smem + 25344 + 16896);
  const int h = hb >> 3, c8 = hb & 7;
  const int tid = threadIdx.x, wid = tid >> 6, lane = tid & 63;
  const int row = c8 * 256 + tid;
  u32x4 kr[3], vr[3], qr[3];
  {
    const u32x4* kp = reinterpret_cast<const u32x4*>(qkv + ((size_t)(32 + h) * S_ + row) * 24);
    const u32x4* vp = reinterpret_cast<const u32x4*>(qkv + ((size_t)(64 + h) * S_ + row) * 24);
    const u32x4* qp = reinterpret_cast<const u32x4*>(qkv + ((size_t)h * S_ + row) * 24);
#pragma unroll
    for (int d = 0; d < 3; ++d) { kr[d] = kp[d]; vr[d] = vp[d]; qr[d] = qp[d]; }
  }
  for (int i = tid; i < 12 * 264; i += 256) phiT[36 * 264 + i] = 0;
  for (int i = tid; i < 14 * 264; i += 256) vT[18 * 264 + i] = (i < 264) ? (u16)0x3f80 : (u16)0;
  {
    float pk[18];
#pragma unroll
    for (int d = 0; d < 9; ++d) {
      pk[2 * d] = lo16(kr[d >> 2][d & 3]);
      pk[2 * d + 1] = hi16(kr[d >> 2][d & 3]);
    }
    float ep[18], en[18];
    softmax_pm(pk, ep, en);
#pragma unroll
    for (int f = 0; f < 18; ++f) {
      phiT[f * 264 + tid] = f2bf(ep[f]);
      phiT[(18 + f) * 264 + tid] = f2bf(en[f]);
      const unsigned w = vr[(f >> 1) >> 2][(f >> 1) & 3];
      vT[f * 264 + tid] = (u16)((f & 1) ? (w >> 16) : (w & 0xffff));
    }
  }
  __syncthreads();
  {
    f32x4 acc[3][2];
#pragma unroll
    for (int a = 0; a < 3; ++a)
#pragma unroll
      for (int b = 0; b < 2; ++b) acc[a][b] = (f32x4){0.f, 0.f, 0.f, 0.f};
#pragma unroll
    for (int ks = 0; ks < 2; ++ks) {
      const int s0 = wid * 64 + ks * 32 + 8 * (lane >> 4);
      bf16x8 bv[2];
#pragma unroll
      for (int b = 0; b < 2; ++b) bv[b] = *reinterpret_cast<const bf16x8*>(vT + (b * 16 + (lane & 15)) * 264 + s0);
#pragma unroll
      for (int a = 0; a < 3; ++a) {
        bf16x8 av = *reinterpret_cast<const bf16x8*>(phiT + (a * 16 + (lane & 15)) * 264 + s0);
#pragma unroll
        for (int b = 0; b < 2; ++b) acc[a][b] = __builtin_amdgcn_mfma_f32_16x16x32_bf16(av, bv[b], acc[a][b], 0, 0, 0);
      }
    }
#pragma unroll
    for (int a = 0; a < 3; ++a)
#pragma unroll
      for (int b = 0; b < 2; ++b)
#pragma unroll
        for (int j = 0; j < 4; ++j)
          kvs[(wid * 48 + a * 16 + (lane >> 4) * 4 + j) * 32 + b * 16 + (lane & 15)] = acc[a][b][j];
  }
  __syncthreads();
  float* mypart = pkv + (size_t)(h * 8 + c8) * 1536;
#pragma unroll
  for (int it = 0; it < 6; ++it) {
    const int i = it * 256 + tid;
    const float v = kvs[i] + kvs[1536 + i] + kvs[2 * 1536 + i] + kvs[3 * 1536 + i];
    __hip_atomic_store(mypart + i, v, __ATOMIC_RELAXED, __HIP_MEMORY_SCOPE_AGENT);
  }
  asm volatile("s_waitcnt vmcnt(0)" ::: "memory");
  __syncthreads();
  if (tid == 0) __hip_atomic_fetch_add(cnt + h * 32, 1u, __ATOMIC_RELAXED, __HIP_MEMORY_SCOPE_AGENT);
  float ep[18], en[18];
  {
    float pk[18];
#pragma unroll
    for (int d = 0; d < 9; ++d) {
      pk[2 * d] = lo16(qr[d >> 2][d & 3]);
      pk[2 * d + 1] = hi16(qr[d >> 2][d & 3]);
    }
    softmax_pm(pk, ep, en);
#pragma unroll
    for (int f = 0; f < 18; ++f) { ep[f] = rbf(ep[f]); en[f] = rbf(en[f]); }
  }
  if (tid == 0) {
    unsigned spins = 0;
    while (__hip_atomic_load(cnt + h * 32, __ATOMIC_RELAXED, __HIP_MEMORY_SCOPE_AGENT) < 8u) {
      __builtin_amdgcn_s_sleep(1);
      if (++spins > (1u << 24)) break;
    }
  }
  __syncthreads();
  u16* kvT = reinterpret_cast<u16*>(smem);
  u16* phiR = reinterpret_cast<u16*>(smem + 4608);
  {
    float* hp = pkv + (size_t)h * 8 * 1536;
    float pv[5][8];
#pragma unroll
    for (int it = 0; it < 5; ++it) {
      const int i = it * 256 + tid;
#pragma unroll
      for (int c = 0; c < 8; ++c) pv[it][c] = __hip_atomic_load(hp + c * 1536 + i, __ATOMIC_RELAXED, __HIP_MEMORY_SCOPE_AGENT);
    }
    {
      unsigned o[32];
#pragma unroll
      for (int f = 0; f < 9; ++f) { o[f] = pack2(ep[2 * f], ep[2 * f + 1]); o[9 + f] = pack2(en[2 * f], en[2 * f + 1]); }
#pragma unroll
      for (int f = 18; f < 32; ++f) o[f] = 0u;
#pragma unroll
      for (int c = 0; c < 8; ++c) {
        u32x4 v = {o[4 * c], o[4 * c + 1], o[4 * c + 2], o[4 * c + 3]};
        *reinterpret_cast<u32x4*>(phiR + tid * 72 + c * 8) = v;
      }
    }
#pragma unroll
    for (int it = 0; it < 8; ++it) {
      const int i = it * 256 + tid;
      const int f = i >> 5, d = i & 31;
      float v = 0.f;
      if (it < 5) {
#pragma unroll
        for (int c = 0; c < 8; ++c) v += pv[it < 5 ? it : 0][c];
      }
      kvT[d * 72 + f] = ((d < 19) && (f < 36)) ? f2bf(v) : (u16)0;
    }
  }
  __syncthreads();
  {
    const int g = lane >> 4;
    bf16x8 ka[2][2];
#pragma unroll
    for (int t = 0; t < 2; ++t)
#pragma unroll
      for (int ks = 0; ks < 2; ++ks)
        ka[t][ks] = *reinterpret_cast<const bf16x8*>(kvT + (t * 16 + (lane & 15)) * 72 + ks * 32 + 8 * g);
#pragma unroll
    for (int rt = 0; rt < 4; ++rt) {
      const int rl = wid * 64 + rt * 16 + (lane & 15);
      f32x4 c0 = {0.f, 0.f, 0.f, 0.f}, c1 = {0.f, 0.f, 0.f, 0.f};
#pragma unroll
      for (int ks = 0; ks < 2; ++ks) {
        const bf16x8 pb = *reinterpret_cast<const bf16x8*>(phiR + rl * 72 + ks * 32 + 8 * g);
        c0 = __builtin_amdgcn_mfma_f32_16x16x32_bf16(ka[0][ks], pb, c0, 0, 0, 0);
        c1 = __builtin_amdgcn_mfma_f32_16x16x32_bf16(ka[1][ks], pb, c1, 0, 0, 0);
      }
      const float den = __shfl(c1[2], lane & 15);
      const float rd = 1.f / (rbf(den) + 1e-6f);
      const int grow = c8 * 256 + rl;
      u16* obase = of + (size_t)(grow >> 4) * 18 * 512 + (grow & 15) * 8;
#pragma unroll
      for (int j2 = 0; j2 < 2; ++j2) {
        const int k = h * 18 + 4 * g + 2 * j2;
        *reinterpret_cast<unsigned*>(obase + (k >> 5) * 512 + ((k >> 3) & 3) * 128 + (k & 7)) =
            pack2(rbf(c0[2 * j2]) * rd, rbf(c0[2 * j2 + 1]) * rd);
      }
      if (g == 0) {
        const int k = h * 18 + 16;
        *reinterpret_cast<unsigned*>(obase + (k >> 5) * 512 + ((k >> 3) & 3) * 128 + (k & 7)) =
            pack2(rbf(c1[0]) * rd, rbf(c1[1]) * rd);
      }
    }
  }
}

template <int BASE, int N0, int CNT, int I = 0>
__device__ __forceinline__ void g2_wreads(WF (&w)[5], unsigned a0, unsigned a1) {
  if constexpr (I < CNT) {
    tr_rd<BASE + (N0 + I) * 32>(w[I].lo, a0);
    tr_rd<BASE + (N0 + I) * 32>(w[I].hi, a1);
    g2_wreads<BASE, N0, CNT, I + 1>(w, a0, a1);
  }
}
template <int N0, int CNT>
__device__ __forceinline__ void g2_mma(WF (&w)[5], const bf16x8 (&ac)[2], f32x4 (&acc)[2][G2_NT]) {
#pragma unroll
  for (int i = 0; i < CNT; ++i) pin(w[i]);
#pragma unroll
  for (int i = 0; i < CNT; ++i)
#pragma unroll
    for (int r = 0; r < 2; ++r)
      acc[r][N0 + i] = __builtin_amdgcn_mfma_f32_16x16x32_bf16(cat(w[i]), ac[r], acc[r][N0 + i], 0, 0, 0);
}
constexpr int K2_HEADBLK = 256;

__global__ __launch_bounds__(256, 2) void k2_gemm2(const u16* __restrict__ hf, const u16* __restrict__ w2,
                                                   u16* __restrict__ part, const u16* __restrict__ qkv,
                                                   u16* __restrict__ of, float* pkv, unsigned* cnt) {
  __shared__ __attribute__((aligned(16))) char smem[G2_RING * G2_BUFB];
  const int tid = threadIdx.x, wid = tid >> 6, lane = tid & 63;
  int bid = blockIdx.x;
  if (bid < K2_HEADBLK) { head_block(bid, qkv, of, pkv, cnt, smem); return; }
  bid -= K2_HEADBLK;
  __builtin_amdgcn_s_setprio(3);
  const int xcd = bid & 7, loc = bid >> 3;
  const int sp = xcd >> 1, mt = (xcd & 1) * 8 + (loc >> 2), nt4 = loc & 3;
  const int m0 = mt * 128, n0 = nt4 * 144, k0 = sp * 1152;
  const int g = lane >> 4, q = (lane & 15) >> 2, p = lane & 3;
  const bool odd = g & 1;
  const unsigned wa0 = lds_addr(smem) + (8 * g + 4 * (g & 1) + q) * G2_RB + p * 8;
  const unsigned wa1 = lds_addr(smem) + (8 * g + 4 * (1 - (g & 1)) + q) * G2_RB + p * 8;
  f32x4 acc[2][G2_NT];
#pragma unroll
  for (int r = 0; r < 2; ++r)
#pragma unroll
    for (int n = 0; n < G2_NT; ++n) acc[r][n] = (f32x4){0.f, 0.f, 0.f, 0.f};
  const u16* wp[5];
  int wdst[5];
#pragma unroll
  for (int i = 0; i < 5; ++i) {
    int ins = wid + 4 * i;
    if (ins >= 18) ins -= 18;
    int c = ins * 64 + lane;
    int row = c / 18, cc = c % 18;
    wp[i] = w2 + (size_t)(k0 + row) * D_ + n0 + cc * 8;
    wdst[i] = c * 16;
  }
  const u16* ap[2];
#pragma unroll
  for (int r = 0; r < 2; ++r) ap[r] = hf + ((size_t)((m0 >> 4) + wid * 2 + r) * 144 + (k0 >> 5)) * 512 + lane * 8;
  bf16x8 an[4][2][2];
  auto issue = [&](auto SL) {
    constexpr int slot = decltype(SL)::value;
    char* buf = smem + slot * G2_BUFB;
#pragma unroll
    for (int i = 0; i < 5; ++i) { glds16(wp[i], buf + wdst[i]); wp[i] += 64 * D_; }
#pragma unroll
    for (int r = 0; r < 2; ++r) {
      gld16_asm(an[slot][r][0], ap[r]);
      gld16_asm(an[slot][r][1], ap[r] + 512);
      ap[r] += 1024;
    }
  };
  issue(IC<0>{});
  issue(IC<1>{});
  issue(IC<2>{});
  wait_vm<18>();
  BAR();
  WF wA[5], wB[5];
  g2_wreads<0, 0, 5>(wA, wa0, wa1);
  auto stage = [&](auto U, auto LAST) {
    constexpr int u = decltype(U)::value;
    constexpr int rem = decltype(LAST)::value;
    constexpr int BASE = u * G2_BUFB;
    bf16x8 c0[2], c1[2];
#pragma unroll
    for (int r = 0; r < 2; ++r) {
      pin(an[u][r][0]); pin(an[u][r][1]);
      c0[r] = swap_halves_if(an[u][r][0], odd);
      c1[r] = swap_halves_if(an[u][r][1], odd);
    }
    g2_wreads<BASE, 5, 4>(wB, wa0, wa1);
    wait_lgkm<8>();
    g2_mma<0, 5>(wA, c0, acc);
    g2_wreads<BASE + 32 * G2_RB, 0, 5>(wA, wa0, wa1);
    wait_lgkm<10>();
    g2_mma<5, 4>(wB, c0, acc);
    g2_wreads<BASE + 32 * G2_RB, 5, 4>(wB, wa0, wa1);
    wait_lgkm<8>();
    g2_mma<0, 5>(wA, c1, acc);
    if constexpr (rem >= 1) {
      if constexpr (rem >= 2) wait_vm<9>(); else wait_vm<0>();
      BAR();
      if constexpr (rem >= 3) issue(IC<(u + 3) & 3>{});
      g2_wreads<((u + 1) & 3) * G2_BUFB, 0, 5>(wA, wa0, wa1);
      wait_lgkm<10>();
    } else {
      wait_lgkm<0>();
    }
    g2_mma<5, 4>(wB, c1, acc);
  };
#pragma unroll 1
  for (int it = 0; it < 3; ++it) {
    stage(IC<0>{}, IC<3>{}); stage(IC<1>{}, IC<3>{}); stage(IC<2>{}, IC<3>{}); stage(IC<3>{}, IC<3>{});
  }
  stage(IC<0>{}, IC<3>{}); stage(IC<1>{}, IC<3>{}); stage(IC<2>{}, IC<3>{});
  stage(IC<3>{}, IC<2>{}); stage(IC<0>{}, IC<1>{}); stage(IC<1>{}, IC<0>{});
#pragma unroll
  for (int r = 0; r < 2; ++r) {
    const int m = m0 + wid * 32 + r * 16 + (lane & 15);
    u16* pp = part + ((size_t)sp * S_ + m) * D_ + n0 + 4 * g;
#pragma unroll
    for (int n = 0; n < G2_NT; ++n) {
      u32x2 v = {pack2(acc[r][n][0], acc[r][n][1]), pack2(acc[r][n][2], acc[r][n][3])};
      ST_NT(reinterpret_cast<u32x2*>(pp + n * 16), v);
    }
  }
  asm volatile("s_waitcnt vmcnt(0)" ::: "memory");
}

__global__ __launch_bounds__(512, 2) void k3_final(const u16* __restrict__ of, const u16* __restrict__ wo,
                                                   const u16* __restrict__ part, const u16* __restrict__ b2,
                                                   const u16* __restrict__ x, u16* __restrict__ out) {
  __shared__ __attribute__((aligned(16))) char smem[576 * 128];
  const int tid = threadIdx.x, wid = tid >> 6, lane = tid & 63;
  const int bid = blockIdx.x;
  const int xcd = bid & 7, loc = bid >> 3;
  const int mt = xcd * 2 + loc / 9, nt = loc % 9;
  const int m0 = mt * 128, n0 = nt * 64;
  const int g = lane >> 4, q = (lane & 15) >> 2, p = lane & 3;
  const int wr = wid >> 1, wc = wid & 1;
#pragma unroll
  for (int i = 0; i < 9; ++i) {
    const int row = (wid * 9 + i) * 8 + (lane >> 3);
    const int f = ((row >> 1) & 1) | (((row >> 3) & 1) << 1);
    const int ch = (lane & 7) ^ (f << 1);
    glds16(wo + (size_t)row * D_ + n0 + ch * 8, smem + (wid * 9 + i) * 1024 + lane * 16);
  }
  bf16x8 an[2][18];
#pragma unroll
  for (int ks = 0; ks < 18; ++ks)
#pragma unroll
    for (int r = 0; r < 2; ++r)
      gld16_asm(an[r][ks], of + ((size_t)(((m0 >> 4) + wr * 2 + r) * 18 + ks) * 64 + lane) * 8);
  u32x2 py[2][2][4];
  u32x2 xx[2][2], bb[2];
#pragma unroll
  for (int r = 0; r < 2; ++r) {
    const int m = m0 + wr * 32 + r * 16 + (lane & 15);
#pragma unroll
    for (int n = 0; n < 2; ++n) {
      const int ncol = n0 + wc * 32 + n * 16 + 4 * g;
#pragma unroll
      for (int sI = 0; sI < SPLITK_; ++sI)
        asm volatile("global_load_dwordx2 %0, %1, off" : "=v"(py[r][n][sI]) : "v"(part + ((size_t)sI * S_ + m) * D_ + ncol) : "memory");
      asm volatile("global_load_dwordx2 %0, %1, off" : "=v"(xx[r][n]) : "v"(x + (size_t)m * D_ + ncol) : "memory");
    }
  }
#pragma unroll
  for (int n = 0; n < 2; ++n)
    asm volatile("global_load_dwordx2 %0, %1, off" : "=v"(bb[n]) : "v"(b2 + n0 + wc * 32 + n * 16 + 4 * g) : "memory");
  unsigned ad[2][2];
#pragma unroll
  for (int t = 0; t < 2; ++t) {
    const int row = 8 * g + 4 * t + q;
    const int f = ((row >> 1) & 1) | (((row >> 3) & 1) << 1);
#pragma unroll
    for (int n = 0; n < 2; ++n) ad[n][t] = lds_addr(smem) + row * 128 + (((wc * 2 + n) ^ f) * 32) + p * 8;
  }
  f32x4 acc[2][2];
#pragma unroll
  for (int r = 0; r < 2; ++r)
#pragma unroll
    for (int n = 0; n < 2; ++n) acc[r][n] = (f32x4){0.f, 0.f, 0.f, 0.f};
  wait_vm<22>();
  BAR();
#pragma unroll
  for (int ks = 0; ks < 18; ++ks) { pin(an[0][ks]); pin(an[1][ks]); }
  auto grp = [&](auto KB) {
    constexpr int kb = decltype(KB)::value;
    WF w[6][2];
#pragma unroll
    for (int k = 0; k < 6; ++k)
#pragma unroll
      for (int n = 0; n < 2; ++n) {
        if (k == 0) { tr_rd<0 * 4096>(w[0][n].lo, ad[n][0] + kb * 4096); tr_rd<0 * 4096>(w[0][n].hi, ad[n][1] + kb * 4096); }
        if (k == 1) { tr_rd<1 * 4096>(w[1][n].lo, ad[n][0] + kb * 4096); tr_rd<1 * 4096>(w[1][n].hi, ad[n][1] + kb * 4096); }
        if (k == 2) { tr_rd<2 * 4096>(w[2][n].lo, ad[n][0] + kb * 4096); tr_rd<2 * 4096>(w[2][n].hi, ad[n][1] + kb * 4096); }
        if (k == 3) { tr_rd<3 * 4096>(w[3][n].lo, ad[n][0] + kb * 4096); tr_rd<3 * 4096>(w[3][n].hi, ad[n][1] + kb * 4096); }
        if (k == 4) { tr_rd<4 * 4096>(w[4][n].lo, ad[n][0] + kb * 4096); tr_rd<4 * 4096>(w[4][n].hi, ad[n][1] + kb * 4096); }
        if (k == 5) { tr_rd<5 * 4096>(w[5][n].lo, ad[n][0] + kb * 4096); tr_rd<5 * 4096>(w[5][n].hi, ad[n][1] + kb * 4096); }
      }
    wait_lgkm<12>();
#pragma unroll
    for (int k = 0; k < 3; ++k)
#pragma unroll
      for (int n = 0; n < 2; ++n) pin(w[k][n]);
#pragma unroll
    for (int k = 0; k < 3; ++k)
#pragma unroll
      for (int n = 0; n < 2; ++n)
#pragma unroll
        for (int r = 0; r < 2; ++r)
          acc[r][n] = __builtin_amdgcn_mfma_f32_16x16x32_bf16(cat(w[k][n]), an[r][kb + k], acc[r][n], 0, 0, 0);
    wait_lgkm<0>();
#pragma unroll
    for (int k = 3; k < 6; ++k)
#pragma unroll
      for (int n = 0; n < 2; ++n) pin(w[k][n]);
#pragma unroll
    for (int k = 3; k < 6; ++k)
#pragma unroll
      for (int n = 0; n < 2; ++n)
#pragma unroll
        for (int r = 0; r < 2; ++r)
          acc[r][n] = __builtin_amdgcn_mfma_f32_16x16x32_bf16(cat(w[k][n]), an[r][kb + k], acc[r][n], 0, 0, 0);
  };
  grp(IC<0>{}); grp(IC<6>{}); grp(IC<12>{});
  wait_vm<0>();
#pragma unroll
  for (int n = 0; n < 2; ++n) asm volatile("" : "+v"(bb[n]));
#pragma unroll
  for (int r = 0; r < 2; ++r) {
    const int m = m0 + wr * 32 + r * 16 + (lane & 15);
#pragma unroll
    for (int n = 0; n < 2; ++n) {
      const int ncol = n0 + wc * 32 + n * 16 + 4 * g;
      asm volatile("" : "+v"(xx[r][n]));
#pragma unroll
      for (int sI = 0; sI < SPLITK_; ++sI) asm volatile("" : "+v"(py[r][n][sI]));
      f32x4 y = {0.f, 0.f, 0.f, 0.f};
#pragma unroll
      for (int sI = 0; sI < SPLITK_; ++sI) {
        y[0] += lo16(py[r][n][sI][0]); y[1] += hi16(py[r][n][sI][0]);
        y[2] += lo16(py[r][n][sI][1]); y[3] += hi16(py[r][n][sI][1]);
      }
      float bv[4] = {lo16(bb[n][0]), hi16(bb[n][0]), lo16(bb[n][1]), hi16(bb[n][1])};
      float xv[4] = {lo16(xx[r][n][0]), hi16(xx[r][n][0]), lo16(xx[r][n][1]), hi16(xx[r][n][1])};
      float o[4];
#pragma unroll
      for (int j = 0; j < 4; ++j) {
        float yy = y[j] + bv[j];
        float gg = yy / (1.f + __expf(-yy));
        o[j] = xv[j] + acc[r][n][j] * gg;
      }
      u32x2 v = {pack2(o[0], o[1]), pack2(o[2], o[3])};
      *reinterpret_cast<u32x2*>(out + (size_t)m * D_ + ncol) = v;
    }
  }
}

extern "C" void kernel_launch(void* const* d_in, const int* in_sizes, int n_in, void* d_out, int out_size, void* d_ws,
                              size_t ws_size, hipStream_t stream) {
  const u16* x = (const u16*)d_in[0];
  const u16* wqkv = (const u16*)d_in[1];
  const u16* wphi = (const u16*)d_in[2];
  const u16* wo = (const u16*)d_in[3];
  const u16* w1 = (const u16*)d_in[5];
  const u16* b1 = (const u16*)d_in[6];
  const u16* w2 = (const u16*)d_in[7];
  const u16* b2 = (const u16*)d_in[8];
  u16* out = (u16*)d_out;
  char* ws = (char*)d_ws;
  size_t off = 0;
  u16* xnf = (u16*)(ws + off); off += (size_t)S_ * D_ * 2;
  u16* wfold = (u16*)(ws + off); off += (size_t)D_ * NPH_ * 2;
  u16* qkv = (u16*)(ws + off); off += (size_t)96 * S_ * 24 * 2;
  u16* hf = (u16*)(ws + off); off += (size_t)S_ * F_ * 2;
  u16* part = (u16*)(ws + off); off += (size_t)SPLITK_ * S_ * D_ * 2;
  u16* of = (u16*)(ws + off); off += (size_t)S_ * D_ * 2;
  float* pkv = (float*)(ws + off); off += (size_t)256 * 1536 * 4;
  unsigned* cnt = (unsigned*)(ws + off); off += 32 * 32 * 4;

  k0_prep<<<704 + 344, 256, 0, stream>>>(x, wqkv, wphi, xnf, wfold, cnt, w1, w2, wo);
  k1_gemm1<<<256, 512, 0, stream>>>(xnf, wfold, wqkv, w1, b1, qkv, hf);
  k2_gemm2<<<K2_HEADBLK + 256, 256, 0, stream>>>(hf, w2, part, qkv, of, pkv, cnt);
  k3_final<<<16 * 9, 512, 0, stream>>>(of, wo, part, b2, x, out);
}
```
